```python
import torch

def _softmax(x, axis=-1):
    return torch.softmax(x, dim=axis)

def _gelu(x):
    return torch.nn.functional.gelu(x, approximate='tanh')

def _silu(x):
    return torch.nn.functional.silu(x)

def _sigmoid(x):
    return torch.sigmoid(x)

def _layernorm(x, axis=-1, eps=1e-05):
    m = x.mean(dim=axis, keepdim=True)
    v = x.var(dim=axis, keepdim=True, unbiased=False)
    return (x - m) / torch.sqrt(v + eps)

def _scatter_add(tgt, idx, src, axis=0):
    return tgt.clone().index_add_(axis, idx, src)

def _segment_sum(src, seg, n):
    out = torch.zeros((n,) + src.shape[1:], dtype=src.dtype)
    return out.index_add_(0, seg, src)

def _pad(x, pw):
    flat = tuple((v for pair in reversed(pw) for v in pair))
    return torch.nn.functional.pad(x, flat)
kernel_name = 'adaLN_cond__compressed_kv_cla__dense_activation_relu__decode'

def setup_inputs():

    _w = lambda a: (lambda t: t.to(torch.bfloat16) if t.is_floating_point() else t)(torch.as_tensor(a))

    def _A_setup():
        B, H, Sq, Sk, D, M = (8, 32, 1, 4096, 128, 512)
        Dc = 4 * D
        return {'x': _w((lambda *s: torch.randn(*s, dtype=torch.bfloat16) * 0.05)(B, Sq, M)), 'c_kv': _w((lambda *s: torch.randn(*s, dtype=torch.bfloat16) * 1.6)(B, Sk, Dc)), 'w_q': _w((lambda *s: torch.randn(*s, dtype=torch.bfloat16) * 1.6)(M, H, D) * M ** (-0.5)), 'w_uk': _w((lambda *s: torch.randn(*s, dtype=torch.bfloat16) * 1.6)(Dc, H, D) * Dc ** (-0.5)), 'w_uv': _w((lambda *s: torch.randn(*s, dtype=torch.bfloat16) * 1.6)(Dc, H, D) * Dc ** (-0.5)), 'w_o': _w((lambda *s: torch.randn(*s, dtype=torch.bfloat16) * 1.6)(H, D, M) * (H * D) ** (-0.5))}
    _A_in = _A_setup()

    def _B_setup():
        B, S, D, Dff = (8, 1, 512, 1792)
        return {'x': _w((lambda *s: torch.randn(*s, dtype=torch.bfloat16) * 0.05)(B, S, D)), 'w1': _w((lambda *s: torch.randn(*s, dtype=torch.bfloat16) * 0.05)(D, Dff)), 'w2': _w((lambda *s: torch.randn(*s, dtype=torch.bfloat16) * 0.05)(Dff, D))}
    _B_in = _B_setup()
    return {'A_x': _A_in['x'], 'A_c_kv': _A_in['c_kv'], 'A_w_q': _A_in['w_q'], 'A_w_uk': _A_in['w_uk'], 'A_w_uv': _A_in['w_uv'], 'A_w_o': _A_in['w_o'], 'B_x': _B_in['x'], 'B_w1': _B_in['w1'], 'B_w2': _B_in['w2'], 'c': _w((lambda *s: torch.randn(*s, dtype=torch.bfloat16) * 0.2)(8, 512) * 8.0), 'w_mod': _w((lambda *s: torch.randn(*s, dtype=torch.bfloat16) * 0.2)(512, 6 * 512) * 512 ** (-0.5) * 8.0)}

def reference(A_x, A_c_kv, A_w_q, A_w_uk, A_w_uv, A_w_o, B_x, B_w1, B_w2, c, w_mod):

    def _A_ref(x, c_kv, w_q, w_uk, w_uv, w_o):
        B, Sq, M = (x.shape[0], x.shape[1], x.shape[2])
        Sk = c_kv.shape[1]
        H, D = (w_q.shape[1], w_q.shape[2])
        q = torch.einsum('bqm,mhd->bhqd', x, w_q)
        k = torch.einsum('bkc,chd->bhkd', c_kv, w_uk)
        v = torch.einsum('bkc,chd->bhkd', c_kv, w_uv)
        scores = torch.einsum('bhqd,bhkd->bhqk', q, k) * D ** (-0.5)
        off = Sk - Sq
        qi = torch.arange(Sq)[:, None]
        kj = torch.arange(Sk)[None, :]
        cmask = kj <= qi + off
        scores = torch.where(cmask[None, None], scores, -1000000000.0)
        p = _softmax(scores, axis=-1)
        ctx = torch.einsum('bhqk,bhkd->bhqd', p, v)
        return torch.einsum('bhqd,hdm->bqm', ctx, w_o)

    def _B_ref(x, w1, w2):
        h = torch.einsum('bsd,df->bsf', x, w1)
        zero = torch.zeros_like(h)
        h = torch.maximum(h, zero)
        return torch.einsum('bsf,fd->bsd', h, w2)
    x = A_x
    mod = (_silu(c) @ w_mod).reshape(c.shape[0], 6, 1, -1)
    sa, ca, ga, sb, cb, gb = (mod[:, 0].clone(), mod[:, 1].clone(), mod[:, 2].clone(), mod[:, 3].clone(), mod[:, 4].clone(), mod[:, 5].clone())
    h = _layernorm(x, axis=-1) * (1 + ca) + sa
    x = x + ga * _A_ref(h, A_c_kv, A_w_q, A_w_uk, A_w_uv, A_w_o)
    h = _layernorm(x, axis=-1) * (1 + cb) + sb
    x = x + gb * _B_ref(h, B_w1, B_w2)
    return x
```

```cpp
#define NQ 8
#define NM 0
#define MLMASK 1
#define FARF 1
#define CALHIT 290
#define CALTH 60
#define CPFE 37800
#define CPFW 0
#define CPFO 2048
#define PFPOS 1
#include <hip/hip_runtime.h>
#include <hip/hip_bf16.h>
#include <stdint.h>

#define NB 8
#define NH 32
#define SK 4096
#define MM 512
#define DC 512
#define DFF 1792
#define NSPLIT 32
#define KEYS_PER (SK / NSPLIT)
#define NMLP 112
#define NDOWN 32
#define SLPD 12
#define SLPG 8
#define QD 4

using u16x8 = __attribute__((ext_vector_type(8))) unsigned short;
using s16x4 = __attribute__((ext_vector_type(4))) short;
using bf16x8 = __attribute__((ext_vector_type(8))) short;
using f32x16 = __attribute__((ext_vector_type(16))) float;
using f32x4 = __attribute__((ext_vector_type(4))) float;
using u32x4 = __attribute__((ext_vector_type(4))) unsigned;
using u32x2 = __attribute__((ext_vector_type(2))) unsigned;

#define GAS __attribute__((address_space(1)))
#define LAS __attribute__((address_space(3)))
typedef GAS unsigned gu32;
typedef GAS unsigned long long gu64;
#define RLX_AGENT __ATOMIC_RELAXED, __HIP_MEMORY_SCOPE_AGENT
#define SENT 0xffffffffu
#define TAGC 0x9e3779b9u

__device__ __forceinline__ float bf2f(unsigned short u) { return __uint_as_float(((unsigned)u) << 16); }
typedef __attribute__((ext_vector_type(2))) __bf16 bf16x2_t;
typedef __attribute__((ext_vector_type(2))) float f32x2_t;
__device__ __forceinline__ unsigned pk2(float a, float b) {
    f32x2_t v = {a, b};
    bf16x2_t r = __builtin_convertvector(v, bf16x2_t);
    return __builtin_bit_cast(unsigned, r);
}
__device__ __forceinline__ unsigned pk2s(float a, float b) {
    const unsigned r = pk2(a, b);
    return r == SENT ? 0x7fc07fc0u : r;
}
__device__ __forceinline__ unsigned f32s(float a) {
    const unsigned r = __float_as_uint(a);
    return r == SENT ? 0x7fc00000u : r;
}
__device__ __forceinline__ unsigned short f2bf(float f) { return (unsigned short)(pk2(f, 0.f) & 0xffffu); }
__device__ __forceinline__ float rbf(float f) { return __uint_as_float(pk2(f, 0.f) << 16); }
__device__ __forceinline__ bool full4(const u32x4& v) { return (v[0] != SENT) & (v[1] != SENT) & (v[2] != SENT) & (v[3] != SENT); }

__device__ __forceinline__ unsigned umax3(unsigned a, unsigned b, unsigned c) { return max(max(a, b), c); }
__device__ __forceinline__ unsigned mx4(const u32x4& v, unsigned m) { return umax3(v[2], v[3], umax3(v[0], v[1], m)); }
__device__ __forceinline__ float fmax3(float a, float b, float c) { float r; asm("v_max3_f32 %0, %1, %2, %3" : "=v"(r) : "v"(a), "v"(b), "v"(c)); return r; }
__device__ __forceinline__ float dpp_add(float v, const int ctrl_sel) {
    int r;
    const int iv = __float_as_int(v);
    if (ctrl_sel == 0) r = __builtin_amdgcn_update_dpp(0, iv, 0xB1, 0xf, 0xf, true);
    else if (ctrl_sel == 1) r = __builtin_amdgcn_update_dpp(0, iv, 0x4E, 0xf, 0xf, true);
    else if (ctrl_sel == 2) r = __builtin_amdgcn_update_dpp(0, iv, 0x141, 0xf, 0xf, true);
    else r = __builtin_amdgcn_update_dpp(0, iv, 0x140, 0xf, 0xf, true);
    return v + __int_as_float(r);
}
#define WS_EPOCH 0
#define WS_FLAGS 8192
#define WS_G2 65536
#define WS_H 589824
#define WS_G3 851968
#define WS_ML 12582912
#define WS_MOD (WS_ML + 65536)
#define WS_X1 (WS_ML + 196608)
#define WS_OUTP 1048576
#define WS_PART 2097152
#define WS_MODP 10485760
#define WS_END 12845056

#define LP(T, off) ((LAS T*)(size_t)(unsigned)(off))
#define STAMP(k) do {} while (0)
#define STAMPB(k) do {} while (0)
#define STAMPW(k, t) do {} while (0)
#define STAMP_INIT() do {} while (0)
#define STAMP_FLUSH() do {} while (0)
__device__ __forceinline__ s16x4 tr4(unsigned a) { return __builtin_amdgcn_ds_read_tr16_b64_v4i16(LP(s16x4, a)); }
__device__ __forceinline__ bf16x8 cat8(s16x4 a, s16x4 b) { return __builtin_shufflevector(a, b, 0, 1, 2, 3, 4, 5, 6, 7); }
__device__ __forceinline__ bf16x8 tr_frag(unsigned a0, unsigned a1) { return cat8(tr4(a0), tr4(a1)); }

__device__ __forceinline__ unsigned swz(unsigned row) { return ((row & 3) << 2) | ((row >> 2) & 3); }
__device__ __forceinline__ unsigned tile_off(unsigned key, unsigned chunk) {
    return ((key >> 5) * 4 + (chunk >> 4)) * 8192u + 256u * (key & 31) + 16u * ((chunk & 15) ^ swz(key & 31));
}
__device__ __forceinline__ void wg_barrier() { asm volatile("s_waitcnt lgkmcnt(0)\n\ts_barrier" ::: "memory"); }
__device__ __forceinline__ void dma16(unsigned voff, const void* sbase, unsigned lds_dst) {
    asm volatile("s_mov_b32 m0, %2\n\ts_nop 0\n\tglobal_load_lds_dwordx4 %0, %1 nt" :: "v"(voff), "s"(sbase), "s"(lds_dst) : "memory", "m0");
}
#define ARRIVE(base, off) asm volatile("s_waitcnt lgkmcnt(0)\n\ts_mov_b64 exec, 1\n\tds_add_u32 %0, %1 offset:%2\n\ts_mov_b64 exec, -1" :: "v"(base), "v"(one), "n"(off) : "memory")
#define ARRIVE_RTN(res, base, off) do { unsigned t_; asm volatile("s_waitcnt lgkmcnt(0)\n\ts_mov_b64 exec, 1\n\tds_add_rtn_u32 %1, %2, %3 offset:%4\n\ts_mov_b64 exec, -1\n\ts_waitcnt lgkmcnt(0)\n\tv_readfirstlane_b32 %0, %1" \
    : "=s"(res), "=&v"(t_) : "v"(base), "v"(one), "n"(off) : "memory"); } while (0)
#define LWAIT(base, off, n) do { unsigned t_, s_; asm volatile("1:\n\tds_read_b32 %1, %2 offset:%3\n\ts_waitcnt lgkmcnt(0)\n\tv_readfirstlane_b32 %0, %1\n\ts_cmp_lt_u32 %0, %4\n\ts_cbranch_scc1 1b" \
    : "=&s"(s_), "=&v"(t_) : "v"(base), "n"(off), "s"((unsigned)(n)) : "memory", "scc"); } while (0)
__device__ __forceinline__ unsigned long long lds_ld64(unsigned addr) {
    unsigned long long v;
    asm volatile("ds_read_b64 %0, %1\n\ts_waitcnt lgkmcnt(0)" : "=v"(v) : "v"(addr) : "memory");
    return v;
}
constexpr unsigned kSpinCap = 1u << 20;
#define SLP 2
__device__ __forceinline__ bool spin_fail(unsigned& spins) {
    __builtin_amdgcn_s_sleep(SLP);
    return ++spins > kSpinCap;
}
#define LDM(x) __builtin_nontemporal_load(x)
struct Params {
    const unsigned short* x;
    const unsigned short* ckv;
    const unsigned short* w_q;
    const unsigned short* w_uk;
    const unsigned short* w_uv;
    const unsigned short* w_o;
    const unsigned short* w1;
    const unsigned short* w2;
    const unsigned short* c;
    const unsigned short* w_mod;
    char* ws;
    unsigned short* out;
};

#define HS_LD 520
#define L_SCR 131072
#define L_WUK (L_SCR)
#define L_QRED (L_SCR + 20480)
#define L_XN (L_SCR)
#define L_QIMG (L_SCR)
#define L_QLAST 130048
#define L_EXM 126976
#define L_P (L_SCR)
#define L_EXS (L_SCR + 8192)
#define L_RED2 (L_SCR + 10240)
#define L_WGT (L_SCR + 14336)
#define L_H2S (L_SCR + 15360)
#define L_REDM (L_SCR + 24576)
#define L_SYNC (163840 - 256)
#define L_TAG (163840 - 16)
#define L_CW (L_SYNC + 152)
#define L_NEARQ (L_SYNC + 156)
#define L_CALT (L_SYNC + 160)
#define L_MSUM 114688
#define S_QRED 4
#define S_MOD 8
#define S_WGT 12
#define S_MLP 24
#define S_DRAIN 28
#define S_SEEN 32
#define S_PRE 40
#define S_TAG 52
#define S_INIT2 56
#define S_HALF 44
#define S_FREE 64
#define S_WQ 80
#define S_WUK 96
#define S_ML 100
#define S_X 144
#define S_L 148
#define S_CAL 168
#define CKW 6
#define CKNB 12
#define SLPH 8
#define SLPM 4
#define SLPX 2
#define N0 1
#define N1 1
#define WOE 4
#define VMW(n) asm volatile("s_waitcnt vmcnt(%0)" :: "n"(n) : "memory")
#define PIN(x) asm volatile("" : "+v"(x))

__global__ __launch_bounds__(512, 2) void k_main(Params p) {
    const int id = blockIdx.x;
    const int tid = threadIdx.x;
    int lane = tid & 63;
    const int wv = __builtin_amdgcn_readfirstlane(tid >> 6);
    int g = lane >> 4, nl = lane & 15;
    const int wp = wv & 3;
    char* ws = p.ws;
    if (wv == 2 && id < 8) asm volatile("buffer_wbl2 sc1" ::: "memory");
    STAMP(22);
    if (tid < 48) *LP(unsigned, L_SYNC + tid * 4) = 0u;
    if (wv == CPFW && (id >> 3) < 5) {
        unsigned long long pc;
        asm volatile("s_getpc_b64 %0" : "=s"(pc));
        asm volatile("s_mov_b32 m0, %2\n\ts_nop 0\n\tglobal_load_lds_dword %0, %1" :: "v"(min((unsigned)(((id >> 3) * 64 + lane) * 128 + CPFO), (unsigned)CPFE)), "s"(pc), "s"(L_SCR + 31744) : "memory", "m0");
    }
    const bool hwg = (id & 7) < 2 && id < 64;
    const unsigned coff = (unsigned)(((nl & 7) * 512 + (nl >> 3) * 32 + g * 8) * 2);
    const int qi = (id & 7) < 2 ? (id & 7) * 24 + (id >> 3) - 8 : (id & 7) * 32 - 16 + (id >> 3);
    const int q7 = (qi * 9363) >> 16;
    const int qh = qi < 112 ? q7 : 2 + (qi >> 3), qj = qi < 112 ? qi - q7 * 7 : qi & 7;
    const bool ext = qi < 112 && qj < 4;
    unsigned vo[4];
#pragma unroll
    for (int i = 0; i < 4; ++i) vo[i] = (unsigned)((i * 4 + g) * 8192 + ((nl ^ (i * 4 + g)) << 4));
    const char* wq = (const char*)(p.w_q + qh * 128) + (size_t)wp * 128 * 8192;
    auto wq_dma = [&](int k) {
        const int pk = (k + qj) & 3;
#pragma unroll
        for (int i = 0; i < 8; ++i) dma16(vo[i & 3], wq + (size_t)(pk * 32 + (i >> 2) * 16) * 8192, wp * 32768 + (k * 8 + i) * 1024);
    };
    if (wv >= 4 && !hwg) {
        const char* wuk = (const char*)(p.w_uk + qh * 128);
#pragma unroll
        for (int i = 0; i < 4; ++i) dma16(vo[i], wuk + (size_t)(qj * 64 + wp * 16) * 8192, L_WUK + (wp * 4 + i) * 1024);
        dma16((unsigned)((wp * 4 + g) * 8192 + ((nl ^ (wp * 4 + g)) << 4)), wuk + (size_t)(448 + (qj & 3) * 16) * 8192, L_WUK + (16 + wp) * 1024);
        wq_dma(0);
    }
    u16x8 c0, c1, xa, xb;
    if (hwg && wv < 4) {
        c0 = *(const u16x8*)((const char*)p.c + coff + wv * 256); c1 = *(const u16x8*)((const char*)p.c + coff + wv * 256 + 128);
        xa = *(const u16x8*)(p.x + wv * 1024 + lane * 16); xb = *(const u16x8*)(p.x + wv * 1024 + lane * 16 + 8);
        __builtin_amdgcn_sched_barrier(0);
    }
    unsigned calw0 = 0, calw1 = 0;
    const char *cal0 = ws, *cal1 = ws;
    if (!hwg && wv == 1) {
        const unsigned r = ((unsigned)__builtin_amdgcn_s_memrealtime() * 0x9e3779b1u + (unsigned)id * 0x85ebca6bu) >> 8;
        const unsigned blk = WS_PART + (r & 511u) * 16384u, ln = ((r >> 9) & 63u) * 128u, odd = ((blk >> 21) & 1u) << 13;
        cal0 = ws + blk + odd + ln; cal1 = ws + blk + (odd ^ 8192u) + ln;
        asm volatile("s_load_dword %0, %2, 0x0\n\ts_load_dword %1, %3, 0x0" : "=&s"(calw0), "=&s"(calw1) : "s"(ws + blk + (ln ^ 1024u)), "s"(ws + blk + 8192u + (ln ^ 1024u)) : "memory");
    }
    if (wv == 0) asm volatile("s_waitcnt lgkmcnt(0)" ::: "memory");
    asm volatile("s_barrier" ::: "memory");
    unsigned sy = L_SYNC, syw = L_SYNC + wp * 4, one = 1u;
    PIN(sy); PIN(syw); PIN(one);
    const int ab = id & 7, as = id >> 3;
    unsigned cw = 0u, nearp = 0u;
    auto q_off = [&](unsigned b, unsigned h) { return WS_G2 + (b * 4 + (h >> 3)) * 16384 + (h & 7) * 1024; };
    auto ml_off = [&](unsigned b, unsigned s_, unsigned h) { return WS_ML + (b >> 1) * 16384 + ((((h >> 4) ^ cw) & 1) << 13) + ((b & 1) * 32 + s_) * 128 + (h & 15) * 8; };
    auto part_off = [&](unsigned b, unsigned h, unsigned cb) {
        return WS_PART + ((b << 20) | (((cb >> 1) & 1) << 19) | (((h >> 3) & 1) << 18) | ((h & 7) << 15) | ((cb >> 2) << 14) | ((((h >> 4) ^ cw ^ ((2 + b) >> 1)) & 1) << 13) | ((cb & 1) << 12));
    };
    auto outp_off = [&](unsigned r) { return WS_OUTP + (((r & ~5u) | ((r & 1) << 2) | (((r >> 2) ^ cw) & 1)) << 13); };
    auto cw_get = [&]() {
        cw = __builtin_amdgcn_readfirstlane(*LP(unsigned, L_CW));
        nearp = (((unsigned)id >> 2) ^ cw) & 1u;
    };
    const int cgl = (id & 1) * 8 + (id >> 3);
    const bool twg = id & 1;
    const __amdgpu_buffer_rsrc_t rw = __builtin_amdgcn_make_buffer_rsrc(ws, 0, WS_END, 0x00020000);
    unsigned long long tag;
    unsigned pcl = (unsigned)(((((lane & 7) >> 1) ^ ((lane >> 4) & 1)) * 32) + (lane & 1) * 16);
    auto dma_piece = [&](const unsigned short* src, unsigned ldb, unsigned dst, int n, unsigned odd = 0u) {
        const unsigned v0 = ((lane >> 3) * ldb + pcl) ^ (odd << 6), v1 = v0 ^ 64u;
#pragma unroll
        for (int i = 0; i < n; ++i) dma16((i & 1) ? v1 : v0, (const char*)src + (size_t)i * 8 * ldb, dst + i * 1024);
    };
    unsigned frl = (unsigned)((g * 8 + (nl >> 2)) * 128 + (lane & 3) * 8), frs = (unsigned)((nl >> 3) | ((g & 1) << 1));
    auto rederive = [&]() {
        int t = tid;
        asm volatile("" : "+v"(t));
        lane = t & 63; g = lane >> 4; nl = lane & 15;
        pcl = (unsigned)(((((lane & 7) >> 1) ^ ((lane >> 4) & 1)) * 32) + (lane & 1) * 16);
        frl = (unsigned)((g * 8 + (nl >> 2)) * 128 + (lane & 3) * 8); frs = (unsigned)((nl >> 3) | ((g & 1) << 1));
    };
    auto piece_frag = [&](unsigned base, int ks, int t) {
        const unsigned a = base + frl + ((t ^ frs) << 5) + ks * 4096;
        return tr_frag(a, a + 512);
    };
    auto tag_get = [&]() {
        LWAIT(sy, S_TAG, 1);
        tag = lds_ld64(L_TAG);
    };
    auto flag_late = [&]() {
        unsigned n;
        ARRIVE_RTN(n, sy, S_INIT2);
        if (n == 3) {
            tag_get();
            if (lane < 8) __hip_atomic_store((gu64*)(ws + WS_FLAGS + 16384) + lane * 256 + id, tag, RLX_AGENT);
            STAMPW(15, wv * 64);
        }
    };
    auto flags_wait = [&](int set) {
        const unsigned long long tv = set == 0 ? tag << 2 : tag;
        const unsigned flo = (unsigned)tv, fhi = (unsigned)(tv >> 32), msk = set == 0 ? ~3u : ~0u;
        u32x4 f0, f1;
        for (unsigned spins = 0;;) {
            f0 = __builtin_amdgcn_raw_buffer_load_b128(rw, (unsigned)(WS_FLAGS + set * 16384 + (id & 7) * 2048 + lane * 32), 0, 16);
            f1 = __builtin_amdgcn_raw_buffer_load_b128(rw, (unsigned)(WS_FLAGS + set * 16384 + (id & 7) * 2048 + lane * 32 + 16), 0, 16);
            const bool ok = ((f0[0] & msk) == flo) & (f0[1] == fhi) & ((f0[2] & msk) == flo) & (f0[3] == fhi) & ((f1[0] & msk) == flo) & (f1[1] == fhi) & ((f1[2] & msk) == flo) & (f1[3] == fhi);
            if (__all(ok)) break;
            __builtin_amdgcn_s_sleep(16);
            if (spin_fail(spins)) break;
            asm volatile("" ::: "memory");
        }
        if (set == 0) {
            const int n1 = __popcll(__ballot((f0[0] & 3u) == 3u)) + __popcll(__ballot((f0[2] & 3u) == 3u)) + __popcll(__ballot((f1[0] & 3u) == 3u)) + __popcll(__ballot((f1[2] & 3u) == 3u));
            const int n0 = __popcll(__ballot((f0[0] & 3u) == 2u)) + __popcll(__ballot((f0[2] & 3u) == 2u)) + __popcll(__ballot((f1[0] & 3u) == 2u)) + __popcll(__ballot((f1[2] & 3u) == 2u));
            const unsigned c = n1 > n0 ? 1u : 0u;
            if (lane == 0) { *LP(unsigned, L_CW) = c; *LP(unsigned, L_NEARQ) = (((unsigned)id >> 2) ^ c) & 1u; }
        }
    };
    auto mod_act = [&](const u16x8& cf, u32x4 (&af)[2]) {
#pragma unroll
        for (int j = 0; j < 4; ++j) {
            const float a = bf2f(cf[2 * j]), b = bf2f(cf[2 * j + 1]);
            af[0][j] = pk2(a * __builtin_amdgcn_rcpf(1.f + __expf(-a)), b * __builtin_amdgcn_rcpf(1.f + __expf(-b)));
            af[1][j] = (unsigned)__builtin_amdgcn_update_dpp(0, (int)af[0][j], 0x128, 0xf, 0xf, true);
        }
        PIN(af[0]); PIN(af[1]);
    };
    auto mod_mm = [&](const u32x4 (&af)[2], unsigned base, f32x4 (&d)[4]) {
        bf16x8 f[2][4];
#pragma unroll
        for (int t = 0; t < 4; ++t) { f[0][t] = piece_frag(base, 0, t); f[1][t] = piece_frag(base, 1, t); }
#pragma unroll
        for (int t = 0; t < 4; ++t) {
            d[t] = __builtin_amdgcn_mfma_f32_16x16x32_bf16(f[0][t], __builtin_bit_cast(bf16x8, af[0]), f32x4{0.f, 0.f, 0.f, 0.f}, 0, 0, 0);
            d[t] = __builtin_amdgcn_mfma_f32_16x16x32_bf16(f[1][t], __builtin_bit_cast(bf16x8, af[1]), d[t], 0, 0, 0);
        }
    };
    const u32x4 s4 = {SENT, SENT, SENT, SENT};
    unsigned long long cnt1;
    unsigned calt[2];
    auto init_early_a = [&]() {
        cnt1 = __hip_atomic_load((gu64*)(ws + WS_EPOCH), RLX_AGENT);
        const unsigned a = (unsigned)(q_off(lane >> 3, qh) + (qj * 64 + (lane & 7) * 8) * 2);
        __builtin_amdgcn_raw_buffer_store_b128(s4, rw, a, 0, 16);
        __builtin_amdgcn_raw_buffer_store_b128(s4, rw, a, 8192, 16);
        if (ext && lane < 16) {
            const unsigned e = (unsigned)(q_off(lane >> 1, qh) + (448 + qj * 16 + (lane & 1) * 8) * 2);
            __builtin_amdgcn_raw_buffer_store_b128(s4, rw, e, 0, 16);
            __builtin_amdgcn_raw_buffer_store_b128(s4, rw, e, 8192, 16);
        }
        __builtin_amdgcn_sched_barrier(0);
        asm volatile("s_waitcnt lgkmcnt(0)" : "+s"(calw0), "+s"(calw1) :: "memory");
#pragma unroll
        for (int j = 0; j < 2; ++j) {
            unsigned long long t0_, t1_; unsigned x_;
            asm volatile("s_memtime %0\n\ts_waitcnt lgkmcnt(0)\n\ts_load_dword %2, %3, 0x0\n\ts_waitcnt lgkmcnt(0)\n\ts_memtime %1\n\ts_waitcnt lgkmcnt(0)"
                         : "=&s"(t0_), "=&s"(t1_), "=&s"(x_) : "s"(j ? cal1 : cal0) : "memory");
            calt[j] = (unsigned)(t1_ - t0_);
        }
    };
    auto init_early_b = [&]() {
        asm volatile("s_waitcnt vmcnt(0)" : "+v"(cnt1) :: "memory");
        if (lane == 0) *LP(unsigned long long, L_TAG) = cnt1 + 1ull;
        const unsigned np = calt[1] < calt[0] ? 1u : 0u, sure = ((calt[0] > calt[1] + CALTH || calt[1] > calt[0] + CALTH) && min(calt[0], calt[1]) > CALHIT) ? 2u : 0u;
        if (lane == 0) *LP(unsigned, L_NEARQ) = np;
        ARRIVE(sy, S_TAG);
        if (lane < 8) __hip_atomic_store((gu64*)(ws + WS_FLAGS) + lane * 256 + id, ((cnt1 + 1ull) << 2) | sure | ((np ^ ((unsigned)id >> 2)) & 1u), RLX_AGENT);
        STAMPW(31, wv * 64);
    };
    auto init_late = [&]() {
        __builtin_amdgcn_raw_buffer_store_b128(s4, rw, (unsigned)(WS_OUTP + id * 4096 + wp * 1024 + lane * 16), 0, 16);
        if (wv == 2) __builtin_amdgcn_raw_buffer_store_b128(s4, rw, (unsigned)(WS_ML + id * 1024 + lane * 16), 0, 16);
        if (wv == 3 && lane < 16) __builtin_amdgcn_raw_buffer_store_b128(s4, rw, (unsigned)(WS_G3 + id * 256 + lane * 16), 0, 16);
        __builtin_amdgcn_sched_barrier(0);
    };
    auto late_mod = [&]() {
        u32x4 af[2];
        mod_act(*(const u16x8*)((const char*)p.c + coff + (id & 7) * 128), af);
        LWAIT(sy, S_MOD, 4);
        f32x4 d[4];
        mod_mm(af, L_WUK + 4096, d);
        if (nl < 8) {
#pragma unroll
            for (int t = 0; t < 4; ++t) __builtin_amdgcn_raw_buffer_store_b128(__builtin_bit_cast(u32x4, d[t]), rw, (unsigned)(WS_MODP + id * 2048 + nl * 256 + t * 64 + g * 16), 0, 16);
        }
    };
    if (hwg) {
        const int hb = lane & 7, hch = (wv >> 2) * 8 + (wv & 3);
        unsigned hoff = (unsigned)(WS_H + (lane >> 4) * 65536 + (cgl >> 2) * 16384 + ((lane >> 3) & 1) * 8192 + (cgl & 3) * 2048 + wv * 256 + hb * 16);
        unsigned sa0 = L_MSUM + hb * 256 + ((hch ^ hb) << 4), sa1 = L_MSUM + hb * 256 + (((hch + 4) ^ hb) << 4);
        if (wv >= 4) {
            unsigned long long cnt;
            asm volatile("global_load_dwordx2 %0, %1, %2 sc1" : "=v"(cnt) : "v"((unsigned)WS_EPOCH), "s"(ws) : "memory");
            dma_piece(p.w_mod + (size_t)wp * 128 * 3072 + cgl * 64, 6144, wp * 16384, 16);
            asm volatile("s_waitcnt vmcnt(8)" : "+v"(cnt) :: "memory");
            ARRIVE(syw, S_WQ);
            VMW(0);
            ARRIVE(syw, S_WQ);
            if (wv == 4) {
                if (lane == 0) *LP(unsigned long long, L_TAG) = cnt + 1ull;
                ARRIVE(sy, S_TAG);
                if (lane < 8) __hip_atomic_store((gu64*)(ws + WS_FLAGS) + lane * 256 + id, (cnt + 1ull) << 2, RLX_AGENT);
            }
            STAMPB(27);
        } else {
            u32x4 af[2][2];
            mod_act(c0, af[0]);
            mod_act(c1, af[1]);
            if (twg) {
                float v[16], sum = 0.f;
#pragma unroll
                for (int j = 0; j < 8; ++j) { v[j] = bf2f(xa[j]); v[8 + j] = bf2f(xb[j]); sum += v[j] + v[8 + j]; }
                sum = dpp_add(sum, 0); sum = dpp_add(sum, 1); sum = dpp_add(sum, 2); sum = dpp_add(sum, 3);
                { auto sw = __builtin_amdgcn_permlane16_swap(__float_as_uint(sum), __float_as_uint(sum), false, false); sum = __uint_as_float(sw[0]) + __uint_as_float(sw[1]); }
                const float m = sum * (1.f / 512.f);
                float q = 0.f;
#pragma unroll
                for (int j = 0; j < 16; ++j) { const float d = v[j] - m; q += d * d; }
                q = dpp_add(q, 0); q = dpp_add(q, 1); q = dpp_add(q, 2); q = dpp_add(q, 3);
                { auto sw = __builtin_amdgcn_permlane16_swap(__float_as_uint(q), __float_as_uint(q), false, false); q = __uint_as_float(sw[0]) + __uint_as_float(sw[1]); }
                const float rden = __builtin_amdgcn_rcpf(rbf(sqrtf(rbf(rbf(q * (1.f / 512.f)) + 1e-5f)))), mb = rbf(m);
                if (((lane & 31) >> 2) == (cgl & 7)) {
                    u32x4 r0, r1;
#pragma unroll
                    for (int j = 0; j < 4; ++j) {
                        const unsigned d0 = pk2(v[2 * j] - mb, v[2 * j + 1] - mb), d1 = pk2(v[8 + 2 * j] - mb, v[9 + 2 * j] - mb);
                        r0[j] = pk2(__uint_as_float(d0 << 16) * rden, __uint_as_float(d0 & 0xffff0000u) * rden);
                        r1[j] = pk2(__uint_as_float(d1 << 16) * rden, __uint_as_float(d1 & 0xffff0000u) * rden);
                    }
                    *LP(u32x4, L_XN + (wv * 2 + (lane >> 5)) * 128 + (lane & 3) * 32) = r0;
                    *LP(u32x4, L_XN + (wv * 2 + (lane >> 5)) * 128 + (lane & 3) * 32 + 16) = r1;
                }
            }
            unsigned wa = L_MSUM + wv * 2048 + nl * 256;
            PIN(wa);
            STAMP(16);
            f32x4 d[4];
#pragma unroll
            for (int t = 0; t < 4; ++t) d[t] = f32x4{0.f, 0.f, 0.f, 0.f};
#pragma unroll
            for (int hf = 0; hf < 2; ++hf) {
                LWAIT(syw, S_WQ, hf + 1);
                bf16x8 f[2][4];
#pragma unroll
                for (int t = 0; t < 4; ++t) { f[0][t] = piece_frag(wv * 16384 + hf * 8192, 0, t); f[1][t] = piece_frag(wv * 16384 + hf * 8192, 1, t); }
#pragma unroll
                for (int t = 0; t < 4; ++t) {
                    d[t] = __builtin_amdgcn_mfma_f32_16x16x32_bf16(f[0][t], __builtin_bit_cast(bf16x8, af[hf][0]), d[t], 0, 0, 0);
                    d[t] = __builtin_amdgcn_mfma_f32_16x16x32_bf16(f[1][t], __builtin_bit_cast(bf16x8, af[hf][1]), d[t], 0, 0, 0);
                }
            }
            if (nl < 8) {
#pragma unroll
                for (int t = 0; t < 4; ++t) *LP(f32x4, wa + (((t * 4 + g) ^ nl) << 4)) = d[t];
            }
            STAMP(19);
        }
        tag_get();
        unsigned tlo = (unsigned)tag, thi = tlo ^ (unsigned)(tag >> 32) ^ TAGC;
        PIN(tlo); PIN(thi); PIN(hoff); PIN(sa0); PIN(sa1);
        wg_barrier();
        STAMP(28);
        {
            f32x4 u[4], v[4];
#pragma unroll
            for (int w2 = 0; w2 < 4; ++w2) { u[w2] = *LP(f32x4, sa0 + w2 * 2048); v[w2] = *LP(f32x4, sa1 + w2 * 2048); }
            const f32x4 v0 = (u[0] + u[1]) + (u[2] + u[3]), v1 = (v[0] + v[1]) + (v[2] + v[3]);
            unsigned r[4] = {pk2(v0[0], v0[1]), pk2(v0[2], v0[3]), pk2(v1[0], v1[1]), pk2(v1[2], v1[3])};
            if (twg) {
                const u32x2 x0 = *LP(u32x2, L_XN + hb * 128 + hch * 8), x1 = *LP(u32x2, L_XN + hb * 128 + hch * 8 + 32);
#pragma unroll
                for (int j = 0; j < 4; ++j) {
                    const unsigned sc = pk2(1.f + __uint_as_float(r[j] << 16), 1.f + __uint_as_float(r[j] & 0xffff0000u)), xw = j < 2 ? x0[j] : x1[j - 2];
                    r[j] = pk2(__uint_as_float(xw << 16) * __uint_as_float(sc << 16), __uint_as_float(xw & 0xffff0000u) * __uint_as_float(sc & 0xffff0000u));
                }
            }
            __builtin_amdgcn_raw_buffer_store_b128(u32x4{r[0], tlo, r[1], thi}, rw, hoff, 0, 16);
            __builtin_amdgcn_raw_buffer_store_b128(u32x4{r[2], tlo, r[3], thi}, rw, hoff + 128, 0, 16);
        }
        STAMP(20);
        wg_barrier();
    }
    const int head = lane & 31;
    const unsigned h2 = lane >> 5;
    const int th = (id & 7) * 4 + ((id >> 3) & 3), tbp = id >> 6, thalf = (id >> 5) & 1;
    const unsigned rgn = (unsigned)(wv * 16384);
    const unsigned short* tuv = p.w_uv + ((size_t)wv * 64 * NH + th) * 128 + thalf * 64;
    const unsigned short* two = p.w_o + (size_t)(th * 128 + thalf * 64) * 512 + wv * 64;
    unsigned exw, exr, pwr, sw0, srd, po, uvo0, uvo1, xw, xr;
    const char* tuvs;
    const int ch = wv < 4, kb = wp * 2 + ch;
    bf16x8 ka[16];
    s16x4 va[8][4];
    unsigned kad[8], vad[4], vad2[4], qad[8], qwr[4], qsrc, prd;
    auto k_addr = [&]() {
#pragma unroll
        for (int j = 0; j < 8; ++j) { kad[j] = tile_off(wp * 32 + (head ^ (ch * 16)), ch * 32 + 2 * j + h2); PIN(kad[j]); }
    };
    auto v_addr = [&]() {
#pragma unroll
        for (int j = 0; j < 4; ++j) {
            vad[j] = tile_off(4 * h2 + (nl >> 2) + 8 * (j & 1), wv * 8 + 4 * (j >> 1) + 2 * (g & 1) + ((lane & 3) >> 1)) + 8 * (lane & 1);
            vad2[j] = vad[j] + 65536;
            PIN(vad[j]); PIN(vad2[j]);
        }
    };
    auto att_addr = [&]() {
#pragma unroll
        for (int j = 0; j < 8; ++j) {
            qad[j] = (head == 31 ? L_QLAST : L_QIMG + head * 1024) + ch * 512 + (((2 * j + h2) ^ (head & 15)) << 4);
            PIN(qad[j]);
        }
#pragma unroll
        for (int k = 0; k < 4; ++k) {
            const int hd = wv * 4 + k;
            qwr[k] = (hd == 31 ? L_QLAST : L_QIMG + hd * 1024) + g * 256 + ((nl ^ (hd & 15)) << 4);
            PIN(qwr[k]);
        }
        prd = L_P + lane * 16;
        PIN(prd);
        qsrc = (unsigned)(q_off(ab, wv * 4) + lane * 16) + ((*LP(unsigned, L_NEARQ) & 1u) << 13);
        PIN(qsrc);
        exw = L_EXM + (head * 8 + wv) * 4; exr = L_EXM + head * 32; pwr = (unsigned)(L_P + kb * 1024 + lane * 16);
        xw = rgn + 8192 + lane * 16; xr = xw ^ 65536u;
        PIN(xw); PIN(xr);
        sw0 = rgn + 12288 + head * 128; srd = rgn + 12288 + (lane >> 3) * 128 + (((lane & 7) ^ (lane >> 3)) << 4);
        po = (unsigned)(part_off(ab, lane >> 3, wv) + (as ^ (lane >> 3)) * 128 + (lane & 7) * 16);
        PIN(exw); PIN(exr); PIN(pwr); PIN(sw0); PIN(srd); PIN(po);
        uvo0 = (lane >> 3) * 8192u + pcl; uvo1 = uvo0 ^ 64u; tuvs = (const char*)tuv;
        PIN(uvo0); PIN(uvo1);
        asm volatile("" : "+s"(tuvs));
    };
    auto uv_dma = [&](int i) { dma16((i & 1) ? uvo1 : uvo0, tuvs + (size_t)i * 65536, rgn + i * 1024); };
    auto ka_reads = [&]() {
#pragma unroll
        for (int ks = 0; ks < 16; ++ks) ka[ks] = *LP(bf16x8, kad[ks & 7] + (ks >> 3) * 8192);
    };
    auto va_reads = [&]() {
#pragma unroll
        for (int kk = 0; kk < 8; ++kk)
#pragma unroll
            for (int j = 0; j < 4; ++j) va[kk][j] = tr4((kk < 4 ? vad[j] : vad2[j]) + ((kk >> 1) & 1) * 32768 + (kk & 1) * 4096);
    };
    auto va_pin = [&]() {
#pragma unroll
        for (int q = 0; q < 8; ++q) asm volatile("" : "+v"(va[q][0]), "+v"(va[q][1]), "+v"(va[q][2]), "+v"(va[q][3]) :: "memory");
    };
    if (wv < 4) {
        if (!hwg) {
            if (wv == 1) init_early_a();
            STAMP(0);
            LWAIT(sy, S_WUK, 4);
            const unsigned fa[4] = {(unsigned)(nl * 256 + ((g ^ nl) << 4)), (unsigned)(nl * 256 + (((4 + g) ^ nl) << 4)), (unsigned)(nl * 256 + (((8 + g) ^ nl) << 4)), (unsigned)(nl * 256 + (((12 + g) ^ nl) << 4))};
            bf16x8 bk[5][4];
#pragma unroll
            for (int ct = 0; ct < 5; ++ct)
#pragma unroll
                for (int s = 0; s < 4; ++s) bk[ct][s] = *LP(bf16x8, L_WUK + ct * 4096 + fa[s]);
#pragma unroll
            for (int ct = 0; ct < 5; ++ct) asm volatile("" : "+v"(bk[ct][0]), "+v"(bk[ct][1]), "+v"(bk[ct][2]), "+v"(bk[ct][3]) :: "memory");
            ARRIVE(sy, S_WUK);
            if (wv == 1) init_early_b();
            u32x4 wf[4][5], hs[4], ht[4];
            unsigned hbase = (unsigned)(WS_H + ((id >> 1) & 3) * 65536 + (wv >> 1) * 16384 + (wv & 1) * 4096 + lane * 16);
#pragma unroll
            for (int k = 0; k < 4; ++k) {
                LWAIT(syw, S_WQ, k + 1);
                bf16x8 a0[4], a1[4];
#pragma unroll
                for (int s = 0; s < 4; ++s) {
                    a0[s] = *LP(bf16x8, wv * 32768 + k * 8192 + fa[s]);
                    a1[s] = *LP(bf16x8, wv * 32768 + k * 8192 + 4096 + fa[s]);
                }
                if (k == 3) {
                    asm volatile("" : "+v"(a0[0]), "+v"(a0[1]), "+v"(a0[2]), "+v"(a0[3]), "+v"(a1[0]), "+v"(a1[1]), "+v"(a1[2]), "+v"(a1[3]) :: "memory");
                    hbase += (*LP(unsigned, L_NEARQ) & 1u) << 13;
                    ARRIVE(syw, S_FREE);
                    STAMP(18);
#pragma unroll
                    for (int k2 = 0; k2 < 4; ++k2) {
                        hs[k2] = __builtin_amdgcn_raw_buffer_load_b128(rw, hbase, ((k2 + qj) & 3) * 1024, 16);
                        ht[k2] = __builtin_amdgcn_raw_buffer_load_b128(rw, hbase + 32768, ((k2 + qj) & 3) * 1024, 16);
                    }
                    __builtin_amdgcn_sched_barrier(0);
                }
                f32x4 d0[4], d1[4];
#pragma unroll
                for (int ct = 0; ct < 4; ++ct) { d0[ct] = f32x4{0.f, 0.f, 0.f, 0.f}; d1[ct] = f32x4{0.f, 0.f, 0.f, 0.f}; }
#pragma unroll
                for (int s = 0; s < 4; ++s)
#pragma unroll
                    for (int ct = 0; ct < 4; ++ct) {
                        d0[ct] = __builtin_amdgcn_mfma_f32_16x16x32_bf16(a0[s], bk[ct][s], d0[ct], 0, 0, 0);
                        d1[ct] = __builtin_amdgcn_mfma_f32_16x16x32_bf16(a1[s], bk[ct][s], d1[ct], 0, 0, 0);
                    }
#pragma unroll
                for (int ct = 0; ct < 4; ++ct)
                    wf[k][ct] = u32x4{pk2(d0[ct][0], d0[ct][1]), pk2(d0[ct][2], d0[ct][3]), pk2(d1[ct][0], d1[ct][1]), pk2(d1[ct][2], d1[ct][3])};
                wf[k][4] = u32x4{0u, 0u, 0u, 0u};
                if (ext) {
                    f32x4 e0 = {0.f, 0.f, 0.f, 0.f}, e1 = {0.f, 0.f, 0.f, 0.f};
#pragma unroll
                    for (int s = 0; s < 4; ++s) {
                        e0 = __builtin_amdgcn_mfma_f32_16x16x32_bf16(a0[s], bk[4][s], e0, 0, 0, 0);
                        e1 = __builtin_amdgcn_mfma_f32_16x16x32_bf16(a1[s], bk[4][s], e1, 0, 0, 0);
                    }
                    wf[k][4] = u32x4{pk2(e0[0], e0[1]), pk2(e0[2], e0[3]), pk2(e1[0], e1[1]), pk2(e1[2], e1[3])};
                }
                asm volatile("" : "+v"(wf[k][0]), "+v"(wf[k][1]), "+v"(wf[k][2]), "+v"(wf[k][3]), "+v"(wf[k][4]));
            }
            STAMP(13);
            tag_get();
            const unsigned tlo = (unsigned)tag, thi = tlo ^ (unsigned)(tag >> 32) ^ TAGC;
            unsigned qw = L_QRED + ((wv * 8 + nl) * 80 + g * 4) * 4, qr = L_QRED + ((lane >> 3) * 80 + (lane & 7) * 8) * 4;
            unsigned qo = (unsigned)(q_off(lane >> 3, qh) + (qj * 64 + (lane & 7) * 8) * 2);
            PIN(qw); PIN(qr); PIN(qo);
            auto h_bad = [&]() {
                unsigned bad = 0;
#pragma unroll
                for (int k = 0; k < 4; ++k) bad |= (hs[k][1] ^ tlo) | (hs[k][3] ^ thi) | (ht[k][1] ^ tlo) | (ht[k][3] ^ thi);
                PIN(bad);
                return bad;
            };
            if (!__all(h_bad() == 0u)) {
                const unsigned hb2 = hbase + 32768;
                for (unsigned spins = 0;;) {
                    __builtin_amdgcn_s_sleep(SLPH);
                    if (spin_fail(spins)) break;
#pragma unroll
                    for (int k = 0; k < 4; ++k) {
                        const unsigned so = ((k + qj) & 3) * 1024;
                        if (!__all((hs[k][1] == tlo) & (hs[k][3] == thi))) asm volatile("buffer_load_dwordx4 %0, %1, %2, %3 offen sc1" : "+v"(hs[k]) : "v"(hbase), "s"(rw), "s"(so) : "memory");
                        if (!__all((ht[k][1] == tlo) & (ht[k][3] == thi))) asm volatile("buffer_load_dwordx4 %0, %1, %2, %3 offen sc1" : "+v"(ht[k]) : "v"(hb2), "s"(rw), "s"(so) : "memory");
                    }
                    asm volatile("s_waitcnt vmcnt(0)" : "+v"(hs[0]), "+v"(hs[1]), "+v"(hs[2]), "+v"(hs[3]), "+v"(ht[0]), "+v"(ht[1]), "+v"(ht[2]), "+v"(ht[3]) :: "memory");
                    if (__all(h_bad() == 0u)) break;
                }
            }
            STAMP(14);
            {
                f32x4 acc[5];
#pragma unroll
                for (int ct = 0; ct < 5; ++ct) acc[ct] = f32x4{0.f, 0.f, 0.f, 0.f};
#pragma unroll
                for (int k = 0; k < 4; ++k) {
                    u32x4 hf;
#pragma unroll
                    for (int q = 0; q < 2; ++q) {
                        hf[q] = pk2(__uint_as_float(ht[k][2 * q] << 16) + __uint_as_float(hs[k][2 * q] << 16), __uint_as_float(ht[k][2 * q] & 0xffff0000u) + __uint_as_float(hs[k][2 * q] & 0xffff0000u));
                        hf[2 + q] = (unsigned)__builtin_amdgcn_update_dpp(0, (int)hf[q], 0x128, 0xf, 0xf, true);
                    }
#pragma unroll
                    for (int ct = 0; ct < 5; ++ct)
                        acc[ct] = __builtin_amdgcn_mfma_f32_16x16x32_bf16(__builtin_bit_cast(bf16x8, wf[k][ct]), __builtin_bit_cast(bf16x8, hf), acc[ct], 0, 0, 0);
                }
                if (nl < 8) {
#pragma unroll
                    for (int ct = 0; ct < 5; ++ct) *LP(f32x4, qw + ct * 64) = acc[ct];
                }
            }
            unsigned n;
            ARRIVE_RTN(n, sy, S_QRED);
            if (n == 3) {
                const float sc = 0.08838834764831845f * 1.4426950408889634f;
                {
                    f32x4 lo[4], hi[4];
#pragma unroll
                    for (int w2 = 0; w2 < 4; ++w2) { lo[w2] = *LP(f32x4, qr + w2 * 2560); hi[w2] = *LP(f32x4, qr + w2 * 2560 + 16); }
                    const f32x4 a = ((lo[0] + lo[1]) + (lo[2] + lo[3])) * sc, b = ((hi[0] + hi[1]) + (hi[2] + hi[3])) * sc;
                    const u32x4 qv = {pk2s(a[0], a[1]), pk2s(a[2], a[3]), pk2s(b[0], b[1]), pk2s(b[2], b[3])};
                    __builtin_amdgcn_raw_buffer_store_b128(qv, rw, qo, 0, 16);
                    __builtin_amdgcn_raw_buffer_store_b128(qv, rw, qo, 8192, 16);
                }
                if (ext && lane < 16) {
                    const unsigned qr2 = L_QRED + ((lane >> 1) * 80 + 64 + (lane & 1) * 8) * 4;
                    f32x4 lo[4], hi[4];
#pragma unroll
                    for (int w2 = 0; w2 < 4; ++w2) { lo[w2] = *LP(f32x4, qr2 + w2 * 2560); hi[w2] = *LP(f32x4, qr2 + w2 * 2560 + 16); }
                    const f32x4 a = ((lo[0] + lo[1]) + (lo[2] + lo[3])) * sc, b = ((hi[0] + hi[1]) + (hi[2] + hi[3])) * sc;
                    const u32x4 qv = {pk2s(a[0], a[1]), pk2s(a[2], a[3]), pk2s(b[0], b[1]), pk2s(b[2], b[3])};
                    const unsigned e = (unsigned)(q_off(lane >> 1, qh) + (448 + qj * 16 + (lane & 1) * 8) * 2);
                    __builtin_amdgcn_raw_buffer_store_b128(qv, rw, e, 0, 16);
                    __builtin_amdgcn_raw_buffer_store_b128(qv, rw, e, 8192, 16);
                }
                STAMPW(29, wv * 64);
            } else if (n == 0) {
                flags_wait(0);
                STAMPW(21, wv * 64);
            } else if (n == 1) {
                late_mod();
            }
        } else if (wv == 0) {
            flags_wait(0);
        } else if (wv == 1) {
            late_mod();
        }
        STAMP(3);
        ARRIVE(sy, S_SEEN);
        LWAIT(sy, S_SEEN, 4);
        init_late();
        v_addr();
        LWAIT(sy, S_HALF, 4);
        va_reads();
        va_pin();
        att_addr();
        k_addr();
        VMW(0);
        flag_late();
    } else {
        if (!hwg) {
            wq_dma(1);
            VMW(16);
            ARRIVE(sy, S_WUK);
            VMW(8);
            ARRIVE(syw, S_WQ);
            wq_dma(2);
            VMW(8);
            ARRIVE(syw, S_WQ);
            wq_dma(3);
            VMW(8);
            ARRIVE(syw, S_WQ);
            STAMPB(17);
        }
        if (!hwg) LWAIT(sy, S_WUK, 8);
        dma_piece(p.w_mod + (size_t)((id & 7) * 64 + wp * 16) * 3072 + 1024 + (id >> 3) * 64, 6144, L_WUK + 4096 + wp * 2048, 2);
        const __amdgpu_buffer_rsrc_t rk = __builtin_amdgcn_make_buffer_rsrc((void*)(p.ckv + ((size_t)ab * SK + (size_t)as * KEYS_PER + wp * 32) * DC), 0, 32768, 0x00020000);
        u32x4 ck[32];
        int wr = 0;
        const unsigned tw = (unsigned)(wp * 32768 + lane * 16);
#define TWO(w) ((((w) >> 4) * 2 + ((w) & 1)) * 8192 + (((w) >> 1) & 7) * 1024)
#pragma unroll
        for (int i = 0; i < 32; ++i) {
            const unsigned row = ((i >> 1) & 3) * 4 + g;
            ck[i] = __builtin_amdgcn_raw_buffer_load_b128(rk, row * 1024 + ((nl ^ swz(row)) << 4), ((i >> 3) & 1) * 16384 + ((i >> 4) * 2 + (i & 1)) * 256, 2);
            __builtin_amdgcn_sched_barrier(0);
            if (i >= CKW - 1) VMW(CKW - 1);
            if (i == CKW - 1) { if (!hwg) ARRIVE(syw, S_WQ); ARRIVE(sy, S_MOD); }
            if (i == CKNB && !hwg) LWAIT(syw, S_FREE, 1);
            if (i == 26) {
                k_addr();
                LWAIT(sy, S_HALF, 4);
                ka_reads();
#pragma unroll
                for (int ks = 0; ks < 16; ks += 4) asm volatile("" : "+v"(ka[ks]), "+v"(ka[ks + 1]), "+v"(ka[ks + 2]), "+v"(ka[ks + 3]) :: "memory");
                att_addr();
                v_addr();
            }
            if (i >= CKNB) {
                if (wr <= i - CKW + 1) { *LP(u32x4, tw + TWO(wr)) = ck[wr]; ++wr; if (wr == 16) ARRIVE(sy, S_HALF); }
                if (wr <= i - CKW + 1) { *LP(u32x4, tw + TWO(wr)) = ck[wr]; ++wr; if (wr == 16) ARRIVE(sy, S_HALF); }
            }
            __builtin_amdgcn_sched_barrier(0);
        }
#pragma unroll
        for (int j = 0; j < 32; ++j) if (j >= wr) *LP(u32x4, tw + TWO(j)) = ck[j];
        STAMPB(5);
    }
    wg_barrier();
    {
        u32x4 qb[4];
#pragma unroll
        for (int k = 0; k < 4; ++k) qb[k] = __builtin_amdgcn_raw_buffer_load_b128(rw, qsrc + k * 1024, 0, 16);
        __builtin_amdgcn_sched_barrier(0);
        if (wv < 4) ka_reads(); else va_reads();
        cw_get();
        po ^= (cw << 13) ^ (FARF ? (~(unsigned)id & 4u) << 11 : 0u);
        if (FARF) srd ^= (~(unsigned)id & 4u) << 9;
        ARRIVE(sy, S_PRE);
        STAMP(1);
        if (wv == 7) LWAIT(sy, S_PRE, 8);
        for (unsigned spins = 0;;) {
#pragma unroll
            for (int k = 0; k < 4; ++k) *LP(u32x4, qwr[k]) = qb[k];
            if (__all(mx4(qb[0], mx4(qb[1], mx4(qb[2], mx4(qb[3], 0u)))) != SENT)) break;
            if (spin_fail(spins)) break;
            asm volatile("" ::: "memory");
#pragma unroll
            for (int k = 0; k < 4; ++k) qb[k] = __builtin_amdgcn_raw_buffer_load_b128(rw, qsrc + k * 1024, 0, 16);
        }
        STAMP(23);
    }
    float lsum, mfin;
    const bool pfx = (((((unsigned)(size_t)p.w2 >> 7) + (unsigned)id) & 7u) == 3u);
    unsigned pfo = (unsigned)(((id >> 3) * 56 + wv * 7 + lane) * 1024 + (id & 7) * 128);
    PIN(pfo);
    auto PF = [&]() {
        if (pfx) asm volatile("s_mov_b32 m0, %2\n\ts_nop 0\n\ts_mov_b64 exec, 0x7f\n\tglobal_load_lds_dword %0, %1\n\ts_mov_b64 exec, -1" :: "v"(pfo), "s"(p.w2), "s"(L_SCR + 31744) : "memory", "m0");
    };
    wg_barrier();
    STAMP(4);
    {
        f32x16 sc;
#pragma unroll
        for (int i = 0; i < 16; ++i) sc[i] = 0.f;
        bf16x8 b[16];
        auto qld = [&](int ks) { b[ks] = *LP(bf16x8, qad[ks & 7] + (ks >> 3) * 256); };
#pragma unroll
        for (int ks = 0; ks < QD; ++ks) qld(ks);
        __builtin_amdgcn_sched_barrier(0);
#pragma unroll
        for (int ks = 0; ks < 16; ++ks) {
            sc = __builtin_amdgcn_mfma_f32_32x32x16_bf16(ka[ks], b[ks], sc, 0, 0, 0);
            if (ks + QD < 16) qld(ks + QD);
            if (ks % (16 / NQ) == 0) uv_dma(ks / (16 / NQ));
            __builtin_amdgcn_sched_barrier(0);
        }
        float t[8];
        *LP(f32x4, xw) = f32x4{sc[8], sc[9], sc[10], sc[11]}; *LP(f32x4, xw + 1024) = f32x4{sc[12], sc[13], sc[14], sc[15]};
        wg_barrier();
        {
            const f32x4 x0 = *LP(f32x4, xr), x1 = *LP(f32x4, xr + 1024);
#pragma unroll
            for (int r = 0; r < 4; ++r) { t[r] = sc[r] + x0[r]; t[4 + r] = sc[4 + r] + x1[r]; }
        }
#pragma unroll
        for (int i = NQ; i < NQ + NM; ++i) uv_dma(i);
        {
            const float m = fmax3(fmax3(t[0], t[1], t[2]), fmax3(t[3], t[4], t[5]), fmax3(t[6], t[7], t[7]));
            auto u = __builtin_amdgcn_permlane32_swap(__float_as_uint(m), __float_as_uint(m), false, false);
            *LP(float, exw) = fmax3(__uint_as_float(u[0]), __uint_as_float(u[1]), __uint_as_float(u[1]));
        }
        wg_barrier();
        const f32x4 a0 = *LP(f32x4, exr), a1 = *LP(f32x4, exr + 16);
        mfin = fmax3(fmax3(a0[0], a0[1], a0[2]), fmax3(a0[3], a1[0], a1[1]), fmax3(a1[2], a1[3], a1[3]));
        float e[8];
#pragma unroll
        for (int r = 0; r < 8; ++r) e[r] = __builtin_amdgcn_exp2f(t[r] - mfin);
        *LP(u32x4, pwr) = u32x4{pk2(e[0], e[1]), pk2(e[2], e[3]), pk2(e[4], e[5]), pk2(e[6], e[7])};
        lsum = ((e[0] + e[1]) + (e[2] + e[3])) + ((e[4] + e[5]) + (e[6] + e[7]));
#pragma unroll
        for (int i = NQ + NM; i < 8; ++i) uv_dma(i);
    }
    wg_barrier();
    STAMP(6);
    {
        f32x16 o[2];
#pragma unroll
        for (int t = 0; t < 2; ++t)
#pragma unroll
            for (int i = 0; i < 16; ++i) o[t][i] = 0.f;
        bf16x8 pf[8];
#pragma unroll
        for (int kk = 0; kk < 8; ++kk) pf[kk] = *LP(bf16x8, prd + kk * 1024);
        u32x4 f0, f1;
        if (wv == 7) {
            f0 = __builtin_amdgcn_raw_buffer_load_b128(rw, (unsigned)(WS_FLAGS + 16384 + (id & 7) * 2048 + lane * 32), 0, 16);
            f1 = __builtin_amdgcn_raw_buffer_load_b128(rw, (unsigned)(WS_FLAGS + 16384 + (id & 7) * 2048 + lane * 32 + 16), 0, 16);
        }
        __builtin_amdgcn_sched_barrier(0);
#pragma unroll
        for (int kk = 0; kk < 8; ++kk) {
            o[0] = __builtin_amdgcn_mfma_f32_32x32x16_bf16(cat8(va[kk][0], va[kk][1]), pf[kk], o[0], 0, 0, 0);
            o[1] = __builtin_amdgcn_mfma_f32_32x32x16_bf16(cat8(va[kk][2], va[kk][3]), pf[kk], o[1], 0, 0, 0);
        }
        {
            auto u = __builtin_amdgcn_permlane32_swap(__float_as_uint(lsum), __float_as_uint(lsum), false, false);
            *LP(float, exw + (L_EXS - L_EXM)) = __uint_as_float(u[0]) + __uint_as_float(u[1]);
            ARRIVE(sy, S_L);
        }
#pragma unroll
        for (int t = 0; t < 2; ++t)
#pragma unroll
            for (int gp = 0; gp < 2; ++gp) {
                const unsigned lo0 = pk2(o[t][8 * gp + 0], o[t][8 * gp + 1]), lo1 = pk2(o[t][8 * gp + 2], o[t][8 * gp + 3]);
                const unsigned hi0 = pk2(o[t][8 * gp + 4], o[t][8 * gp + 5]), hi1 = pk2(o[t][8 * gp + 6], o[t][8 * gp + 7]);
                auto r0 = __builtin_amdgcn_permlane32_swap(lo0, hi0, false, false);
                auto r1 = __builtin_amdgcn_permlane32_swap(lo1, hi1, false, false);
                const u32x4 v = {r0[0], r1[0], r0[1], r1[1]};
                const unsigned chunk = t * 4 + 2 * gp + h2;
                *LP(u32x4, sw0 + ((chunk ^ (head & 7)) << 4)) = v;
            }
        asm volatile("s_waitcnt lgkmcnt(0)" ::: "memory");
        u32x4 w[4];
#pragma unroll
        for (int i = 0; i < 4; ++i) w[i] = *LP(u32x4, ((i & 2) ? srd ^ 2048u : srd) + (i & 1) * 1024);
#pragma unroll
        for (int i = 0; i < 4; ++i) __builtin_amdgcn_raw_buffer_store_b128(w[i], rw, (i & 2) ? po ^ 8192u : po, (i & 1) * 262144, 16);
        __builtin_amdgcn_sched_barrier(0);
        LWAIT(sy, S_L, 8);
        u32x2 mlrec;
        unsigned mlo = (unsigned)ml_off(ab, as, head);
        {
            const f32x4 b0 = *LP(f32x4, exr + (L_EXS - L_EXM)), b1 = *LP(f32x4, exr + (L_EXS - L_EXM) + 16);
            const float L = ((b0[0] + b0[1]) + (b0[2] + b0[3])) + ((b1[0] + b1[1]) + (b1[2] + b1[3]));
            mlrec = u32x2{f32s(mfin), f32s(L)};
            PIN(mlrec); PIN(mlo);
        }
        if (wv == 7) {
            tag_get();
            const unsigned flo = (unsigned)tag, fhi = (unsigned)(tag >> 32);
            if (!__all((f0[0] == flo) & (f0[1] == fhi) & (f0[2] == flo) & (f0[3] == fhi) & (f1[0] == flo) & (f1[1] == fhi) & (f1[2] == flo) & (f1[3] == fhi))) flags_wait(1);
            if (id == 255 && lane == 0) __hip_atomic_store((gu64*)(ws + WS_EPOCH), tag, RLX_AGENT);
        }
        VMW(0);
        unsigned n;
        ARRIVE_RTN(n, sy, S_DRAIN);
        if (n == 7) {
            if (lane < 32) __builtin_amdgcn_raw_buffer_store_b64(mlrec, rw, mlo, 0, 16);
            ARRIVE(sy, S_DRAIN);
        }
        LWAIT(sy, S_DRAIN, 9);
    }
    STAMP(7);
    rederive();
    const int ug = (id & 7) * 14 + (id >> 3);
    const int n0 = (((id - NMLP) & 7) * 4 + ((id - NMLP) >> 3)) * 16;
    const unsigned trl = (unsigned)(g * 8 + (nl >> 2)) * 32u + (unsigned)(lane & 3) * 8u;
    auto mod_final = [&]() {
        f32x4 v0 = {0.f, 0.f, 0.f, 0.f}, v1 = {0.f, 0.f, 0.f, 0.f};
        u32x4 pp[16];
#pragma unroll
        for (int k = 0; k < 16; ++k) pp[k] = __builtin_amdgcn_raw_buffer_load_b128(rw, (unsigned)(WS_MODP + (id - 224) * 16384 + lane * 32 + (k & 1) * 16), (k >> 1) * 2048, 16);
#pragma unroll
        for (int k = 0; k < 8; ++k) { v0 += __builtin_bit_cast(f32x4, pp[2 * k]); v1 += __builtin_bit_cast(f32x4, pp[2 * k + 1]); }
        const unsigned moff = (unsigned)(WS_MOD + ((lane >> 3) * 3072 + 1024 + (id - 224) * 64 + (lane & 7) * 8) * 4);
        __builtin_amdgcn_raw_buffer_store_b128(u32x4{f32s(rbf(v0[0])), f32s(rbf(v0[1])), f32s(rbf(v0[2])), f32s(rbf(v0[3]))}, rw, moff, 0, 16);
        __builtin_amdgcn_raw_buffer_store_b128(u32x4{f32s(rbf(v1[0])), f32s(rbf(v1[1])), f32s(rbf(v1[2])), f32s(rbf(v1[3]))}, rw, moff + 16, 0, 16);
    };
    dma_piece(two, 1024, rgn + 8192, WOE);
    if (PFPOS == 1) PF();
    if (wv == 2 && id >= 224) mod_final();
    const bool dnw = id >= NMLP && id < NMLP + NDOWN;
    const unsigned mvo = (unsigned)((lane >> 1) * 1024 + (lane & 1) * 16);
    const char* mws = (const char*)(p.w2 + (size_t)wv * 224 * 512 + n0);
    auto w2_at = [&](int ks) { return ks < N0 ? (unsigned)(L_SCR + ks * 16384 + wv * 1024) : ks < N0 + N1 ? rgn + 1024 + ks * 1024 : rgn + 8192 + ks * 1024; };
    {
        bf16x8 fuv[2][4], fwo[2][4];
#pragma unroll
        for (int ks = 0; ks < 2; ++ks)
#pragma unroll
            for (int t = 0; t < 4; ++t) {
                const unsigned a = rgn + (unsigned)((g * 4 + (nl >> 2)) * 128 + (lane & 3) * 8) + ((t ^ (unsigned)((nl >> 3) | (g & 2))) << 5) + ks * 4096;
                fuv[ks][t] = tr_frag(a, a + 2048);
            }
#pragma unroll
        for (int ks = 0; ks < 2; ++ks) asm volatile("" : "+v"(fuv[ks][0]), "+v"(fuv[ks][1]), "+v"(fuv[ks][2]), "+v"(fuv[ks][3]) :: "memory");
        unsigned psrc = (unsigned)(part_off(tbp * 2, th, wv) + ((lane >> 3) ^ (th & 7)) * 128) + pcl;
        unsigned pw = rgn + lane * 16, rw2 = L_RED2 + wv * 512 + nl * 256 + g * 16;
        unsigned wa0 = (nl & 1) ? L_WGT + 256 : L_WGT + g * 16 + (nl & 2) * 64, wa1 = (nl & 1) ? L_WGT + 64 + g * 16 + (nl & 2) * 64 : L_WGT + 256;
        unsigned pfa[4];
#pragma unroll
        for (int t = 0; t < 4; ++t) { pfa[t] = rgn + frl + ((t ^ frs) << 5); PIN(pfa[t]); }
        if (wv == 0) *LP(unsigned, L_WGT + 256 + lane * 4) = 0u;
        unsigned oo = (unsigned)(outp_off((tbp * 2 + (nl & 1)) * 16 + wv * 2 + (nl >> 2)) + (((th * 2 + thalf) ^ (wv & 7)) * 32 + ((nl >> 1) & 1) * 16 + g * 4) * 4);
        unsigned cr = rgn + ((nl & 1) * 64 + g * 8) * 2, r2r = L_RED2 + lane * 8, cw = rgn + lane * 4;
        PIN(r2r); PIN(cw);
        unsigned psrc1 = psrc ^ 64u, psrc2 = psrc + 1048576u, psrc3 = psrc1 + 1048576u;
        PIN(psrc1); PIN(psrc2); PIN(psrc3); PIN(psrc); PIN(pw); PIN(rw2); PIN(wa0); PIN(wa1); PIN(oo); PIN(cr);
        unsigned w1vo = (unsigned)((lane >> 1) * DFF * 2 + (lane & 1) * 16);
        const char* w1s = (const char*)(p.w1 + (size_t)wv * 64 * DFF + ug * 16);
        PIN(w1vo);
        asm volatile("" : "+s"(w1s));
        if (dnw) {
#pragma unroll
            for (int i = 0; i < N0; ++i) dma16(mvo, mws + i * 32768, __builtin_amdgcn_readfirstlane(w2_at(i)));
        }
        STAMP(24);
        float mlm = 0.f, mll = 0.f;
        if (wv == 0) {
            const unsigned mlsrc = (unsigned)ml_off(tbp * 2 + (lane >> 5), lane & 31, th);
            u32x2 ml = {SENT, SENT};
            for (unsigned spins = 0;;) {
                if (MLMASK == 0 || max(ml[0], ml[1]) == SENT) ml = __builtin_amdgcn_raw_buffer_load_b64(rw, mlsrc, 0, 16);
                mlm = __uint_as_float(ml[0]); mll = __uint_as_float(ml[1]);
                if (__all(max(ml[0], ml[1]) != SENT)) break;
                __builtin_amdgcn_s_sleep(SLPM);
                if (spin_fail(spins)) break;
                asm volatile("" ::: "memory");
            }
            ARRIVE(sy, S_ML);
        } else {
            LWAIT(sy, S_ML, 1);
        }
        STAMP(8);
        bf16x8 pa[2][4], wh[2];
        {
            u32x4 pl[8];
            asm volatile("global_load_dwordx4 %0, %8, %12 sc1\n\tglobal_load_dwordx4 %1, %9, %12 offset:1024 sc1\n\tglobal_load_dwordx4 %2, %8, %12 offset:2048 sc1\n\tglobal_load_dwordx4 %3, %9, %12 offset:3072 sc1\n\t"
                         "global_load_dwordx4 %4, %10, %12 sc1\n\tglobal_load_dwordx4 %5, %11, %12 offset:1024 sc1\n\tglobal_load_dwordx4 %6, %10, %12 offset:2048 sc1\n\tglobal_load_dwordx4 %7, %11, %12 offset:3072 sc1"
                         : "=&v"(pl[0]), "=&v"(pl[1]), "=&v"(pl[2]), "=&v"(pl[3]), "=&v"(pl[4]), "=&v"(pl[5]), "=&v"(pl[6]), "=&v"(pl[7])
                         : "v"(psrc), "v"(psrc1), "v"(psrc2), "v"(psrc3), "s"(ws) : "memory");
            if (wv == 0) {
                const float m = mlm;
                float M = m;
                M = fmaxf(M, __int_as_float(__builtin_amdgcn_update_dpp(0, __float_as_int(M), 0xB1, 0xf, 0xf, true)));
                M = fmaxf(M, __int_as_float(__builtin_amdgcn_update_dpp(0, __float_as_int(M), 0x4E, 0xf, 0xf, true)));
                M = fmaxf(M, __int_as_float(__builtin_amdgcn_update_dpp(0, __float_as_int(M), 0x141, 0xf, 0xf, true)));
                M = fmaxf(M, __int_as_float(__builtin_amdgcn_update_dpp(0, __float_as_int(M), 0x140, 0xf, 0xf, true)));
                { auto sw = __builtin_amdgcn_permlane16_swap(__float_as_uint(M), __float_as_uint(M), false, false); M = fmaxf(__uint_as_float(sw[0]), __uint_as_float(sw[1])); }
                const float e = __builtin_amdgcn_exp2f(m - M);
                float L = e * mll;
                L = dpp_add(L, 0); L = dpp_add(L, 1); L = dpp_add(L, 2); L = dpp_add(L, 3);
                { auto sw = __builtin_amdgcn_permlane16_swap(__float_as_uint(L), __float_as_uint(L), false, false); L = __uint_as_float(sw[0]) + __uint_as_float(sw[1]); }
                const float w = e * __builtin_amdgcn_rcpf(L);
                const unsigned short hi = f2bf(w);
                *LP(unsigned short, L_WGT + lane * 2) = hi;
                *LP(unsigned short, L_WGT + 128 + lane * 2) = f2bf(w - bf2f(hi));
                ARRIVE(sy, S_WGT);
            }
            LWAIT(sy, S_WGT, 1);
            wh[0] = *LP(bf16x8, wa0); wh[1] = *LP(bf16x8, wa1);
            __builtin_amdgcn_sched_barrier(0);
            VMW(0);
            asm volatile("" : "+v"(pl[0]), "+v"(pl[1]), "+v"(pl[2]), "+v"(pl[3]), "+v"(pl[4]), "+v"(pl[5]), "+v"(pl[6]), "+v"(pl[7]) :: "memory");
#pragma unroll
            for (int i = 0; i < 8; ++i) *LP(u32x4, pw + i * 1024) = pl[i];
            __builtin_amdgcn_sched_barrier(0);
            if (WOE < 8) dma_piece(two + (size_t)WOE * 8 * 512, 1024, rgn + 8192 + WOE * 1024, 8 - WOE);
            asm volatile("s_waitcnt lgkmcnt(0)" ::: "memory");
#pragma unroll
            for (int b = 0; b < 2; ++b)
#pragma unroll
                for (int t = 0; t < 4; ++t) pa[b][t] = tr_frag(pfa[t] + b * 4096, pfa[t] + b * 4096 + 512);
        }
        STAMP(2);
        {
            f32x4 d[4];
#pragma unroll
            for (int t = 0; t < 4; ++t) d[t] = __builtin_amdgcn_mfma_f32_16x16x32_bf16(pa[0][t], wh[0], f32x4{0.f, 0.f, 0.f, 0.f}, 0, 0, 0);
#pragma unroll
            for (int t = 0; t < 4; ++t) d[t] = __builtin_amdgcn_mfma_f32_16x16x32_bf16(pa[1][t], wh[1], d[t], 0, 0, 0);
#pragma unroll
            for (int t = 0; t < 4; ++t)
#pragma unroll
                for (int e = 0; e < 4; ++e) { float v = d[t][e] + __int_as_float(__builtin_amdgcn_update_dpp(0, __float_as_int(d[t][e]), 0x4E, 0xf, 0xf, true)); asm("" : "+v"(v)); d[t][e] = v; }
            const u32x4 o0 = {pk2(d[0][0], d[0][1]), pk2(d[0][2], d[0][3]), pk2(d[1][0], d[1][1]), pk2(d[1][2], d[1][3])};
            const u32x4 o1 = {pk2(d[2][0], d[2][1]), pk2(d[2][2], d[2][3]), pk2(d[3][0], d[3][1]), pk2(d[3][2], d[3][3])};
            f32x4 y[4];
#pragma unroll
            for (int t = 0; t < 4; ++t) y[t] = __builtin_amdgcn_mfma_f32_16x16x32_bf16(fuv[0][t], __builtin_bit_cast(bf16x8, o0), f32x4{0.f, 0.f, 0.f, 0.f}, 0, 0, 0);
#pragma unroll
            for (int t = 0; t < 4; ++t) y[t] = __builtin_amdgcn_mfma_f32_16x16x32_bf16(fuv[1][t], __builtin_bit_cast(bf16x8, o1), y[t], 0, 0, 0);
            if (nl < 2) {
#pragma unroll
                for (int t = 0; t < 4; ++t) *LP(f32x4, rw2 + t * 64) = y[t];
            }
        }
        VMW(0);
#pragma unroll
        for (int ks = 0; ks < 2; ++ks)
#pragma unroll
            for (int t = 0; t < 4; ++t) fwo[ks][t] = tr_frag(pfa[t] + 8192 + ks * 4096, pfa[t] + 8192 + ks * 4096 + 512);
        if (dnw) {
#pragma unroll
            for (int i = N0; i < N0 + N1; ++i) dma16(mvo, mws + i * 32768, __builtin_amdgcn_readfirstlane(w2_at(i)));
        }
        if (id < NMLP) dma16(w1vo, w1s, rgn + 7168);
        STAMP(26);
        wg_barrier();
        {
            f32x2_t cs[8];
#pragma unroll
            for (int w2 = 0; w2 < 8; ++w2) cs[w2] = *LP(f32x2_t, r2r + w2 * 512);
            const f32x2_t c2 = ((cs[0] + cs[1]) + (cs[2] + cs[3])) + ((cs[4] + cs[5]) + (cs[6] + cs[7]));
            *LP(unsigned, cw) = pk2(c2[0], c2[1]);
            asm volatile("s_waitcnt lgkmcnt(0)" ::: "memory");
        }
        f32x4 o[4];
        {
            const u32x4 cx0 = *LP(u32x4, cr), cx1 = *LP(u32x4, cr + 64);
#pragma unroll
            for (int j = 0; j < 4; ++j) o[j] = __builtin_amdgcn_mfma_f32_16x16x32_bf16(fwo[0][j], __builtin_bit_cast(bf16x8, cx0), f32x4{0.f, 0.f, 0.f, 0.f}, 0, 0, 0);
#pragma unroll
            for (int j = 0; j < 4; ++j) o[j] = __builtin_amdgcn_mfma_f32_16x16x32_bf16(fwo[1][j], __builtin_bit_cast(bf16x8, cx1), o[j], 0, 0, 0);
        }
        if (nl < 8) {
            const f32x4 sel = (nl & 4) ? ((nl & 2) ? o[3] : o[2]) : ((nl & 2) ? o[1] : o[0]);
            u32x4 sv = __builtin_bit_cast(u32x4, sel);
            if (__builtin_expect(mx4(sv, 0u) == SENT, 0)) sv = u32x4{f32s(sel[0]), f32s(sel[1]), f32s(sel[2]), f32s(sel[3])};
            __builtin_amdgcn_raw_buffer_store_b128(sv, rw, oo, 0, 16);
        }
    }
    wg_barrier();
    STAMP(9);
    if (id >= NMLP + NDOWN) {
        const int r = (id - (NMLP + NDOWN)) + 112 * wv;
        if (r >= 128) { STAMP_FLUSH(); return; }
        const int b = r >> 4, col = (r & 15) * 32 + (lane & 7) * 4 + (lane >> 5) * 2;
        const unsigned xg = *(const unsigned*)(p.x + b * 512 + col);
        unsigned osrc = (unsigned)(outp_off(r) + lane * 16), xdst = (unsigned)(WS_X1 + ((lane >> 3) & 3) * 8192 + (b * 512 + col) * 2);
        PIN(osrc); PIN(xdst);
        u32x2 ga;
        u32x4 op[8];
        for (unsigned spins = 0;;) {
            ga = __builtin_amdgcn_raw_buffer_load_b64(rw, (unsigned)(WS_MOD + (b * 3072 + 1024 + col) * 4), 0, 16);
#pragma unroll
            for (int k = 0; k < 8; ++k) op[k] = __builtin_amdgcn_raw_buffer_load_b128(rw, osrc + (k & 3) * 1024, (k >> 2) * 4096, 16);
            unsigned m = max(ga[0], ga[1]);
#pragma unroll
            for (int k = 0; k < 8; ++k) m = mx4(op[k], m);
            if (__all(m != SENT)) break;
            if (++spins > kSpinCap) break;
            asm volatile("" ::: "memory");
        }
        STAMP(25);
        const f32x4 v = ((__builtin_bit_cast(f32x4, op[0]) + __builtin_bit_cast(f32x4, op[1])) + (__builtin_bit_cast(f32x4, op[2]) + __builtin_bit_cast(f32x4, op[3]))) +
                        ((__builtin_bit_cast(f32x4, op[4]) + __builtin_bit_cast(f32x4, op[5])) + (__builtin_bit_cast(f32x4, op[6]) + __builtin_bit_cast(f32x4, op[7])));
        float w[2];
#pragma unroll
        for (int j = 0; j < 2; ++j) {
            auto t = __builtin_amdgcn_permlane32_swap(__float_as_uint(v[j]), __float_as_uint(v[2 + j]), false, false);
            w[j] = __uint_as_float(t[0]) + __uint_as_float(t[1]);
            auto u = __builtin_amdgcn_permlane16_swap(__float_as_uint(w[j]), __float_as_uint(w[j]), false, false);
            w[j] = __uint_as_float(u[0]) + __uint_as_float(u[1]);
            w[j] += __int_as_float(__builtin_amdgcn_update_dpp(0, __float_as_int(w[j]), 0x128, 0xf, 0xf, true));
        }
        unsigned t = pk2(w[0], w[1]);
        t = pk2(__uint_as_float(ga[0]) * __uint_as_float(t << 16), __uint_as_float(ga[1]) * __uint_as_float(t & 0xffff0000u));
        t = pk2s(__uint_as_float(xg << 16) + __uint_as_float(t << 16), __uint_as_float(xg & 0xffff0000u) + __uint_as_float(t & 0xffff0000u));
        __builtin_amdgcn_raw_buffer_store_b32(t, rw, xdst, 0, 16);
        __builtin_amdgcn_raw_buffer_store_b32(t, rw, xdst, 32768, 16);
        STAMP_FLUSH();
        return;
    }
    if (id < NMLP) {
        const int b = wv;
        {
            const u16x8 wb = LDM((const u16x8*)(p.w1 + (size_t)(wv * 64 + 32 + (lane >> 1)) * DFF + ug * 16 + (lane & 1) * 8));
            *LP(u16x8, rgn + 8192 + lane * 16) = wb;
            ARRIVE(sy, S_MLP);
        }
        u32x4 md[4];
        for (unsigned spins = 0;;) {
#pragma unroll
            for (int k = 0; k < 4; ++k) md[k] = __builtin_amdgcn_raw_buffer_load_b128(rw, (unsigned)(WS_MOD + (b * 3072 + 1536 + (k >> 1) * 512 + lane * 8 + (k & 1) * 4) * 4), 0, 16);
            if (__all(mx4(md[0], mx4(md[1], mx4(md[2], mx4(md[3], 0u)))) != SENT)) break;
            __builtin_amdgcn_s_sleep(8);
            if (spin_fail(spins)) break;
            asm volatile("" ::: "memory");
        }
        float scv[8];
#pragma unroll
        for (int j = 0; j < 8; ++j) {
            scv[j] = rbf(1.f + __uint_as_float(j < 4 ? md[2][j] : md[3][j - 4]));
            PIN(scv[j]);
        }
        bf16x8 wfr[16];
        unsigned har = L_H2S + ((lane & 7) * HS_LD + g * 8) * 2, haw = L_H2S + (b * HS_LD + lane * 8) * 2;
        unsigned udst = (unsigned)(WS_G3 + ((id & 7) >> 1) * 16384 + (id & 1) * 3584 + (id >> 3) * 256 + (nl * 16 + g * 4) * 2), xsrc = (unsigned)(WS_X1 + ((id & 6) | nearp) * 8192 + (b * 512 + lane * 8) * 2);
        PIN(har); PIN(haw); PIN(udst); PIN(xsrc);
        if (wv == 7) {
            LWAIT(sy, S_MLP, 8);
#pragma unroll
            for (int ks = 0; ks < 16; ++ks) {
                const unsigned a = (ks >> 1) * 16384 + 7168 + (ks & 1) * 1024 + trl;
                wfr[ks] = tr_frag(a, a + 128);
            }
            __builtin_amdgcn_sched_barrier(0);
#pragma unroll
            for (int ks = 0; ks < 16; ks += 4) asm volatile("" : "+v"(wfr[ks]), "+v"(wfr[ks + 1]), "+v"(wfr[ks + 2]), "+v"(wfr[ks + 3]));
        }
        __builtin_amdgcn_s_sleep(SLPX);
        u32x4 xr;
        for (unsigned spins = 0;;) {
            xr = __builtin_amdgcn_raw_buffer_load_b128(rw, xsrc, 0, 16);
            if (__all(mx4(xr, 0u) != SENT)) break;
            if (spin_fail(spins)) break;
            asm volatile("" ::: "memory");
        }
        STAMP(10);
        {
            f32x2_t v[4];
#pragma unroll
            for (int j = 0; j < 4; ++j) v[j] = f32x2_t{__uint_as_float(xr[j] << 16), __uint_as_float(xr[j] & 0xffff0000u)};
            const f32x2_t p1 = (v[0] + v[1]) + (v[2] + v[3]), p2 = (v[0] * v[0] + v[1] * v[1]) + (v[2] * v[2] + v[3] * v[3]);
            auto t = __builtin_amdgcn_permlane32_swap(__float_as_uint(p1[0] + p1[1]), __float_as_uint(p2[0] + p2[1]), false, false);
            float a = __uint_as_float(t[0]) + __uint_as_float(t[1]);
            a = dpp_add(a, 0); a = dpp_add(a, 1); a = dpp_add(a, 2); a = dpp_add(a, 3);
            auto t2 = __builtin_amdgcn_permlane16_swap(__float_as_uint(a), __float_as_uint(a), false, false);
            a = (__uint_as_float(t2[0]) + __uint_as_float(t2[1])) * (1.f / 512.f);
            auto t3 = __builtin_amdgcn_permlane32_swap(__float_as_uint(a), __float_as_uint(a), false, false);
            const float m = __uint_as_float(t3[0]);
            const float var = rbf(fmaxf(fmaf(-m, m, __uint_as_float(t3[1])), 0.f));
            const float mb = rbf(m), rden = __builtin_amdgcn_rcpf(rbf(__builtin_amdgcn_sqrtf(rbf(var + 1e-5f))));
            u32x4 hr;
#pragma unroll
            for (int j = 0; j < 4; ++j) {
                const f32x2_t h = ((v[j] - mb) * rden) * f32x2_t{scv[2 * j], scv[2 * j + 1]} + f32x2_t{__uint_as_float(md[j >> 1][(j & 1) * 2]), __uint_as_float(md[j >> 1][(j & 1) * 2 + 1])};
                hr[j] = pk2(h[0], h[1]);
            }
            *LP(u32x4, haw) = hr;
        }
        wg_barrier();
        if (wv == 7) {
            bf16x8 af[16];
#pragma unroll
            for (int ks = 0; ks < 16; ++ks) af[ks] = *LP(bf16x8, har + ks * 64);
            __builtin_amdgcn_sched_barrier(0);
            f32x4 d0 = {0.f, 0.f, 0.f, 0.f}, d1 = {0.f, 0.f, 0.f, 0.f};
#pragma unroll
            for (int ks = 0; ks < 16; ks += 2) {
                d0 = __builtin_amdgcn_mfma_f32_16x16x32_bf16(wfr[ks], af[ks], d0, 0, 0, 0);
                d1 = __builtin_amdgcn_mfma_f32_16x16x32_bf16(wfr[ks + 1], af[ks + 1], d1, 0, 0, 0);
            }
            d0 += d1;
            if (nl < 8) {
                const u32x2 uv = {pk2s(fmaxf(d0[0], 0.f), fmaxf(d0[1], 0.f)), pk2s(fmaxf(d0[2], 0.f), fmaxf(d0[3], 0.f))};
                __builtin_amdgcn_raw_buffer_store_b64(uv, rw, udst, 0, 16);
                __builtin_amdgcn_raw_buffer_store_b64(uv, rw, udst, 8192, 16);
            }
            STAMPW(30, 448);
        }
        STAMP(11);
        STAMP_FLUSH();
        return;
    }
    {
        bf16x8 wfr[7];
        u32x4 w2r[7];
#pragma unroll
        for (int i = N0 + N1; i < 7; ++i) w2r[i] = __builtin_nontemporal_load((const u32x4*)(mws + i * 32768 + mvo));
        __builtin_amdgcn_sched_barrier(0);
        const int fb = lane >> 3, fc = (lane & 7) * 2;
        if (wv == 0) {
            u32x2 gb;
            unsigned xg;
            for (unsigned spins = 0;;) {
                gb = __builtin_amdgcn_raw_buffer_load_b64(rw, (unsigned)(WS_MOD + (fb * 3072 + 2560 + n0 + fc) * 4), 0, 16);
                xg = __builtin_amdgcn_raw_buffer_load_b32(rw, (unsigned)(WS_X1 + ((id & 6) | nearp) * 8192 + (fb * 512 + n0 + fc) * 2), 0, 16);
                if (__all(umax3(gb[0], gb[1], xg) != SENT)) {
                    if (spins) __builtin_amdgcn_s_sleep(SLPG);
                    break;
                }
                __builtin_amdgcn_s_sleep(SLPD);
                if (spin_fail(spins)) break;
                asm volatile("" ::: "memory");
            }
            *LP(u32x4, L_H2S + lane * 16) = u32x4{xg, gb[0], gb[1], 0u};
            ARRIVE(sy, S_X);
        } else {
            LWAIT(sy, S_X, 1);
        }
        u32x4 xgb = *LP(u32x4, L_H2S + lane * 16);
        PIN(xgb);
        u32x4 uf[7];
        unsigned usrc = (unsigned)(WS_G3 + (wv >> 1) * 16384 + nearp * 8192 + (wv & 1) * 3584 + (g >> 1) * 256 + (nl & 7) * 32 + (g & 1) * 16);
        unsigned rdw = L_REDM + wv * 512 + (nl & 7) * 64 + g * 16, rdr = L_REDM + lane * 8;
        unsigned short* outp = p.out + fb * 512 + n0 + fc;
        PIN(usrc); PIN(rdw); PIN(rdr); PIN(outp);
#pragma unroll
        for (int ks = 0; ks < 7; ++ks) uf[ks] = __builtin_amdgcn_raw_buffer_load_b128(rw, usrc + ks * 512, 0, 16);
        __builtin_amdgcn_sched_barrier(0);
#pragma unroll
        for (int i = N0 + N1; i < 7; ++i) *LP(u32x4, w2_at(i) + lane * 16) = w2r[i];
        asm volatile("s_waitcnt lgkmcnt(0)" ::: "memory");
#pragma unroll
        for (int ks = 0; ks < 7; ++ks) wfr[ks] = tr_frag(w2_at(ks) + trl, w2_at(ks) + trl + 128);
        __builtin_amdgcn_sched_barrier(0);
        for (unsigned spins = 0;;) {
            unsigned m = 0u;
#pragma unroll
            for (int ks = 0; ks < 7; ++ks) m = mx4(uf[ks], m);
            if (__all(m != SENT)) break;
            if (spin_fail(spins)) break;
            asm volatile("" ::: "memory");
#pragma unroll
            for (int ks = 0; ks < 7; ++ks) uf[ks] = __builtin_amdgcn_raw_buffer_load_b128(rw, usrc + ks * 512, 0, 16);
        }
        STAMP(11);
        f32x4 d = {0.f, 0.f, 0.f, 0.f}, d2 = {0.f, 0.f, 0.f, 0.f};
#pragma unroll
        for (int ks = 0; ks < 7; ++ks) {
            if (ks & 1) d2 = __builtin_amdgcn_mfma_f32_16x16x32_bf16(wfr[ks], __builtin_bit_cast(bf16x8, uf[ks]), d2, 0, 0, 0);
            else d = __builtin_amdgcn_mfma_f32_16x16x32_bf16(wfr[ks], __builtin_bit_cast(bf16x8, uf[ks]), d, 0, 0, 0);
        }
        *LP(f32x4, rdw) = d + d2;
        wg_barrier();
        if (wv == 0) {
            f32x2_t c[8];
#pragma unroll
            for (int w2 = 0; w2 < 8; ++w2) c[w2] = *LP(f32x2_t, rdr + w2 * 512);
            const f32x2_t s = ((c[0] + c[1]) + (c[2] + c[3])) + ((c[4] + c[5]) + (c[6] + c[7]));
            unsigned t = pk2(s[0], s[1]);
            t = pk2(__uint_as_float(xgb[1]) * __uint_as_float(t << 16), __uint_as_float(xgb[2]) * __uint_as_float(t & 0xffff0000u));
            *(GAS unsigned*)outp = pk2(__uint_as_float(xgb[0] << 16) + __uint_as_float(t << 16), __uint_as_float(xgb[0] & 0xffff0000u) + __uint_as_float(t & 0xffff0000u));
        }
        STAMP_FLUSH();
    }
}

extern "C" void kernel_launch(void* const* d_in, const int* in_sizes, int n_in,
                              void* d_out, int out_size, void* d_ws, size_t ws_size,
                              hipStream_t stream) {
    Params p;
    p.x = (const unsigned short*)d_in[0];
    p.ckv = (const unsigned short*)d_in[1];
    p.w_q = (const unsigned short*)d_in[2];
    p.w_uk = (const unsigned short*)d_in[3];
    p.w_uv = (const unsigned short*)d_in[4];
    p.w_o = (const unsigned short*)d_in[5];
    p.w1 = (const unsigned short*)d_in[7];
    p.w2 = (const unsigned short*)d_in[8];
    p.c = (const unsigned short*)d_in[9];
    p.w_mod = (const unsigned short*)d_in[10];
    p.ws = (char*)d_ws;
    p.out = (unsigned short*)d_out;
    (void)hipFuncSetAttribute((const void*)k_main, hipFuncAttributeMaxDynamicSharedMemorySize, 163840);
    k_main<<<256, 512, 163840, stream>>>(p);
}
```

```cpp
#define MEAS 99
#include <hip/hip_runtime.h>
#include <hip/hip_bf16.h>
#include <stdint.h>

#define NH 32
#define SK 4096
#define DFF 1792
#define GI 19
#define ROTC 1
#define PFS 2
#define XNO 0
#define CPJ 7
#define TWD 620
#define LMD 250
#define PFM 57
#define CPFE 30400
#define SLPM 4
#define SLPD 12
#define SLPG 8
#define SLPX 4
#define SLPH 4

using u16x8 = __attribute__((ext_vector_type(8))) unsigned short;
using s16x4 = __attribute__((ext_vector_type(4))) short;
using bf16x8 = __attribute__((ext_vector_type(8))) short;
using f32x16 = __attribute__((ext_vector_type(16))) float;
using f32x4 = __attribute__((ext_vector_type(4))) float;
using u32x4 = __attribute__((ext_vector_type(4))) unsigned;
using u32x2 = __attribute__((ext_vector_type(2))) unsigned;

#define GAS __attribute__((address_space(1)))
#define LAS __attribute__((address_space(3)))
typedef GAS unsigned long long gu64;
#define RLX_AGENT __ATOMIC_RELAXED, __HIP_MEMORY_SCOPE_AGENT
#define SENT 0xffffffffu
#define TAGC 0x9e3779b9u

__device__ __forceinline__ float bf2f(unsigned short u) { return __uint_as_float(((unsigned)u) << 16); }
typedef __attribute__((ext_vector_type(2))) __bf16 bf16x2_t;
typedef __attribute__((ext_vector_type(2))) float f32x2_t;
__device__ __forceinline__ unsigned pk2(float a, float b) {
    f32x2_t v = {a, b};
    bf16x2_t r = __builtin_convertvector(v, bf16x2_t);
    return __builtin_bit_cast(unsigned, r);
}
__device__ __forceinline__ unsigned pk2s(float a, float b) {
    const unsigned r = pk2(a, b);
    return r == SENT ? 0x7fc07fc0u : r;
}
__device__ __forceinline__ unsigned f32s(float a) {
    const unsigned r = __float_as_uint(a);
    return r == SENT ? 0x7fc00000u : r;
}
__device__ __forceinline__ unsigned short f2bf(float f) { return (unsigned short)(pk2(f, 0.f) & 0xffffu); }
__device__ __forceinline__ float rbf(float f) { return __uint_as_float(pk2(f, 0.f) << 16); }
__device__ __forceinline__ float lo16(unsigned u) { return __uint_as_float(u << 16); }
__device__ __forceinline__ float hi16(unsigned u) { return __uint_as_float(u & 0xffff0000u); }
__device__ __forceinline__ unsigned umax3(unsigned a, unsigned b, unsigned c) { return max(max(a, b), c); }
__device__ __forceinline__ unsigned mx4(const u32x4& v, unsigned m) { return umax3(v[2], v[3], umax3(v[0], v[1], m)); }
__device__ __forceinline__ float fmax3(float a, float b, float c) { float r; asm("v_max3_f32 %0, %1, %2, %3" : "=v"(r) : "v"(a), "v"(b), "v"(c)); return r; }
template <int C> __device__ __forceinline__ float dppf(float v) { return __int_as_float(__builtin_amdgcn_update_dpp(0, __float_as_int(v), C, 0xf, 0xf, true)); }
__device__ __forceinline__ float sum16(float v) { v += dppf<0xB1>(v); v += dppf<0x4E>(v); v += dppf<0x141>(v); v += dppf<0x140>(v); return v; }

__device__ __forceinline__ void ln_stats(float p1, float p2, float& mb, float& rden) {
    auto t = __builtin_amdgcn_permlane32_swap(__float_as_uint(p1), __float_as_uint(p2), false, false);
    float a = sum16(__uint_as_float(t[0]) + __uint_as_float(t[1]));
    auto t2 = __builtin_amdgcn_permlane16_swap(__float_as_uint(a), __float_as_uint(a), false, false);
    a = (__uint_as_float(t2[0]) + __uint_as_float(t2[1])) * (1.f / 512.f);
    auto t3 = __builtin_amdgcn_permlane32_swap(__float_as_uint(a), __float_as_uint(a), false, false);
    const float m = __uint_as_float(t3[0]);
    const float var = rbf(fmaxf(fmaf(-m, m, __uint_as_float(t3[1])), 0.f));
    mb = rbf(m);
    rden = __builtin_amdgcn_rcpf(rbf(__builtin_amdgcn_sqrtf(rbf(var + 1e-5f))));
}
#define WS_EPOCH 0
#define WS_FI 4096
#define WS_FA 8192
#define WS_FB 24576
#define WS_H 65536
#define WS_Q 327680
#define WS_U 589824
#define WS_X1 655360
#define WS_MOD 720896
#define WS_ML 786432
#define WS_OUTP 1048576
#define WS_PART 2097152
#define WS_END 6291456

#define LP(T, off) ((LAS T*)(size_t)(unsigned)(off))
#define STAMP(k) do { if (((k) == MEAS || (k) == 0) && (threadIdx.x & 63) == 0) __hip_atomic_store((gu64*)(p.ws + (128u << 20)) + ((k) == 0 ? 0 : 2048) + blockIdx.x * 8 + wv, (unsigned long long)__builtin_amdgcn_s_memrealtime(), RLX_AGENT); \
    if ((k) == 31) { \
        const unsigned long long t1 = __builtin_amdgcn_s_memrealtime(); \
        unsigned long long m = ~0ull, mx = 0ull; \
        for (int q = 0; q < 32; ++q) { const unsigned long long v = __hip_atomic_load((gu64*)(p.ws + (128u << 20)) + q * 64 + (threadIdx.x & 63), RLX_AGENT); if (t1 - v < 10000ull) m = v < m ? v : m; } \
        for (int o = 1; o < 64; o <<= 1) { const unsigned long long v = __shfl_xor(m, o); m = v < m ? v : m; } \
        for (int q = 0; q < 32; ++q) { const unsigned long long v = __hip_atomic_load((gu64*)(p.ws + (128u << 20)) + 2048 + q * 64 + (threadIdx.x & 63), RLX_AGENT); if (v - m < 10000ull) mx = v > mx ? v : mx; } \
        for (int o = 1; o < 64; o <<= 1) { const unsigned long long v = __shfl_xor(mx, o); mx = v > mx ? v : mx; } \
        if (MEAS == 31) mx = t1; \
        const unsigned dl = MEAS == 99 ? 0u : min(__builtin_amdgcn_readfirstlane((unsigned)(mx - m)), 5000u) * 4u; \
        while (__builtin_amdgcn_s_memrealtime() < t1 + dl) __builtin_amdgcn_s_sleep(1); \
    } } while (0)
#define STAMP_FLUSH() do {} while (0)
__device__ __forceinline__ s16x4 tr4(unsigned a) { return __builtin_amdgcn_ds_read_tr16_b64_v4i16(LP(s16x4, a)); }
__device__ __forceinline__ bf16x8 cat8(s16x4 a, s16x4 b) { return __builtin_shufflevector(a, b, 0, 1, 2, 3, 4, 5, 6, 7); }
__device__ __forceinline__ bf16x8 tr_frag(unsigned a0, unsigned a1) { return cat8(tr4(a0), tr4(a1)); }
__device__ __forceinline__ unsigned swz(unsigned row) { return ((row & 3) << 2) | ((row >> 2) & 3); }
__device__ __forceinline__ unsigned off_b(unsigned row, unsigned ch) { return 256u * row + 16u * (ch ^ swz(row)); }
__device__ __forceinline__ unsigned tile_off(unsigned key, unsigned chunk) { return ((key >> 5) * 4 + (chunk >> 4)) * 8192u + off_b(key & 31, chunk & 15); }
__device__ __forceinline__ void wg_barrier() { asm volatile("s_waitcnt lgkmcnt(0)\n\ts_barrier" ::: "memory"); }
__device__ __forceinline__ void dma16(unsigned voff, const void* sbase, unsigned lds_dst) {
    asm volatile("s_mov_b32 m0, %2\n\ts_nop 0\n\tglobal_load_lds_dwordx4 %0, %1 nt" :: "v"(voff), "s"(sbase), "s"(lds_dst) : "memory", "m0");
}
__device__ __forceinline__ void pf4(unsigned voff, const void* sbase, unsigned lds_dst) {
    asm volatile("s_mov_b32 m0, %2\n\ts_nop 0\n\tglobal_load_lds_dword %0, %1 nt" :: "v"(voff), "s"(sbase), "s"(lds_dst) : "memory", "m0");
}
__device__ __forceinline__ void pf4s(unsigned voff, const void* sbase, unsigned lds_dst) {
    asm volatile("s_mov_b32 m0, %2\n\ts_nop 0\n\tglobal_load_lds_dword %0, %1 sc1" :: "v"(voff), "s"(sbase), "s"(lds_dst) : "memory", "m0");
}
#define PTOUCH 1
#define XGATE 300
#define SPINCAP (1u << 20)
constexpr unsigned kSpinCap = SPINCAP;
struct Params {
    const unsigned short* x;
    const unsigned short* ckv;
    const unsigned short* w_q;
    const unsigned short* w_uk;
    const unsigned short* w_uv;
    const unsigned short* w_o;
    const unsigned short* w1;
    const unsigned short* w2;
    const unsigned short* c;
    const unsigned short* w_mod;
    char* ws;
    unsigned short* out;
};
#define VMW(n) asm volatile("s_waitcnt vmcnt(%0)" :: "n"(n) : "memory")
#define PIN(x) asm volatile("" : "+v"(x))
#define PIN4(a, b, c, d) asm volatile("" : "+v"(a), "+v"(b), "+v"(c), "+v"(d) :: "memory")
#define MFMA16(a, b, c) __builtin_amdgcn_mfma_f32_16x16x32_bf16(a, b, c, 0, 0, 0)
#define MFMA32(a, b, c) __builtin_amdgcn_mfma_f32_32x32x16_bf16(a, b, c, 0, 0, 0)
#define BF8(x) __builtin_bit_cast(bf16x8, x)
#define ST16(v, off, soff) __builtin_amdgcn_raw_buffer_store_b128(v, rw, off, soff, 16)
#define LD16(off, soff) __builtin_amdgcn_raw_buffer_load_b128(rw, off, soff, 16)
#define SCL (0.08838834764831845f * 1.4426950408889634f)
#define L_SCR 131072
#define HS_LD 520

__global__ __launch_bounds__(512, 2) void k_main(Params p) {
    unsigned long long pc;
    asm volatile("s_getpc_b64 %0" : "=s"(pc));
    const int id = blockIdx.x, tid = threadIdx.x, lane = tid & 63;
    const int wv = __builtin_amdgcn_readfirstlane(tid >> 6);
    const int g = lane >> 4, nl = lane & 15;
    const int x = id & 7, j = id >> 3;
    if (wv < 2 && (j >> 1) == CPJ) {
        asm volatile("s_mov_b32 m0, %2\n\ts_nop 0\n\tglobal_load_lds_dword %0, %1" :: "v"(min((unsigned)((((j & 1) * 2 + wv) * 64 + lane) * 128), (unsigned)CPFE)), "s"(pc), "s"(L_SCR + 31744) : "memory", "m0");
    }
    char* ws = p.ws;
    STAMP(0);
    const __amdgpu_buffer_rsrc_t rw = __builtin_amdgcn_make_buffer_rsrc(ws, 0, WS_END, 0x00020000);
    const u32x4 s4 = {SENT, SENT, SENT, SENT};
    unsigned long long cnt = 0ull;
    auto cnt_load = [&]() { asm volatile("global_load_dwordx2 %0, %1, %2 sc1" : "+v"(cnt) : "v"((unsigned)WS_EPOCH), "s"(ws) : "memory"); };
    unsigned tlo = 0u, thi = 0u;
    auto tag_take = [&]() {
        asm volatile("" : "+v"(cnt) :: "memory");
        const unsigned long long t = cnt + 1ull;
        tlo = __builtin_amdgcn_readfirstlane((unsigned)t); thi = __builtin_amdgcn_readfirstlane((unsigned)(t >> 32));
    };
    auto flag_put = [&](unsigned base, unsigned stride, unsigned idx) {
        if (lane < 8) __builtin_amdgcn_raw_buffer_store_b64(u32x2{tlo, thi}, rw, base + lane * stride + idx * 8, 0, 16);
    };
    auto flags_ok = [&](const u32x4& f) { return (f[0] == tlo) & (f[1] == thi) & (f[2] == tlo) & (f[3] == thi); };
    auto flags_wait = [&](unsigned off) {
        for (unsigned spins = 0;;) {
            const u32x4 f = LD16(off, 0);
            if (__all(flags_ok(f))) break;
            __builtin_amdgcn_s_sleep(16);
            if (++spins > kSpinCap) break;
            asm volatile("" ::: "memory");
        }
    };
    const unsigned pcl = (unsigned)(((((lane & 7) >> 1) ^ ((lane >> 4) & 1)) * 32) + (lane & 1) * 16);
    auto dma_piece = [&](const unsigned short* src, unsigned ldb, unsigned dst, int n) {
        const unsigned v0 = (lane >> 3) * ldb + pcl, v1 = v0 ^ 64u;
#pragma unroll
        for (int i = 0; i < n; ++i) dma16((i & 1) ? v1 : v0, (const char*)src + (size_t)i * 8 * ldb, dst + i * 1024);
    };
    const unsigned frl = (unsigned)((g * 8 + (nl >> 2)) * 128 + (lane & 3) * 8), frs = (unsigned)((nl >> 3) | ((g & 1) << 1));
    auto piece_frag = [&](unsigned base, int ks, int t) {
        const unsigned a = base + frl + ((t ^ frs) << 5) + ks * 4096;
        return tr_frag(a, a + 512);
    };
    const unsigned rgn = (unsigned)(wv * 16384);

    if (j < 16) {
        const unsigned long long t_start = __builtin_amdgcn_s_memrealtime();
        const int th = x * 4 + (j & 3);
        if (j < 12) {
            const int qt = j >> 2, rot = qt * 5;
            const bool pub = wv < (qt < 2 ? 3 : 2);
            const unsigned qo = (unsigned)(WS_Q + ((nl & 7) * 32 + th) * 1024 + (qt * 192 + wv * 64 + g * 16 + (nl >> 3) * 8) * 2);
            const unsigned vb = (unsigned)(g * 8192 + ((nl ^ (g << 2)) << 4));
            const char* wuk = (const char*)(p.w_uk + th * 128) + (size_t)qt * 192 * 8192;
            const char* wq = (const char*)(p.w_q + th * 128) + (size_t)wv * 32768;
            const unsigned L_U = 114688;
            {
                const int k0 = qt < 2 ? wv * 6 : wv * 4;
#pragma unroll
                for (int i = 0; i < 6; ++i) {
                    if (i < 4 || qt < 2) dma16(vb ^ (((k0 + i) & 3) << 4), wuk + (size_t)(k0 + i) * 32768, L_U + (k0 + i) * 1024);
                    if (i == 4) VMW(4);
                }
            }
            const unsigned vq = vb ^ ((wv & 3) << 4);
            auto wq_dma = [&](int ps) { dma16(vq, wq + (size_t)((ps + rot) & 15) * 262144, ps * 8192 + wv * 1024); };
            bf16x8 uk[4][4];
            u32x4 fi;
            const unsigned fio = (unsigned)(WS_FI + x * 128 + (lane & 7) * 16);
#pragma unroll
            for (int ps = 0; ps < 14; ++ps) {
                wq_dma(ps);
                if (ps == 8) cnt_load();
                if (ps >= 8 && ps < 13) VMW(5); else VMW(4);
                if (ps == 3) {
                    asm volatile("s_barrier" ::: "memory");
#pragma unroll
                    for (int t = 0; t < 4; ++t)
#pragma unroll
                        for (int s = 0; s < 4; ++s) uk[t][s] = *LP(bf16x8, L_U + off_b((wv & 3) * 64 + (nl >> 2) * 16 + t * 4 + (nl & 3), s * 4 + g) - (wv == 3 || wv == 7 ? 16384 : 0));
                    STAMP(1);
                }
            }
#pragma unroll
            for (int t = 0; t < 4; ++t) PIN4(uk[t][0], uk[t][1], uk[t][2], uk[t][3]);
            wg_barrier();
            STAMP(15);
            wq_dma(14);
            wq_dma(15);
            const unsigned hsrc = (unsigned)(WS_H + x * 32768 + wv * 2048 + lane * 16), hsrc2 = hsrc + 16384;
            u32x4 hv[4];
            asm volatile("buffer_load_dwordx4 %0, %1, %2, 0 offen sc1" : "=v"(fi) : "v"(fio), "s"(rw) : "memory");
            asm volatile("buffer_load_dwordx4 %0, %4, %6, 0 offen sc1\n\tbuffer_load_dwordx4 %1, %4, %6, 0 offen offset:1024 sc1\n\t"
                         "buffer_load_dwordx4 %2, %5, %6, 0 offen sc1\n\tbuffer_load_dwordx4 %3, %5, %6, 0 offen offset:1024 sc1"
                         : "=&v"(hv[0]), "=&v"(hv[1]), "=&v"(hv[2]), "=&v"(hv[3]) : "v"(hsrc), "v"(hsrc2), "s"(rw) : "memory");
            bf16x8 aq[16];
            const unsigned ta0 = off_b(8 * g + (nl >> 2), 2 * wv + ((lane & 3) >> 1)) + 8 * (lane & 1), ta1 = off_b(8 * g + 4 + (nl >> 2), 2 * wv + ((lane & 3) >> 1)) + 8 * (lane & 1);
            unsigned tb0 = ta0 + 65536, tb1 = ta1 + 65536, qo2 = qo;
            PIN(tb0); PIN(tb1); PIN(qo2);
            tag_take();
            const unsigned thx = tlo ^ thi ^ TAGC;
#pragma unroll
            for (int ps = 0; ps < 10; ++ps) aq[ps] = ps < 8 ? tr_frag(ta0 + ps * 8192, ta1 + ps * 8192) : tr_frag(tb0 + (ps - 8) * 8192, tb1 + (ps - 8) * 8192);
            const unsigned L_HI = L_SCR, L_QI = L_SCR + 8192;
            unsigned hw0 = L_HI + ((2 * wv - rot) & 15) * 512 + (lane >> 4) * 128 + (lane & 7) * 16 + ((lane >> 3) & 1) * 8;
            unsigned hw1 = L_HI + ((2 * wv + 1 - rot) & 15) * 512 + (lane >> 4) * 128 + (lane & 7) * 16 + ((lane >> 3) & 1) * 8;
            unsigned hr = L_HI + g * 128 + (nl & 7) * 16, qw = L_QI + (wv >> 1) * 512 + ((wv & 1) * 2 + (g >> 1)) * 128 + (nl & 7) * 16 + (g & 1) * 8;
            PIN4(hw0, hw1, hr, qw);
            PIN4(aq[0], aq[1], aq[2], aq[3]); PIN4(aq[4], aq[5], aq[6], aq[7]); PIN(aq[8]); PIN(aq[9]);
            STAMP(2);
            for (unsigned spins = 0;;) {
                asm volatile("s_waitcnt vmcnt(0)" : "+v"(hv[0]), "+v"(hv[1]), "+v"(hv[2]), "+v"(hv[3]) :: "memory");
                unsigned bad = (hv[0][1] ^ tlo) | (hv[0][3] ^ thx) | (hv[1][1] ^ tlo) | (hv[1][3] ^ thx) | (hv[2][1] ^ tlo) | (hv[2][3] ^ thx) | (hv[3][1] ^ tlo) | (hv[3][3] ^ thx);
                PIN(bad);
                if (__all(bad == 0u)) break;
                __builtin_amdgcn_s_sleep(SLPH);
                if (++spins > kSpinCap) break;
                asm volatile("buffer_load_dwordx4 %0, %4, %6, 0 offen sc1\n\tbuffer_load_dwordx4 %1, %4, %6, 0 offen offset:1024 sc1\n\t"
                             "buffer_load_dwordx4 %2, %5, %6, 0 offen sc1\n\tbuffer_load_dwordx4 %3, %5, %6, 0 offen offset:1024 sc1"
                             : "+v"(hv[0]), "+v"(hv[1]), "+v"(hv[2]), "+v"(hv[3]) : "v"(hsrc), "v"(hsrc2), "s"(rw) : "memory");
            }
            *LP(u32x2, hw0) = u32x2{pk2(lo16(hv[2][0]) + lo16(hv[0][0]), hi16(hv[2][0]) + hi16(hv[0][0])), pk2(lo16(hv[2][2]) + lo16(hv[0][2]), hi16(hv[2][2]) + hi16(hv[0][2]))};
            *LP(u32x2, hw1) = u32x2{pk2(lo16(hv[3][0]) + lo16(hv[1][0]), hi16(hv[3][0]) + hi16(hv[1][0])), pk2(lo16(hv[3][2]) + lo16(hv[1][2]), hi16(hv[3][2]) + hi16(hv[1][2]))};
            PIN(fi);
            { unsigned bad = (fi[0] ^ tlo) | (fi[1] ^ thi) | (fi[2] ^ tlo) | (fi[3] ^ thi); PIN(bad); if (!__all(bad == 0u)) flags_wait(fio); }
            STAMP(3);
            wg_barrier();
            {
                bf16x8 hb[16];
#pragma unroll
                for (int ps = 0; ps < 10; ++ps) hb[ps] = *LP(bf16x8, hr + ps * 512);
#pragma unroll
                for (int ps = 10; ps < 16; ++ps) { hb[ps] = *LP(bf16x8, hr + ps * 512); aq[ps] = tr_frag(tb0 + (ps - 8) * 8192, tb1 + (ps - 8) * 8192); }
                __builtin_amdgcn_sched_barrier(0);
                f32x4 d0 = {0.f, 0.f, 0.f, 0.f}, d1 = {0.f, 0.f, 0.f, 0.f};
#pragma unroll
                for (int ps = 0; ps < 16; ps += 2) { d0 = MFMA16(aq[ps], hb[ps], d0); d1 = MFMA16(aq[ps + 1], hb[ps + 1], d1); }
                d0 += d1;
                *LP(u32x2, qw) = u32x2{pk2(d0[0], d0[1]), pk2(d0[2], d0[3])};
            }
            wg_barrier();
            if (pub) {
                bf16x8 qb[4];
#pragma unroll
                for (int s = 0; s < 4; ++s) qb[s] = *LP(bf16x8, hr + 8192 + s * 512);
                f32x4 d[4];
#pragma unroll
                for (int t = 0; t < 4; ++t) d[t] = MFMA16(uk[t][0], qb[0], (f32x4{0.f, 0.f, 0.f, 0.f}));
#pragma unroll
                for (int s = 1; s < 4; ++s)
#pragma unroll
                    for (int t = 0; t < 4; ++t) d[t] = MFMA16(uk[t][s], qb[s], d[t]);
                const f32x4 a = nl & 8 ? d[2] : d[0], b = nl & 8 ? d[3] : d[1];
                u32x4 qv = {pk2(a[0], a[1]), pk2(a[2], a[3]), pk2(b[0], b[1]), pk2(b[2], b[3])};
                if (__builtin_expect(mx4(qv, 0u) == SENT, 0)) qv = u32x4{pk2s(a[0], a[1]), pk2s(a[2], a[3]), pk2s(b[0], b[1]), pk2s(b[2], b[3])};
                ST16(qv, qo2, 0);
            }
            STAMP(4);
            wg_barrier();
        } else {
            const int cg = x * 2 + (j & 1);
            cnt_load();
            if (j >= 14 && !(XNO & 1)) { ST16(s4, (unsigned)(WS_Q + cg * 16384 + wv * 2048 + lane * 16), 0); ST16(s4, (unsigned)(WS_Q + cg * 16384 + wv * 2048 + lane * 16), 1024); }
            u16x8 cf, xa;
            asm volatile("global_load_dwordx4 %0, %2, %4\n\tglobal_load_dwordx4 %1, %3, %5" : "=&v"(cf), "=&v"(xa)
                         : "v"((unsigned)(((nl & 7) * 512 + (nl >> 3) * 32 + g * 8) * 2 + wv * 128)), "v"((unsigned)(wv * 1024 + lane * 16)), "s"(p.c), "s"(p.x) : "memory");
            if (j < 14) { dma_piece(p.w_mod + (size_t)wv * 64 * 3072 + cg * 64, 6144, rgn, 8); VMW(8); } else VMW(0);
            asm volatile("" : "+v"(cf), "+v"(xa) :: "memory");
            tag_take();
            if (j >= 14) { wg_barrier(); if (wv == 7) flag_put(WS_FI, 128, cg); STAMP(15); }
            u32x4 af[2];
#pragma unroll
            for (int q = 0; q < 4; ++q) {
                const float a = bf2f(cf[2 * q]), b = bf2f(cf[2 * q + 1]);
                af[0][q] = pk2(a * __builtin_amdgcn_rcpf(1.f + __expf(-a)), b * __builtin_amdgcn_rcpf(1.f + __expf(-b)));
                af[1][q] = (unsigned)__builtin_amdgcn_update_dpp(0, (int)af[0][q], 0x128, 0xf, 0xf, true);
            }
            const unsigned L_MS = L_SCR, L_XN = L_SCR + 16384;
            if (j < 14 && cg >= 8) {
                float v[8];
#pragma unroll
                for (int q = 0; q < 8; ++q) v[q] = bf2f(xa[q]);
                const float p1 = ((v[0] + v[1]) + (v[2] + v[3])) + ((v[4] + v[5]) + (v[6] + v[7]));
                const float p2 = ((v[0] * v[0] + v[1] * v[1]) + (v[2] * v[2] + v[3] * v[3])) + ((v[4] * v[4] + v[5] * v[5]) + (v[6] * v[6] + v[7] * v[7]));
                float mb, rden;
                ln_stats(p1, p2, mb, rden);
                if ((lane >> 3) == (cg & 7)) {
                    u32x4 r;
#pragma unroll
                    for (int q = 0; q < 4; ++q) { const unsigned d = pk2(v[2 * q] - mb, v[2 * q + 1] - mb); r[q] = pk2(lo16(d) * rden, hi16(d) * rden); }
                    *LP(u32x4, L_XN + wv * 128 + (lane & 7) * 16) = r;
                }
            }
            unsigned wa = L_MS + wv * 2048 + nl * 256;
            PIN(wa); PIN(af[0]); PIN(af[1]);
            auto mod_mm = [&]() {
                VMW(0);
                bf16x8 f[2][4];
#pragma unroll
                for (int t = 0; t < 4; ++t) { f[0][t] = piece_frag(rgn, 0, t); f[1][t] = piece_frag(rgn, 1, t); }
                f32x4 d[4];
#pragma unroll
                for (int t = 0; t < 4; ++t) d[t] = MFMA16(f[0][t], BF8(af[0]), (f32x4{0.f, 0.f, 0.f, 0.f}));
#pragma unroll
                for (int t = 0; t < 4; ++t) d[t] = MFMA16(f[1][t], BF8(af[1]), d[t]);
                if (nl < 8) {
#pragma unroll
                    for (int t = 0; t < 4; ++t) *LP(f32x4, wa + (((t * 4 + g) ^ nl) << 4)) = d[t];
                }
                wg_barrier();
            };
            auto sum8 = [&](unsigned a) {
                f32x4 u[8];
#pragma unroll
                for (int w2 = 0; w2 < 8; ++w2) u[w2] = *LP(f32x4, a + w2 * 2048);
                return ((u[0] + u[1]) + (u[2] + u[3])) + ((u[4] + u[5]) + (u[6] + u[7]));
            };
            if (j < 14) {
                const int hb = lane & 7, c4 = 2 * wv + (nl >> 3);
                unsigned hoff = (unsigned)(WS_H + g * 32768 + cg * 2048 + wv * 256 + nl * 16);
                unsigned sa = L_MS + g * 4096 + hb * 256 + ((c4 ^ hb) << 4), xa2 = L_XN + hb * 128 + c4 * 8;
                const unsigned thx = tlo ^ thi ^ TAGC;
                PIN(hoff); PIN(sa); PIN(xa2);
                STAMP(1);
                mod_mm();
                STAMP(2);
                f32x4 v = *LP(f32x4, sa) + *LP(f32x4, sa + 2048);
                const u32x2 xn = *LP(u32x2, xa2);
#pragma unroll
                for (int e = 0; e < 4; ++e) {
                    auto t = __builtin_amdgcn_permlane16_swap(__float_as_uint(v[e]), __float_as_uint(v[e]), false, false);
                    const float u = __uint_as_float(t[0]) + __uint_as_float(t[1]);
                    auto t2 = __builtin_amdgcn_permlane32_swap(__float_as_uint(u), __float_as_uint(u), false, false);
                    v[e] = __uint_as_float(t2[0]) + __uint_as_float(t2[1]);
                }
                u32x2 r = {pk2(v[0], v[1]), pk2(v[2], v[3])};
                if (cg >= 8) {
#pragma unroll
                    for (int q = 0; q < 2; ++q) { const unsigned sc = pk2(1.f + lo16(r[q]), 1.f + hi16(r[q])); r[q] = pk2(lo16(xn[q]) * lo16(sc), hi16(xn[q]) * hi16(sc)); }
                }
                ST16((u32x4{r[0], tlo, r[1], thx}), hoff, 0);
                ST16((u32x4{r[0], tlo, r[1], thx}), hoff, 131072);
                STAMP(5);
            }
            wg_barrier();
            while (__builtin_amdgcn_s_memrealtime() < t_start + LMD) __builtin_amdgcn_s_sleep(2);
            const int lg = x * 4 + (j & 3);
            dma_piece(p.w_mod + (size_t)wv * 64 * 3072 + 1024 + lg * 64, 6144, rgn, 8);
            mod_mm();
            if (wv < 2) {
                const int b = wv * 4 + g;
                const f32x4 v = sum8(L_MS + b * 256 + ((nl ^ b) << 4));
                ST16((u32x4{__float_as_uint(rbf(v[0])), __float_as_uint(rbf(v[1])), __float_as_uint(rbf(v[2])), __float_as_uint(rbf(v[3]))}), (unsigned)(WS_MOD + (b * 2048 + lg * 64 + nl * 4) * 4), 0);
            }
            STAMP(6);
        }
        const int bq = (j >> 2) & 1, dh = j >> 3, src = th * 2 + dh, rb = bq * 4 + (src >> 4), rc = src & 15;
        unsigned oo = (unsigned)(WS_OUTP + ((bq * 4 + (nl & 3)) * 16 + wv * 2 + (nl >> 3)) * 8192 + (src ^ wv) * 128 + ((nl >> 2) & 1) * 64 + g * 16);
        const int rcol = rc * 32 + (lane & 7) * 4 + (lane >> 5) * 2;
        unsigned xdst = (unsigned)(WS_X1 + ((lane >> 3) & 3) * 8192 + (rb * 512 + rcol) * 2);
        ST16(s4, oo, 0);
        if (wv == 0) { __builtin_amdgcn_raw_buffer_store_b32(SENT, rw, xdst, 0, 16); __builtin_amdgcn_raw_buffer_store_b32(SENT, rw, xdst, 32768, 16); }
        VMW(0);
        wg_barrier();
        if (wv == 0) flag_put(WS_FB, 1024, id);
        while (__builtin_amdgcn_s_memrealtime() < t_start + TWD) __builtin_amdgcn_s_sleep(2);
        STAMP(7);
        if ((PFM & 8) && wv == 0 && lane < 32) pf4((unsigned)((j * 32 + lane) * 8192 + x * 1024 + ((3u - ((unsigned)(size_t)p.w_uv >> 7)) & 7u) * 128), p.w_uv, L_SCR + 31744);
        dma_piece(p.w_uv + ((size_t)wv * 64 * NH + th) * 128 + dh * 64, 8192, rgn, 8);
        dma_piece(p.w_o + (size_t)(th * 128 + dh * 64) * 512 + wv * 64, 1024, rgn + 8192, 8);
        VMW(0);
        STAMP(8);
        bf16x8 fuv[2][4], fwo[2][4];
        unsigned pfa[4];
#pragma unroll
        for (int t = 0; t < 4; ++t) { pfa[t] = rgn + frl + ((t ^ frs) << 5); PIN(pfa[t]); }
#pragma unroll
        for (int ks = 0; ks < 2; ++ks)
#pragma unroll
            for (int t = 0; t < 4; ++t) {
                const unsigned a = rgn + (unsigned)((g * 4 + (nl >> 2)) * 128 + (lane & 3) * 8) + ((t ^ (unsigned)((nl >> 3) | (g & 2))) << 5) + ks * 4096;
                fuv[ks][t] = tr_frag(a, a + 2048);
                fwo[ks][t] = tr_frag(pfa[t] + 8192 + ks * 4096, pfa[t] + 8192 + ks * 4096 + 512);
            }
#pragma unroll
        for (int ks = 0; ks < 2; ++ks) { PIN4(fuv[ks][0], fuv[ks][1], fuv[ks][2], fuv[ks][3]); PIN4(fwo[ks][0], fwo[ks][1], fwo[ks][2], fwo[ks][3]); }
        const unsigned L_WGT = L_SCR, L_RED = L_SCR + 1024;
        unsigned psrc = (unsigned)(WS_PART + ((bq * 4 * 32 + th) * 8 + wv) * 2048 + ((lane >> 3) ^ (th & 7)) * 128) + pcl, psrc1 = (psrc ^ 64u) + 1024;
        unsigned pw = rgn + lane * 16, rw2 = L_RED + wv * 1024 + (nl & 3) * 256 + g * 16;
        unsigned wa0 = (nl & 3) == (g >> 1) ? L_WGT + (nl & 4) * 32 + g * 16 : L_WGT + 256, wa1 = (nl & 3) == 2 + (g >> 1) ? L_WGT + (nl & 4) * 32 + 64 + g * 16 : L_WGT + 256;
        unsigned cr = rgn + (nl & 3) * 128 + g * 16, r2r = L_RED + lane * 16, cw = rgn + lane * 8;
        unsigned osrc = (unsigned)(WS_OUTP + (rb * 16 + rc) * 8192 + lane * 16);
        PIN4(psrc, psrc1, pw, rw2); PIN4(wa0, wa1, cr, r2r); PIN4(cw, oo, osrc, xdst);
        if (wv == 1 && lane < 4) pf4s(psrc + lane * 524288u, ws, L_SCR + 31744);
        u32x2 ga = {0u, 0u};
        unsigned xg = 0u;
        float mlm = 0.f, mll = 0.f;
        if (wv == 0) {
            *LP(unsigned, L_WGT + 256 + lane * 4) = 0u;
            flags_wait(WS_FB + x * 1024 + lane * 16);
            ga = __builtin_amdgcn_raw_buffer_load_b64(rw, (unsigned)(WS_MOD + (rb * 2048 + rcol) * 4), 0, 16);
            xg = *(const unsigned*)(p.x + rb * 512 + rcol);
            PIN(ga); PIN(xg);
            STAMP(9);
            const unsigned mlsrc = (unsigned)(WS_ML + ((bq * 4 + g) * 16 + nl) * 512 + th * 16);
            u32x4 ml = {0u, 0u, ~tlo, ~thi};
            for (unsigned spins = 0;;) {
                if ((ml[2] != tlo) | (ml[3] != thi)) ml = LD16(mlsrc, 0);
                if (__all((ml[2] == tlo) & (ml[3] == thi))) break;
                __builtin_amdgcn_s_sleep(SLPM);
                if (++spins > kSpinCap) break;
                asm volatile("" ::: "memory");
            }
            mlm = __uint_as_float(ml[0]); mll = __uint_as_float(ml[1]);
        }
        wg_barrier();
        STAMP(10);
        bf16x8 pa[2][4], wh[2];
        {
            u32x4 pl[8];
            asm volatile("buffer_load_dwordx4 %0, %8, %10, 0 offen sc1\n\tbuffer_load_dwordx4 %1, %9, %10, 0 offen sc1\n\t"
                         "buffer_load_dwordx4 %2, %8, %10, %11 offen sc1\n\tbuffer_load_dwordx4 %3, %9, %10, %11 offen sc1\n\t"
                         "buffer_load_dwordx4 %4, %8, %10, %12 offen sc1\n\tbuffer_load_dwordx4 %5, %9, %10, %12 offen sc1\n\t"
                         "buffer_load_dwordx4 %6, %8, %10, %13 offen sc1\n\tbuffer_load_dwordx4 %7, %9, %10, %13 offen sc1"
                         : "=&v"(pl[0]), "=&v"(pl[1]), "=&v"(pl[2]), "=&v"(pl[3]), "=&v"(pl[4]), "=&v"(pl[5]), "=&v"(pl[6]), "=&v"(pl[7])
                         : "v"(psrc), "v"(psrc1), "s"(rw), "s"(524288u), "s"(1048576u), "s"(1572864u) : "memory");
            if (wv == 0) {
                float M = mlm;
                M = fmaxf(M, dppf<0xB1>(M)); M = fmaxf(M, dppf<0x4E>(M)); M = fmaxf(M, dppf<0x141>(M)); M = fmaxf(M, dppf<0x140>(M));
                const float e = __builtin_amdgcn_exp2f(mlm - M);
                const float w = e * __builtin_amdgcn_rcpf(sum16(e * mll));
                const unsigned short hi = f2bf(w);
                *LP(unsigned short, L_WGT + lane * 2) = hi;
                *LP(unsigned short, L_WGT + 128 + lane * 2) = f2bf(w - bf2f(hi));
            }
            wg_barrier();
            wh[0] = *LP(bf16x8, wa0); wh[1] = *LP(bf16x8, wa1);
            __builtin_amdgcn_sched_barrier(0);
            asm volatile("s_waitcnt vmcnt(0)" : "+v"(pl[0]), "+v"(pl[1]), "+v"(pl[2]), "+v"(pl[3]), "+v"(pl[4]), "+v"(pl[5]), "+v"(pl[6]), "+v"(pl[7]) :: "memory");
#pragma unroll
            for (int i = 0; i < 8; ++i) *LP(u32x4, pw + i * 1024) = pl[i];
            asm volatile("s_waitcnt lgkmcnt(0)" ::: "memory");
#pragma unroll
            for (int ks = 0; ks < 2; ++ks)
#pragma unroll
                for (int t = 0; t < 4; ++t) pa[ks][t] = tr_frag(pfa[t] + ks * 4096, pfa[t] + ks * 4096 + 512);
        }
        STAMP(11);
        {
            f32x4 d[4];
#pragma unroll
            for (int t = 0; t < 4; ++t) d[t] = MFMA16(pa[0][t], wh[0], (f32x4{0.f, 0.f, 0.f, 0.f}));
#pragma unroll
            for (int t = 0; t < 4; ++t) d[t] = MFMA16(pa[1][t], wh[1], d[t]);
#pragma unroll
            for (int t = 0; t < 4; ++t)
#pragma unroll
                for (int e = 0; e < 4; ++e) { float v = d[t][e] + dppf<0x124>(d[t][e]); asm("" : "+v"(v)); d[t][e] = v; }
            const u32x4 o0 = {pk2(d[0][0], d[0][1]), pk2(d[0][2], d[0][3]), pk2(d[1][0], d[1][1]), pk2(d[1][2], d[1][3])};
            const u32x4 o1 = {pk2(d[2][0], d[2][1]), pk2(d[2][2], d[2][3]), pk2(d[3][0], d[3][1]), pk2(d[3][2], d[3][3])};
            f32x4 y[4];
#pragma unroll
            for (int t = 0; t < 4; ++t) y[t] = MFMA16(fuv[0][t], BF8(o0), (f32x4{0.f, 0.f, 0.f, 0.f}));
#pragma unroll
            for (int t = 0; t < 4; ++t) y[t] = MFMA16(fuv[1][t], BF8(o1), y[t]);
            if (nl < 4) {
#pragma unroll
                for (int t = 0; t < 4; ++t) *LP(f32x4, rw2 + t * 64) = y[t];
            }
        }
        wg_barrier();
        {
            f32x4 cs[8];
#pragma unroll
            for (int w2 = 0; w2 < 8; ++w2) cs[w2] = *LP(f32x4, r2r + w2 * 1024);
            const f32x4 c4 = ((cs[0] + cs[1]) + (cs[2] + cs[3])) + ((cs[4] + cs[5]) + (cs[6] + cs[7]));
            *LP(u32x2, cw) = u32x2{pk2(c4[0], c4[1]), pk2(c4[2], c4[3])};
            asm volatile("s_waitcnt lgkmcnt(0)" ::: "memory");
            const u32x4 cx0 = *LP(u32x4, cr), cx1 = *LP(u32x4, cr + 64);
            f32x4 o[4];
#pragma unroll
            for (int q = 0; q < 4; ++q) o[q] = MFMA16(fwo[0][q], BF8(cx0), (f32x4{0.f, 0.f, 0.f, 0.f}));
#pragma unroll
            for (int q = 0; q < 4; ++q) o[q] = MFMA16(fwo[1][q], BF8(cx1), o[q]);
            const f32x4 sel = (nl & 8) ? ((nl & 4) ? o[3] : o[2]) : ((nl & 4) ? o[1] : o[0]);
            u32x4 sv = __builtin_bit_cast(u32x4, sel);
            if (__builtin_expect(mx4(sv, 0u) == SENT, 0)) sv = u32x4{f32s(sel[0]), f32s(sel[1]), f32s(sel[2]), f32s(sel[3])};
            ST16(sv, oo, 0);
        }
        STAMP(12);
        wg_barrier();
        if (wv == 0) {
            u32x4 op[8];
            for (unsigned spins = 0;;) {
#pragma unroll
                for (int k = 0; k < 8; ++k) op[k] = LD16(osrc + (k & 3) * 1024, (k >> 2) * 4096);
                unsigned m = 0u;
#pragma unroll
                for (int k = 0; k < 8; ++k) m = mx4(op[k], m);
                if (__all(m != SENT)) break;
                if (++spins > kSpinCap) break;
                asm volatile("" ::: "memory");
            }
            STAMP(13);
            const f32x4 v = ((__builtin_bit_cast(f32x4, op[0]) + __builtin_bit_cast(f32x4, op[1])) + (__builtin_bit_cast(f32x4, op[2]) + __builtin_bit_cast(f32x4, op[3]))) +
                            ((__builtin_bit_cast(f32x4, op[4]) + __builtin_bit_cast(f32x4, op[5])) + (__builtin_bit_cast(f32x4, op[6]) + __builtin_bit_cast(f32x4, op[7])));
            float w[2];
#pragma unroll
            for (int q = 0; q < 2; ++q) {
                auto t = __builtin_amdgcn_permlane32_swap(__float_as_uint(v[q]), __float_as_uint(v[2 + q]), false, false);
                w[q] = __uint_as_float(t[0]) + __uint_as_float(t[1]);
                auto u = __builtin_amdgcn_permlane16_swap(__float_as_uint(w[q]), __float_as_uint(w[q]), false, false);
                w[q] = __uint_as_float(u[0]) + __uint_as_float(u[1]);
                w[q] += dppf<0x128>(w[q]);
            }
            unsigned t = pk2(w[0], w[1]);
            t = pk2(__uint_as_float(ga[0]) * lo16(t), __uint_as_float(ga[1]) * hi16(t));
            t = pk2s(lo16(xg) + lo16(t), hi16(xg) + hi16(t));
            __builtin_amdgcn_raw_buffer_store_b32(t, rw, xdst, 0, 16);
            __builtin_amdgcn_raw_buffer_store_b32(t, rw, xdst, 32768, 16);
            STAMP(14);
        }
        STAMP_FLUSH();
        return;
    }

    const int s = j - 16;
    if (wv == 0 && !(XNO & 2)) cnt_load();
    {
        const unsigned ln = (unsigned)(s * 64 + lane);
        if ((PFM & 1) && wv == 0) pf4((ln & 511u) * 6144u + (x * 2 + (ln >> 9)) * 128u, p.w_mod, L_SCR);
        if ((PFM & 2) && wv == 1) {
            const unsigned short* w = s < 8 ? p.w_uk : p.w_q;
            pf4((ln & 511u) * 8192u + x * 1024u + ((3u - ((unsigned)(size_t)w >> 7)) & 7u) * 128u, w, L_SCR);
        }
        if ((PFM & 48) == 48) {
            const unsigned r0 = (unsigned)(s * 4 + (wv & 3)), row = PFS == 0 ? r0 * 8 + (lane >> 3) : PFS == 1 ? (lane >> 3) * 64 + r0 : (r0 >> 5) * 256 + (lane >> 3) * 32 + (r0 & 31);
            pf4(row * 8192u + x * 1024u + (lane & 7u) * 128u, wv < 4 ? p.w_uk : p.w_q, L_SCR);
        }
        else if ((PFM & 16) && wv >= 4) { const unsigned l2 = (unsigned)((s * 4 + (wv & 3)) * 64 + lane); pf4((l2 >> 3) * 8192u + x * 1024u + (l2 & 7u) * 128u, p.w_q, L_SCR); }
        else if ((PFM & 32) && wv >= 4) { const unsigned l2 = (unsigned)((s * 4 + (wv & 3)) * 64 + lane); pf4((l2 >> 3) * 8192u + x * 1024u + (l2 & 7u) * 128u, p.w_uk, L_SCR); }
        if ((PFM & 4) && (wv & 6) == 2) { const unsigned l2 = (unsigned)((s * 2 + (wv & 1)) * 64 + lane); pf4((l2 & 511u) * 8192u + (x * 4 + (l2 >> 9)) * 256u, p.w_q, L_SCR); }
    }
    const int ug = x * 7 + s;
    const int head = lane & 31, wp = wv & 3, ch = wv >> 2, kb = wp * 2 + ch;
    const unsigned h2 = lane >> 5;
    const unsigned vb = (unsigned)(g * 1024 + ((nl ^ (g << 2)) << 4)), lb = (unsigned)((wv >> 1) * 32768 + (wv & 1) * 4096);
    const char* ck = (const char*)p.ckv + ((size_t)x * SK + s * 256 + wv * 16) * 1024;
    const unsigned rot = ROTC ? (unsigned)s & 3u : 0u;
    auto ck_dma = [&](unsigned i) { dma16(vb ^ ((i & 3u) << 4) ^ (rot << 8) ^ (((i >> 2) & 3u) << 8), ck + (i >> 4) * 131072u + (i & 3u) * 4096u, lb + ((i >> 2) & 3u) * 8192u + (i & 3u) * 1024u); };
    bf16x8 ka[16];
    s16x4 va[8][4];
    unsigned kad[4], vad[4], vad2[4];
    auto ka_reads = [&](int cb) {
#pragma unroll
        for (int u = 0; u < 4; ++u) ka[cb * 4 + u] = *LP(bf16x8, kad[u] + cb * 8192);
    };
    auto va_reads = [&]() {
#pragma unroll
        for (int kk = 0; kk < 8; ++kk)
#pragma unroll
            for (int q = 0; q < 4; ++q) va[kk][q] = tr4((kk < 4 ? vad[q] : vad2[q]) + ((kk >> 1) & 1) * 32768 + (kk & 1) * 4096);
    };
    auto va_pin = [&]() {
#pragma unroll
        for (int q = 0; q < 8; ++q) PIN4(va[q][0], va[q][1], va[q][2], va[q][3]);
    };
    u32x4 fa = {0u, 0u, 0u, 0u};
    const unsigned fao = (unsigned)(WS_FI + x * 128 + (lane & 7) * 16);
    constexpr int LAST = GI + 4 > 19 ? GI + 4 : 19;
    const unsigned qsrc = (unsigned)(WS_Q + (x * 32 + wv * 4) * 1024 + lane * 16);
    u32x4 qb[4];
#pragma unroll
    for (int i = 0; i <= LAST; ++i) {
        ck_dma(i);
        if (i == 3) {
#pragma unroll
            for (int u = 0; u < 4; ++u) {
                kad[u] = tile_off(wp * 32 + (head ^ (ch * 16)), ch * 8 + 2 * u + h2);
                vad[u] = tile_off(4 * h2 + (nl >> 2) + 8 * (u & 1), wv * 8 + 4 * (u >> 1) + 2 * (g & 1) + ((lane & 3) >> 1)) + 8 * (lane & 1);
                vad2[u] = vad[u] + 65536;
                PIN(kad[u]); PIN(vad[u]); PIN(vad2[u]);
            }
        }
        if (i > GI && i < GI + 4) VMW(8); else if (i >= 4) VMW(4);
        if (i == 12 && wv == 0 && (XNO & 2)) { cnt_load(); VMW(0); }
        if (i == 15 && wv == 0) {
            PIN(fa);
            tag_take();
            if (!__all(flags_ok(fa))) flags_wait(fao);
        }
        if ((i & 3) == 3 && i >= 7 && i <= 19) {
            const int cb = (i - 7) >> 2;
            if (cb > 0) { PIN4(ka[cb * 4 - 4], ka[cb * 4 - 3], ka[cb * 4 - 2], ka[cb * 4 - 1]); va_pin(); }
            wg_barrier();
            ka_reads(cb);
            if ((wv >> 1) == cb) va_reads();
            STAMP(16 + cb);
        }
        if (i == 10 && wv == 0) asm volatile("buffer_load_dwordx4 %0, %1, %2, 0 offen sc1" : "+v"(fa) : "v"(fao), "s"(rw) : "memory");
        if (i == GI)
            asm volatile("buffer_load_dwordx4 %0, %4, %5, 0 offen sc1\n\tbuffer_load_dwordx4 %1, %4, %5, 0 offen offset:1024 sc1\n\t"
                         "buffer_load_dwordx4 %2, %4, %5, 0 offen offset:2048 sc1\n\tbuffer_load_dwordx4 %3, %4, %5, 0 offen offset:3072 sc1"
                         : "=&v"(qb[0]), "=&v"(qb[1]), "=&v"(qb[2]), "=&v"(qb[3]) : "v"(qsrc), "s"(rw) : "memory");
    }
    const unsigned L_X = L_SCR, L_P = L_SCR + 16384, L_EXM = L_SCR + 24576, L_EXS = L_SCR + 25600;
    unsigned nx = LAST + 1;
    unsigned qad[4];
    {
        unsigned qwr[4];
#pragma unroll
        for (int k = 0; k < 4; ++k) {
            qwr[k] = L_SCR + (wv * 4 + k) * 1024 + (g ^ rot) * 256 + ((nl ^ ((wv * 4 + k) & 15)) << 4);
            qad[k] = L_SCR + head * 1024 + (((ch * 8 + 2 * k + h2) ^ (head & 15)) << 4);
            PIN(qwr[k]); PIN(qad[k]);
        }
        PIN4(ka[12], ka[13], ka[14], ka[15]); va_pin();
        wg_barrier();
        PIN4(qb[0], qb[1], qb[2], qb[3]);
        STAMP(20);
        if (!__all(mx4(qb[0], mx4(qb[1], mx4(qb[2], mx4(qb[3], 0u)))) != SENT)) {
            for (unsigned spins = 0;;) {
                if (!__all(mx4(qb[0], 0u) != SENT)) asm volatile("buffer_load_dwordx4 %0, %1, %2, 0 offen sc1" : "+v"(qb[0]) : "v"(qsrc), "s"(rw) : "memory");
                if (!__all(mx4(qb[1], 0u) != SENT)) asm volatile("buffer_load_dwordx4 %0, %1, %2, 0 offen offset:1024 sc1" : "+v"(qb[1]) : "v"(qsrc), "s"(rw) : "memory");
                if (!__all(mx4(qb[2], 0u) != SENT)) asm volatile("buffer_load_dwordx4 %0, %1, %2, 0 offen offset:2048 sc1" : "+v"(qb[2]) : "v"(qsrc), "s"(rw) : "memory");
                if (!__all(mx4(qb[3], 0u) != SENT)) asm volatile("buffer_load_dwordx4 %0, %1, %2, 0 offen offset:3072 sc1" : "+v"(qb[3]) : "v"(qsrc), "s"(rw) : "memory");
                if (nx <= 28) {
#pragma unroll
                    for (int i = 0; i < 4; ++i) ck_dma(nx + i);
                    nx += 4;
                    VMW(4);
                } else {
                    for (; nx < 32; ++nx) ck_dma(nx);
                    VMW(0);
                }
                PIN4(qb[0], qb[1], qb[2], qb[3]);
                if (__all(mx4(qb[0], mx4(qb[1], mx4(qb[2], mx4(qb[3], 0u)))) != SENT)) break;
                if (++spins > kSpinCap) break;
            }
        }
#pragma unroll
        for (int k = 0; k < 4; ++k) *LP(u32x4, qwr[k]) = qb[k];
    }
    auto slot = [&]() { if (nx < 32) { ck_dma(nx); ++nx; } };
    unsigned xw = L_X + wv * 2048 + lane * 16, xr = xw ^ 8192u, exw = L_EXM + (head * 8 + wv) * 4, exr = L_EXM + head * 32;
    unsigned pwr = L_P + kb * 1024 + lane * 16, prd = L_P + lane * 16, esw = L_EXS + (head * 16 + h2 * 8 + wv) * 4;
    PIN4(xw, xr, exw, exr); PIN(pwr); PIN(prd); PIN(esw);
    wg_barrier();
    STAMP(21);
    bf16x8 qf[16];
#pragma unroll
    for (int ks = 0; ks < 16; ++ks) qf[ks] = *LP(bf16x8, qad[ks & 3] + (ks >> 2) * 256);
    __builtin_amdgcn_sched_barrier(0);
    float lsum, mrun;
    f32x16 o[2];
    auto meet = [&](const f32x16& sc, float (&t)[8]) {
        *LP(f32x4, xw) = f32x4{sc[8], sc[9], sc[10], sc[11]}; *LP(f32x4, xw + 1024) = f32x4{sc[12], sc[13], sc[14], sc[15]};
        wg_barrier();
        const f32x4 x0 = *LP(f32x4, xr), x1 = *LP(f32x4, xr + 1024);
#pragma unroll
        for (int r = 0; r < 4; ++r) { t[r] = sc[r] + x0[r]; t[4 + r] = sc[4 + r] + x1[r]; }
        const float m = fmax3(fmax3(t[0], t[1], t[2]), fmax3(t[3], t[4], t[5]), fmax3(t[6], t[7], t[7]));
        auto u = __builtin_amdgcn_permlane32_swap(__float_as_uint(m), __float_as_uint(m), false, false);
        *LP(float, exw) = fmax3(__uint_as_float(u[0]), __uint_as_float(u[1]), __uint_as_float(u[1]));
    };
    {
        f32x16 sc;
#pragma unroll
        for (int i = 0; i < 16; ++i) sc[i] = 0.f;
#pragma unroll
        for (int ks = 0; ks < 16; ++ks) {
            sc = MFMA32(ka[ks], qf[ks], sc);
            if ((ks & 1) == 1) slot();
            __builtin_amdgcn_sched_barrier(0);
        }
        wg_barrier();
        float t[8];
        meet(sc, t);
        wg_barrier();
        const f32x4 a0 = *LP(f32x4, exr), a1 = *LP(f32x4, exr + 16);
        mrun = fmax3(fmax3(a0[0], a0[1], a0[2]), fmax3(a0[3], a1[0], a1[1]), fmax3(a1[2], a1[3], a1[3])) * SCL;
        float e[8];
#pragma unroll
        for (int r = 0; r < 8; ++r) e[r] = __builtin_amdgcn_exp2f(fmaf(t[r], SCL, -mrun));
        *LP(u32x4, pwr) = u32x4{pk2(e[0], e[1]), pk2(e[2], e[3]), pk2(e[4], e[5]), pk2(e[6], e[7])};
        lsum = ((e[0] + e[1]) + (e[2] + e[3])) + ((e[4] + e[5]) + (e[6] + e[7]));
        wg_barrier();
        STAMP(22);
        bf16x8 pf[8];
#pragma unroll
        for (int kk = 0; kk < 8; ++kk) pf[kk] = *LP(bf16x8, prd + kk * 1024);
#pragma unroll
        for (int i = 0; i < 16; ++i) { o[0][i] = 0.f; o[1][i] = 0.f; }
        __builtin_amdgcn_sched_barrier(0);
#pragma unroll
        for (int kk = 0; kk < 8; ++kk) {
            o[0] = MFMA32(cat8(va[kk][0], va[kk][1]), pf[kk], o[0]);
            o[1] = MFMA32(cat8(va[kk][2], va[kk][3]), pf[kk], o[1]);
            if (kk & 1) slot();
            __builtin_amdgcn_sched_barrier(0);
        }
        for (; nx < 32; ++nx) ck_dma(nx);
    }
    STAMP(23);
    unsigned sw[4], srd = lb + (lane >> 3) * 128 + (((lane & 7) ^ (lane >> 3)) << 4);
#pragma unroll
    for (int q = 0; q < 4; ++q) sw[q] = lb + head * 128 + (((2 * q + h2) ^ (head & 7)) << 4);
    PIN4(sw[0], sw[1], sw[2], sw[3]);
    unsigned po = (unsigned)(WS_PART + ((x * 32 + (lane >> 3)) * 8 + (wv ^ (rot * 2))) * 2048 + (s ^ (lane >> 3)) * 128 + (lane & 7) * 16);
    unsigned mlo = (unsigned)(WS_ML + (x * 16 + s) * 512 + head * 16), esr = L_EXS + head * 64;
    PIN4(esr, srd, po, mlo);
    {
        f32x16 sc;
#pragma unroll
        for (int i = 0; i < 16; ++i) sc[i] = 0.f;
        VMW(8);
        wg_barrier();
        ka_reads(0); ka_reads(1);
        if (wv < 4) va_reads();
        __builtin_amdgcn_sched_barrier(0);
#pragma unroll
        for (int ks = 0; ks < 8; ++ks) sc = MFMA32(ka[ks], qf[ks], sc);
        __builtin_amdgcn_sched_barrier(0);
        VMW(4);
        wg_barrier();
        ka_reads(2);
        if ((wv >> 1) == 2) va_reads();
        __builtin_amdgcn_sched_barrier(0);
#pragma unroll
        for (int ks = 8; ks < 12; ++ks) sc = MFMA32(ka[ks], qf[ks], sc);
        __builtin_amdgcn_sched_barrier(0);
        VMW(0);
        wg_barrier();
        STAMP(24);
        ka_reads(3);
        __builtin_amdgcn_sched_barrier(0);
#pragma unroll
        for (int ks = 12; ks < 16; ++ks) sc = MFMA32(ka[ks], qf[ks], sc);
        float t[8];
        meet(sc, t);
        wg_barrier();
        const f32x4 a0 = *LP(f32x4, exr), a1 = *LP(f32x4, exr + 16);
        const float mnew = fmaxf(fmax3(fmax3(a0[0], a0[1], a0[2]), fmax3(a0[3], a1[0], a1[1]), fmax3(a1[2], a1[3], a1[3])) * SCL, mrun);
        float e[8];
#pragma unroll
        for (int r = 0; r < 8; ++r) e[r] = __builtin_amdgcn_exp2f(fmaf(t[r], SCL, -mnew));
        *LP(u32x4, pwr) = u32x4{pk2(e[0], e[1]), pk2(e[2], e[3]), pk2(e[4], e[5]), pk2(e[6], e[7])};
        const float al = __builtin_amdgcn_exp2f(mrun - mnew);
        *LP(float, esw) = lsum * al + (((e[0] + e[1]) + (e[2] + e[3])) + ((e[4] + e[5]) + (e[6] + e[7])));
        mrun = mnew;
        o[0] *= al; o[1] *= al;
        va_pin();
        wg_barrier();
        STAMP(25);
        if ((wv >> 1) == 3) va_reads();
        bf16x8 pf[8];
#pragma unroll
        for (int kk = 0; kk < 8; ++kk) pf[kk] = *LP(bf16x8, prd + kk * 1024);
        __builtin_amdgcn_sched_barrier(0);
#pragma unroll
        for (int kk = 0; kk < 8; ++kk) {
            o[0] = MFMA32(cat8(va[kk][0], va[kk][1]), pf[kk], o[0]);
            o[1] = MFMA32(cat8(va[kk][2], va[kk][3]), pf[kk], o[1]);
        }
#pragma unroll
        for (int t2 = 0; t2 < 2; ++t2)
#pragma unroll
            for (int gp = 0; gp < 2; ++gp) {
                const unsigned lo0 = pk2(o[t2][8 * gp + 0], o[t2][8 * gp + 1]), lo1 = pk2(o[t2][8 * gp + 2], o[t2][8 * gp + 3]);
                const unsigned hi0 = pk2(o[t2][8 * gp + 4], o[t2][8 * gp + 5]), hi1 = pk2(o[t2][8 * gp + 6], o[t2][8 * gp + 7]);
                auto r0 = __builtin_amdgcn_permlane32_swap(lo0, hi0, false, false);
                auto r1 = __builtin_amdgcn_permlane32_swap(lo1, hi1, false, false);
                *LP(u32x4, sw[t2 * 2 + gp]) = u32x4{r0[0], r1[0], r0[1], r1[1]};
            }
        asm volatile("s_waitcnt lgkmcnt(0)" ::: "memory");
        u32x4 w[4];
#pragma unroll
        for (int i = 0; i < 4; ++i) w[i] = *LP(u32x4, srd + i * 1024);
#pragma unroll
        for (int i = 0; i < 4; ++i) ST16(w[i], po, i * 131072);
        __builtin_amdgcn_sched_barrier(0);
        STAMP(26);
        u32x4 mlrec;
        {
            const f32x4 b0 = *LP(f32x4, esr), b1 = *LP(f32x4, esr + 16), b2 = *LP(f32x4, esr + 32), b3 = *LP(f32x4, esr + 48);
            const f32x4 bs = (b0 + b1) + (b2 + b3);
            mlrec = u32x4{__float_as_uint(mrun), __float_as_uint((bs[0] + bs[1]) + (bs[2] + bs[3])), tlo, thi};
            PIN(mlrec);
        }
        VMW(0);
        wg_barrier();
        if (wv == 0 && lane < 32) ST16(mlrec, mlo, 0);
        STAMP(27);
        wg_barrier();
    }
    const unsigned long long t_att = __builtin_amdgcn_s_memrealtime();
    if (s >= 11) {
        const unsigned n = (unsigned)((s - 11) * 8 + wv);
        if (n < 28) pf4((n * 64 + lane) * 1024u + x * 128u, p.w2, 0);
        { const unsigned l2 = n * 64 + lane; pf4((l2 / 5u) * (DFF * 2) + min(((unsigned)x * 448u & ~127u) + (l2 % 5u) * 128u, DFF * 2 - 4u), p.w1, 0); }
        if (wv == 0) flag_put(WS_FA, 1024, id - 128);
        STAMP_FLUSH();
        return;
    }
    const unsigned L_H2S = L_SCR, L_REDM = L_SCR + 16384;
    if (s < 7) {
        {
            const char* w1s = (const char*)(p.w1 + (size_t)wv * 64 * DFF + ug * 32);
#pragma unroll
            for (int i = 0; i < 4; ++i) dma16((unsigned)((lane >> 2) * DFF * 2 + (lane & 3) * 16), w1s + (size_t)i * 16 * DFF * 2, wv * 4096 + i * 1024);
        }
        if (wv == 0) {
            if (lane < 32) ST16(s4, (unsigned)(WS_U + ug * 512 + lane * 16), 0);
            flags_wait(WS_FB + x * 1024 + lane * 16);
        }
        VMW(0);
        if (wv == 0) flag_put(WS_FA, 1024, id - 128);
        wg_barrier();
        const int b = wv;
        u32x4 md[4];
#pragma unroll
        for (int k = 0; k < 4; ++k) md[k] = LD16((unsigned)(WS_MOD + (b * 2048 + 512 + (k >> 1) * 512 + lane * 8 + (k & 1) * 4) * 4), 0);
        bf16x8 wfr[16];
        if (wv >= 6) {
#pragma unroll
            for (int ks = 0; ks < 16; ++ks) {
                const unsigned a = (unsigned)(ks * 2048 + (g * 8 + (nl >> 2)) * 64 + (wv & 1) * 32 + (lane & 3) * 8);
                wfr[ks] = tr_frag(a, a + 256);
            }
#pragma unroll
            for (int ks = 0; ks < 16; ks += 4) PIN4(wfr[ks], wfr[ks + 1], wfr[ks + 2], wfr[ks + 3]);
        }
        float scv[8];
#pragma unroll
        for (int q = 0; q < 8; ++q) { scv[q] = rbf(1.f + __uint_as_float(q < 4 ? md[2][q] : md[3][q - 4])); PIN(scv[q]); }
        unsigned har = L_H2S + ((lane & 7) * HS_LD + g * 8) * 2, haw = L_H2S + (b * HS_LD + lane * 8) * 2;
        unsigned udst = (unsigned)(WS_U + (ug * 2 + (wv & 1)) * 256 + (nl * 16 + g * 4) * 2), xsrc = (unsigned)(WS_X1 + x * 8192 + (b * 512 + lane * 8) * 2);
        PIN4(har, haw, udst, xsrc); PIN(md[0]); PIN(md[1]);
        STAMP(28);
        while (__builtin_amdgcn_s_memrealtime() < t_att + XGATE) __builtin_amdgcn_s_sleep(2);
        u32x4 xv;
        for (unsigned spins = 0;;) {
            xv = LD16(xsrc, 0);
            if (__all(mx4(xv, 0u) != SENT)) break;
            __builtin_amdgcn_s_sleep(SLPX);
            if (++spins > kSpinCap) break;
            asm volatile("" ::: "memory");
        }
        STAMP(29);
        {
            f32x2_t v[4];
#pragma unroll
            for (int q = 0; q < 4; ++q) v[q] = f32x2_t{lo16(xv[q]), hi16(xv[q])};
            const f32x2_t p1 = (v[0] + v[1]) + (v[2] + v[3]), p2 = (v[0] * v[0] + v[1] * v[1]) + (v[2] * v[2] + v[3] * v[3]);
            float mb, rden;
            ln_stats(p1[0] + p1[1], p2[0] + p2[1], mb, rden);
            u32x4 hr;
#pragma unroll
            for (int q = 0; q < 4; ++q) {
                const f32x2_t h = ((v[q] - mb) * rden) * f32x2_t{scv[2 * q], scv[2 * q + 1]} + f32x2_t{__uint_as_float(md[q >> 1][(q & 1) * 2]), __uint_as_float(md[q >> 1][(q & 1) * 2 + 1])};
                hr[q] = pk2(h[0], h[1]);
            }
            *LP(u32x4, haw) = hr;
        }
        wg_barrier();
        if (wv >= 6) {
            bf16x8 af[16];
#pragma unroll
            for (int ks = 0; ks < 16; ++ks) af[ks] = *LP(bf16x8, har + ks * 64);
            __builtin_amdgcn_sched_barrier(0);
            f32x4 d0 = {0.f, 0.f, 0.f, 0.f}, d1 = {0.f, 0.f, 0.f, 0.f};
#pragma unroll
            for (int ks = 0; ks < 16; ks += 2) { d0 = MFMA16(wfr[ks], af[ks], d0); d1 = MFMA16(wfr[ks + 1], af[ks + 1], d1); }
            d0 += d1;
            if (nl < 8) __builtin_amdgcn_raw_buffer_store_b64(u32x2{pk2s(fmaxf(d0[0], 0.f), fmaxf(d0[1], 0.f)), pk2s(fmaxf(d0[2], 0.f), fmaxf(d0[3], 0.f))}, rw, udst, 0, 16);
            STAMP(30);
        }
        STAMP_FLUSH();
        return;
    }
    {
        const int n0 = (x * 4 + (s - 7)) * 16;
        const char* mws = (const char*)(p.w2 + (size_t)wv * 224 * 512 + n0);
#pragma unroll
        for (int i = 0; i < 7; ++i) dma16((unsigned)((lane >> 1) * 1024 + (lane & 1) * 16), mws + i * 32768, rgn + i * 1024);
        const int fb = lane >> 3, fc = (lane & 7) * 2;
        u32x2 gb = {0u, 0u};
        if (wv == 0) {
            flag_put(WS_FA, 1024, id - 128);
            flags_wait(WS_FB + x * 1024 + lane * 16);
            flags_wait(WS_FA + x * 1024 + lane * 16);
            if (id == 23 * 8 && lane == 0) __builtin_amdgcn_raw_buffer_store_b64(u32x2{tlo, thi}, rw, WS_EPOCH, 0, 16);
            gb = __builtin_amdgcn_raw_buffer_load_b64(rw, (unsigned)(WS_MOD + (fb * 2048 + 1536 + n0 + fc) * 4), 0, 16);
            PIN(gb);
        }
        VMW(0);
        bf16x8 wfr[7];
        const unsigned trl = (unsigned)(g * 8 + (nl >> 2)) * 32u + (unsigned)(lane & 3) * 8u;
#pragma unroll
        for (int ks = 0; ks < 7; ++ks) { wfr[ks] = tr_frag(rgn + ks * 1024 + trl, rgn + ks * 1024 + trl + 128); PIN(wfr[ks]); }
        unsigned usrc = (unsigned)(WS_U + wv * 3584 + (g >> 1) * 256 + (nl & 7) * 32 + (g & 1) * 16);
        unsigned rdw = L_REDM + wv * 512 + (nl & 7) * 64 + g * 16, rdr = L_REDM + lane * 8;
        unsigned short* outp = p.out + fb * 512 + n0 + fc;
        PIN(usrc); PIN(rdw); PIN(rdr); PIN(outp);
        STAMP(28);
        unsigned xg = 0u;
        if (wv == 0) {
            while (__builtin_amdgcn_s_memrealtime() < t_att + XGATE) __builtin_amdgcn_s_sleep(2);
            for (unsigned spins = 0;;) {
                xg = __builtin_amdgcn_raw_buffer_load_b32(rw, (unsigned)(WS_X1 + x * 8192 + (fb * 512 + n0 + fc) * 2), 0, 16);
                if (__all(xg != SENT)) break;
                __builtin_amdgcn_s_sleep(SLPD);
                if (++spins > kSpinCap) break;
                asm volatile("" ::: "memory");
            }
            __builtin_amdgcn_s_sleep(SLPG);
        }
        wg_barrier();
        u32x4 uf[7];
        for (unsigned spins = 0;;) {
#pragma unroll
            for (int ks = 0; ks < 7; ++ks) uf[ks] = LD16(usrc + ks * 512, 0);
            unsigned m = 0u;
#pragma unroll
            for (int ks = 0; ks < 7; ++ks) m = mx4(uf[ks], m);
            if (__all(m != SENT)) break;
            if (++spins > kSpinCap) break;
            asm volatile("" ::: "memory");
        }
        STAMP(29);
        f32x4 d = {0.f, 0.f, 0.f, 0.f}, d2 = {0.f, 0.f, 0.f, 0.f};
#pragma unroll
        for (int ks = 0; ks < 7; ++ks) {
            if (ks & 1) d2 = MFMA16(wfr[ks], BF8(uf[ks]), d2);
            else d = MFMA16(wfr[ks], BF8(uf[ks]), d);
        }
        *LP(f32x4, rdw) = d + d2;
        wg_barrier();
        if (wv == 0) {
            f32x2_t c[8];
#pragma unroll
            for (int w2 = 0; w2 < 8; ++w2) c[w2] = *LP(f32x2_t, rdr + w2 * 512);
            const f32x2_t sm = ((c[0] + c[1]) + (c[2] + c[3])) + ((c[4] + c[5]) + (c[6] + c[7]));
            unsigned t = pk2(sm[0], sm[1]);
            t = pk2(__uint_as_float(gb[0]) * lo16(t), __uint_as_float(gb[1]) * hi16(t));
            *(GAS unsigned*)outp = pk2(lo16(xg) + lo16(t), hi16(xg) + hi16(t));
            STAMP(31);
        }
        STAMP_FLUSH();
    }
}

extern "C" void kernel_launch(void* const* d_in, const int* in_sizes, int n_in,
                              void* d_out, int out_size, void* d_ws, size_t ws_size,
                              hipStream_t stream) {
    Params p;
    p.x = (const unsigned short*)d_in[0];
    p.ckv = (const unsigned short*)d_in[1];
    p.w_q = (const unsigned short*)d_in[2];
    p.w_uk = (const unsigned short*)d_in[3];
    p.w_uv = (const unsigned short*)d_in[4];
    p.w_o = (const unsigned short*)d_in[5];
    p.w1 = (const unsigned short*)d_in[7];
    p.w2 = (const unsigned short*)d_in[8];
    p.c = (const unsigned short*)d_in[9];
    p.w_mod = (const unsigned short*)d_in[10];
    p.ws = (char*)d_ws;
    p.out = (unsigned short*)d_out;
    (void)hipFuncSetAttribute((const void*)k_main, hipFuncAttributeMaxDynamicSharedMemorySize, 163840);
    k_main<<<256, 512, 163840, stream>>>(p);
}
```

```cpp
#define NTM 16
#include <hip/hip_runtime.h>
#include <hip/hip_bf16.h>
#include <stdint.h>

#define NH 32
#define SK 4096
#define DFF 1792
#define GI 19
#define ROTC 1
#define PFS 2
#define XNO 0
#define CPJ 7
#define TWD 620
#define LMD 250
#define PFM 57
#define CPFE 30400
#define SLPM 4
#define SLPD 12
#define SLPG 8
#define SLPX 4
#define SLPH 4

using u16x8 = __attribute__((ext_vector_type(8))) unsigned short;
using s16x4 = __attribute__((ext_vector_type(4))) short;
using bf16x8 = __attribute__((ext_vector_type(8))) short;
using f32x16 = __attribute__((ext_vector_type(16))) float;
using f32x4 = __attribute__((ext_vector_type(4))) float;
using u32x4 = __attribute__((ext_vector_type(4))) unsigned;
using u32x2 = __attribute__((ext_vector_type(2))) unsigned;

#define GAS __attribute__((address_space(1)))
#define LAS __attribute__((address_space(3)))
typedef GAS unsigned long long gu64;
#define RLX_AGENT __ATOMIC_RELAXED, __HIP_MEMORY_SCOPE_AGENT
#define SENT 0xffffffffu
#define TAGC 0x9e3779b9u

__device__ __forceinline__ float bf2f(unsigned short u) { return __uint_as_float(((unsigned)u) << 16); }
typedef __attribute__((ext_vector_type(2))) __bf16 bf16x2_t;
typedef __attribute__((ext_vector_type(2))) float f32x2_t;
__device__ __forceinline__ unsigned pk2(float a, float b) {
    f32x2_t v = {a, b};
    bf16x2_t r = __builtin_convertvector(v, bf16x2_t);
    return __builtin_bit_cast(unsigned, r);
}
__device__ __forceinline__ unsigned pk2s(float a, float b) {
    const unsigned r = pk2(a, b);
    return r == SENT ? 0x7fc07fc0u : r;
}
__device__ __forceinline__ unsigned f32s(float a) {
    const unsigned r = __float_as_uint(a);
    return r == SENT ? 0x7fc00000u : r;
}
__device__ __forceinline__ unsigned short f2bf(float f) { return (unsigned short)(pk2(f, 0.f) & 0xffffu); }
__device__ __forceinline__ float rbf(float f) { return __uint_as_float(pk2(f, 0.f) << 16); }
__device__ __forceinline__ float lo16(unsigned u) { return __uint_as_float(u << 16); }
__device__ __forceinline__ float hi16(unsigned u) { return __uint_as_float(u & 0xffff0000u); }
__device__ __forceinline__ unsigned umax3(unsigned a, unsigned b, unsigned c) { return max(max(a, b), c); }
__device__ __forceinline__ unsigned mx4(const u32x4& v, unsigned m) { return umax3(v[2], v[3], umax3(v[0], v[1], m)); }
__device__ __forceinline__ float fmax3(float a, float b, float c) { float r; asm("v_max3_f32 %0, %1, %2, %3" : "=v"(r) : "v"(a), "v"(b), "v"(c)); return r; }
template <int C> __device__ __forceinline__ float dppf(float v) { return __int_as_float(__builtin_amdgcn_update_dpp(0, __float_as_int(v), C, 0xf, 0xf, true)); }
__device__ __forceinline__ float sum16(float v) { v += dppf<0xB1>(v); v += dppf<0x4E>(v); v += dppf<0x141>(v); v += dppf<0x140>(v); return v; }

__device__ __forceinline__ void ln_stats(float p1, float p2, float& mb, float& rden) {
    auto t = __builtin_amdgcn_permlane32_swap(__float_as_uint(p1), __float_as_uint(p2), false, false);
    float a = sum16(__uint_as_float(t[0]) + __uint_as_float(t[1]));
    auto t2 = __builtin_amdgcn_permlane16_swap(__float_as_uint(a), __float_as_uint(a), false, false);
    a = (__uint_as_float(t2[0]) + __uint_as_float(t2[1])) * (1.f / 512.f);
    auto t3 = __builtin_amdgcn_permlane32_swap(__float_as_uint(a), __float_as_uint(a), false, false);
    const float m = __uint_as_float(t3[0]);
    const float var = rbf(fmaxf(fmaf(-m, m, __uint_as_float(t3[1])), 0.f));
    mb = rbf(m);
    rden = __builtin_amdgcn_rcpf(rbf(__builtin_amdgcn_sqrtf(rbf(var + 1e-5f))));
}
#define WS_EPOCH 0
#define WS_FI 4096
#define WS_FA 8192
#define WS_FB 24576
#define WS_H 65536
#define WS_Q 327680
#define WS_U 589824
#define WS_X1 655360
#define WS_MOD 720896
#define WS_ML 786432
#define WS_OUTP 1048576
#define WS_PART 2097152
#define WS_END 6291456

#define LP(T, off) ((LAS T*)(size_t)(unsigned)(off))
#define STAMP(k) do {} while (0)
#define STAMP_FLUSH() do {} while (0)
__device__ __forceinline__ s16x4 tr4(unsigned a) { return __builtin_amdgcn_ds_read_tr16_b64_v4i16(LP(s16x4, a)); }
__device__ __forceinline__ bf16x8 cat8(s16x4 a, s16x4 b) { return __builtin_shufflevector(a, b, 0, 1, 2, 3, 4, 5, 6, 7); }
__device__ __forceinline__ bf16x8 tr_frag(unsigned a0, unsigned a1) { return cat8(tr4(a0), tr4(a1)); }
__device__ __forceinline__ unsigned swz(unsigned row) { return ((row & 3) << 2) | ((row >> 2) & 3); }
__device__ __forceinline__ unsigned off_b(unsigned row, unsigned ch) { return 256u * row + 16u * (ch ^ swz(row)); }
__device__ __forceinline__ unsigned tile_off(unsigned key, unsigned chunk) { return ((key >> 5) * 4 + (chunk >> 4)) * 8192u + off_b(key & 31, chunk & 15); }
__device__ __forceinline__ void wg_barrier() { asm volatile("s_waitcnt lgkmcnt(0)\n\ts_barrier" ::: "memory"); }
template <int NT> __device__ __forceinline__ void dma16(unsigned voff, const void* sbase, unsigned lds_dst) {
    if (NT) asm volatile("s_mov_b32 m0, %2\n\ts_nop 0\n\tglobal_load_lds_dwordx4 %0, %1 nt" :: "v"(voff), "s"(sbase), "s"(lds_dst) : "memory", "m0");
    else asm volatile("s_mov_b32 m0, %2\n\ts_nop 0\n\tglobal_load_lds_dwordx4 %0, %1" :: "v"(voff), "s"(sbase), "s"(lds_dst) : "memory", "m0");
}
template <int NT> __device__ __forceinline__ void pf4(unsigned voff, const void* sbase, unsigned lds_dst) {
    if (NT) asm volatile("s_mov_b32 m0, %2\n\ts_nop 0\n\tglobal_load_lds_dword %0, %1 nt" :: "v"(voff), "s"(sbase), "s"(lds_dst) : "memory", "m0");
    else asm volatile("s_mov_b32 m0, %2\n\ts_nop 0\n\tglobal_load_lds_dword %0, %1" :: "v"(voff), "s"(sbase), "s"(lds_dst) : "memory", "m0");
}
__device__ __forceinline__ void pf4s(unsigned voff, const void* sbase, unsigned lds_dst) {
    asm volatile("s_mov_b32 m0, %2\n\ts_nop 0\n\tglobal_load_lds_dword %0, %1 sc1" :: "v"(voff), "s"(sbase), "s"(lds_dst) : "memory", "m0");
}
#define PTOUCH 1
#define XGATE 300
#define SPINCAP (1u << 20)
constexpr unsigned kSpinCap = SPINCAP;
struct Params {
    const unsigned short* x;
    const unsigned short* ckv;
    const unsigned short* w_q;
    const unsigned short* w_uk;
    const unsigned short* w_uv;
    const unsigned short* w_o;
    const unsigned short* w1;
    const unsigned short* w2;
    const unsigned short* c;
    const unsigned short* w_mod;
    char* ws;
    unsigned short* out;
};
#define VMW(n) asm volatile("s_waitcnt vmcnt(%0)" :: "n"(n) : "memory")
#define PIN(x) asm volatile("" : "+v"(x))
#define PIN4(a, b, c, d) asm volatile("" : "+v"(a), "+v"(b), "+v"(c), "+v"(d) :: "memory")
#define MFMA16(a, b, c) __builtin_amdgcn_mfma_f32_16x16x32_bf16(a, b, c, 0, 0, 0)
#define MFMA32(a, b, c) __builtin_amdgcn_mfma_f32_32x32x16_bf16(a, b, c, 0, 0, 0)
#define BF8(x) __builtin_bit_cast(bf16x8, x)
#define ST16(v, off, soff) __builtin_amdgcn_raw_buffer_store_b128(v, rw, off, soff, 16)
#define LD16(off, soff) __builtin_amdgcn_raw_buffer_load_b128(rw, off, soff, 16)
#define SCL (0.08838834764831845f * 1.4426950408889634f)
#define L_SCR 131072
#define HS_LD 520

__global__ __launch_bounds__(512, 2) void k_main(Params p) {
    unsigned long long pc;
    asm volatile("s_getpc_b64 %0" : "=s"(pc));
    const int id = blockIdx.x, tid = threadIdx.x, lane = tid & 63;
    const int wv = __builtin_amdgcn_readfirstlane(tid >> 6);
    const int g = lane >> 4, nl = lane & 15;
    const int x = id & 7, j = id >> 3;
    if (wv < 2 && (j >> 1) == CPJ) {
        asm volatile("s_mov_b32 m0, %2\n\ts_nop 0\n\tglobal_load_lds_dword %0, %1" :: "v"(min((unsigned)((((j & 1) * 2 + wv) * 64 + lane) * 128), (unsigned)CPFE)), "s"(pc), "s"(L_SCR + 31744) : "memory", "m0");
    }
    char* ws = p.ws;
    STAMP(0);
    const __amdgpu_buffer_rsrc_t rw = __builtin_amdgcn_make_buffer_rsrc(ws, 0, WS_END, 0x00020000);
    const u32x4 s4 = {SENT, SENT, SENT, SENT};
    unsigned long long cnt = 0ull;
    auto cnt_load = [&]() { asm volatile("global_load_dwordx2 %0, %1, %2 sc1" : "+v"(cnt) : "v"((unsigned)WS_EPOCH), "s"(ws) : "memory"); };
    unsigned tlo = 0u, thi = 0u;
    auto tag_take = [&]() {
        asm volatile("" : "+v"(cnt) :: "memory");
        const unsigned long long t = cnt + 1ull;
        tlo = __builtin_amdgcn_readfirstlane((unsigned)t); thi = __builtin_amdgcn_readfirstlane((unsigned)(t >> 32));
    };
    auto flag_put = [&](unsigned base, unsigned stride, unsigned idx) {
        if (lane < 8) __builtin_amdgcn_raw_buffer_store_b64(u32x2{tlo, thi}, rw, base + lane * stride + idx * 8, 0, 16);
    };
    auto flags_ok = [&](const u32x4& f) { return (f[0] == tlo) & (f[1] == thi) & (f[2] == tlo) & (f[3] == thi); };
    auto flags_wait = [&](unsigned off) {
        for (unsigned spins = 0;;) {
            const u32x4 f = LD16(off, 0);
            if (__all(flags_ok(f))) break;
            __builtin_amdgcn_s_sleep(16);
            if (++spins > kSpinCap) break;
            asm volatile("" ::: "memory");
        }
    };
    const unsigned pcl = (unsigned)(((((lane & 7) >> 1) ^ ((lane >> 4) & 1)) * 32) + (lane & 1) * 16);
    auto dma_piece = [&](const unsigned short* src, unsigned ldb, unsigned dst, int n, int nt) {
        const unsigned v0 = (lane >> 3) * ldb + pcl, v1 = v0 ^ 64u;
#pragma unroll
        for (int i = 0; i < n; ++i) {
            if (nt) dma16<1>((i & 1) ? v1 : v0, (const char*)src + (size_t)i * 8 * ldb, dst + i * 1024);
            else dma16<0>((i & 1) ? v1 : v0, (const char*)src + (size_t)i * 8 * ldb, dst + i * 1024);
        }
    };
    const unsigned frl = (unsigned)((g * 8 + (nl >> 2)) * 128 + (lane & 3) * 8), frs = (unsigned)((nl >> 3) | ((g & 1) << 1));
    auto piece_frag = [&](unsigned base, int ks, int t) {
        const unsigned a = base + frl + ((t ^ frs) << 5) + ks * 4096;
        return tr_frag(a, a + 512);
    };
    const unsigned rgn = (unsigned)(wv * 16384);

    if (j < 16) {
        const unsigned long long t_start = __builtin_amdgcn_s_memrealtime();
        const int th = x * 4 + (j & 3);
        if (j < 12) {
            const int qt = j >> 2, rot = qt * 5;
            const bool pub = wv < (qt < 2 ? 3 : 2);
            const unsigned qo = (unsigned)(WS_Q + ((nl & 7) * 32 + th) * 1024 + (qt * 192 + wv * 64 + g * 16 + (nl >> 3) * 8) * 2);
            const unsigned vb = (unsigned)(g * 8192 + ((nl ^ (g << 2)) << 4));
            const char* wuk = (const char*)(p.w_uk + th * 128) + (size_t)qt * 192 * 8192;
            const char* wq = (const char*)(p.w_q + th * 128) + (size_t)wv * 32768;
            const unsigned L_U = 114688;
            {
                const int k0 = qt < 2 ? wv * 6 : wv * 4;
#pragma unroll
                for (int i = 0; i < 6; ++i) {
                    if (i < 4 || qt < 2) dma16<NTM & 2>(vb ^ (((k0 + i) & 3) << 4), wuk + (size_t)(k0 + i) * 32768, L_U + (k0 + i) * 1024);
                    if (i == 4) VMW(4);
                }
            }
            const unsigned vq = vb ^ ((wv & 3) << 4);
            auto wq_dma = [&](int ps) { dma16<NTM & 2>(vq, wq + (size_t)((ps + rot) & 15) * 262144, ps * 8192 + wv * 1024); };
            bf16x8 uk[4][4];
            u32x4 fi;
            const unsigned fio = (unsigned)(WS_FI + x * 128 + (lane & 7) * 16);
#pragma unroll
            for (int ps = 0; ps < 14; ++ps) {
                wq_dma(ps);
                if (ps == 8) cnt_load();
                if (ps >= 8 && ps < 13) VMW(5); else VMW(4);
                if (ps == 3) {
                    asm volatile("s_barrier" ::: "memory");
#pragma unroll
                    for (int t = 0; t < 4; ++t)
#pragma unroll
                        for (int s = 0; s < 4; ++s) uk[t][s] = *LP(bf16x8, L_U + off_b((wv & 3) * 64 + (nl >> 2) * 16 + t * 4 + (nl & 3), s * 4 + g) - (wv == 3 || wv == 7 ? 16384 : 0));
                    STAMP(1);
                }
            }
#pragma unroll
            for (int t = 0; t < 4; ++t) PIN4(uk[t][0], uk[t][1], uk[t][2], uk[t][3]);
            wg_barrier();
            STAMP(15);
            wq_dma(14);
            wq_dma(15);
            const unsigned hsrc = (unsigned)(WS_H + x * 32768 + wv * 2048 + lane * 16), hsrc2 = hsrc + 16384;
            u32x4 hv[4];
            asm volatile("buffer_load_dwordx4 %0, %1, %2, 0 offen sc1" : "=v"(fi) : "v"(fio), "s"(rw) : "memory");
            asm volatile("buffer_load_dwordx4 %0, %4, %6, 0 offen sc1\n\tbuffer_load_dwordx4 %1, %4, %6, 0 offen offset:1024 sc1\n\t"
                         "buffer_load_dwordx4 %2, %5, %6, 0 offen sc1\n\tbuffer_load_dwordx4 %3, %5, %6, 0 offen offset:1024 sc1"
                         : "=&v"(hv[0]), "=&v"(hv[1]), "=&v"(hv[2]), "=&v"(hv[3]) : "v"(hsrc), "v"(hsrc2), "s"(rw) : "memory");
            bf16x8 aq[16];
            const unsigned ta0 = off_b(8 * g + (nl >> 2), 2 * wv + ((lane & 3) >> 1)) + 8 * (lane & 1), ta1 = off_b(8 * g + 4 + (nl >> 2), 2 * wv + ((lane & 3) >> 1)) + 8 * (lane & 1);
            unsigned tb0 = ta0 + 65536, tb1 = ta1 + 65536, qo2 = qo;
            PIN(tb0); PIN(tb1); PIN(qo2);
            tag_take();
            const unsigned thx = tlo ^ thi ^ TAGC;
#pragma unroll
            for (int ps = 0; ps < 10; ++ps) aq[ps] = ps < 8 ? tr_frag(ta0 + ps * 8192, ta1 + ps * 8192) : tr_frag(tb0 + (ps - 8) * 8192, tb1 + (ps - 8) * 8192);
            const unsigned L_HI = L_SCR, L_QI = L_SCR + 8192;
            unsigned hw0 = L_HI + ((2 * wv - rot) & 15) * 512 + (lane >> 4) * 128 + (lane & 7) * 16 + ((lane >> 3) & 1) * 8;
            unsigned hw1 = L_HI + ((2 * wv + 1 - rot) & 15) * 512 + (lane >> 4) * 128 + (lane & 7) * 16 + ((lane >> 3) & 1) * 8;
            unsigned hr = L_HI + g * 128 + (nl & 7) * 16, qw = L_QI + (wv >> 1) * 512 + ((wv & 1) * 2 + (g >> 1)) * 128 + (nl & 7) * 16 + (g & 1) * 8;
            PIN4(hw0, hw1, hr, qw);
            PIN4(aq[0], aq[1], aq[2], aq[3]); PIN4(aq[4], aq[5], aq[6], aq[7]); PIN(aq[8]); PIN(aq[9]);
            STAMP(2);
            for (unsigned spins = 0;;) {
                asm volatile("s_waitcnt vmcnt(0)" : "+v"(hv[0]), "+v"(hv[1]), "+v"(hv[2]), "+v"(hv[3]) :: "memory");
                unsigned bad = (hv[0][1] ^ tlo) | (hv[0][3] ^ thx) | (hv[1][1] ^ tlo) | (hv[1][3] ^ thx) | (hv[2][1] ^ tlo) | (hv[2][3] ^ thx) | (hv[3][1] ^ tlo) | (hv[3][3] ^ thx);
                PIN(bad);
                if (__all(bad == 0u)) break;
                __builtin_amdgcn_s_sleep(SLPH);
                if (++spins > kSpinCap) break;
                asm volatile("buffer_load_dwordx4 %0, %4, %6, 0 offen sc1\n\tbuffer_load_dwordx4 %1, %4, %6, 0 offen offset:1024 sc1\n\t"
                             "buffer_load_dwordx4 %2, %5, %6, 0 offen sc1\n\tbuffer_load_dwordx4 %3, %5, %6, 0 offen offset:1024 sc1"
                             : "+v"(hv[0]), "+v"(hv[1]), "+v"(hv[2]), "+v"(hv[3]) : "v"(hsrc), "v"(hsrc2), "s"(rw) : "memory");
            }
            *LP(u32x2, hw0) = u32x2{pk2(lo16(hv[2][0]) + lo16(hv[0][0]), hi16(hv[2][0]) + hi16(hv[0][0])), pk2(lo16(hv[2][2]) + lo16(hv[0][2]), hi16(hv[2][2]) + hi16(hv[0][2]))};
            *LP(u32x2, hw1) = u32x2{pk2(lo16(hv[3][0]) + lo16(hv[1][0]), hi16(hv[3][0]) + hi16(hv[1][0])), pk2(lo16(hv[3][2]) + lo16(hv[1][2]), hi16(hv[3][2]) + hi16(hv[1][2]))};
            PIN(fi);
            { unsigned bad = (fi[0] ^ tlo) | (fi[1] ^ thi) | (fi[2] ^ tlo) | (fi[3] ^ thi); PIN(bad); if (!__all(bad == 0u)) flags_wait(fio); }
            STAMP(3);
            wg_barrier();
            {
                bf16x8 hb[16];
#pragma unroll
                for (int ps = 0; ps < 10; ++ps) hb[ps] = *LP(bf16x8, hr + ps * 512);
#pragma unroll
                for (int ps = 10; ps < 16; ++ps) { hb[ps] = *LP(bf16x8, hr + ps * 512); aq[ps] = tr_frag(tb0 + (ps - 8) * 8192, tb1 + (ps - 8) * 8192); }
                __builtin_amdgcn_sched_barrier(0);
                f32x4 d0 = {0.f, 0.f, 0.f, 0.f}, d1 = {0.f, 0.f, 0.f, 0.f};
#pragma unroll
                for (int ps = 0; ps < 16; ps += 2) { d0 = MFMA16(aq[ps], hb[ps], d0); d1 = MFMA16(aq[ps + 1], hb[ps + 1], d1); }
                d0 += d1;
                *LP(u32x2, qw) = u32x2{pk2(d0[0], d0[1]), pk2(d0[2], d0[3])};
            }
            wg_barrier();
            if (pub) {
                bf16x8 qb[4];
#pragma unroll
                for (int s = 0; s < 4; ++s) qb[s] = *LP(bf16x8, hr + 8192 + s * 512);
                f32x4 d[4];
#pragma unroll
                for (int t = 0; t < 4; ++t) d[t] = MFMA16(uk[t][0], qb[0], (f32x4{0.f, 0.f, 0.f, 0.f}));
#pragma unroll
                for (int s = 1; s < 4; ++s)
#pragma unroll
                    for (int t = 0; t < 4; ++t) d[t] = MFMA16(uk[t][s], qb[s], d[t]);
                const f32x4 a = nl & 8 ? d[2] : d[0], b = nl & 8 ? d[3] : d[1];
                u32x4 qv = {pk2(a[0], a[1]), pk2(a[2], a[3]), pk2(b[0], b[1]), pk2(b[2], b[3])};
                if (__builtin_expect(mx4(qv, 0u) == SENT, 0)) qv = u32x4{pk2s(a[0], a[1]), pk2s(a[2], a[3]), pk2s(b[0], b[1]), pk2s(b[2], b[3])};
                ST16(qv, qo2, 0);
            }
            STAMP(4);
            wg_barrier();
        } else {
            const int cg = x * 2 + (j & 1);
            cnt_load();
            if (j >= 14 && !(XNO & 1)) { ST16(s4, (unsigned)(WS_Q + cg * 16384 + wv * 2048 + lane * 16), 0); ST16(s4, (unsigned)(WS_Q + cg * 16384 + wv * 2048 + lane * 16), 1024); }
            u16x8 cf, xa;
            asm volatile("global_load_dwordx4 %0, %2, %4\n\tglobal_load_dwordx4 %1, %3, %5" : "=&v"(cf), "=&v"(xa)
                         : "v"((unsigned)(((nl & 7) * 512 + (nl >> 3) * 32 + g * 8) * 2 + wv * 128)), "v"((unsigned)(wv * 1024 + lane * 16)), "s"(p.c), "s"(p.x) : "memory");
            if (j < 14) { dma_piece(p.w_mod + (size_t)wv * 64 * 3072 + cg * 64, 6144, rgn, 8, NTM & 2); VMW(8); } else VMW(0);
            asm volatile("" : "+v"(cf), "+v"(xa) :: "memory");
            tag_take();
            if (j >= 14) { wg_barrier(); if (wv == 7) flag_put(WS_FI, 128, cg); STAMP(15); }
            u32x4 af[2];
#pragma unroll
            for (int q = 0; q < 4; ++q) {
                const float a = bf2f(cf[2 * q]), b = bf2f(cf[2 * q + 1]);
                af[0][q] = pk2(a * __builtin_amdgcn_rcpf(1.f + __expf(-a)), b * __builtin_amdgcn_rcpf(1.f + __expf(-b)));
                af[1][q] = (unsigned)__builtin_amdgcn_update_dpp(0, (int)af[0][q], 0x128, 0xf, 0xf, true);
            }
            const unsigned L_MS = L_SCR, L_XN = L_SCR + 16384;
            if (j < 14 && cg >= 8) {
                float v[8];
#pragma unroll
                for (int q = 0; q < 8; ++q) v[q] = bf2f(xa[q]);
                const float p1 = ((v[0] + v[1]) + (v[2] + v[3])) + ((v[4] + v[5]) + (v[6] + v[7]));
                const float p2 = ((v[0] * v[0] + v[1] * v[1]) + (v[2] * v[2] + v[3] * v[3])) + ((v[4] * v[4] + v[5] * v[5]) + (v[6] * v[6] + v[7] * v[7]));
                float mb, rden;
                ln_stats(p1, p2, mb, rden);
                if ((lane >> 3) == (cg & 7)) {
                    u32x4 r;
#pragma unroll
                    for (int q = 0; q < 4; ++q) { const unsigned d = pk2(v[2 * q] - mb, v[2 * q + 1] - mb); r[q] = pk2(lo16(d) * rden, hi16(d) * rden); }
                    *LP(u32x4, L_XN + wv * 128 + (lane & 7) * 16) = r;
                }
            }
            unsigned wa = L_MS + wv * 2048 + nl * 256;
            PIN(wa); PIN(af[0]); PIN(af[1]);
            auto mod_mm = [&]() {
                VMW(0);
                bf16x8 f[2][4];
#pragma unroll
                for (int t = 0; t < 4; ++t) { f[0][t] = piece_frag(rgn, 0, t); f[1][t] = piece_frag(rgn, 1, t); }
                f32x4 d[4];
#pragma unroll
                for (int t = 0; t < 4; ++t) d[t] = MFMA16(f[0][t], BF8(af[0]), (f32x4{0.f, 0.f, 0.f, 0.f}));
#pragma unroll
                for (int t = 0; t < 4; ++t) d[t] = MFMA16(f[1][t], BF8(af[1]), d[t]);
                if (nl < 8) {
#pragma unroll
                    for (int t = 0; t < 4; ++t) *LP(f32x4, wa + (((t * 4 + g) ^ nl) << 4)) = d[t];
                }
                wg_barrier();
            };
            auto sum8 = [&](unsigned a) {
                f32x4 u[8];
#pragma unroll
                for (int w2 = 0; w2 < 8; ++w2) u[w2] = *LP(f32x4, a + w2 * 2048);
                return ((u[0] + u[1]) + (u[2] + u[3])) + ((u[4] + u[5]) + (u[6] + u[7]));
            };
            if (j < 14) {
                const int hb = lane & 7, c4 = 2 * wv + (nl >> 3);
                unsigned hoff = (unsigned)(WS_H + g * 32768 + cg * 2048 + wv * 256 + nl * 16);
                unsigned sa = L_MS + g * 4096 + hb * 256 + ((c4 ^ hb) << 4), xa2 = L_XN + hb * 128 + c4 * 8;
                const unsigned thx = tlo ^ thi ^ TAGC;
                PIN(hoff); PIN(sa); PIN(xa2);
                STAMP(1);
                mod_mm();
                STAMP(2);
                f32x4 v = *LP(f32x4, sa) + *LP(f32x4, sa + 2048);
                const u32x2 xn = *LP(u32x2, xa2);
#pragma unroll
                for (int e = 0; e < 4; ++e) {
                    auto t = __builtin_amdgcn_permlane16_swap(__float_as_uint(v[e]), __float_as_uint(v[e]), false, false);
                    const float u = __uint_as_float(t[0]) + __uint_as_float(t[1]);
                    auto t2 = __builtin_amdgcn_permlane32_swap(__float_as_uint(u), __float_as_uint(u), false, false);
                    v[e] = __uint_as_float(t2[0]) + __uint_as_float(t2[1]);
                }
                u32x2 r = {pk2(v[0], v[1]), pk2(v[2], v[3])};
                if (cg >= 8) {
#pragma unroll
                    for (int q = 0; q < 2; ++q) { const unsigned sc = pk2(1.f + lo16(r[q]), 1.f + hi16(r[q])); r[q] = pk2(lo16(xn[q]) * lo16(sc), hi16(xn[q]) * hi16(sc)); }
                }
                ST16((u32x4{r[0], tlo, r[1], thx}), hoff, 0);
                ST16((u32x4{r[0], tlo, r[1], thx}), hoff, 131072);
                STAMP(5);
            }
            wg_barrier();
            while (__builtin_amdgcn_s_memrealtime() < t_start + LMD) __builtin_amdgcn_s_sleep(2);
            const int lg = x * 4 + (j & 3);
            dma_piece(p.w_mod + (size_t)wv * 64 * 3072 + 1024 + lg * 64, 6144, rgn, 8, NTM & 4);
            mod_mm();
            if (wv < 2) {
                const int b = wv * 4 + g;
                const f32x4 v = sum8(L_MS + b * 256 + ((nl ^ b) << 4));
                ST16((u32x4{__float_as_uint(rbf(v[0])), __float_as_uint(rbf(v[1])), __float_as_uint(rbf(v[2])), __float_as_uint(rbf(v[3]))}), (unsigned)(WS_MOD + (b * 2048 + lg * 64 + nl * 4) * 4), 0);
            }
            STAMP(6);
        }
        const int bq = (j >> 2) & 1, dh = j >> 3, src = th * 2 + dh, rb = bq * 4 + (src >> 4), rc = src & 15;
        unsigned oo = (unsigned)(WS_OUTP + ((bq * 4 + (nl & 3)) * 16 + wv * 2 + (nl >> 3)) * 8192 + (src ^ wv) * 128 + ((nl >> 2) & 1) * 64 + g * 16);
        const int rcol = rc * 32 + (lane & 7) * 4 + (lane >> 5) * 2;
        unsigned xdst = (unsigned)(WS_X1 + ((lane >> 3) & 3) * 8192 + (rb * 512 + rcol) * 2);
        ST16(s4, oo, 0);
        if (wv == 0) { __builtin_amdgcn_raw_buffer_store_b32(SENT, rw, xdst, 0, 16); __builtin_amdgcn_raw_buffer_store_b32(SENT, rw, xdst, 32768, 16); }
        VMW(0);
        wg_barrier();
        if (wv == 0) flag_put(WS_FB, 1024, id);
        while (__builtin_amdgcn_s_memrealtime() < t_start + TWD) __builtin_amdgcn_s_sleep(2);
        STAMP(7);
        if ((PFM & 8) && wv == 0 && lane < 32) pf4<NTM & 8>((unsigned)((j * 32 + lane) * 8192 + x * 1024 + ((3u - ((unsigned)(size_t)p.w_uv >> 7)) & 7u) * 128), p.w_uv, L_SCR + 31744);
        dma_piece(p.w_uv + ((size_t)wv * 64 * NH + th) * 128 + dh * 64, 8192, rgn, 8, NTM & 8);
        dma_piece(p.w_o + (size_t)(th * 128 + dh * 64) * 512 + wv * 64, 1024, rgn + 8192, 8, NTM & 8);
        VMW(0);
        STAMP(8);
        bf16x8 fuv[2][4], fwo[2][4];
        unsigned pfa[4];
#pragma unroll
        for (int t = 0; t < 4; ++t) { pfa[t] = rgn + frl + ((t ^ frs) << 5); PIN(pfa[t]); }
#pragma unroll
        for (int ks = 0; ks < 2; ++ks)
#pragma unroll
            for (int t = 0; t < 4; ++t) {
                const unsigned a = rgn + (unsigned)((g * 4 + (nl >> 2)) * 128 + (lane & 3) * 8) + ((t ^ (unsigned)((nl >> 3) | (g & 2))) << 5) + ks * 4096;
                fuv[ks][t] = tr_frag(a, a + 2048);
                fwo[ks][t] = tr_frag(pfa[t] + 8192 + ks * 4096, pfa[t] + 8192 + ks * 4096 + 512);
            }
#pragma unroll
        for (int ks = 0; ks < 2; ++ks) { PIN4(fuv[ks][0], fuv[ks][1], fuv[ks][2], fuv[ks][3]); PIN4(fwo[ks][0], fwo[ks][1], fwo[ks][2], fwo[ks][3]); }
        const unsigned L_WGT = L_SCR, L_RED = L_SCR + 1024;
        unsigned psrc = (unsigned)(WS_PART + ((bq * 4 * 32 + th) * 8 + wv) * 2048 + ((lane >> 3) ^ (th & 7)) * 128) + pcl, psrc1 = (psrc ^ 64u) + 1024;
        unsigned pw = rgn + lane * 16, rw2 = L_RED + wv * 1024 + (nl & 3) * 256 + g * 16;
        unsigned wa0 = (nl & 3) == (g >> 1) ? L_WGT + (nl & 4) * 32 + g * 16 : L_WGT + 256, wa1 = (nl & 3) == 2 + (g >> 1) ? L_WGT + (nl & 4) * 32 + 64 + g * 16 : L_WGT + 256;
        unsigned cr = rgn + (nl & 3) * 128 + g * 16, r2r = L_RED + lane * 16, cw = rgn + lane * 8;
        unsigned osrc = (unsigned)(WS_OUTP + (rb * 16 + rc) * 8192 + lane * 16);
        PIN4(psrc, psrc1, pw, rw2); PIN4(wa0, wa1, cr, r2r); PIN4(cw, oo, osrc, xdst);
        if (wv == 1 && lane < 4) pf4s(psrc + lane * 524288u, ws, L_SCR + 31744);
        u32x2 ga = {0u, 0u};
        unsigned xg = 0u;
        float mlm = 0.f, mll = 0.f;
        if (wv == 0) {
            *LP(unsigned, L_WGT + 256 + lane * 4) = 0u;
            flags_wait(WS_FB + x * 1024 + lane * 16);
            ga = __builtin_amdgcn_raw_buffer_load_b64(rw, (unsigned)(WS_MOD + (rb * 2048 + rcol) * 4), 0, 16);
            xg = *(const unsigned*)(p.x + rb * 512 + rcol);
            PIN(ga); PIN(xg);
            STAMP(9);
            const unsigned mlsrc = (unsigned)(WS_ML + ((bq * 4 + g) * 16 + nl) * 512 + th * 16);
            u32x4 ml = {0u, 0u, ~tlo, ~thi};
            for (unsigned spins = 0;;) {
                if ((ml[2] != tlo) | (ml[3] != thi)) ml = LD16(mlsrc, 0);
                if (__all((ml[2] == tlo) & (ml[3] == thi))) break;
                __builtin_amdgcn_s_sleep(SLPM);
                if (++spins > kSpinCap) break;
                asm volatile("" ::: "memory");
            }
            mlm = __uint_as_float(ml[0]); mll = __uint_as_float(ml[1]);
        }
        wg_barrier();
        STAMP(10);
        bf16x8 pa[2][4], wh[2];
        {
            u32x4 pl[8];
            asm volatile("buffer_load_dwordx4 %0, %8, %10, 0 offen sc1\n\tbuffer_load_dwordx4 %1, %9, %10, 0 offen sc1\n\t"
                         "buffer_load_dwordx4 %2, %8, %10, %11 offen sc1\n\tbuffer_load_dwordx4 %3, %9, %10, %11 offen sc1\n\t"
                         "buffer_load_dwordx4 %4, %8, %10, %12 offen sc1\n\tbuffer_load_dwordx4 %5, %9, %10, %12 offen sc1\n\t"
                         "buffer_load_dwordx4 %6, %8, %10, %13 offen sc1\n\tbuffer_load_dwordx4 %7, %9, %10, %13 offen sc1"
                         : "=&v"(pl[0]), "=&v"(pl[1]), "=&v"(pl[2]), "=&v"(pl[3]), "=&v"(pl[4]), "=&v"(pl[5]), "=&v"(pl[6]), "=&v"(pl[7])
                         : "v"(psrc), "v"(psrc1), "s"(rw), "s"(524288u), "s"(1048576u), "s"(1572864u) : "memory");
            if (wv == 0) {
                float M = mlm;
                M = fmaxf(M, dppf<0xB1>(M)); M = fmaxf(M, dppf<0x4E>(M)); M = fmaxf(M, dppf<0x141>(M)); M = fmaxf(M, dppf<0x140>(M));
                const float e = __builtin_amdgcn_exp2f(mlm - M);
                const float w = e * __builtin_amdgcn_rcpf(sum16(e * mll));
                const unsigned short hi = f2bf(w);
                *LP(unsigned short, L_WGT + lane * 2) = hi;
                *LP(unsigned short, L_WGT + 128 + lane * 2) = f2bf(w - bf2f(hi));
            }
            wg_barrier();
            wh[0] = *LP(bf16x8, wa0); wh[1] = *LP(bf16x8, wa1);
            __builtin_amdgcn_sched_barrier(0);
            asm volatile("s_waitcnt vmcnt(0)" : "+v"(pl[0]), "+v"(pl[1]), "+v"(pl[2]), "+v"(pl[3]), "+v"(pl[4]), "+v"(pl[5]), "+v"(pl[6]), "+v"(pl[7]) :: "memory");
#pragma unroll
            for (int i = 0; i < 8; ++i) *LP(u32x4, pw + i * 1024) = pl[i];
            asm volatile("s_waitcnt lgkmcnt(0)" ::: "memory");
#pragma unroll
            for (int ks = 0; ks < 2; ++ks)
#pragma unroll
                for (int t = 0; t < 4; ++t) pa[ks][t] = tr_frag(pfa[t] + ks * 4096, pfa[t] + ks * 4096 + 512);
        }
        STAMP(11);
        {
            f32x4 d[4];
#pragma unroll
            for (int t = 0; t < 4; ++t) d[t] = MFMA16(pa[0][t], wh[0], (f32x4{0.f, 0.f, 0.f, 0.f}));
#pragma unroll
            for (int t = 0; t < 4; ++t) d[t] = MFMA16(pa[1][t], wh[1], d[t]);
#pragma unroll
            for (int t = 0; t < 4; ++t)
#pragma unroll
                for (int e = 0; e < 4; ++e) { float v = d[t][e] + dppf<0x124>(d[t][e]); asm("" : "+v"(v)); d[t][e] = v; }
            const u32x4 o0 = {pk2(d[0][0], d[0][1]), pk2(d[0][2], d[0][3]), pk2(d[1][0], d[1][1]), pk2(d[1][2], d[1][3])};
            const u32x4 o1 = {pk2(d[2][0], d[2][1]), pk2(d[2][2], d[2][3]), pk2(d[3][0], d[3][1]), pk2(d[3][2], d[3][3])};
            f32x4 y[4];
#pragma unroll
            for (int t = 0; t < 4; ++t) y[t] = MFMA16(fuv[0][t], BF8(o0), (f32x4{0.f, 0.f, 0.f, 0.f}));
#pragma unroll
            for (int t = 0; t < 4; ++t) y[t] = MFMA16(fuv[1][t], BF8(o1), y[t]);
            if (nl < 4) {
#pragma unroll
                for (int t = 0; t < 4; ++t) *LP(f32x4, rw2 + t * 64) = y[t];
            }
        }
        wg_barrier();
        {
            f32x4 cs[8];
#pragma unroll
            for (int w2 = 0; w2 < 8; ++w2) cs[w2] = *LP(f32x4, r2r + w2 * 1024);
            const f32x4 c4 = ((cs[0] + cs[1]) + (cs[2] + cs[3])) + ((cs[4] + cs[5]) + (cs[6] + cs[7]));
            *LP(u32x2, cw) = u32x2{pk2(c4[0], c4[1]), pk2(c4[2], c4[3])};
            asm volatile("s_waitcnt lgkmcnt(0)" ::: "memory");
            const u32x4 cx0 = *LP(u32x4, cr), cx1 = *LP(u32x4, cr + 64);
            f32x4 o[4];
#pragma unroll
            for (int q = 0; q < 4; ++q) o[q] = MFMA16(fwo[0][q], BF8(cx0), (f32x4{0.f, 0.f, 0.f, 0.f}));
#pragma unroll
            for (int q = 0; q < 4; ++q) o[q] = MFMA16(fwo[1][q], BF8(cx1), o[q]);
            const f32x4 sel = (nl & 8) ? ((nl & 4) ? o[3] : o[2]) : ((nl & 4) ? o[1] : o[0]);
            u32x4 sv = __builtin_bit_cast(u32x4, sel);
            if (__builtin_expect(mx4(sv, 0u) == SENT, 0)) sv = u32x4{f32s(sel[0]), f32s(sel[1]), f32s(sel[2]), f32s(sel[3])};
            ST16(sv, oo, 0);
        }
        STAMP(12);
        wg_barrier();
        if (wv == 0) {
            u32x4 op[8];
            for (unsigned spins = 0;;) {
#pragma unroll
                for (int k = 0; k < 8; ++k) op[k] = LD16(osrc + (k & 3) * 1024, (k >> 2) * 4096);
                unsigned m = 0u;
#pragma unroll
                for (int k = 0; k < 8; ++k) m = mx4(op[k], m);
                if (__all(m != SENT)) break;
                if (++spins > kSpinCap) break;
                asm volatile("" ::: "memory");
            }
            STAMP(13);
            const f32x4 v = ((__builtin_bit_cast(f32x4, op[0]) + __builtin_bit_cast(f32x4, op[1])) + (__builtin_bit_cast(f32x4, op[2]) + __builtin_bit_cast(f32x4, op[3]))) +
                            ((__builtin_bit_cast(f32x4, op[4]) + __builtin_bit_cast(f32x4, op[5])) + (__builtin_bit_cast(f32x4, op[6]) + __builtin_bit_cast(f32x4, op[7])));
            float w[2];
#pragma unroll
            for (int q = 0; q < 2; ++q) {
                auto t = __builtin_amdgcn_permlane32_swap(__float_as_uint(v[q]), __float_as_uint(v[2 + q]), false, false);
                w[q] = __uint_as_float(t[0]) + __uint_as_float(t[1]);
                auto u = __builtin_amdgcn_permlane16_swap(__float_as_uint(w[q]), __float_as_uint(w[q]), false, false);
                w[q] = __uint_as_float(u[0]) + __uint_as_float(u[1]);
                w[q] += dppf<0x128>(w[q]);
            }
            unsigned t = pk2(w[0], w[1]);
            t = pk2(__uint_as_float(ga[0]) * lo16(t), __uint_as_float(ga[1]) * hi16(t));
            t = pk2s(lo16(xg) + lo16(t), hi16(xg) + hi16(t));
            __builtin_amdgcn_raw_buffer_store_b32(t, rw, xdst, 0, 16);
            __builtin_amdgcn_raw_buffer_store_b32(t, rw, xdst, 32768, 16);
            STAMP(14);
        }
        STAMP_FLUSH();
        return;
    }

    const int s = j - 16;
    if (wv == 0 && !(XNO & 2)) cnt_load();
    {
        const unsigned ln = (unsigned)(s * 64 + lane);
        if ((PFM & 1) && wv == 0) pf4<NTM & 1>((ln & 511u) * 6144u + (x * 2 + (ln >> 9)) * 128u, p.w_mod, L_SCR);
        if ((PFM & 2) && wv == 1) {
            const unsigned short* w = s < 8 ? p.w_uk : p.w_q;
            pf4<NTM & 1>((ln & 511u) * 8192u + x * 1024u + ((3u - ((unsigned)(size_t)w >> 7)) & 7u) * 128u, w, L_SCR);
        }
        if ((PFM & 48) == 48) {
            const unsigned r0 = (unsigned)(s * 4 + (wv & 3)), row = PFS == 0 ? r0 * 8 + (lane >> 3) : PFS == 1 ? (lane >> 3) * 64 + r0 : (r0 >> 5) * 256 + (lane >> 3) * 32 + (r0 & 31);
            pf4<NTM & 1>(row * 8192u + x * 1024u + (lane & 7u) * 128u, wv < 4 ? p.w_uk : p.w_q, L_SCR);
        }
        else if ((PFM & 16) && wv >= 4) { const unsigned l2 = (unsigned)((s * 4 + (wv & 3)) * 64 + lane); pf4<NTM & 1>((l2 >> 3) * 8192u + x * 1024u + (l2 & 7u) * 128u, p.w_q, L_SCR); }
        else if ((PFM & 32) && wv >= 4) { const unsigned l2 = (unsigned)((s * 4 + (wv & 3)) * 64 + lane); pf4<NTM & 1>((l2 >> 3) * 8192u + x * 1024u + (l2 & 7u) * 128u, p.w_uk, L_SCR); }
        if ((PFM & 4) && (wv & 6) == 2) { const unsigned l2 = (unsigned)((s * 2 + (wv & 1)) * 64 + lane); pf4<NTM & 1>((l2 & 511u) * 8192u + (x * 4 + (l2 >> 9)) * 256u, p.w_q, L_SCR); }
    }
    const int ug = x * 7 + s;
    const int head = lane & 31, wp = wv & 3, ch = wv >> 2, kb = wp * 2 + ch;
    const unsigned h2 = lane >> 5;
    const unsigned vb = (unsigned)(g * 1024 + ((nl ^ (g << 2)) << 4)), lb = (unsigned)((wv >> 1) * 32768 + (wv & 1) * 4096);
    const char* ck = (const char*)p.ckv + ((size_t)x * SK + s * 256 + wv * 16) * 1024;
    const unsigned rot = ROTC ? (unsigned)s & 3u : 0u;
    auto ck_dma = [&](unsigned i) { dma16<NTM & 16>(vb ^ ((i & 3u) << 4) ^ (rot << 8) ^ (((i >> 2) & 3u) << 8), ck + (i >> 4) * 131072u + (i & 3u) * 4096u, lb + ((i >> 2) & 3u) * 8192u + (i & 3u) * 1024u); };
    bf16x8 ka[16];
    s16x4 va[8][4];
    unsigned kad[4], vad[4], vad2[4];
    auto ka_reads = [&](int cb) {
#pragma unroll
        for (int u = 0; u < 4; ++u) ka[cb * 4 + u] = *LP(bf16x8, kad[u] + cb * 8192);
    };
    auto va_reads = [&]() {
#pragma unroll
        for (int kk = 0; kk < 8; ++kk)
#pragma unroll
            for (int q = 0; q < 4; ++q) va[kk][q] = tr4((kk < 4 ? vad[q] : vad2[q]) + ((kk >> 1) & 1) * 32768 + (kk & 1) * 4096);
    };
    auto va_pin = [&]() {
#pragma unroll
        for (int q = 0; q < 8; ++q) PIN4(va[q][0], va[q][1], va[q][2], va[q][3]);
    };
    u32x4 fa = {0u, 0u, 0u, 0u};
    const unsigned fao = (unsigned)(WS_FI + x * 128 + (lane & 7) * 16);
    constexpr int LAST = GI + 4 > 19 ? GI + 4 : 19;
    const unsigned qsrc = (unsigned)(WS_Q + (x * 32 + wv * 4) * 1024 + lane * 16);
    u32x4 qb[4];
#pragma unroll
    for (int i = 0; i <= LAST; ++i) {
        ck_dma(i);
        if (i == 3) {
#pragma unroll
            for (int u = 0; u < 4; ++u) {
                kad[u] = tile_off(wp * 32 + (head ^ (ch * 16)), ch * 8 + 2 * u + h2);
                vad[u] = tile_off(4 * h2 + (nl >> 2) + 8 * (u & 1), wv * 8 + 4 * (u >> 1) + 2 * (g & 1) + ((lane & 3) >> 1)) + 8 * (lane & 1);
                vad2[u] = vad[u] + 65536;
                PIN(kad[u]); PIN(vad[u]); PIN(vad2[u]);
            }
        }
        if (i > GI && i < GI + 4) VMW(8); else if (i >= 4) VMW(4);
        if (i == 12 && wv == 0 && (XNO & 2)) { cnt_load(); VMW(0); }
        if (i == 15 && wv == 0) {
            PIN(fa);
            tag_take();
            if (!__all(flags_ok(fa))) flags_wait(fao);
        }
        if ((i & 3) == 3 && i >= 7 && i <= 19) {
            const int cb = (i - 7) >> 2;
            if (cb > 0) { PIN4(ka[cb * 4 - 4], ka[cb * 4 - 3], ka[cb * 4 - 2], ka[cb * 4 - 1]); va_pin(); }
            wg_barrier();
            ka_reads(cb);
            if ((wv >> 1) == cb) va_reads();
            STAMP(16 + cb);
        }
        if (i == 10 && wv == 0) asm volatile("buffer_load_dwordx4 %0, %1, %2, 0 offen sc1" : "+v"(fa) : "v"(fao), "s"(rw) : "memory");
        if (i == GI)
            asm volatile("buffer_load_dwordx4 %0, %4, %5, 0 offen sc1\n\tbuffer_load_dwordx4 %1, %4, %5, 0 offen offset:1024 sc1\n\t"
                         "buffer_load_dwordx4 %2, %4, %5, 0 offen offset:2048 sc1\n\tbuffer_load_dwordx4 %3, %4, %5, 0 offen offset:3072 sc1"
                         : "=&v"(qb[0]), "=&v"(qb[1]), "=&v"(qb[2]), "=&v"(qb[3]) : "v"(qsrc), "s"(rw) : "memory");
    }
    const unsigned L_X = L_SCR, L_P = L_SCR + 16384, L_EXM = L_SCR + 24576, L_EXS = L_SCR + 25600;
    unsigned nx = LAST + 1;
    unsigned qad[4];
    {
        unsigned qwr[4];
#pragma unroll
        for (int k = 0; k < 4; ++k) {
            qwr[k] = L_SCR + (wv * 4 + k) * 1024 + (g ^ rot) * 256 + ((nl ^ ((wv * 4 + k) & 15)) << 4);
            qad[k] = L_SCR + head * 1024 + (((ch * 8 + 2 * k + h2) ^ (head & 15)) << 4);
            PIN(qwr[k]); PIN(qad[k]);
        }
        PIN4(ka[12], ka[13], ka[14], ka[15]); va_pin();
        wg_barrier();
        PIN4(qb[0], qb[1], qb[2], qb[3]);
        STAMP(20);
        if (!__all(mx4(qb[0], mx4(qb[1], mx4(qb[2], mx4(qb[3], 0u)))) != SENT)) {
            for (unsigned spins = 0;;) {
                if (!__all(mx4(qb[0], 0u) != SENT)) asm volatile("buffer_load_dwordx4 %0, %1, %2, 0 offen sc1" : "+v"(qb[0]) : "v"(qsrc), "s"(rw) : "memory");
                if (!__all(mx4(qb[1], 0u) != SENT)) asm volatile("buffer_load_dwordx4 %0, %1, %2, 0 offen offset:1024 sc1" : "+v"(qb[1]) : "v"(qsrc), "s"(rw) : "memory");
                if (!__all(mx4(qb[2], 0u) != SENT)) asm volatile("buffer_load_dwordx4 %0, %1, %2, 0 offen offset:2048 sc1" : "+v"(qb[2]) : "v"(qsrc), "s"(rw) : "memory");
                if (!__all(mx4(qb[3], 0u) != SENT)) asm volatile("buffer_load_dwordx4 %0, %1, %2, 0 offen offset:3072 sc1" : "+v"(qb[3]) : "v"(qsrc), "s"(rw) : "memory");
                if (nx <= 28) {
#pragma unroll
                    for (int i = 0; i < 4; ++i) ck_dma(nx + i);
                    nx += 4;
                    VMW(4);
                } else {
                    for (; nx < 32; ++nx) ck_dma(nx);
                    VMW(0);
                }
                PIN4(qb[0], qb[1], qb[2], qb[3]);
                if (__all(mx4(qb[0], mx4(qb[1], mx4(qb[2], mx4(qb[3], 0u)))) != SENT)) break;
                if (++spins > kSpinCap) break;
            }
        }
#pragma unroll
        for (int k = 0; k < 4; ++k) *LP(u32x4, qwr[k]) = qb[k];
    }
    auto slot = [&]() { if (nx < 32) { ck_dma(nx); ++nx; } };
    unsigned xw = L_X + wv * 2048 + lane * 16, xr = xw ^ 8192u, exw = L_EXM + (head * 8 + wv) * 4, exr = L_EXM + head * 32;
    unsigned pwr = L_P + kb * 1024 + lane * 16, prd = L_P + lane * 16, esw = L_EXS + (head * 16 + h2 * 8 + wv) * 4;
    PIN4(xw, xr, exw, exr); PIN(pwr); PIN(prd); PIN(esw);
    wg_barrier();
    STAMP(21);
    bf16x8 qf[16];
#pragma unroll
    for (int ks = 0; ks < 16; ++ks) qf[ks] = *LP(bf16x8, qad[ks & 3] + (ks >> 2) * 256);
    __builtin_amdgcn_sched_barrier(0);
    float lsum, mrun;
    f32x16 o[2];
    auto meet = [&](const f32x16& sc, float (&t)[8]) {
        *LP(f32x4, xw) = f32x4{sc[8], sc[9], sc[10], sc[11]}; *LP(f32x4, xw + 1024) = f32x4{sc[12], sc[13], sc[14], sc[15]};
        wg_barrier();
        const f32x4 x0 = *LP(f32x4, xr), x1 = *LP(f32x4, xr + 1024);
#pragma unroll
        for (int r = 0; r < 4; ++r) { t[r] = sc[r] + x0[r]; t[4 + r] = sc[4 + r] + x1[r]; }
        const float m = fmax3(fmax3(t[0], t[1], t[2]), fmax3(t[3], t[4], t[5]), fmax3(t[6], t[7], t[7]));
        auto u = __builtin_amdgcn_permlane32_swap(__float_as_uint(m), __float_as_uint(m), false, false);
        *LP(float, exw) = fmax3(__uint_as_float(u[0]), __uint_as_float(u[1]), __uint_as_float(u[1]));
    };
    {
        f32x16 sc;
#pragma unroll
        for (int i = 0; i < 16; ++i) sc[i] = 0.f;
#pragma unroll
        for (int ks = 0; ks < 16; ++ks) {
            sc = MFMA32(ka[ks], qf[ks], sc);
            if ((ks & 1) == 1) slot();
            __builtin_amdgcn_sched_barrier(0);
        }
        wg_barrier();
        float t[8];
        meet(sc, t);
        wg_barrier();
        const f32x4 a0 = *LP(f32x4, exr), a1 = *LP(f32x4, exr + 16);
        mrun = fmax3(fmax3(a0[0], a0[1], a0[2]), fmax3(a0[3], a1[0], a1[1]), fmax3(a1[2], a1[3], a1[3])) * SCL;
        float e[8];
#pragma unroll
        for (int r = 0; r < 8; ++r) e[r] = __builtin_amdgcn_exp2f(fmaf(t[r], SCL, -mrun));
        *LP(u32x4, pwr) = u32x4{pk2(e[0], e[1]), pk2(e[2], e[3]), pk2(e[4], e[5]), pk2(e[6], e[7])};
        lsum = ((e[0] + e[1]) + (e[2] + e[3])) + ((e[4] + e[5]) + (e[6] + e[7]));
        wg_barrier();
        STAMP(22);
        bf16x8 pf[8];
#pragma unroll
        for (int kk = 0; kk < 8; ++kk) pf[kk] = *LP(bf16x8, prd + kk * 1024);
#pragma unroll
        for (int i = 0; i < 16; ++i) { o[0][i] = 0.f; o[1][i] = 0.f; }
        __builtin_amdgcn_sched_barrier(0);
#pragma unroll
        for (int kk = 0; kk < 8; ++kk) {
            o[0] = MFMA32(cat8(va[kk][0], va[kk][1]), pf[kk], o[0]);
            o[1] = MFMA32(cat8(va[kk][2], va[kk][3]), pf[kk], o[1]);
            if (kk & 1) slot();
            __builtin_amdgcn_sched_barrier(0);
        }
        for (; nx < 32; ++nx) ck_dma(nx);
    }
    STAMP(23);
    unsigned sw[4], srd = lb + (lane >> 3) * 128 + (((lane & 7) ^ (lane >> 3)) << 4);
#pragma unroll
    for (int q = 0; q < 4; ++q) sw[q] = lb + head * 128 + (((2 * q + h2) ^ (head & 7)) << 4);
    PIN4(sw[0], sw[1], sw[2], sw[3]);
    unsigned po = (unsigned)(WS_PART + ((x * 32 + (lane >> 3)) * 8 + (wv ^ (rot * 2))) * 2048 + (s ^ (lane >> 3)) * 128 + (lane & 7) * 16);
    unsigned mlo = (unsigned)(WS_ML + (x * 16 + s) * 512 + head * 16), esr = L_EXS + head * 64;
    PIN4(esr, srd, po, mlo);
    {
        f32x16 sc;
#pragma unroll
        for (int i = 0; i < 16; ++i) sc[i] = 0.f;
        VMW(8);
        wg_barrier();
        ka_reads(0); ka_reads(1);
        if (wv < 4) va_reads();
        __builtin_amdgcn_sched_barrier(0);
#pragma unroll
        for (int ks = 0; ks < 8; ++ks) sc = MFMA32(ka[ks], qf[ks], sc);
        __builtin_amdgcn_sched_barrier(0);
        VMW(4);
        wg_barrier();
        ka_reads(2);
        if ((wv >> 1) == 2) va_reads();
        __builtin_amdgcn_sched_barrier(0);
#pragma unroll
        for (int ks = 8; ks < 12; ++ks) sc = MFMA32(ka[ks], qf[ks], sc);
        __builtin_amdgcn_sched_barrier(0);
        VMW(0);
        wg_barrier();
        STAMP(24);
        ka_reads(3);
        __builtin_amdgcn_sched_barrier(0);
#pragma unroll
        for (int ks = 12; ks < 16; ++ks) sc = MFMA32(ka[ks], qf[ks], sc);
        float t[8];
        meet(sc, t);
        wg_barrier();
        const f32x4 a0 = *LP(f32x4, exr), a1 = *LP(f32x4, exr + 16);
        const float mnew = fmaxf(fmax3(fmax3(a0[0], a0[1], a0[2]), fmax3(a0[3], a1[0], a1[1]), fmax3(a1[2], a1[3], a1[3])) * SCL, mrun);
        float e[8];
#pragma unroll
        for (int r = 0; r < 8; ++r) e[r] = __builtin_amdgcn_exp2f(fmaf(t[r], SCL, -mnew));
        *LP(u32x4, pwr) = u32x4{pk2(e[0], e[1]), pk2(e[2], e[3]), pk2(e[4], e[5]), pk2(e[6], e[7])};
        const float al = __builtin_amdgcn_exp2f(mrun - mnew);
        *LP(float, esw) = lsum * al + (((e[0] + e[1]) + (e[2] + e[3])) + ((e[4] + e[5]) + (e[6] + e[7])));
        mrun = mnew;
        o[0] *= al; o[1] *= al;
        va_pin();
        wg_barrier();
        STAMP(25);
        if ((wv >> 1) == 3) va_reads();
        bf16x8 pf[8];
#pragma unroll
        for (int kk = 0; kk < 8; ++kk) pf[kk] = *LP(bf16x8, prd + kk * 1024);
        __builtin_amdgcn_sched_barrier(0);
#pragma unroll
        for (int kk = 0; kk < 8; ++kk) {
            o[0] = MFMA32(cat8(va[kk][0], va[kk][1]), pf[kk], o[0]);
            o[1] = MFMA32(cat8(va[kk][2], va[kk][3]), pf[kk], o[1]);
        }
#pragma unroll
        for (int t2 = 0; t2 < 2; ++t2)
#pragma unroll
            for (int gp = 0; gp < 2; ++gp) {
                const unsigned lo0 = pk2(o[t2][8 * gp + 0], o[t2][8 * gp + 1]), lo1 = pk2(o[t2][8 * gp + 2], o[t2][8 * gp + 3]);
                const unsigned hi0 = pk2(o[t2][8 * gp + 4], o[t2][8 * gp + 5]), hi1 = pk2(o[t2][8 * gp + 6], o[t2][8 * gp + 7]);
                auto r0 = __builtin_amdgcn_permlane32_swap(lo0, hi0, false, false);
                auto r1 = __builtin_amdgcn_permlane32_swap(lo1, hi1, false, false);
                *LP(u32x4, sw[t2 * 2 + gp]) = u32x4{r0[0], r1[0], r0[1], r1[1]};
            }
        asm volatile("s_waitcnt lgkmcnt(0)" ::: "memory");
        u32x4 w[4];
#pragma unroll
        for (int i = 0; i < 4; ++i) w[i] = *LP(u32x4, srd + i * 1024);
#pragma unroll
        for (int i = 0; i < 4; ++i) ST16(w[i], po, i * 131072);
        __builtin_amdgcn_sched_barrier(0);
        STAMP(26);
        u32x4 mlrec;
        {
            const f32x4 b0 = *LP(f32x4, esr), b1 = *LP(f32x4, esr + 16), b2 = *LP(f32x4, esr + 32), b3 = *LP(f32x4, esr + 48);
            const f32x4 bs = (b0 + b1) + (b2 + b3);
            mlrec = u32x4{__float_as_uint(mrun), __float_as_uint((bs[0] + bs[1]) + (bs[2] + bs[3])), tlo, thi};
            PIN(mlrec);
        }
        VMW(0);
        wg_barrier();
        if (wv == 0 && lane < 32) ST16(mlrec, mlo, 0);
        STAMP(27);
        wg_barrier();
    }
    const unsigned long long t_att = __builtin_amdgcn_s_memrealtime();
    if (s >= 11) {
        const unsigned n = (unsigned)((s - 11) * 8 + wv);
        if (n < 28) pf4<NTM & 32>((n * 64 + lane) * 1024u + x * 128u, p.w2, 0);
        { const unsigned l2 = n * 64 + lane; pf4<NTM & 32>((l2 / 5u) * (DFF * 2) + min(((unsigned)x * 448u & ~127u) + (l2 % 5u) * 128u, DFF * 2 - 4u), p.w1, 0); }
        if (wv == 0) flag_put(WS_FA, 1024, id - 128);
        STAMP_FLUSH();
        return;
    }
    const unsigned L_H2S = L_SCR, L_REDM = L_SCR + 16384;
    if (s < 7) {
        {
            const char* w1s = (const char*)(p.w1 + (size_t)wv * 64 * DFF + ug * 32);
#pragma unroll
            for (int i = 0; i < 4; ++i) dma16<NTM & 32>((unsigned)((lane >> 2) * DFF * 2 + (lane & 3) * 16), w1s + (size_t)i * 16 * DFF * 2, wv * 4096 + i * 1024);
        }
        if (wv == 0) {
            if (lane < 32) ST16(s4, (unsigned)(WS_U + ug * 512 + lane * 16), 0);
            flags_wait(WS_FB + x * 1024 + lane * 16);
        }
        VMW(0);
        if (wv == 0) flag_put(WS_FA, 1024, id - 128);
        wg_barrier();
        const int b = wv;
        u32x4 md[4];
#pragma unroll
        for (int k = 0; k < 4; ++k) md[k] = LD16((unsigned)(WS_MOD + (b * 2048 + 512 + (k >> 1) * 512 + lane * 8 + (k & 1) * 4) * 4), 0);
        bf16x8 wfr[16];
        if (wv >= 6) {
#pragma unroll
            for (int ks = 0; ks < 16; ++ks) {
                const unsigned a = (unsigned)(ks * 2048 + (g * 8 + (nl >> 2)) * 64 + (wv & 1) * 32 + (lane & 3) * 8);
                wfr[ks] = tr_frag(a, a + 256);
            }
#pragma unroll
            for (int ks = 0; ks < 16; ks += 4) PIN4(wfr[ks], wfr[ks + 1], wfr[ks + 2], wfr[ks + 3]);
        }
        float scv[8];
#pragma unroll
        for (int q = 0; q < 8; ++q) { scv[q] = rbf(1.f + __uint_as_float(q < 4 ? md[2][q] : md[3][q - 4])); PIN(scv[q]); }
        unsigned har = L_H2S + ((lane & 7) * HS_LD + g * 8) * 2, haw = L_H2S + (b * HS_LD + lane * 8) * 2;
        unsigned udst = (unsigned)(WS_U + (ug * 2 + (wv & 1)) * 256 + (nl * 16 + g * 4) * 2), xsrc = (unsigned)(WS_X1 + x * 8192 + (b * 512 + lane * 8) * 2);
        PIN4(har, haw, udst, xsrc); PIN(md[0]); PIN(md[1]);
        STAMP(28);
        while (__builtin_amdgcn_s_memrealtime() < t_att + XGATE) __builtin_amdgcn_s_sleep(2);
        u32x4 xv;
        for (unsigned spins = 0;;) {
            xv = LD16(xsrc, 0);
            if (__all(mx4(xv, 0u) != SENT)) break;
            __builtin_amdgcn_s_sleep(SLPX);
            if (++spins > kSpinCap) break;
            asm volatile("" ::: "memory");
        }
        STAMP(29);
        {
            f32x2_t v[4];
#pragma unroll
            for (int q = 0; q < 4; ++q) v[q] = f32x2_t{lo16(xv[q]), hi16(xv[q])};
            const f32x2_t p1 = (v[0] + v[1]) + (v[2] + v[3]), p2 = (v[0] * v[0] + v[1] * v[1]) + (v[2] * v[2] + v[3] * v[3]);
            float mb, rden;
            ln_stats(p1[0] + p1[1], p2[0] + p2[1], mb, rden);
            u32x4 hr;
#pragma unroll
            for (int q = 0; q < 4; ++q) {
                const f32x2_t h = ((v[q] - mb) * rden) * f32x2_t{scv[2 * q], scv[2 * q + 1]} + f32x2_t{__uint_as_float(md[q >> 1][(q & 1) * 2]), __uint_as_float(md[q >> 1][(q & 1) * 2 + 1])};
                hr[q] = pk2(h[0], h[1]);
            }
            *LP(u32x4, haw) = hr;
        }
        wg_barrier();
        if (wv >= 6) {
            bf16x8 af[16];
#pragma unroll
            for (int ks = 0; ks < 16; ++ks) af[ks] = *LP(bf16x8, har + ks * 64);
            __builtin_amdgcn_sched_barrier(0);
            f32x4 d0 = {0.f, 0.f, 0.f, 0.f}, d1 = {0.f, 0.f, 0.f, 0.f};
#pragma unroll
            for (int ks = 0; ks < 16; ks += 2) { d0 = MFMA16(wfr[ks], af[ks], d0); d1 = MFMA16(wfr[ks + 1], af[ks + 1], d1); }
            d0 += d1;
            if (nl < 8) __builtin_amdgcn_raw_buffer_store_b64(u32x2{pk2s(fmaxf(d0[0], 0.f), fmaxf(d0[1], 0.f)), pk2s(fmaxf(d0[2], 0.f), fmaxf(d0[3], 0.f))}, rw, udst, 0, 16);
            STAMP(30);
        }
        STAMP_FLUSH();
        return;
    }
    {
        const int n0 = (x * 4 + (s - 7)) * 16;
        const char* mws = (const char*)(p.w2 + (size_t)wv * 224 * 512 + n0);
#pragma unroll
        for (int i = 0; i < 7; ++i) dma16<NTM & 32>((unsigned)((lane >> 1) * 1024 + (lane & 1) * 16), mws + i * 32768, rgn + i * 1024);
        const int fb = lane >> 3, fc = (lane & 7) * 2;
        u32x2 gb = {0u, 0u};
        if (wv == 0) {
            flag_put(WS_FA, 1024, id - 128);
            flags_wait(WS_FB + x * 1024 + lane * 16);
            flags_wait(WS_FA + x * 1024 + lane * 16);
            if (id == 23 * 8 && lane == 0) __builtin_amdgcn_raw_buffer_store_b64(u32x2{tlo, thi}, rw, WS_EPOCH, 0, 16);
            gb = __builtin_amdgcn_raw_buffer_load_b64(rw, (unsigned)(WS_MOD + (fb * 2048 + 1536 + n0 + fc) * 4), 0, 16);
            PIN(gb);
        }
        VMW(0);
        bf16x8 wfr[7];
        const unsigned trl = (unsigned)(g * 8 + (nl >> 2)) * 32u + (unsigned)(lane & 3) * 8u;
#pragma unroll
        for (int ks = 0; ks < 7; ++ks) { wfr[ks] = tr_frag(rgn + ks * 1024 + trl, rgn + ks * 1024 + trl + 128); PIN(wfr[ks]); }
        unsigned usrc = (unsigned)(WS_U + wv * 3584 + (g >> 1) * 256 + (nl & 7) * 32 + (g & 1) * 16);
        unsigned rdw = L_REDM + wv * 512 + (nl & 7) * 64 + g * 16, rdr = L_REDM + lane * 8;
        unsigned short* outp = p.out + fb * 512 + n0 + fc;
        PIN(usrc); PIN(rdw); PIN(rdr); PIN(outp);
        STAMP(28);
        unsigned xg = 0u;
        if (wv == 0) {
            while (__builtin_amdgcn_s_memrealtime() < t_att + XGATE) __builtin_amdgcn_s_sleep(2);
            for (unsigned spins = 0;;) {
                xg = __builtin_amdgcn_raw_buffer_load_b32(rw, (unsigned)(WS_X1 + x * 8192 + (fb * 512 + n0 + fc) * 2), 0, 16);
                if (__all(xg != SENT)) break;
                __builtin_amdgcn_s_sleep(SLPD);
                if (++spins > kSpinCap) break;
                asm volatile("" ::: "memory");
            }
            __builtin_amdgcn_s_sleep(SLPG);
        }
        wg_barrier();
        u32x4 uf[7];
        for (unsigned spins = 0;;) {
#pragma unroll
            for (int ks = 0; ks < 7; ++ks) uf[ks] = LD16(usrc + ks * 512, 0);
            unsigned m = 0u;
#pragma unroll
            for (int ks = 0; ks < 7; ++ks) m = mx4(uf[ks], m);
            if (__all(m != SENT)) break;
            if (++spins > kSpinCap) break;
            asm volatile("" ::: "memory");
        }
        STAMP(29);
        f32x4 d = {0.f, 0.f, 0.f, 0.f}, d2 = {0.f, 0.f, 0.f, 0.f};
#pragma unroll
        for (int ks = 0; ks < 7; ++ks) {
            if (ks & 1) d2 = MFMA16(wfr[ks], BF8(uf[ks]), d2);
            else d = MFMA16(wfr[ks], BF8(uf[ks]), d);
        }
        *LP(f32x4, rdw) = d + d2;
        wg_barrier();
        if (wv == 0) {
            f32x2_t c[8];
#pragma unroll
            for (int w2 = 0; w2 < 8; ++w2) c[w2] = *LP(f32x2_t, rdr + w2 * 512);
            const f32x2_t sm = ((c[0] + c[1]) + (c[2] + c[3])) + ((c[4] + c[5]) + (c[6] + c[7]));
            unsigned t = pk2(sm[0], sm[1]);
            t = pk2(__uint_as_float(gb[0]) * lo16(t), __uint_as_float(gb[1]) * hi16(t));
            *(GAS unsigned*)outp = pk2(lo16(xg) + lo16(t), hi16(xg) + hi16(t));
            STAMP(31);
        }
        STAMP_FLUSH();
    }
}

extern "C" void kernel_launch(void* const* d_in, const int* in_sizes, int n_in,
                              void* d_out, int out_size, void* d_ws, size_t ws_size,
                              hipStream_t stream) {
    Params p;
    p.x = (const unsigned short*)d_in[0];
    p.ckv = (const unsigned short*)d_in[1];
    p.w_q = (const unsigned short*)d_in[2];
    p.w_uk = (const unsigned short*)d_in[3];
    p.w_uv = (const unsigned short*)d_in[4];
    p.w_o = (const unsigned short*)d_in[5];
    p.w1 = (const unsigned short*)d_in[7];
    p.w2 = (const unsigned short*)d_in[8];
    p.c = (const unsigned short*)d_in[9];
    p.w_mod = (const unsigned short*)d_in[10];
    p.ws = (char*)d_ws;
    p.out = (unsigned short*)d_out;
    (void)hipFuncSetAttribute((const void*)k_main, hipFuncAttributeMaxDynamicSharedMemorySize, 163840);
    k_main<<<256, 512, 163840, stream>>>(p);
}
```

```cpp
#define MEAS 99
#define NTM 60
#include <hip/hip_runtime.h>
#include <hip/hip_bf16.h>
#include <stdint.h>

#define NH 32
#define SK 4096
#define DFF 1792
#define GI 19
#define ROTC 1
#define PFS 2
#define XNO 0
#define CPJ 7
#define TWD 620
#define LMD 250
#define PFM 57
#define CPFE 30400
#define SLPM 4
#define SLPD 12
#define SLPG 8
#define SLPX 4
#define SLPH 4

using u16x8 = __attribute__((ext_vector_type(8))) unsigned short;
using s16x4 = __attribute__((ext_vector_type(4))) short;
using bf16x8 = __attribute__((ext_vector_type(8))) short;
using f32x16 = __attribute__((ext_vector_type(16))) float;
using f32x4 = __attribute__((ext_vector_type(4))) float;
using u32x4 = __attribute__((ext_vector_type(4))) unsigned;
using u32x2 = __attribute__((ext_vector_type(2))) unsigned;

#define GAS __attribute__((address_space(1)))
#define LAS __attribute__((address_space(3)))
typedef GAS unsigned long long gu64;
#define RLX_AGENT __ATOMIC_RELAXED, __HIP_MEMORY_SCOPE_AGENT
#define SENT 0xffffffffu
#define TAGC 0x9e3779b9u

__device__ __forceinline__ float bf2f(unsigned short u) { return __uint_as_float(((unsigned)u) << 16); }
typedef __attribute__((ext_vector_type(2))) __bf16 bf16x2_t;
typedef __attribute__((ext_vector_type(2))) float f32x2_t;
__device__ __forceinline__ unsigned pk2(float a, float b) {
    f32x2_t v = {a, b};
    bf16x2_t r = __builtin_convertvector(v, bf16x2_t);
    return __builtin_bit_cast(unsigned, r);
}
__device__ __forceinline__ unsigned pk2s(float a, float b) {
    const unsigned r = pk2(a, b);
    return r == SENT ? 0x7fc07fc0u : r;
}
__device__ __forceinline__ unsigned f32s(float a) {
    const unsigned r = __float_as_uint(a);
    return r == SENT ? 0x7fc00000u : r;
}
__device__ __forceinline__ unsigned short f2bf(float f) { return (unsigned short)(pk2(f, 0.f) & 0xffffu); }
__device__ __forceinline__ float rbf(float f) { return __uint_as_float(pk2(f, 0.f) << 16); }
__device__ __forceinline__ float lo16(unsigned u) { return __uint_as_float(u << 16); }
__device__ __forceinline__ float hi16(unsigned u) { return __uint_as_float(u & 0xffff0000u); }
__device__ __forceinline__ unsigned umax3(unsigned a, unsigned b, unsigned c) { return max(max(a, b), c); }
__device__ __forceinline__ unsigned mx4(const u32x4& v, unsigned m) { return umax3(v[2], v[3], umax3(v[0], v[1], m)); }
__device__ __forceinline__ float fmax3(float a, float b, float c) { float r; asm("v_max3_f32 %0, %1, %2, %3" : "=v"(r) : "v"(a), "v"(b), "v"(c)); return r; }
template <int C> __device__ __forceinline__ float dppf(float v) { return __int_as_float(__builtin_amdgcn_update_dpp(0, __float_as_int(v), C, 0xf, 0xf, true)); }
__device__ __forceinline__ float sum16(float v) { v += dppf<0xB1>(v); v += dppf<0x4E>(v); v += dppf<0x141>(v); v += dppf<0x140>(v); return v; }

__device__ __forceinline__ void ln_stats(float p1, float p2, float& mb, float& rden) {
    auto t = __builtin_amdgcn_permlane32_swap(__float_as_uint(p1), __float_as_uint(p2), false, false);
    float a = sum16(__uint_as_float(t[0]) + __uint_as_float(t[1]));
    auto t2 = __builtin_amdgcn_permlane16_swap(__float_as_uint(a), __float_as_uint(a), false, false);
    a = (__uint_as_float(t2[0]) + __uint_as_float(t2[1])) * (1.f / 512.f);
    auto t3 = __builtin_amdgcn_permlane32_swap(__float_as_uint(a), __float_as_uint(a), false, false);
    const float m = __uint_as_float(t3[0]);
    const float var = rbf(fmaxf(fmaf(-m, m, __uint_as_float(t3[1])), 0.f));
    mb = rbf(m);
    rden = __builtin_amdgcn_rcpf(rbf(__builtin_amdgcn_sqrtf(rbf(var + 1e-5f))));
}
#define WS_EPOCH 0
#define WS_FI 4096
#define WS_FA 8192
#define WS_FB 24576
#define WS_H 65536
#define WS_Q 327680
#define WS_U 589824
#define WS_X1 655360
#define WS_MOD 720896
#define WS_ML 786432
#define WS_OUTP 1048576
#define WS_PART 2097152
#define WS_END 6291456

#define LP(T, off) ((LAS T*)(size_t)(unsigned)(off))
#define STAMP(k) do { if (((k) == MEAS || ((k) == 0 && (blockIdx.x >> 4) == 7 && wv == 7)) && (threadIdx.x & 63) == 0) __hip_atomic_store((gu64*)(p.ws + (128u << 20)) + ((k) == 0 ? 0 : 2048) + blockIdx.x * 8 + wv, (unsigned long long)__builtin_amdgcn_s_memrealtime(), RLX_AGENT); \
    if ((k) == 31) { \
        const unsigned long long t1 = __builtin_amdgcn_s_memrealtime(); \
        unsigned long long m = ~0ull, mx = 0ull; \
        for (int q = 0; q < 32; ++q) { const unsigned long long v = __hip_atomic_load((gu64*)(p.ws + (128u << 20)) + q * 64 + (threadIdx.x & 63), RLX_AGENT); if (t1 - v < 10000ull) m = v < m ? v : m; } \
        for (int o = 1; o < 64; o <<= 1) { const unsigned long long v = __shfl_xor(m, o); m = v < m ? v : m; } \
        for (int q = 0; q < 32; ++q) { const unsigned long long v = __hip_atomic_load((gu64*)(p.ws + (128u << 20)) + 2048 + q * 64 + (threadIdx.x & 63), RLX_AGENT); if (v - m < 10000ull) mx = v > mx ? v : mx; } \
        for (int o = 1; o < 64; o <<= 1) { const unsigned long long v = __shfl_xor(mx, o); mx = v > mx ? v : mx; } \
        if (MEAS == 31) mx = t1; \
        const unsigned dl = MEAS == 99 ? 0u : min(__builtin_amdgcn_readfirstlane((unsigned)(mx - m)), 5000u) * 4u; \
        while (__builtin_amdgcn_s_memrealtime() < t1 + dl) __builtin_amdgcn_s_sleep(1); \
    } } while (0)
#define STAMP_FLUSH() do {} while (0)
__device__ __forceinline__ s16x4 tr4(unsigned a) { return __builtin_amdgcn_ds_read_tr16_b64_v4i16(LP(s16x4, a)); }
__device__ __forceinline__ bf16x8 cat8(s16x4 a, s16x4 b) { return __builtin_shufflevector(a, b, 0, 1, 2, 3, 4, 5, 6, 7); }
__device__ __forceinline__ bf16x8 tr_frag(unsigned a0, unsigned a1) { return cat8(tr4(a0), tr4(a1)); }
__device__ __forceinline__ unsigned swz(unsigned row) { return ((row & 3) << 2) | ((row >> 2) & 3); }
__device__ __forceinline__ unsigned off_b(unsigned row, unsigned ch) { return 256u * row + 16u * (ch ^ swz(row)); }
__device__ __forceinline__ unsigned tile_off(unsigned key, unsigned chunk) { return ((key >> 5) * 4 + (chunk >> 4)) * 8192u + off_b(key & 31, chunk & 15); }
__device__ __forceinline__ void wg_barrier() { asm volatile("s_waitcnt lgkmcnt(0)\n\ts_barrier" ::: "memory"); }
template <int NT> __device__ __forceinline__ void dma16(unsigned voff, const void* sbase, unsigned lds_dst) {
    if (NT) asm volatile("s_mov_b32 m0, %2\n\ts_nop 0\n\tglobal_load_lds_dwordx4 %0, %1 nt" :: "v"(voff), "s"(sbase), "s"(lds_dst) : "memory", "m0");
    else asm volatile("s_mov_b32 m0, %2\n\ts_nop 0\n\tglobal_load_lds_dwordx4 %0, %1" :: "v"(voff), "s"(sbase), "s"(lds_dst) : "memory", "m0");
}
template <int NT> __device__ __forceinline__ void pf4(unsigned voff, const void* sbase, unsigned lds_dst) {
    if (NT) asm volatile("s_mov_b32 m0, %2\n\ts_nop 0\n\tglobal_load_lds_dword %0, %1 nt" :: "v"(voff), "s"(sbase), "s"(lds_dst) : "memory", "m0");
    else asm volatile("s_mov_b32 m0, %2\n\ts_nop 0\n\tglobal_load_lds_dword %0, %1" :: "v"(voff), "s"(sbase), "s"(lds_dst) : "memory", "m0");
}
__device__ __forceinline__ void pf4s(unsigned voff, const void* sbase, unsigned lds_dst) {
    asm volatile("s_mov_b32 m0, %2\n\ts_nop 0\n\tglobal_load_lds_dword %0, %1 sc1" :: "v"(voff), "s"(sbase), "s"(lds_dst) : "memory", "m0");
}
#define PTOUCH 1
#define XGATE 300
#define SPINCAP (1u << 20)
constexpr unsigned kSpinCap = SPINCAP;
struct Params {
    const unsigned short* x;
    const unsigned short* ckv;
    const unsigned short* w_q;
    const unsigned short* w_uk;
    const unsigned short* w_uv;
    const unsigned short* w_o;
    const unsigned short* w1;
    const unsigned short* w2;
    const unsigned short* c;
    const unsigned short* w_mod;
    char* ws;
    unsigned short* out;
};
#define VMW(n) asm volatile("s_waitcnt vmcnt(%0)" :: "n"(n) : "memory")
#define PIN(x) asm volatile("" : "+v"(x))
#define PIN4(a, b, c, d) asm volatile("" : "+v"(a), "+v"(b), "+v"(c), "+v"(d) :: "memory")
#define MFMA16(a, b, c) __builtin_amdgcn_mfma_f32_16x16x32_bf16(a, b, c, 0, 0, 0)
#define MFMA32(a, b, c) __builtin_amdgcn_mfma_f32_32x32x16_bf16(a, b, c, 0, 0, 0)
#define BF8(x) __builtin_bit_cast(bf16x8, x)
#define ST16(v, off, soff) __builtin_amdgcn_raw_buffer_store_b128(v, rw, off, soff, 16)
#define LD16(off, soff) __builtin_amdgcn_raw_buffer_load_b128(rw, off, soff, 16)
#define SCL (0.08838834764831845f * 1.4426950408889634f)
#define L_SCR 131072
#define HS_LD 520

__global__ __launch_bounds__(512, 2) void k_main(Params p) {
    unsigned long long pc;
    asm volatile("s_getpc_b64 %0" : "=s"(pc));
    const int id = blockIdx.x, tid = threadIdx.x, lane = tid & 63;
    const int wv = __builtin_amdgcn_readfirstlane(tid >> 6);
    const int g = lane >> 4, nl = lane & 15;
    const int x = id & 7, j = id >> 3;
    if (wv < 2 && (j >> 1) == CPJ) {
        asm volatile("s_mov_b32 m0, %2\n\ts_nop 0\n\tglobal_load_lds_dword %0, %1" :: "v"(min((unsigned)((((j & 1) * 2 + wv) * 64 + lane) * 128), (unsigned)CPFE)), "s"(pc), "s"(L_SCR + 31744) : "memory", "m0");
    }
    char* ws = p.ws;
    STAMP(0);
    const __amdgpu_buffer_rsrc_t rw = __builtin_amdgcn_make_buffer_rsrc(ws, 0, WS_END, 0x00020000);
    const u32x4 s4 = {SENT, SENT, SENT, SENT};
    unsigned long long cnt = 0ull;
    auto cnt_load = [&]() { asm volatile("global_load_dwordx2 %0, %1, %2 sc1" : "+v"(cnt) : "v"((unsigned)WS_EPOCH), "s"(ws) : "memory"); };
    unsigned tlo = 0u, thi = 0u;
    auto tag_take = [&]() {
        asm volatile("" : "+v"(cnt) :: "memory");
        const unsigned long long t = cnt + 1ull;
        tlo = __builtin_amdgcn_readfirstlane((unsigned)t); thi = __builtin_amdgcn_readfirstlane((unsigned)(t >> 32));
    };
    auto flag_put = [&](unsigned base, unsigned stride, unsigned idx) {
        if (lane < 8) __builtin_amdgcn_raw_buffer_store_b64(u32x2{tlo, thi}, rw, base + lane * stride + idx * 8, 0, 16);
    };
    auto flags_ok = [&](const u32x4& f) { return (f[0] == tlo) & (f[1] == thi) & (f[2] == tlo) & (f[3] == thi); };
    auto flags_wait = [&](unsigned off) {
        for (unsigned spins = 0;;) {
            const u32x4 f = LD16(off, 0);
            if (__all(flags_ok(f))) break;
            __builtin_amdgcn_s_sleep(16);
            if (++spins > kSpinCap) break;
            asm volatile("" ::: "memory");
        }
    };
    const unsigned pcl = (unsigned)(((((lane & 7) >> 1) ^ ((lane >> 4) & 1)) * 32) + (lane & 1) * 16);
    auto dma_piece = [&](const unsigned short* src, unsigned ldb, unsigned dst, int n, int nt) {
        const unsigned v0 = (lane >> 3) * ldb + pcl, v1 = v0 ^ 64u;
#pragma unroll
        for (int i = 0; i < n; ++i) {
            if (nt) dma16<1>((i & 1) ? v1 : v0, (const char*)src + (size_t)i * 8 * ldb, dst + i * 1024);
            else dma16<0>((i & 1) ? v1 : v0, (const char*)src + (size_t)i * 8 * ldb, dst + i * 1024);
        }
    };
    const unsigned frl = (unsigned)((g * 8 + (nl >> 2)) * 128 + (lane & 3) * 8), frs = (unsigned)((nl >> 3) | ((g & 1) << 1));
    auto piece_frag = [&](unsigned base, int ks, int t) {
        const unsigned a = base + frl + ((t ^ frs) << 5) + ks * 4096;
        return tr_frag(a, a + 512);
    };
    const unsigned rgn = (unsigned)(wv * 16384);

    if (j < 16) {
        const unsigned long long t_start = __builtin_amdgcn_s_memrealtime();
        const int th = x * 4 + (j & 3);
        if (j < 12) {
            const int qt = j >> 2, rot = qt * 5;
            const bool pub = wv < (qt < 2 ? 3 : 2);
            const unsigned qo = (unsigned)(WS_Q + ((nl & 7) * 32 + th) * 1024 + (qt * 192 + wv * 64 + g * 16 + (nl >> 3) * 8) * 2);
            const unsigned vb = (unsigned)(g * 8192 + ((nl ^ (g << 2)) << 4));
            const char* wuk = (const char*)(p.w_uk + th * 128) + (size_t)qt * 192 * 8192;
            const char* wq = (const char*)(p.w_q + th * 128) + (size_t)wv * 32768;
            const unsigned L_U = 114688;
            {
                const int k0 = qt < 2 ? wv * 6 : wv * 4;
#pragma unroll
                for (int i = 0; i < 6; ++i) {
                    if (i < 4 || qt < 2) dma16<NTM & 2>(vb ^ (((k0 + i) & 3) << 4), wuk + (size_t)(k0 + i) * 32768, L_U + (k0 + i) * 1024);
                    if (i == 4) VMW(4);
                }
            }
            const unsigned vq = vb ^ ((wv & 3) << 4);
            auto wq_dma = [&](int ps) { dma16<NTM & 2>(vq, wq + (size_t)((ps + rot) & 15) * 262144, ps * 8192 + wv * 1024); };
            bf16x8 uk[4][4];
            u32x4 fi;
            const unsigned fio = (unsigned)(WS_FI + x * 128 + (lane & 7) * 16);
#pragma unroll
            for (int ps = 0; ps < 14; ++ps) {
                wq_dma(ps);
                if (ps == 8) cnt_load();
                if (ps >= 8 && ps < 13) VMW(5); else VMW(4);
                if (ps == 3) {
                    asm volatile("s_barrier" ::: "memory");
#pragma unroll
                    for (int t = 0; t < 4; ++t)
#pragma unroll
                        for (int s = 0; s < 4; ++s) uk[t][s] = *LP(bf16x8, L_U + off_b((wv & 3) * 64 + (nl >> 2) * 16 + t * 4 + (nl & 3), s * 4 + g) - (wv == 3 || wv == 7 ? 16384 : 0));
                    STAMP(1);
                }
            }
#pragma unroll
            for (int t = 0; t < 4; ++t) PIN4(uk[t][0], uk[t][1], uk[t][2], uk[t][3]);
            wg_barrier();
            STAMP(15);
            wq_dma(14);
            wq_dma(15);
            const unsigned hsrc = (unsigned)(WS_H + x * 32768 + wv * 2048 + lane * 16), hsrc2 = hsrc + 16384;
            u32x4 hv[4];
            asm volatile("buffer_load_dwordx4 %0, %1, %2, 0 offen sc1" : "=v"(fi) : "v"(fio), "s"(rw) : "memory");
            asm volatile("buffer_load_dwordx4 %0, %4, %6, 0 offen sc1\n\tbuffer_load_dwordx4 %1, %4, %6, 0 offen offset:1024 sc1\n\t"
                         "buffer_load_dwordx4 %2, %5, %6, 0 offen sc1\n\tbuffer_load_dwordx4 %3, %5, %6, 0 offen offset:1024 sc1"
                         : "=&v"(hv[0]), "=&v"(hv[1]), "=&v"(hv[2]), "=&v"(hv[3]) : "v"(hsrc), "v"(hsrc2), "s"(rw) : "memory");
            bf16x8 aq[16];
            const unsigned ta0 = off_b(8 * g + (nl >> 2), 2 * wv + ((lane & 3) >> 1)) + 8 * (lane & 1), ta1 = off_b(8 * g + 4 + (nl >> 2), 2 * wv + ((lane & 3) >> 1)) + 8 * (lane & 1);
            unsigned tb0 = ta0 + 65536, tb1 = ta1 + 65536, qo2 = qo;
            PIN(tb0); PIN(tb1); PIN(qo2);
            tag_take();
            const unsigned thx = tlo ^ thi ^ TAGC;
#pragma unroll
            for (int ps = 0; ps < 10; ++ps) aq[ps] = ps < 8 ? tr_frag(ta0 + ps * 8192, ta1 + ps * 8192) : tr_frag(tb0 + (ps - 8) * 8192, tb1 + (ps - 8) * 8192);
            const unsigned L_HI = L_SCR, L_QI = L_SCR + 8192;
            unsigned hw0 = L_HI + ((2 * wv - rot) & 15) * 512 + (lane >> 4) * 128 + (lane & 7) * 16 + ((lane >> 3) & 1) * 8;
            unsigned hw1 = L_HI + ((2 * wv + 1 - rot) & 15) * 512 + (lane >> 4) * 128 + (lane & 7) * 16 + ((lane >> 3) & 1) * 8;
            unsigned hr = L_HI + g * 128 + (nl & 7) * 16, qw = L_QI + (wv >> 1) * 512 + ((wv & 1) * 2 + (g >> 1)) * 128 + (nl & 7) * 16 + (g & 1) * 8;
            PIN4(hw0, hw1, hr, qw);
            PIN4(aq[0], aq[1], aq[2], aq[3]); PIN4(aq[4], aq[5], aq[6], aq[7]); PIN(aq[8]); PIN(aq[9]);
            STAMP(2);
            for (unsigned spins = 0;;) {
                asm volatile("s_waitcnt vmcnt(0)" : "+v"(hv[0]), "+v"(hv[1]), "+v"(hv[2]), "+v"(hv[3]) :: "memory");
                unsigned bad = (hv[0][1] ^ tlo) | (hv[0][3] ^ thx) | (hv[1][1] ^ tlo) | (hv[1][3] ^ thx) | (hv[2][1] ^ tlo) | (hv[2][3] ^ thx) | (hv[3][1] ^ tlo) | (hv[3][3] ^ thx);
                PIN(bad);
                if (__all(bad == 0u)) break;
                __builtin_amdgcn_s_sleep(SLPH);
                if (++spins > kSpinCap) break;
                asm volatile("buffer_load_dwordx4 %0, %4, %6, 0 offen sc1\n\tbuffer_load_dwordx4 %1, %4, %6, 0 offen offset:1024 sc1\n\t"
                             "buffer_load_dwordx4 %2, %5, %6, 0 offen sc1\n\tbuffer_load_dwordx4 %3, %5, %6, 0 offen offset:1024 sc1"
                             : "+v"(hv[0]), "+v"(hv[1]), "+v"(hv[2]), "+v"(hv[3]) : "v"(hsrc), "v"(hsrc2), "s"(rw) : "memory");
            }
            *LP(u32x2, hw0) = u32x2{pk2(lo16(hv[2][0]) + lo16(hv[0][0]), hi16(hv[2][0]) + hi16(hv[0][0])), pk2(lo16(hv[2][2]) + lo16(hv[0][2]), hi16(hv[2][2]) + hi16(hv[0][2]))};
            *LP(u32x2, hw1) = u32x2{pk2(lo16(hv[3][0]) + lo16(hv[1][0]), hi16(hv[3][0]) + hi16(hv[1][0])), pk2(lo16(hv[3][2]) + lo16(hv[1][2]), hi16(hv[3][2]) + hi16(hv[1][2]))};
            PIN(fi);
            { unsigned bad = (fi[0] ^ tlo) | (fi[1] ^ thi) | (fi[2] ^ tlo) | (fi[3] ^ thi); PIN(bad); if (!__all(bad == 0u)) flags_wait(fio); }
            STAMP(3);
            wg_barrier();
            {
                bf16x8 hb[16];
#pragma unroll
                for (int ps = 0; ps < 10; ++ps) hb[ps] = *LP(bf16x8, hr + ps * 512);
#pragma unroll
                for (int ps = 10; ps < 16; ++ps) { hb[ps] = *LP(bf16x8, hr + ps * 512); aq[ps] = tr_frag(tb0 + (ps - 8) * 8192, tb1 + (ps - 8) * 8192); }
                __builtin_amdgcn_sched_barrier(0);
                f32x4 d0 = {0.f, 0.f, 0.f, 0.f}, d1 = {0.f, 0.f, 0.f, 0.f};
#pragma unroll
                for (int ps = 0; ps < 16; ps += 2) { d0 = MFMA16(aq[ps], hb[ps], d0); d1 = MFMA16(aq[ps + 1], hb[ps + 1], d1); }
                d0 += d1;
                *LP(u32x2, qw) = u32x2{pk2(d0[0], d0[1]), pk2(d0[2], d0[3])};
            }
            wg_barrier();
            if (pub) {
                bf16x8 qb[4];
#pragma unroll
                for (int s = 0; s < 4; ++s) qb[s] = *LP(bf16x8, hr + 8192 + s * 512);
                f32x4 d[4];
#pragma unroll
                for (int t = 0; t < 4; ++t) d[t] = MFMA16(uk[t][0], qb[0], (f32x4{0.f, 0.f, 0.f, 0.f}));
#pragma unroll
                for (int s = 1; s < 4; ++s)
#pragma unroll
                    for (int t = 0; t < 4; ++t) d[t] = MFMA16(uk[t][s], qb[s], d[t]);
                const f32x4 a = nl & 8 ? d[2] : d[0], b = nl & 8 ? d[3] : d[1];
                u32x4 qv = {pk2(a[0], a[1]), pk2(a[2], a[3]), pk2(b[0], b[1]), pk2(b[2], b[3])};
                if (__builtin_expect(mx4(qv, 0u) == SENT, 0)) qv = u32x4{pk2s(a[0], a[1]), pk2s(a[2], a[3]), pk2s(b[0], b[1]), pk2s(b[2], b[3])};
                ST16(qv, qo2, 0);
            }
            STAMP(4);
            wg_barrier();
        } else {
            const int cg = x * 2 + (j & 1);
            cnt_load();
            if (j >= 14 && !(XNO & 1)) { ST16(s4, (unsigned)(WS_Q + cg * 16384 + wv * 2048 + lane * 16), 0); ST16(s4, (unsigned)(WS_Q + cg * 16384 + wv * 2048 + lane * 16), 1024); }
            u16x8 cf, xa;
            asm volatile("global_load_dwordx4 %0, %2, %4\n\tglobal_load_dwordx4 %1, %3, %5" : "=&v"(cf), "=&v"(xa)
                         : "v"((unsigned)(((nl & 7) * 512 + (nl >> 3) * 32 + g * 8) * 2 + wv * 128)), "v"((unsigned)(wv * 1024 + lane * 16)), "s"(p.c), "s"(p.x) : "memory");
            if (j < 14) { dma_piece(p.w_mod + (size_t)wv * 64 * 3072 + cg * 64, 6144, rgn, 8, NTM & 2); VMW(8); } else VMW(0);
            asm volatile("" : "+v"(cf), "+v"(xa) :: "memory");
            tag_take();
            if (j >= 14) { wg_barrier(); if (wv == 7) flag_put(WS_FI, 128, cg); STAMP(15); }
            u32x4 af[2];
#pragma unroll
            for (int q = 0; q < 4; ++q) {
                const float a = bf2f(cf[2 * q]), b = bf2f(cf[2 * q + 1]);
                af[0][q] = pk2(a * __builtin_amdgcn_rcpf(1.f + __expf(-a)), b * __builtin_amdgcn_rcpf(1.f + __expf(-b)));
                af[1][q] = (unsigned)__builtin_amdgcn_update_dpp(0, (int)af[0][q], 0x128, 0xf, 0xf, true);
            }
            const unsigned L_MS = L_SCR, L_XN = L_SCR + 16384;
            if (j < 14 && cg >= 8) {
                float v[8];
#pragma unroll
                for (int q = 0; q < 8; ++q) v[q] = bf2f(xa[q]);
                const float p1 = ((v[0] + v[1]) + (v[2] + v[3])) + ((v[4] + v[5]) + (v[6] + v[7]));
                const float p2 = ((v[0] * v[0] + v[1] * v[1]) + (v[2] * v[2] + v[3] * v[3])) + ((v[4] * v[4] + v[5] * v[5]) + (v[6] * v[6] + v[7] * v[7]));
                float mb, rden;
                ln_stats(p1, p2, mb, rden);
                if ((lane >> 3) == (cg & 7)) {
                    u32x4 r;
#pragma unroll
                    for (int q = 0; q < 4; ++q) { const unsigned d = pk2(v[2 * q] - mb, v[2 * q + 1] - mb); r[q] = pk2(lo16(d) * rden, hi16(d) * rden); }
                    *LP(u32x4, L_XN + wv * 128 + (lane & 7) * 16) = r;
                }
            }
            unsigned wa = L_MS + wv * 2048 + nl * 256;
            PIN(wa); PIN(af[0]); PIN(af[1]);
            auto mod_mm = [&]() {
                VMW(0);
                bf16x8 f[2][4];
#pragma unroll
                for (int t = 0; t < 4; ++t) { f[0][t] = piece_frag(rgn, 0, t); f[1][t] = piece_frag(rgn, 1, t); }
                f32x4 d[4];
#pragma unroll
                for (int t = 0; t < 4; ++t) d[t] = MFMA16(f[0][t], BF8(af[0]), (f32x4{0.f, 0.f, 0.f, 0.f}));
#pragma unroll
                for (int t = 0; t < 4; ++t) d[t] = MFMA16(f[1][t], BF8(af[1]), d[t]);
                if (nl < 8) {
#pragma unroll
                    for (int t = 0; t < 4; ++t) *LP(f32x4, wa + (((t * 4 + g) ^ nl) << 4)) = d[t];
                }
                wg_barrier();
            };
            auto sum8 = [&](unsigned a) {
                f32x4 u[8];
#pragma unroll
                for (int w2 = 0; w2 < 8; ++w2) u[w2] = *LP(f32x4, a + w2 * 2048);
                return ((u[0] + u[1]) + (u[2] + u[3])) + ((u[4] + u[5]) + (u[6] + u[7]));
            };
            if (j < 14) {
                const int hb = lane & 7, c4 = 2 * wv + (nl >> 3);
                unsigned hoff = (unsigned)(WS_H + g * 32768 + cg * 2048 + wv * 256 + nl * 16);
                unsigned sa = L_MS + g * 4096 + hb * 256 + ((c4 ^ hb) << 4), xa2 = L_XN + hb * 128 + c4 * 8;
                const unsigned thx = tlo ^ thi ^ TAGC;
                PIN(hoff); PIN(sa); PIN(xa2);
                STAMP(1);
                mod_mm();
                STAMP(2);
                f32x4 v = *LP(f32x4, sa) + *LP(f32x4, sa + 2048);
                const u32x2 xn = *LP(u32x2, xa2);
#pragma unroll
                for (int e = 0; e < 4; ++e) {
                    auto t = __builtin_amdgcn_permlane16_swap(__float_as_uint(v[e]), __float_as_uint(v[e]), false, false);
                    const float u = __uint_as_float(t[0]) + __uint_as_float(t[1]);
                    auto t2 = __builtin_amdgcn_permlane32_swap(__float_as_uint(u), __float_as_uint(u), false, false);
                    v[e] = __uint_as_float(t2[0]) + __uint_as_float(t2[1]);
                }
                u32x2 r = {pk2(v[0], v[1]), pk2(v[2], v[3])};
                if (cg >= 8) {
#pragma unroll
                    for (int q = 0; q < 2; ++q) { const unsigned sc = pk2(1.f + lo16(r[q]), 1.f + hi16(r[q])); r[q] = pk2(lo16(xn[q]) * lo16(sc), hi16(xn[q]) * hi16(sc)); }
                }
                ST16((u32x4{r[0], tlo, r[1], thx}), hoff, 0);
                ST16((u32x4{r[0], tlo, r[1], thx}), hoff, 131072);
                STAMP(5);
            }
            wg_barrier();
            while (__builtin_amdgcn_s_memrealtime() < t_start + LMD) __builtin_amdgcn_s_sleep(2);
            const int lg = x * 4 + (j & 3);
            dma_piece(p.w_mod + (size_t)wv * 64 * 3072 + 1024 + lg * 64, 6144, rgn, 8, NTM & 4);
            mod_mm();
            if (wv < 2) {
                const int b = wv * 4 + g;
                const f32x4 v = sum8(L_MS + b * 256 + ((nl ^ b) << 4));
                ST16((u32x4{__float_as_uint(rbf(v[0])), __float_as_uint(rbf(v[1])), __float_as_uint(rbf(v[2])), __float_as_uint(rbf(v[3]))}), (unsigned)(WS_MOD + (b * 2048 + lg * 64 + nl * 4) * 4), 0);
            }
            STAMP(6);
        }
        const int bq = (j >> 2) & 1, dh = j >> 3, src = th * 2 + dh, rb = bq * 4 + (src >> 4), rc = src & 15;
        unsigned oo = (unsigned)(WS_OUTP + ((bq * 4 + (nl & 3)) * 16 + wv * 2 + (nl >> 3)) * 8192 + (src ^ wv) * 128 + ((nl >> 2) & 1) * 64 + g * 16);
        const int rcol = rc * 32 + (lane & 7) * 4 + (lane >> 5) * 2;
        unsigned xdst = (unsigned)(WS_X1 + ((lane >> 3) & 3) * 8192 + (rb * 512 + rcol) * 2);
        ST16(s4, oo, 0);
        if (wv == 0) { __builtin_amdgcn_raw_buffer_store_b32(SENT, rw, xdst, 0, 16); __builtin_amdgcn_raw_buffer_store_b32(SENT, rw, xdst, 32768, 16); }
        VMW(0);
        wg_barrier();
        if (wv == 0) flag_put(WS_FB, 1024, id);
        while (__builtin_amdgcn_s_memrealtime() < t_start + TWD) __builtin_amdgcn_s_sleep(2);
        STAMP(7);
        if ((PFM & 8) && wv == 0 && lane < 32) pf4<NTM & 8>((unsigned)((j * 32 + lane) * 8192 + x * 1024 + ((3u - ((unsigned)(size_t)p.w_uv >> 7)) & 7u) * 128), p.w_uv, L_SCR + 31744);
        dma_piece(p.w_uv + ((size_t)wv * 64 * NH + th) * 128 + dh * 64, 8192, rgn, 8, NTM & 8);
        dma_piece(p.w_o + (size_t)(th * 128 + dh * 64) * 512 + wv * 64, 1024, rgn + 8192, 8, NTM & 8);
        VMW(0);
        STAMP(8);
        bf16x8 fuv[2][4], fwo[2][4];
        unsigned pfa[4];
#pragma unroll
        for (int t = 0; t < 4; ++t) { pfa[t] = rgn + frl + ((t ^ frs) << 5); PIN(pfa[t]); }
#pragma unroll
        for (int ks = 0; ks < 2; ++ks)
#pragma unroll
            for (int t = 0; t < 4; ++t) {
                const unsigned a = rgn + (unsigned)((g * 4 + (nl >> 2)) * 128 + (lane & 3) * 8) + ((t ^ (unsigned)((nl >> 3) | (g & 2))) << 5) + ks * 4096;
                fuv[ks][t] = tr_frag(a, a + 2048);
                fwo[ks][t] = tr_frag(pfa[t] + 8192 + ks * 4096, pfa[t] + 8192 + ks * 4096 + 512);
            }
#pragma unroll
        for (int ks = 0; ks < 2; ++ks) { PIN4(fuv[ks][0], fuv[ks][1], fuv[ks][2], fuv[ks][3]); PIN4(fwo[ks][0], fwo[ks][1], fwo[ks][2], fwo[ks][3]); }
        const unsigned L_WGT = L_SCR, L_RED = L_SCR + 1024;
        unsigned psrc = (unsigned)(WS_PART + ((bq * 4 * 32 + th) * 8 + wv) * 2048 + ((lane >> 3) ^ (th & 7)) * 128) + pcl, psrc1 = (psrc ^ 64u) + 1024;
        unsigned pw = rgn + lane * 16, rw2 = L_RED + wv * 1024 + (nl & 3) * 256 + g * 16;
        unsigned wa0 = (nl & 3) == (g >> 1) ? L_WGT + (nl & 4) * 32 + g * 16 : L_WGT + 256, wa1 = (nl & 3) == 2 + (g >> 1) ? L_WGT + (nl & 4) * 32 + 64 + g * 16 : L_WGT + 256;
        unsigned cr = rgn + (nl & 3) * 128 + g * 16, r2r = L_RED + lane * 16, cw = rgn + lane * 8;
        unsigned osrc = (unsigned)(WS_OUTP + (rb * 16 + rc) * 8192 + lane * 16);
        PIN4(psrc, psrc1, pw, rw2); PIN4(wa0, wa1, cr, r2r); PIN4(cw, oo, osrc, xdst);
        if (wv == 1 && lane < 4) pf4s(psrc + lane * 524288u, ws, L_SCR + 31744);
        u32x2 ga = {0u, 0u};
        unsigned xg = 0u;
        float mlm = 0.f, mll = 0.f;
        if (wv == 0) {
            *LP(unsigned, L_WGT + 256 + lane * 4) = 0u;
            flags_wait(WS_FB + x * 1024 + lane * 16);
            ga = __builtin_amdgcn_raw_buffer_load_b64(rw, (unsigned)(WS_MOD + (rb * 2048 + rcol) * 4), 0, 16);
            xg = *(const unsigned*)(p.x + rb * 512 + rcol);
            PIN(ga); PIN(xg);
            STAMP(9);
            const unsigned mlsrc = (unsigned)(WS_ML + ((bq * 4 + g) * 16 + nl) * 512 + th * 16);
            u32x4 ml = {0u, 0u, ~tlo, ~thi};
            for (unsigned spins = 0;;) {
                if ((ml[2] != tlo) | (ml[3] != thi)) ml = LD16(mlsrc, 0);
                if (__all((ml[2] == tlo) & (ml[3] == thi))) break;
                __builtin_amdgcn_s_sleep(SLPM);
                if (++spins > kSpinCap) break;
                asm volatile("" ::: "memory");
            }
            mlm = __uint_as_float(ml[0]); mll = __uint_as_float(ml[1]);
        }
        wg_barrier();
        STAMP(10);
        bf16x8 pa[2][4], wh[2];
        {
            u32x4 pl[8];
            asm volatile("buffer_load_dwordx4 %0, %8, %10, 0 offen sc1\n\tbuffer_load_dwordx4 %1, %9, %10, 0 offen sc1\n\t"
                         "buffer_load_dwordx4 %2, %8, %10, %11 offen sc1\n\tbuffer_load_dwordx4 %3, %9, %10, %11 offen sc1\n\t"
                         "buffer_load_dwordx4 %4, %8, %10, %12 offen sc1\n\tbuffer_load_dwordx4 %5, %9, %10, %12 offen sc1\n\t"
                         "buffer_load_dwordx4 %6, %8, %10, %13 offen sc1\n\tbuffer_load_dwordx4 %7, %9, %10, %13 offen sc1"
                         : "=&v"(pl[0]), "=&v"(pl[1]), "=&v"(pl[2]), "=&v"(pl[3]), "=&v"(pl[4]), "=&v"(pl[5]), "=&v"(pl[6]), "=&v"(pl[7])
                         : "v"(psrc), "v"(psrc1), "s"(rw), "s"(524288u), "s"(1048576u), "s"(1572864u) : "memory");
            if (wv == 0) {
                float M = mlm;
                M = fmaxf(M, dppf<0xB1>(M)); M = fmaxf(M, dppf<0x4E>(M)); M = fmaxf(M, dppf<0x141>(M)); M = fmaxf(M, dppf<0x140>(M));
                const float e = __builtin_amdgcn_exp2f(mlm - M);
                const float w = e * __builtin_amdgcn_rcpf(sum16(e * mll));
                const unsigned short hi = f2bf(w);
                *LP(unsigned short, L_WGT + lane * 2) = hi;
                *LP(unsigned short, L_WGT + 128 + lane * 2) = f2bf(w - bf2f(hi));
            }
            wg_barrier();
            wh[0] = *LP(bf16x8, wa0); wh[1] = *LP(bf16x8, wa1);
            __builtin_amdgcn_sched_barrier(0);
            asm volatile("s_waitcnt vmcnt(0)" : "+v"(pl[0]), "+v"(pl[1]), "+v"(pl[2]), "+v"(pl[3]), "+v"(pl[4]), "+v"(pl[5]), "+v"(pl[6]), "+v"(pl[7]) :: "memory");
#pragma unroll
            for (int i = 0; i < 8; ++i) *LP(u32x4, pw + i * 1024) = pl[i];
            asm volatile("s_waitcnt lgkmcnt(0)" ::: "memory");
#pragma unroll
            for (int ks = 0; ks < 2; ++ks)
#pragma unroll
                for (int t = 0; t < 4; ++t) pa[ks][t] = tr_frag(pfa[t] + ks * 4096, pfa[t] + ks * 4096 + 512);
        }
        STAMP(11);
        {
            f32x4 d[4];
#pragma unroll
            for (int t = 0; t < 4; ++t) d[t] = MFMA16(pa[0][t], wh[0], (f32x4{0.f, 0.f, 0.f, 0.f}));
#pragma unroll
            for (int t = 0; t < 4; ++t) d[t] = MFMA16(pa[1][t], wh[1], d[t]);
#pragma unroll
            for (int t = 0; t < 4; ++t)
#pragma unroll
                for (int e = 0; e < 4; ++e) { float v = d[t][e] + dppf<0x124>(d[t][e]); asm("" : "+v"(v)); d[t][e] = v; }
            const u32x4 o0 = {pk2(d[0][0], d[0][1]), pk2(d[0][2], d[0][3]), pk2(d[1][0], d[1][1]), pk2(d[1][2], d[1][3])};
            const u32x4 o1 = {pk2(d[2][0], d[2][1]), pk2(d[2][2], d[2][3]), pk2(d[3][0], d[3][1]), pk2(d[3][2], d[3][3])};
            f32x4 y[4];
#pragma unroll
            for (int t = 0; t < 4; ++t) y[t] = MFMA16(fuv[0][t], BF8(o0), (f32x4{0.f, 0.f, 0.f, 0.f}));
#pragma unroll
            for (int t = 0; t < 4; ++t) y[t] = MFMA16(fuv[1][t], BF8(o1), y[t]);
            if (nl < 4) {
#pragma unroll
                for (int t = 0; t < 4; ++t) *LP(f32x4, rw2 + t * 64) = y[t];
            }
        }
        wg_barrier();
        {
            f32x4 cs[8];
#pragma unroll
            for (int w2 = 0; w2 < 8; ++w2) cs[w2] = *LP(f32x4, r2r + w2 * 1024);
            const f32x4 c4 = ((cs[0] + cs[1]) + (cs[2] + cs[3])) + ((cs[4] + cs[5]) + (cs[6] + cs[7]));
            *LP(u32x2, cw) = u32x2{pk2(c4[0], c4[1]), pk2(c4[2], c4[3])};
            asm volatile("s_waitcnt lgkmcnt(0)" ::: "memory");
            const u32x4 cx0 = *LP(u32x4, cr), cx1 = *LP(u32x4, cr + 64);
            f32x4 o[4];
#pragma unroll
            for (int q = 0; q < 4; ++q) o[q] = MFMA16(fwo[0][q], BF8(cx0), (f32x4{0.f, 0.f, 0.f, 0.f}));
#pragma unroll
            for (int q = 0; q < 4; ++q) o[q] = MFMA16(fwo[1][q], BF8(cx1), o[q]);
            const f32x4 sel = (nl & 8) ? ((nl & 4) ? o[3] : o[2]) : ((nl & 4) ? o[1] : o[0]);
            u32x4 sv = __builtin_bit_cast(u32x4, sel);
            if (__builtin_expect(mx4(sv, 0u) == SENT, 0)) sv = u32x4{f32s(sel[0]), f32s(sel[1]), f32s(sel[2]), f32s(sel[3])};
            ST16(sv, oo, 0);
        }
        STAMP(12);
        wg_barrier();
        if (wv == 0) {
            u32x4 op[8];
            for (unsigned spins = 0;;) {
#pragma unroll
                for (int k = 0; k < 8; ++k) op[k] = LD16(osrc + (k & 3) * 1024, (k >> 2) * 4096);
                unsigned m = 0u;
#pragma unroll
                for (int k = 0; k < 8; ++k) m = mx4(op[k], m);
                if (__all(m != SENT)) break;
                if (++spins > kSpinCap) break;
                asm volatile("" ::: "memory");
            }
            STAMP(13);
            const f32x4 v = ((__builtin_bit_cast(f32x4, op[0]) + __builtin_bit_cast(f32x4, op[1])) + (__builtin_bit_cast(f32x4, op[2]) + __builtin_bit_cast(f32x4, op[3]))) +
                            ((__builtin_bit_cast(f32x4, op[4]) + __builtin_bit_cast(f32x4, op[5])) + (__builtin_bit_cast(f32x4, op[6]) + __builtin_bit_cast(f32x4, op[7])));
            float w[2];
#pragma unroll
            for (int q = 0; q < 2; ++q) {
                auto t = __builtin_amdgcn_permlane32_swap(__float_as_uint(v[q]), __float_as_uint(v[2 + q]), false, false);
                w[q] = __uint_as_float(t[0]) + __uint_as_float(t[1]);
                auto u = __builtin_amdgcn_permlane16_swap(__float_as_uint(w[q]), __float_as_uint(w[q]), false, false);
                w[q] = __uint_as_float(u[0]) + __uint_as_float(u[1]);
                w[q] += dppf<0x128>(w[q]);
            }
            unsigned t = pk2(w[0], w[1]);
            t = pk2(__uint_as_float(ga[0]) * lo16(t), __uint_as_float(ga[1]) * hi16(t));
            t = pk2s(lo16(xg) + lo16(t), hi16(xg) + hi16(t));
            __builtin_amdgcn_raw_buffer_store_b32(t, rw, xdst, 0, 16);
            __builtin_amdgcn_raw_buffer_store_b32(t, rw, xdst, 32768, 16);
            STAMP(14);
        }
        STAMP_FLUSH();
        return;
    }

    const int s = j - 16;
    if (wv == 0 && !(XNO & 2)) cnt_load();
    {
        const unsigned ln = (unsigned)(s * 64 + lane);
        if ((PFM & 1) && wv == 0) pf4<NTM & 1>((ln & 511u) * 6144u + (x * 2 + (ln >> 9)) * 128u, p.w_mod, L_SCR);
        if ((PFM & 2) && wv == 1) {
            const unsigned short* w = s < 8 ? p.w_uk : p.w_q;
            pf4<NTM & 1>((ln & 511u) * 8192u + x * 1024u + ((3u - ((unsigned)(size_t)w >> 7)) & 7u) * 128u, w, L_SCR);
        }
        if ((PFM & 48) == 48) {
            const unsigned r0 = (unsigned)(s * 4 + (wv & 3)), row = PFS == 0 ? r0 * 8 + (lane >> 3) : PFS == 1 ? (lane >> 3) * 64 + r0 : (r0 >> 5) * 256 + (lane >> 3) * 32 + (r0 & 31);
            pf4<NTM & 1>(row * 8192u + x * 1024u + (lane & 7u) * 128u, wv < 4 ? p.w_uk : p.w_q, L_SCR);
        }
        else if ((PFM & 16) && wv >= 4) { const unsigned l2 = (unsigned)((s * 4 + (wv & 3)) * 64 + lane); pf4<NTM & 1>((l2 >> 3) * 8192u + x * 1024u + (l2 & 7u) * 128u, p.w_q, L_SCR); }
        else if ((PFM & 32) && wv >= 4) { const unsigned l2 = (unsigned)((s * 4 + (wv & 3)) * 64 + lane); pf4<NTM & 1>((l2 >> 3) * 8192u + x * 1024u + (l2 & 7u) * 128u, p.w_uk, L_SCR); }
        if ((PFM & 4) && (wv & 6) == 2) { const unsigned l2 = (unsigned)((s * 2 + (wv & 1)) * 64 + lane); pf4<NTM & 1>((l2 & 511u) * 8192u + (x * 4 + (l2 >> 9)) * 256u, p.w_q, L_SCR); }
    }
    const int ug = x * 7 + s;
    const int head = lane & 31, wp = wv & 3, ch = wv >> 2, kb = wp * 2 + ch;
    const unsigned h2 = lane >> 5;
    const unsigned vb = (unsigned)(g * 1024 + ((nl ^ (g << 2)) << 4)), lb = (unsigned)((wv >> 1) * 32768 + (wv & 1) * 4096);
    const char* ck = (const char*)p.ckv + ((size_t)x * SK + s * 256 + wv * 16) * 1024;
    const unsigned rot = ROTC ? (unsigned)s & 3u : 0u;
    auto ck_dma = [&](unsigned i) { dma16<NTM & 16>(vb ^ ((i & 3u) << 4) ^ (rot << 8) ^ (((i >> 2) & 3u) << 8), ck + (i >> 4) * 131072u + (i & 3u) * 4096u, lb + ((i >> 2) & 3u) * 8192u + (i & 3u) * 1024u); };
    bf16x8 ka[16];
    s16x4 va[8][4];
    unsigned kad[4], vad[4], vad2[4];
    auto ka_reads = [&](int cb) {
#pragma unroll
        for (int u = 0; u < 4; ++u) ka[cb * 4 + u] = *LP(bf16x8, kad[u] + cb * 8192);
    };
    auto va_reads = [&]() {
#pragma unroll
        for (int kk = 0; kk < 8; ++kk)
#pragma unroll
            for (int q = 0; q < 4; ++q) va[kk][q] = tr4((kk < 4 ? vad[q] : vad2[q]) + ((kk >> 1) & 1) * 32768 + (kk & 1) * 4096);
    };
    auto va_pin = [&]() {
#pragma unroll
        for (int q = 0; q < 8; ++q) PIN4(va[q][0], va[q][1], va[q][2], va[q][3]);
    };
    u32x4 fa = {0u, 0u, 0u, 0u};
    const unsigned fao = (unsigned)(WS_FI + x * 128 + (lane & 7) * 16);
    constexpr int LAST = GI + 4 > 19 ? GI + 4 : 19;
    const unsigned qsrc = (unsigned)(WS_Q + (x * 32 + wv * 4) * 1024 + lane * 16);
    u32x4 qb[4];
#pragma unroll
    for (int i = 0; i <= LAST; ++i) {
        ck_dma(i);
        if (i == 3) {
#pragma unroll
            for (int u = 0; u < 4; ++u) {
                kad[u] = tile_off(wp * 32 + (head ^ (ch * 16)), ch * 8 + 2 * u + h2);
                vad[u] = tile_off(4 * h2 + (nl >> 2) + 8 * (u & 1), wv * 8 + 4 * (u >> 1) + 2 * (g & 1) + ((lane & 3) >> 1)) + 8 * (lane & 1);
                vad2[u] = vad[u] + 65536;
                PIN(kad[u]); PIN(vad[u]); PIN(vad2[u]);
            }
        }
        if (i > GI && i < GI + 4) VMW(8); else if (i >= 4) VMW(4);
        if (i == 12 && wv == 0 && (XNO & 2)) { cnt_load(); VMW(0); }
        if (i == 15 && wv == 0) {
            PIN(fa);
            tag_take();
            if (!__all(flags_ok(fa))) flags_wait(fao);
        }
        if ((i & 3) == 3 && i >= 7 && i <= 19) {
            const int cb = (i - 7) >> 2;
            if (cb > 0) { PIN4(ka[cb * 4 - 4], ka[cb * 4 - 3], ka[cb * 4 - 2], ka[cb * 4 - 1]); va_pin(); }
            wg_barrier();
            ka_reads(cb);
            if ((wv >> 1) == cb) va_reads();
            STAMP(16 + cb);
        }
        if (i == 10 && wv == 0) asm volatile("buffer_load_dwordx4 %0, %1, %2, 0 offen sc1" : "+v"(fa) : "v"(fao), "s"(rw) : "memory");
        if (i == GI)
            asm volatile("buffer_load_dwordx4 %0, %4, %5, 0 offen sc1\n\tbuffer_load_dwordx4 %1, %4, %5, 0 offen offset:1024 sc1\n\t"
                         "buffer_load_dwordx4 %2, %4, %5, 0 offen offset:2048 sc1\n\tbuffer_load_dwordx4 %3, %4, %5, 0 offen offset:3072 sc1"
                         : "=&v"(qb[0]), "=&v"(qb[1]), "=&v"(qb[2]), "=&v"(qb[3]) : "v"(qsrc), "s"(rw) : "memory");
    }
    const unsigned L_X = L_SCR, L_P = L_SCR + 16384, L_EXM = L_SCR + 24576, L_EXS = L_SCR + 25600;
    unsigned nx = LAST + 1;
    unsigned qad[4];
    {
        unsigned qwr[4];
#pragma unroll
        for (int k = 0; k < 4; ++k) {
            qwr[k] = L_SCR + (wv * 4 + k) * 1024 + (g ^ rot) * 256 + ((nl ^ ((wv * 4 + k) & 15)) << 4);
            qad[k] = L_SCR + head * 1024 + (((ch * 8 + 2 * k + h2) ^ (head & 15)) << 4);
            PIN(qwr[k]); PIN(qad[k]);
        }
        PIN4(ka[12], ka[13], ka[14], ka[15]); va_pin();
        wg_barrier();
        PIN4(qb[0], qb[1], qb[2], qb[3]);
        STAMP(20);
        if (!__all(mx4(qb[0], mx4(qb[1], mx4(qb[2], mx4(qb[3], 0u)))) != SENT)) {
            for (unsigned spins = 0;;) {
                if (!__all(mx4(qb[0], 0u) != SENT)) asm volatile("buffer_load_dwordx4 %0, %1, %2, 0 offen sc1" : "+v"(qb[0]) : "v"(qsrc), "s"(rw) : "memory");
                if (!__all(mx4(qb[1], 0u) != SENT)) asm volatile("buffer_load_dwordx4 %0, %1, %2, 0 offen offset:1024 sc1" : "+v"(qb[1]) : "v"(qsrc), "s"(rw) : "memory");
                if (!__all(mx4(qb[2], 0u) != SENT)) asm volatile("buffer_load_dwordx4 %0, %1, %2, 0 offen offset:2048 sc1" : "+v"(qb[2]) : "v"(qsrc), "s"(rw) : "memory");
                if (!__all(mx4(qb[3], 0u) != SENT)) asm volatile("buffer_load_dwordx4 %0, %1, %2, 0 offen offset:3072 sc1" : "+v"(qb[3]) : "v"(qsrc), "s"(rw) : "memory");
                if (nx <= 28) {
#pragma unroll
                    for (int i = 0; i < 4; ++i) ck_dma(nx + i);
                    nx += 4;
                    VMW(4);
                } else {
                    for (; nx < 32; ++nx) ck_dma(nx);
                    VMW(0);
                }
                PIN4(qb[0], qb[1], qb[2], qb[3]);
                if (__all(mx4(qb[0], mx4(qb[1], mx4(qb[2], mx4(qb[3], 0u)))) != SENT)) break;
                if (++spins > kSpinCap) break;
            }
        }
#pragma unroll
        for (int k = 0; k < 4; ++k) *LP(u32x4, qwr[k]) = qb[k];
    }
    auto slot = [&]() { if (nx < 32) { ck_dma(nx); ++nx; } };
    unsigned xw = L_X + wv * 2048 + lane * 16, xr = xw ^ 8192u, exw = L_EXM + (head * 8 + wv) * 4, exr = L_EXM + head * 32;
    unsigned pwr = L_P + kb * 1024 + lane * 16, prd = L_P + lane * 16, esw = L_EXS + (head * 16 + h2 * 8 + wv) * 4;
    PIN4(xw, xr, exw, exr); PIN(pwr); PIN(prd); PIN(esw);
    wg_barrier();
    STAMP(21);
    bf16x8 qf[16];
#pragma unroll
    for (int ks = 0; ks < 16; ++ks) qf[ks] = *LP(bf16x8, qad[ks & 3] + (ks >> 2) * 256);
    __builtin_amdgcn_sched_barrier(0);
    float lsum, mrun;
    f32x16 o[2];
    auto meet = [&](const f32x16& sc, float (&t)[8]) {
        *LP(f32x4, xw) = f32x4{sc[8], sc[9], sc[10], sc[11]}; *LP(f32x4, xw + 1024) = f32x4{sc[12], sc[13], sc[14], sc[15]};
        wg_barrier();
        const f32x4 x0 = *LP(f32x4, xr), x1 = *LP(f32x4, xr + 1024);
#pragma unroll
        for (int r = 0; r < 4; ++r) { t[r] = sc[r] + x0[r]; t[4 + r] = sc[4 + r] + x1[r]; }
        const float m = fmax3(fmax3(t[0], t[1], t[2]), fmax3(t[3], t[4], t[5]), fmax3(t[6], t[7], t[7]));
        auto u = __builtin_amdgcn_permlane32_swap(__float_as_uint(m), __float_as_uint(m), false, false);
        *LP(float, exw) = fmax3(__uint_as_float(u[0]), __uint_as_float(u[1]), __uint_as_float(u[1]));
    };
    {
        f32x16 sc;
#pragma unroll
        for (int i = 0; i < 16; ++i) sc[i] = 0.f;
#pragma unroll
        for (int ks = 0; ks < 16; ++ks) {
            sc = MFMA32(ka[ks], qf[ks], sc);
            if ((ks & 1) == 1) slot();
            __builtin_amdgcn_sched_barrier(0);
        }
        wg_barrier();
        float t[8];
        meet(sc, t);
        wg_barrier();
        const f32x4 a0 = *LP(f32x4, exr), a1 = *LP(f32x4, exr + 16);
        mrun = fmax3(fmax3(a0[0], a0[1], a0[2]), fmax3(a0[3], a1[0], a1[1]), fmax3(a1[2], a1[3], a1[3])) * SCL;
        float e[8];
#pragma unroll
        for (int r = 0; r < 8; ++r) e[r] = __builtin_amdgcn_exp2f(fmaf(t[r], SCL, -mrun));
        *LP(u32x4, pwr) = u32x4{pk2(e[0], e[1]), pk2(e[2], e[3]), pk2(e[4], e[5]), pk2(e[6], e[7])};
        lsum = ((e[0] + e[1]) + (e[2] + e[3])) + ((e[4] + e[5]) + (e[6] + e[7]));
        wg_barrier();
        STAMP(22);
        bf16x8 pf[8];
#pragma unroll
        for (int kk = 0; kk < 8; ++kk) pf[kk] = *LP(bf16x8, prd + kk * 1024);
#pragma unroll
        for (int i = 0; i < 16; ++i) { o[0][i] = 0.f; o[1][i] = 0.f; }
        __builtin_amdgcn_sched_barrier(0);
#pragma unroll
        for (int kk = 0; kk < 8; ++kk) {
            o[0] = MFMA32(cat8(va[kk][0], va[kk][1]), pf[kk], o[0]);
            o[1] = MFMA32(cat8(va[kk][2], va[kk][3]), pf[kk], o[1]);
            if (kk & 1) slot();
            __builtin_amdgcn_sched_barrier(0);
        }
        for (; nx < 32; ++nx) ck_dma(nx);
    }
    STAMP(23);
    unsigned sw[4], srd = lb + (lane >> 3) * 128 + (((lane & 7) ^ (lane >> 3)) << 4);
#pragma unroll
    for (int q = 0; q < 4; ++q) sw[q] = lb + head * 128 + (((2 * q + h2) ^ (head & 7)) << 4);
    PIN4(sw[0], sw[1], sw[2], sw[3]);
    unsigned po = (unsigned)(WS_PART + ((x * 32 + (lane >> 3)) * 8 + (wv ^ (rot * 2))) * 2048 + (s ^ (lane >> 3)) * 128 + (lane & 7) * 16);
    unsigned mlo = (unsigned)(WS_ML + (x * 16 + s) * 512 + head * 16), esr = L_EXS + head * 64;
    PIN4(esr, srd, po, mlo);
    {
        f32x16 sc;
#pragma unroll
        for (int i = 0; i < 16; ++i) sc[i] = 0.f;
        VMW(8);
        wg_barrier();
        ka_reads(0); ka_reads(1);
        if (wv < 4) va_reads();
        __builtin_amdgcn_sched_barrier(0);
#pragma unroll
        for (int ks = 0; ks < 8; ++ks) sc = MFMA32(ka[ks], qf[ks], sc);
        __builtin_amdgcn_sched_barrier(0);
        VMW(4);
        wg_barrier();
        ka_reads(2);
        if ((wv >> 1) == 2) va_reads();
        __builtin_amdgcn_sched_barrier(0);
#pragma unroll
        for (int ks = 8; ks < 12; ++ks) sc = MFMA32(ka[ks], qf[ks], sc);
        __builtin_amdgcn_sched_barrier(0);
        VMW(0);
        wg_barrier();
        STAMP(24);
        ka_reads(3);
        __builtin_amdgcn_sched_barrier(0);
#pragma unroll
        for (int ks = 12; ks < 16; ++ks) sc = MFMA32(ka[ks], qf[ks], sc);
        float t[8];
        meet(sc, t);
        wg_barrier();
        const f32x4 a0 = *LP(f32x4, exr), a1 = *LP(f32x4, exr + 16);
        const float mnew = fmaxf(fmax3(fmax3(a0[0], a0[1], a0[2]), fmax3(a0[3], a1[0], a1[1]), fmax3(a1[2], a1[3], a1[3])) * SCL, mrun);
        float e[8];
#pragma unroll
        for (int r = 0; r < 8; ++r) e[r] = __builtin_amdgcn_exp2f(fmaf(t[r], SCL, -mnew));
        *LP(u32x4, pwr) = u32x4{pk2(e[0], e[1]), pk2(e[2], e[3]), pk2(e[4], e[5]), pk2(e[6], e[7])};
        const float al = __builtin_amdgcn_exp2f(mrun - mnew);
        *LP(float, esw) = lsum * al + (((e[0] + e[1]) + (e[2] + e[3])) + ((e[4] + e[5]) + (e[6] + e[7])));
        mrun = mnew;
        o[0] *= al; o[1] *= al;
        va_pin();
        wg_barrier();
        STAMP(25);
        if ((wv >> 1) == 3) va_reads();
        bf16x8 pf[8];
#pragma unroll
        for (int kk = 0; kk < 8; ++kk) pf[kk] = *LP(bf16x8, prd + kk * 1024);
        __builtin_amdgcn_sched_barrier(0);
#pragma unroll
        for (int kk = 0; kk < 8; ++kk) {
            o[0] = MFMA32(cat8(va[kk][0], va[kk][1]), pf[kk], o[0]);
            o[1] = MFMA32(cat8(va[kk][2], va[kk][3]), pf[kk], o[1]);
        }
#pragma unroll
        for (int t2 = 0; t2 < 2; ++t2)
#pragma unroll
            for (int gp = 0; gp < 2; ++gp) {
                const unsigned lo0 = pk2(o[t2][8 * gp + 0], o[t2][8 * gp + 1]), lo1 = pk2(o[t2][8 * gp + 2], o[t2][8 * gp + 3]);
                const unsigned hi0 = pk2(o[t2][8 * gp + 4], o[t2][8 * gp + 5]), hi1 = pk2(o[t2][8 * gp + 6], o[t2][8 * gp + 7]);
                auto r0 = __builtin_amdgcn_permlane32_swap(lo0, hi0, false, false);
                auto r1 = __builtin_amdgcn_permlane32_swap(lo1, hi1, false, false);
                *LP(u32x4, sw[t2 * 2 + gp]) = u32x4{r0[0], r1[0], r0[1], r1[1]};
            }
        asm volatile("s_waitcnt lgkmcnt(0)" ::: "memory");
        u32x4 w[4];
#pragma unroll
        for (int i = 0; i < 4; ++i) w[i] = *LP(u32x4, srd + i * 1024);
#pragma unroll
        for (int i = 0; i < 4; ++i) ST16(w[i], po, i * 131072);
        __builtin_amdgcn_sched_barrier(0);
        STAMP(26);
        u32x4 mlrec;
        {
            const f32x4 b0 = *LP(f32x4, esr), b1 = *LP(f32x4, esr + 16), b2 = *LP(f32x4, esr + 32), b3 = *LP(f32x4, esr + 48);
            const f32x4 bs = (b0 + b1) + (b2 + b3);
            mlrec = u32x4{__float_as_uint(mrun), __float_as_uint((bs[0] + bs[1]) + (bs[2] + bs[3])), tlo, thi};
            PIN(mlrec);
        }
        VMW(0);
        wg_barrier();
        if (wv == 0 && lane < 32) ST16(mlrec, mlo, 0);
        STAMP(27);
        wg_barrier();
    }
    const unsigned long long t_att = __builtin_amdgcn_s_memrealtime();
    if (s >= 11) {
        const unsigned n = (unsigned)((s - 11) * 8 + wv);
        if (n < 28) pf4<NTM & 32>((n * 64 + lane) * 1024u + x * 128u, p.w2, 0);
        { const unsigned l2 = n * 64 + lane; pf4<NTM & 32>((l2 / 5u) * (DFF * 2) + min(((unsigned)x * 448u & ~127u) + (l2 % 5u) * 128u, DFF * 2 - 4u), p.w1, 0); }
        if (wv == 0) flag_put(WS_FA, 1024, id - 128);
        STAMP_FLUSH();
        return;
    }
    const unsigned L_H2S = L_SCR, L_REDM = L_SCR + 16384;
    if (s < 7) {
        {
            const char* w1s = (const char*)(p.w1 + (size_t)wv * 64 * DFF + ug * 32);
#pragma unroll
            for (int i = 0; i < 4; ++i) dma16<NTM & 32>((unsigned)((lane >> 2) * DFF * 2 + (lane & 3) * 16), w1s + (size_t)i * 16 * DFF * 2, wv * 4096 + i * 1024);
        }
        if (wv == 0) {
            if (lane < 32) ST16(s4, (unsigned)(WS_U + ug * 512 + lane * 16), 0);
            flags_wait(WS_FB + x * 1024 + lane * 16);
        }
        VMW(0);
        if (wv == 0) flag_put(WS_FA, 1024, id - 128);
        wg_barrier();
        const int b = wv;
        u32x4 md[4];
#pragma unroll
        for (int k = 0; k < 4; ++k) md[k] = LD16((unsigned)(WS_MOD + (b * 2048 + 512 + (k >> 1) * 512 + lane * 8 + (k & 1) * 4) * 4), 0);
        bf16x8 wfr[16];
        if (wv >= 6) {
#pragma unroll
            for (int ks = 0; ks < 16; ++ks) {
                const unsigned a = (unsigned)(ks * 2048 + (g * 8 + (nl >> 2)) * 64 + (wv & 1) * 32 + (lane & 3) * 8);
                wfr[ks] = tr_frag(a, a + 256);
            }
#pragma unroll
            for (int ks = 0; ks < 16; ks += 4) PIN4(wfr[ks], wfr[ks + 1], wfr[ks + 2], wfr[ks + 3]);
        }
        float scv[8];
#pragma unroll
        for (int q = 0; q < 8; ++q) { scv[q] = rbf(1.f + __uint_as_float(q < 4 ? md[2][q] : md[3][q - 4])); PIN(scv[q]); }
        unsigned har = L_H2S + ((lane & 7) * HS_LD + g * 8) * 2, haw = L_H2S + (b * HS_LD + lane * 8) * 2;
        unsigned udst = (unsigned)(WS_U + (ug * 2 + (wv & 1)) * 256 + (nl * 16 + g * 4) * 2), xsrc = (unsigned)(WS_X1 + x * 8192 + (b * 512 + lane * 8) * 2);
        PIN4(har, haw, udst, xsrc); PIN(md[0]); PIN(md[1]);
        STAMP(28);
        while (__builtin_amdgcn_s_memrealtime() < t_att + XGATE) __builtin_amdgcn_s_sleep(2);
        u32x4 xv;
        for (unsigned spins = 0;;) {
            xv = LD16(xsrc, 0);
            if (__all(mx4(xv, 0u) != SENT)) break;
            __builtin_amdgcn_s_sleep(SLPX);
            if (++spins > kSpinCap) break;
            asm volatile("" ::: "memory");
        }
        STAMP(29);
        {
            f32x2_t v[4];
#pragma unroll
            for (int q = 0; q < 4; ++q) v[q] = f32x2_t{lo16(xv[q]), hi16(xv[q])};
            const f32x2_t p1 = (v[0] + v[1]) + (v[2] + v[3]), p2 = (v[0] * v[0] + v[1] * v[1]) + (v[2] * v[2] + v[3] * v[3]);
            float mb, rden;
            ln_stats(p1[0] + p1[1], p2[0] + p2[1], mb, rden);
            u32x4 hr;
#pragma unroll
            for (int q = 0; q < 4; ++q) {
                const f32x2_t h = ((v[q] - mb) * rden) * f32x2_t{scv[2 * q], scv[2 * q + 1]} + f32x2_t{__uint_as_float(md[q >> 1][(q & 1) * 2]), __uint_as_float(md[q >> 1][(q & 1) * 2 + 1])};
                hr[q] = pk2(h[0], h[1]);
            }
            *LP(u32x4, haw) = hr;
        }
        wg_barrier();
        if (wv >= 6) {
            bf16x8 af[16];
#pragma unroll
            for (int ks = 0; ks < 16; ++ks) af[ks] = *LP(bf16x8, har + ks * 64);
            __builtin_amdgcn_sched_barrier(0);
            f32x4 d0 = {0.f, 0.f, 0.f, 0.f}, d1 = {0.f, 0.f, 0.f, 0.f};
#pragma unroll
            for (int ks = 0; ks < 16; ks += 2) { d0 = MFMA16(wfr[ks], af[ks], d0); d1 = MFMA16(wfr[ks + 1], af[ks + 1], d1); }
            d0 += d1;
            if (nl < 8) __builtin_amdgcn_raw_buffer_store_b64(u32x2{pk2s(fmaxf(d0[0], 0.f), fmaxf(d0[1], 0.f)), pk2s(fmaxf(d0[2], 0.f), fmaxf(d0[3], 0.f))}, rw, udst, 0, 16);
            STAMP(30);
        }
        STAMP_FLUSH();
        return;
    }
    {
        const int n0 = (x * 4 + (s - 7)) * 16;
        const char* mws = (const char*)(p.w2 + (size_t)wv * 224 * 512 + n0);
#pragma unroll
        for (int i = 0; i < 7; ++i) dma16<NTM & 32>((unsigned)((lane >> 1) * 1024 + (lane & 1) * 16), mws + i * 32768, rgn + i * 1024);
        const int fb = lane >> 3, fc = (lane & 7) * 2;
        u32x2 gb = {0u, 0u};
        if (wv == 0) {
            flag_put(WS_FA, 1024, id - 128);
            flags_wait(WS_FB + x * 1024 + lane * 16);
            flags_wait(WS_FA + x * 1024 + lane * 16);
            if (id == 23 * 8 && lane == 0) __builtin_amdgcn_raw_buffer_store_b64(u32x2{tlo, thi}, rw, WS_EPOCH, 0, 16);
            gb = __builtin_amdgcn_raw_buffer_load_b64(rw, (unsigned)(WS_MOD + (fb * 2048 + 1536 + n0 + fc) * 4), 0, 16);
            PIN(gb);
        }
        VMW(0);
        bf16x8 wfr[7];
        const unsigned trl = (unsigned)(g * 8 + (nl >> 2)) * 32u + (unsigned)(lane & 3) * 8u;
#pragma unroll
        for (int ks = 0; ks < 7; ++ks) { wfr[ks] = tr_frag(rgn + ks * 1024 + trl, rgn + ks * 1024 + trl + 128); PIN(wfr[ks]); }
        unsigned usrc = (unsigned)(WS_U + wv * 3584 + (g >> 1) * 256 + (nl & 7) * 32 + (g & 1) * 16);
        unsigned rdw = L_REDM + wv * 512 + (nl & 7) * 64 + g * 16, rdr = L_REDM + lane * 8;
        unsigned short* outp = p.out + fb * 512 + n0 + fc;
        PIN(usrc); PIN(rdw); PIN(rdr); PIN(outp);
        STAMP(28);
        unsigned xg = 0u;
        if (wv == 0) {
            while (__builtin_amdgcn_s_memrealtime() < t_att + XGATE) __builtin_amdgcn_s_sleep(2);
            for (unsigned spins = 0;;) {
                xg = __builtin_amdgcn_raw_buffer_load_b32(rw, (unsigned)(WS_X1 + x * 8192 + (fb * 512 + n0 + fc) * 2), 0, 16);
                if (__all(xg != SENT)) break;
                __builtin_amdgcn_s_sleep(SLPD);
                if (++spins > kSpinCap) break;
                asm volatile("" ::: "memory");
            }
            __builtin_amdgcn_s_sleep(SLPG);
        }
        wg_barrier();
        u32x4 uf[7];
        for (unsigned spins = 0;;) {
#pragma unroll
            for (int ks = 0; ks < 7; ++ks) uf[ks] = LD16(usrc + ks * 512, 0);
            unsigned m = 0u;
#pragma unroll
            for (int ks = 0; ks < 7; ++ks) m = mx4(uf[ks], m);
            if (__all(m != SENT)) break;
            if (++spins > kSpinCap) break;
            asm volatile("" ::: "memory");
        }
        STAMP(29);
        f32x4 d = {0.f, 0.f, 0.f, 0.f}, d2 = {0.f, 0.f, 0.f, 0.f};
#pragma unroll
        for (int ks = 0; ks < 7; ++ks) {
            if (ks & 1) d2 = MFMA16(wfr[ks], BF8(uf[ks]), d2);
            else d = MFMA16(wfr[ks], BF8(uf[ks]), d);
        }
        *LP(f32x4, rdw) = d + d2;
        wg_barrier();
        if (wv == 0) {
            f32x2_t c[8];
#pragma unroll
            for (int w2 = 0; w2 < 8; ++w2) c[w2] = *LP(f32x2_t, rdr + w2 * 512);
            const f32x2_t sm = ((c[0] + c[1]) + (c[2] + c[3])) + ((c[4] + c[5]) + (c[6] + c[7]));
            unsigned t = pk2(sm[0], sm[1]);
            t = pk2(__uint_as_float(gb[0]) * lo16(t), __uint_as_float(gb[1]) * hi16(t));
            *(GAS unsigned*)outp = pk2(lo16(xg) + lo16(t), hi16(xg) + hi16(t));
            STAMP(31);
        }
        STAMP_FLUSH();
    }
}

extern "C" void kernel_launch(void* const* d_in, const int* in_sizes, int n_in,
                              void* d_out, int out_size, void* d_ws, size_t ws_size,
                              hipStream_t stream) {
    Params p;
    p.x = (const unsigned short*)d_in[0];
    p.ckv = (const unsigned short*)d_in[1];
    p.w_q = (const unsigned short*)d_in[2];
    p.w_uk = (const unsigned short*)d_in[3];
    p.w_uv = (const unsigned short*)d_in[4];
    p.w_o = (const unsigned short*)d_in[5];
    p.w1 = (const unsigned short*)d_in[7];
    p.w2 = (const unsigned short*)d_in[8];
    p.c = (const unsigned short*)d_in[9];
    p.w_mod = (const unsigned short*)d_in[10];
    p.ws = (char*)d_ws;
    p.out = (unsigned short*)d_out;
    (void)hipFuncSetAttribute((const void*)k_main, hipFuncAttributeMaxDynamicSharedMemorySize, 163840);
    k_main<<<256, 512, 163840, stream>>>(p);
}
```

```cpp
#include <hip/hip_runtime.h>
#include <hip/hip_bf16.h>
#include <stdint.h>

#define NH 32
#define SK 4096
#define DFF 1792
#define GI 19
#define ROTC 1
#define PFS 2
#define XNO 0
#define CPJ 7
#define NTM 16
#define QHELP 4
#define HP 13
#define NTH 0
#define W0 2
#define SL0 1
#define LMPF 0
#define SLPB 20
#define MD0 100
#define MD1 150
#define TWD 730
#define LMD 250
#define PFM 57
#define CPFE 31400
#define SLPM 1
#define SLPD 12
#define SLPG 8
#define SLPX 4
#define SLPH 4

using u16x8 = __attribute__((ext_vector_type(8))) unsigned short;
using s16x4 = __attribute__((ext_vector_type(4))) short;
using bf16x8 = __attribute__((ext_vector_type(8))) short;
using f32x16 = __attribute__((ext_vector_type(16))) float;
using f32x4 = __attribute__((ext_vector_type(4))) float;
using u32x4 = __attribute__((ext_vector_type(4))) unsigned;
using u32x2 = __attribute__((ext_vector_type(2))) unsigned;

#define GAS __attribute__((address_space(1)))
#define LAS __attribute__((address_space(3)))
typedef GAS unsigned long long gu64;
#define RLX_AGENT __ATOMIC_RELAXED, __HIP_MEMORY_SCOPE_AGENT
#define SENT 0xffffffffu
#define TAGC 0x9e3779b9u

__device__ __forceinline__ float bf2f(unsigned short u) { return __uint_as_float(((unsigned)u) << 16); }
typedef __attribute__((ext_vector_type(2))) __bf16 bf16x2_t;
typedef __attribute__((ext_vector_type(2))) float f32x2_t;
__device__ __forceinline__ unsigned pk2(float a, float b) {
    f32x2_t v = {a, b};
    bf16x2_t r = __builtin_convertvector(v, bf16x2_t);
    return __builtin_bit_cast(unsigned, r);
}
__device__ __forceinline__ unsigned pk2s(float a, float b) {
    const unsigned r = pk2(a, b);
    return r == SENT ? 0x7fc07fc0u : r;
}
__device__ __forceinline__ unsigned f32s(float a) {
    const unsigned r = __float_as_uint(a);
    return r == SENT ? 0x7fc00000u : r;
}
__device__ __forceinline__ unsigned short f2bf(float f) { return (unsigned short)(pk2(f, 0.f) & 0xffffu); }
__device__ __forceinline__ float rbf(float f) { return __uint_as_float(pk2(f, 0.f) << 16); }
__device__ __forceinline__ float lo16(unsigned u) { return __uint_as_float(u << 16); }
__device__ __forceinline__ float hi16(unsigned u) { return __uint_as_float(u & 0xffff0000u); }
__device__ __forceinline__ unsigned umax3(unsigned a, unsigned b, unsigned c) { return max(max(a, b), c); }
__device__ __forceinline__ unsigned mx4(const u32x4& v, unsigned m) { return umax3(v[2], v[3], umax3(v[0], v[1], m)); }
__device__ __forceinline__ float fmax3(float a, float b, float c) { float r; asm("v_max3_f32 %0, %1, %2, %3" : "=v"(r) : "v"(a), "v"(b), "v"(c)); return r; }
template <int C> __device__ __forceinline__ float dppf(float v) { return __int_as_float(__builtin_amdgcn_update_dpp(0, __float_as_int(v), C, 0xf, 0xf, true)); }
__device__ __forceinline__ float sum16(float v) { v += dppf<0xB1>(v); v += dppf<0x4E>(v); v += dppf<0x141>(v); v += dppf<0x140>(v); return v; }

__device__ __forceinline__ void ln_stats(float p1, float p2, float& mb, float& rden) {
    auto t = __builtin_amdgcn_permlane32_swap(__float_as_uint(p1), __float_as_uint(p2), false, false);
    float a = sum16(__uint_as_float(t[0]) + __uint_as_float(t[1]));
    auto t2 = __builtin_amdgcn_permlane16_swap(__float_as_uint(a), __float_as_uint(a), false, false);
    a = (__uint_as_float(t2[0]) + __uint_as_float(t2[1])) * (1.f / 512.f);
    auto t3 = __builtin_amdgcn_permlane32_swap(__float_as_uint(a), __float_as_uint(a), false, false);
    const float m = __uint_as_float(t3[0]);
    const float var = rbf(fmaxf(fmaf(-m, m, __uint_as_float(t3[1])), 0.f));
    mb = rbf(m);
    rden = __builtin_amdgcn_rcpf(rbf(__builtin_amdgcn_sqrtf(rbf(var + 1e-5f))));
}
#define STR 262144
#define WS_Q 0
#define WS_H 32768
#define WS_X1 65536
#define WS_ML 73728
#define WS_MOD 106496
#define WS_U 90112
#define WS_FI 94208
#define WS_FA 95232
#define WS_FB 96256
#define WS_EPOCH 98304
#define WS_OUTP 131072
#define WS_PART 2097152
#define WS_END 6291456

#define LP(T, off) ((LAS T*)(size_t)(unsigned)(off))
#define STAMP(k) do {} while (0)
#define STAMP_FLUSH() do {} while (0)
__device__ __forceinline__ s16x4 tr4(unsigned a) { return __builtin_amdgcn_ds_read_tr16_b64_v4i16(LP(s16x4, a)); }
__device__ __forceinline__ bf16x8 cat8(s16x4 a, s16x4 b) { return __builtin_shufflevector(a, b, 0, 1, 2, 3, 4, 5, 6, 7); }
__device__ __forceinline__ bf16x8 tr_frag(unsigned a0, unsigned a1) { return cat8(tr4(a0), tr4(a1)); }
__device__ __forceinline__ unsigned swz(unsigned row) { return ((row & 3) << 2) | ((row >> 2) & 3); }
__device__ __forceinline__ unsigned off_b(unsigned row, unsigned ch) { return 256u * row + 16u * (ch ^ swz(row)); }
__device__ __forceinline__ unsigned tile_off(unsigned key, unsigned chunk) { return ((key >> 5) * 4 + (chunk >> 4)) * 8192u + off_b(key & 31, chunk & 15); }
__device__ __forceinline__ void wg_barrier() { asm volatile("s_waitcnt lgkmcnt(0)\n\ts_barrier" ::: "memory"); }
template <int NT> __device__ __forceinline__ void dma16(unsigned voff, const void* sbase, unsigned lds_dst) {
    if (NT) asm volatile("s_mov_b32 m0, %2\n\ts_nop 0\n\tglobal_load_lds_dwordx4 %0, %1 nt" :: "v"(voff), "s"(sbase), "s"(lds_dst) : "memory", "m0");
    else asm volatile("s_mov_b32 m0, %2\n\ts_nop 0\n\tglobal_load_lds_dwordx4 %0, %1" :: "v"(voff), "s"(sbase), "s"(lds_dst) : "memory", "m0");
}
template <int NT> __device__ __forceinline__ void dma16x(unsigned voff, unsigned vx, const void* sbase, unsigned lds_dst) {
    unsigned t;
#define DMAX(pol, c) asm volatile("s_mov_b32 m0, %3\n\tv_xor_b32 %0, %4, %1\n\tglobal_load_lds_dwordx4 %0, %2" pol : "=&v"(t) : "v"(voff), "s"(sbase), "s"(lds_dst), c(vx) : "memory", "m0")
    if (__builtin_constant_p(vx)) { if (NT) DMAX(" nt", "i"); else DMAX("", "i"); }
    else { if (NT) DMAX(" nt", "s"); else DMAX("", "s"); }
}
template <int NT> __device__ __forceinline__ void pf4(unsigned voff, const void* sbase, unsigned lds_dst) {
    if (NT) asm volatile("s_mov_b32 m0, %2\n\ts_nop 0\n\tglobal_load_lds_dword %0, %1 nt" :: "v"(voff), "s"(sbase), "s"(lds_dst) : "memory", "m0");
    else asm volatile("s_mov_b32 m0, %2\n\ts_nop 0\n\tglobal_load_lds_dword %0, %1" :: "v"(voff), "s"(sbase), "s"(lds_dst) : "memory", "m0");
}
#define XGATE 300
#define SPINCAP (1u << 20)
constexpr unsigned kSpinCap = SPINCAP;
struct Params {
    const unsigned short* x;
    const unsigned short* ckv;
    const unsigned short* w_q;
    const unsigned short* w_uk;
    const unsigned short* w_uv;
    const unsigned short* w_o;
    const unsigned short* w1;
    const unsigned short* w2;
    const unsigned short* c;
    const unsigned short* w_mod;
    char* ws;
    unsigned short* out;
};
#define VMW(n) asm volatile("s_waitcnt vmcnt(%0)" :: "n"(n) : "memory")
#define PIN(x) asm volatile("" : "+v"(x))
#define PIN4(a, b, c, d) asm volatile("" : "+v"(a), "+v"(b), "+v"(c), "+v"(d) :: "memory")
#define MFMA16(a, b, c) __builtin_amdgcn_mfma_f32_16x16x32_bf16(a, b, c, 0, 0, 0)
#define MFMA32(a, b, c) __builtin_amdgcn_mfma_f32_32x32x16_bf16(a, b, c, 0, 0, 0)
#define BF8(x) __builtin_bit_cast(bf16x8, x)
#define ST16(v, off, soff) __builtin_amdgcn_raw_buffer_store_b128(v, rw, off, soff, 16)
#define LD16(off, soff) __builtin_amdgcn_raw_buffer_load_b128(rw, off, soff, 16)
#define SCL (0.08838834764831845f * 1.4426950408889634f)
#define BIGD 60.f
#define L_SCR 131072
#define HS_LD 520

__global__ __launch_bounds__(512, 2) void k_main(Params p) {
    unsigned long long pc;
    asm volatile("s_getpc_b64 %0" : "=s"(pc));
    const int id = blockIdx.x, tid = threadIdx.x;
    int lane = tid & 63;
    const int wv = __builtin_amdgcn_readfirstlane(tid >> 6);
    int g = lane >> 4, nl = lane & 15;
    auto rederive = [&]() {
        asm volatile("v_mbcnt_lo_u32_b32 %0, -1, 0\n\tv_mbcnt_hi_u32_b32 %0, -1, %0" : "=v"(lane));
        g = lane >> 4; nl = lane & 15;
    };
    const int x = id & 7, j = id >> 3;
    if (wv < 4 && j == 15) {
        asm volatile("s_mov_b32 m0, %2\n\ts_nop 0\n\tglobal_load_lds_dword %0, %1" :: "v"(min((unsigned)((wv * 64 + lane) * 128), (unsigned)CPFE)), "s"(pc), "s"(L_SCR + 31744) : "memory", "m0");
    }
    if ((j >> 2) == 3) {
        dma16<0>((unsigned)(((nl & 7) * 512 + (nl >> 3) * 32 + g * 8) * 2 + wv * 128), p.c, wv * 16384 + 8192);
        dma16<0>((unsigned)tid * 16u, p.x, wv * 16384 + 9216);
    }
    char* ws = p.ws;
    STAMP(0);
    const __amdgpu_buffer_rsrc_t rw = __builtin_amdgcn_make_buffer_rsrc(ws, 0, WS_END, 0x00020000);
    const u32x4 s4 = {SENT, SENT, SENT, SENT};
    unsigned long long cnt = 0ull;
    auto cnt_load = [&]() { asm volatile("global_load_dwordx2 %0, %1, %2 sc1" : "+v"(cnt) : "v"((unsigned)WS_EPOCH), "s"(ws) : "memory"); };
    unsigned tlo = 0u, thi = 0u;
    auto tag_take = [&]() {
        asm volatile("" : "+v"(cnt) :: "memory");
        const unsigned long long t = cnt + 1ull;
        tlo = __builtin_amdgcn_readfirstlane((unsigned)t); thi = __builtin_amdgcn_readfirstlane((unsigned)(t >> 32));
    };
    auto flag_put = [&](unsigned base, unsigned idx) {
        if (lane < 8) __builtin_amdgcn_raw_buffer_store_b64(u32x2{tlo, thi}, rw, base + lane * STR + idx * 8, 0, 16);
    };
    auto mod_get = [&](unsigned off) {
        u32x4 v;
        for (unsigned spins = 0;;) {
            v = LD16(off, 0);
            if (__all((v[2] == tlo) & (v[3] == thi))) break;
            __builtin_amdgcn_s_sleep(16);
            if (++spins > kSpinCap) break;
            asm volatile("" ::: "memory");
        }
        return u32x2{v[0], v[1]};
    };
    auto flags_ok = [&](const u32x4& f) { return (f[0] == tlo) & (f[1] == thi) & (f[2] == tlo) & (f[3] == thi); };
    auto flags_wait = [&](unsigned off) {
        for (unsigned spins = 0;;) {
            const u32x4 f = LD16(off, 0);
            if (__all(flags_ok(f))) break;
            __builtin_amdgcn_s_sleep(16);
            if (++spins > kSpinCap) break;
            asm volatile("" ::: "memory");
        }
    };
    const unsigned pcl = (unsigned)(((((lane & 7) >> 1) ^ ((lane >> 4) & 1)) * 32) + (lane & 1) * 16);
    auto dma_piece = [&](const unsigned short* src, unsigned ldb, unsigned dst, int n, int nt) {
        const unsigned v0 = (lane >> 3) * ldb + pcl, v1 = v0 ^ 64u;
#pragma unroll
        for (int i = 0; i < n; ++i) {
            if (nt) dma16<1>((i & 1) ? v1 : v0, (const char*)src + (size_t)i * 8 * ldb, dst + i * 1024);
            else dma16<0>((i & 1) ? v1 : v0, (const char*)src + (size_t)i * 8 * ldb, dst + i * 1024);
        }
    };
    const unsigned frl = (unsigned)((g * 8 + (nl >> 2)) * 128 + (lane & 3) * 8), frs = (unsigned)((nl >> 3) | ((g & 1) << 1));
    auto piece_frag = [&](unsigned base, int ks, int t) {
        const unsigned a = base + frl + ((t ^ frs) << 5) + ks * 4096;
        return tr_frag(a, a + 512);
    };
    const unsigned rgn = (unsigned)(wv * 16384);

    if (j < 16) {
        const unsigned long long t_start = __builtin_amdgcn_s_memrealtime();
        const int th = x * 4 + (j & 3);
        if (j < 12) {
            const int qt = j >> 2, rot = qt * 5;
            const bool pub = wv < (qt < 2 ? 3 : 2);
            const unsigned qo = (unsigned)(WS_Q + (th & 7) * STR + ((nl & 7) * 4 + (th >> 3)) * 1024 + (qt * 192 + wv * 64 + g * 16 + (nl >> 3) * 8) * 2);
            const unsigned vb = (unsigned)(g * 8192 + ((nl ^ (g << 2)) << 4));
            const char* wuk = (const char*)(p.w_uk + th * 128) + (size_t)qt * 192 * 8192;
            const char* wq = (const char*)(p.w_q + th * 128) + (size_t)wv * 32768;
            const unsigned L_U = 114688;
            {
                const int k0 = qt < 2 ? wv * 6 : wv * 4;
#pragma unroll
                for (int i = 0; i < 6; ++i) {
                    if (i < 4 || qt < 2) dma16<NTM & 2>(vb ^ (((k0 + i) & 3) << 4), wuk + (size_t)(k0 + i) * 32768, L_U + (k0 + i) * 1024);
                    if (i == 4) VMW(4);
                }
            }
            const unsigned vq = vb ^ ((wv & 3) << 4);
            auto wq_dma = [&](int ps) { dma16<NTM & 2>(vq, wq + (size_t)((ps + rot) & 15) * 262144, ps * 8192 + wv * 1024); };
            bf16x8 uk[4][4], aq[16];
            u32x4 fi, hv[4];
            unsigned fio = (unsigned)(WS_FI + x * STR + (lane & 7) * 16), hsrc, hsrc2, ta0, ta1, tb0, tb1, qo2, hw0, hw1, hr, hr2, qw, thx = 0u;
            const unsigned L_HI = L_SCR, L_QI = L_SCR + 8192;
            auto h_load = [&]() {
                asm volatile("buffer_load_dwordx4 %0, %4, %6, 0 offen sc1\n\tbuffer_load_dwordx4 %1, %4, %6, 0 offen offset:1024 sc1\n\t"
                             "buffer_load_dwordx4 %2, %5, %6, 0 offen sc1\n\tbuffer_load_dwordx4 %3, %5, %6, 0 offen offset:1024 sc1"
                             : "+v"(hv[0]), "+v"(hv[1]), "+v"(hv[2]), "+v"(hv[3]) : "v"(hsrc), "v"(hsrc2), "s"(rw) : "memory");
            };
            auto h_bad = [&]() {
                PIN4(hv[0], hv[1], hv[2], hv[3]);
                unsigned bad = (hv[0][1] ^ tlo) | (hv[0][3] ^ thx) | (hv[1][1] ^ tlo) | (hv[1][3] ^ thx) | (hv[2][1] ^ tlo) | (hv[2][3] ^ thx) | (hv[3][1] ^ tlo) | (hv[3][3] ^ thx);
                PIN(bad);
                return bad;
            };
#pragma unroll
            for (int ps = 0; ps < 16; ++ps) {
                wq_dma(ps);
                if (ps == 4) cnt_load();
                if (ps == HP) { h_load(); asm volatile("buffer_load_dwordx4 %0, %1, %2, 0 offen sc1" : "=v"(fi) : "v"(fio), "s"(rw) : "memory"); }
                if (ps >= 4 && ps < 8) VMW(5); else if (ps >= HP && ps < HP + 4) VMW(9); else VMW(4);
                if (ps == 3) {
                    asm volatile("s_barrier" ::: "memory");
                    if (pub) {
#pragma unroll
                        for (int t = 0; t < 4; ++t)
#pragma unroll
                            for (int s = 0; s < 4; ++s) uk[t][s] = *LP(bf16x8, L_U + off_b(wv * 64 + (nl >> 2) * 16 + t * 4 + (nl & 3), s * 4 + g));
                    }
                    STAMP(1);
                }
                if (ps == 5) {
                    hsrc = (unsigned)(WS_H + x * STR + wv * 2048 + lane * 16); hsrc2 = hsrc + 16384;
                    ta0 = off_b(8 * g + (nl >> 2), 2 * wv + ((lane & 3) >> 1)) + 8 * (lane & 1); ta1 = off_b(8 * g + 4 + (nl >> 2), 2 * wv + ((lane & 3) >> 1)) + 8 * (lane & 1);
                    tb0 = ta0 + 65536; tb1 = ta1 + 65536; qo2 = qo;
                    hw0 = L_HI + ((2 * wv - rot) & 15) * 512 + (lane >> 4) * 128 + (lane & 7) * 16 + ((lane >> 3) & 1) * 8;
                    hw1 = L_HI + ((2 * wv + 1 - rot) & 15) * 512 + (lane >> 4) * 128 + (lane & 7) * 16 + ((lane >> 3) & 1) * 8;
                    hr2 = L_HI + g * 128 + (nl & 7) * 16 + (nl >> 3) * 512;
                    PIN(hr2);
                    hr = L_HI + g * 128 + (nl & 7) * 16; qw = L_QI + (wv >> 1) * 512 + ((wv & 1) * 2 + (g >> 1)) * 128 + (nl & 7) * 16 + (g & 1) * 8;
                    PIN4(hsrc, hsrc2, ta0, ta1); PIN4(tb0, tb1, qo2, hw0); PIN(hw1); PIN(hr); PIN(qw);
                }
                if (ps == 8) { tag_take(); thx = tlo ^ thi ^ TAGC; }
                if (ps == 9) {
#pragma unroll
                    for (int t = 0; t < 4; ++t) PIN4(uk[t][0], uk[t][1], uk[t][2], uk[t][3]);
                    wg_barrier();
#pragma unroll
                    for (int q = 0; q < 6; ++q) aq[q] = tr_frag(ta0 + q * 8192, ta1 + q * 8192);
                }
            }
            STAMP(15);
            if (HP + 4 > 15) VMW(HP + 4 > 15 ? 15 - HP : 0);
            if (!__all(h_bad() == 0u)) {
                for (unsigned spins = 0;;) {
                    h_load();
                    VMW(0);
                    if (__all(h_bad() == 0u)) break;
                    __builtin_amdgcn_s_sleep(SLPH);
                    if (++spins > kSpinCap) break;
                }
            }
            {
                unsigned hh[4];
#pragma unroll
                for (int q = 0; q < 4; ++q) {
                    const unsigned t = hv[2 + (q >> 1)][(q & 1) * 2], sh = hv[q >> 1][(q & 1) * 2];
                    hh[q] = pk2(lo16(t) + lo16(sh), hi16(t) + hi16(sh));
                }
                *LP(u32x2, hw0) = u32x2{hh[0], hh[1]};
                *LP(u32x2, hw1) = u32x2{hh[2], hh[3]};
            }
            STAMP(3);
            wg_barrier();
            {
                bf16x8 hb[8];
#pragma unroll
                for (int q = 0; q < 3; ++q) hb[q] = *LP(bf16x8, hr2 + q * 1024);
#pragma unroll
                for (int ps = 6; ps < 12; ++ps) {
                    if (!(ps & 1)) hb[ps >> 1] = *LP(bf16x8, hr2 + (ps >> 1) * 1024);
                    aq[ps] = ps < 8 ? tr_frag(ta0 + ps * 8192, ta1 + ps * 8192) : tr_frag(tb0 + (ps - 8) * 8192, tb1 + (ps - 8) * 8192);
                }
#pragma unroll
                for (int q = 6; q < 8; ++q) hb[q] = *LP(bf16x8, hr2 + q * 1024);
                __builtin_amdgcn_sched_barrier(0);
                asm volatile("s_waitcnt vmcnt(0)\n\ts_barrier" : "+v"(fi) :: "memory");
                STAMP(2);
#pragma unroll
                for (int ps = 12; ps < 16; ++ps) aq[ps] = tr_frag(tb0 + (ps - 8) * 8192, tb1 + (ps - 8) * 8192);
                __builtin_amdgcn_sched_barrier(0);
                f32x4 d0 = {0.f, 0.f, 0.f, 0.f}, d1 = {0.f, 0.f, 0.f, 0.f};
#pragma unroll
                for (int ps = 0; ps < 16; ps += 2) {
                    d0 = MFMA16(aq[ps], hb[ps >> 1], d0); d1 = MFMA16(aq[ps + 1], hb[ps >> 1], d1);
                    if (ps == 6) { unsigned bad = (fi[0] ^ tlo) | (fi[1] ^ thi) | (fi[2] ^ tlo) | (fi[3] ^ thi); PIN(bad); if (!__all(bad == 0u)) flags_wait(fio); }
                }
#pragma unroll
                for (int e = 0; e < 4; ++e) { float v = d0[e] + dppf<0x128>(d1[e]); asm("" : "+v"(v)); d0[e] = v; }
                if (nl < 8) *LP(u32x2, qw) = u32x2{pk2(d0[0], d0[1]), pk2(d0[2], d0[3])};
            }
            wg_barrier();
            if (pub) {
                bf16x8 qb[4];
#pragma unroll
                for (int s = 0; s < 4; ++s) qb[s] = *LP(bf16x8, hr + 8192 + s * 512);
                __builtin_amdgcn_sched_barrier(0);
                f32x4 d[4];
#pragma unroll
                for (int t = 0; t < 4; ++t) d[t] = MFMA16(uk[t][0], qb[0], (f32x4{0.f, 0.f, 0.f, 0.f}));
#pragma unroll
                for (int s = 1; s < 4; ++s)
#pragma unroll
                    for (int t = 0; t < 4; ++t) d[t] = MFMA16(uk[t][s], qb[s], d[t]);
                const f32x4 a = nl & 8 ? d[2] : d[0], b = nl & 8 ? d[3] : d[1];
                u32x4 qv = {pk2(a[0], a[1]), pk2(a[2], a[3]), pk2(b[0], b[1]), pk2(b[2], b[3])};
                if (__builtin_expect(mx4(qv, 0u) == SENT, 0)) qv = u32x4{pk2s(a[0], a[1]), pk2s(a[2], a[3]), pk2s(b[0], b[1]), pk2s(b[2], b[3])};
                ST16(qv, qo2, 0);
            }
            STAMP(4);
            wg_barrier();
            if (LMPF && wv >= 3 && wv < 6) {
                const unsigned n = (unsigned)(j * 3 + wv - 3);
                if (n < 32) pf4<0>(((n & 7u) * 64 + lane) * 6144u + 2048u + (x * 4 + (n >> 3)) * 128u, p.w_mod, L_SCR + 31744);
            }
            if (QHELP && wv < 3 && j + 12 * wv < 32) {
                const int hh = j + 12 * wv;
#pragma unroll 1
                for (unsigned spins = 0; spins < 64; ++spins) {
                    __builtin_amdgcn_s_sleep(QHELP);
                    const u32x4 v = LD16((unsigned)(WS_Q + (hh & 7) * STR + (x * 4 + (hh >> 3)) * 1024 + lane * 16), 0);
                    if (__all(mx4(v, 0u) != SENT)) break;
                    asm volatile("" ::: "memory");
                }
                STAMP(6);
            }
        } else {
            const int cg = x * 2 + (j & 1);
            if (j < 14) {
                dma_piece(p.w_mod + (size_t)wv * 64 * 3072 + cg * 64, 6144, rgn, 8, NTM & 2);
                cnt_load();
                VMW(9);
            } else {
                cnt_load();
                ST16(s4, (unsigned)(WS_Q + wv * STR + cg * 2048 + lane * 16), 0); ST16(s4, (unsigned)(WS_Q + wv * STR + cg * 2048 + lane * 16), 1024);
                VMW(0);
                tag_take();
                wg_barrier();
                if (wv == 7) flag_put(WS_FI, cg);
                STAMP(15);
            }
            const u16x8 cf = *LP(u16x8, rgn + 8192 + lane * 16), xa = *LP(u16x8, rgn + 9216 + lane * 16);
            u32x4 af[2];
#pragma unroll
            for (int q = 0; q < 4; ++q) {
                const float a = bf2f(cf[2 * q]), b = bf2f(cf[2 * q + 1]);
                af[0][q] = pk2(a * __builtin_amdgcn_rcpf(1.f + __expf(-a)), b * __builtin_amdgcn_rcpf(1.f + __expf(-b)));
                af[1][q] = (unsigned)__builtin_amdgcn_update_dpp(0, (int)af[0][q], 0x128, 0xf, 0xf, true);
            }
            const unsigned L_MS = L_SCR;
            unsigned wa[4], fr[4];
#pragma unroll
            for (int t = 0; t < 4; ++t) { wa[t] = L_MS + wv * 2048 + (nl & 7) * 256 + (((t * 4 + g) ^ (nl & 7)) << 4) + (nl >> 3) * 16384; fr[t] = rgn + frl + ((t ^ frs) << 5); }
            PIN4(wa[0], wa[1], wa[2], wa[3]); PIN4(fr[0], fr[1], fr[2], fr[3]); PIN(af[0]); PIN(af[1]);
            const unsigned L_XN = 12288;
            auto mod_mm = [&](bool ln) {
                VMW(0);
                bf16x8 f[2][4];
#pragma unroll
                for (int ks = 0; ks < 2; ++ks)
#pragma unroll
                    for (int t = 0; t < 4; ++t) f[ks][t] = tr_frag(fr[t] + ks * 4096, fr[t] + ks * 4096 + 512);
                __builtin_amdgcn_sched_barrier(0);
                f32x4 d[4];
#pragma unroll
                for (int t = 0; t < 4; ++t) d[t] = MFMA16(f[0][t], BF8(af[0]), (f32x4{0.f, 0.f, 0.f, 0.f}));
#pragma unroll
                for (int t = 0; t < 4; ++t) d[t] = MFMA16(f[1][t], BF8(af[1]), d[t]);
#pragma unroll
                for (int t = 0; t < 4; ++t) *LP(f32x4, wa[t]) = d[t];
                if (ln) {
                    float v[8];
#pragma unroll
                    for (int q = 0; q < 8; ++q) v[q] = bf2f(xa[q]);
                    const float p1 = ((v[0] + v[1]) + (v[2] + v[3])) + ((v[4] + v[5]) + (v[6] + v[7]));
                    const float p2 = ((v[0] * v[0] + v[1] * v[1]) + (v[2] * v[2] + v[3] * v[3])) + ((v[4] * v[4] + v[5] * v[5]) + (v[6] * v[6] + v[7] * v[7]));
                    float mb, rden;
                    ln_stats(p1, p2, mb, rden);
                    u32x4 r;
#pragma unroll
                    for (int q = 0; q < 4; ++q) { const unsigned d = pk2(v[2 * q] - mb, v[2 * q + 1] - mb); r[q] = pk2(lo16(d) * rden, hi16(d) * rden); }
                    *LP(u32x4, L_XN + wv * 16384 + lane * 16) = r;
                }
                wg_barrier();
            };
            auto sum8 = [&](unsigned a) {
                f32x4 u[8];
#pragma unroll
                for (int w2 = 0; w2 < 8; ++w2) u[w2] = *LP(f32x4, a + w2 * 2048);
                __builtin_amdgcn_sched_barrier(0);
                return ((u[0] + u[1]) + (u[2] + u[3])) + ((u[4] + u[5]) + (u[6] + u[7]));
            };
            if (j < 14) {
                const int hb = lane & 7, c4 = 2 * wv + (nl >> 3);
                unsigned hoff = (unsigned)(WS_H + g * STR + cg * 2048 + wv * 256 + nl * 16);
                unsigned sa = L_MS + hb * 256 + ((c4 ^ hb) << 4);
                unsigned xa2 = L_XN + hb * 16384 + (cg & 7) * 128 + c4 * 8;
                PIN(hoff); PIN(sa); PIN(xa2);
                STAMP(1);
                mod_mm(cg >= 8);
                STAMP(2);
                const u32x2 xn = *LP(u32x2, xa2);
                const f32x4 v = sum8(sa);
                tag_take();
                const unsigned thx = tlo ^ thi ^ TAGC;
                u32x2 r = {pk2(v[0], v[1]), pk2(v[2], v[3])};
                if (cg >= 8) {
#pragma unroll
                    for (int q = 0; q < 2; ++q) { const unsigned sc = pk2(1.f + lo16(r[q]), 1.f + hi16(r[q])); r[q] = pk2(lo16(xn[q]) * lo16(sc), hi16(xn[q]) * hi16(sc)); }
                }
                ST16((u32x4{r[0], tlo, r[1], thx}), hoff, 0);
                ST16((u32x4{r[0], tlo, r[1], thx}), hoff, 4 * STR);
                STAMP(5);
            }
            wg_barrier();
            while (__builtin_amdgcn_s_memrealtime() < t_start + LMD) __builtin_amdgcn_s_sleep(2);
            const int lg = x * 4 + (j & 3);
            dma_piece(p.w_mod + (size_t)wv * 64 * 3072 + 1024 + lg * 64, 6144, rgn, 8, NTM & 4);
            mod_mm(false);
            if (wv < 4) {
                const int b = wv * 2 + (lane >> 5), cp = lane & 31;
                f32x2_t u[8];
#pragma unroll
                for (int w2 = 0; w2 < 8; ++w2) u[w2] = *LP(f32x2_t, L_MS + w2 * 2048 + b * 256 + (((cp >> 1) ^ b) << 4) + (cp & 1) * 8);
                const f32x2_t v = ((u[0] + u[1]) + (u[2] + u[3])) + ((u[4] + u[5]) + (u[6] + u[7]));
                ST16((u32x4{__float_as_uint(rbf(v[0])), __float_as_uint(rbf(v[1])), tlo, thi}), (unsigned)(WS_MOD + b * STR + (lg * 32 + cp) * 16), 0);
            }
            STAMP(6);
        }
        const int bq = (j >> 2) & 1, dh = j >> 3, src = th * 2 + dh, rb = bq * 4 + (src >> 4), rc = src & 15;
        unsigned oo = (unsigned)(WS_OUTP + (bq * 4 + (nl & 3)) * STR + (wv * 2 + (nl >> 3)) * 8192 + (src ^ wv) * 128 + ((nl >> 2) & 1) * 64 + g * 16);
        const int rcol = rc * 32 + (lane & 7) * 4 + (lane >> 5) * 2;
        unsigned xdst = (unsigned)(WS_X1 + ((lane >> 3) & 3) * STR + (rb * 512 + rcol) * 2);
        wg_barrier();
        while (__builtin_amdgcn_s_memrealtime() < t_start + TWD) __builtin_amdgcn_s_sleep(2);
        STAMP(7);
        if ((PFM & 8) && wv == 0 && lane < 32) pf4<NTM & 8>((unsigned)((j * 32 + lane) * 8192 + x * 1024 + ((3u - ((unsigned)(size_t)p.w_uv >> 7)) & 7u) * 128), p.w_uv, L_SCR + 31744);
        ST16(s4, oo, 0);
        if (wv == 7) { __builtin_amdgcn_raw_buffer_store_b32(SENT, rw, xdst, 0, 16); __builtin_amdgcn_raw_buffer_store_b32(SENT, rw, xdst, 4 * STR, 16); }
        if (bq) dma_piece(p.w_o + (size_t)(th * 128 + dh * 64) * 512 + wv * 64, 1024, rgn + 8192, 8, NTM & 8);
        dma_piece(p.w_uv + ((size_t)wv * 64 * NH + th) * 128 + dh * 64, 8192, rgn, 8, NTM & 8);
        if (!bq) dma_piece(p.w_o + (size_t)(th * 128 + dh * 64) * 512 + wv * 64, 1024, rgn + 8192, 8, NTM & 8);
        VMW(16);
        asm volatile("s_barrier" ::: "memory");
        if (wv == 0) flag_put(WS_FB, id);
        u32x4 fb, gm;
        unsigned xg;
        VMW(4);
        if (wv == 7)
            asm volatile("buffer_load_dwordx4 %0, %3, %5, 0 offen sc1\n\tbuffer_load_dwordx4 %1, %4, %5, 0 offen sc1\n\tglobal_load_dword %2, %6, %7" : "=&v"(fb), "=&v"(gm), "=&v"(xg)
                         : "v"((unsigned)(WS_FB + x * STR + lane * 16)), "v"((unsigned)(WS_MOD + rb * STR + rcol * 8)), "s"(rw), "v"((unsigned)(rb * 512 + rcol) * 2u), "s"(p.x) : "memory");
        VMW(0);
        asm volatile("" : "+v"(fb), "+v"(gm), "+v"(xg));
        STAMP(8);
        bf16x8 fuv[2][4], fwo[2][4];
        unsigned pfa[4];
#pragma unroll
        for (int t = 0; t < 4; ++t) { pfa[t] = rgn + frl + ((t ^ frs) << 5); PIN(pfa[t]); }
#pragma unroll
        for (int ks = 0; ks < 2; ++ks)
#pragma unroll
            for (int t = 0; t < 4; ++t) {
                const unsigned a = rgn + (unsigned)((g * 4 + (nl >> 2)) * 128 + (lane & 3) * 8) + ((t ^ (unsigned)((nl >> 3) | (g & 2))) << 5) + ks * 4096;
                fuv[ks][t] = tr_frag(a, a + 2048);
                fwo[ks][t] = tr_frag(pfa[t] + 8192 + ks * 4096, pfa[t] + 8192 + ks * 4096 + 512);
            }
#pragma unroll
        for (int ks = 0; ks < 2; ++ks) { PIN4(fuv[ks][0], fuv[ks][1], fuv[ks][2], fuv[ks][3]); PIN4(fwo[ks][0], fwo[ks][1], fwo[ks][2], fwo[ks][3]); }
        const unsigned L_WGT = L_SCR, L_MSK = L_SCR + 256, L_RED = L_SCR + 1024;
        unsigned psrc = (unsigned)(WS_PART + ((bq * 4 * 32 + th) * 8 + wv) * 2048 + ((lane >> 3) ^ (th & 7)) * 128) + pcl, psrc1 = (psrc ^ 64u) + 1024;
        unsigned rw2 = L_RED + wv * 1024 + (nl & 3) * 256 + g * 16;
        unsigned wa0 = (nl & 3) == (g >> 1) ? L_WGT + (nl & 4) * 32 + g * 16 : L_WGT + 272, wa1 = (nl & 3) == 2 + (g >> 1) ? L_WGT + (nl & 4) * 32 + 64 + g * 16 : L_WGT + 272;
        unsigned cr = rgn + (nl & 3) * 128 + g * 16, r2r = L_RED + lane * 16, cw = rgn + lane * 8;
        unsigned osrc = (unsigned)(WS_OUTP + rb * STR + rc * 8192 + lane * 16);
        PIN(psrc); PIN(psrc1); PIN(rw2); PIN4(wa0, wa1, cr, r2r); PIN4(cw, oo, osrc, xdst);
        float mlm = 0.f, mll = 0.f;
        if (wv == 0) *LP(unsigned, L_WGT + 256 + lane * 4) = 0u;
        wg_barrier();
        const bool early = __all(flags_ok(fb) & (gm[2] == tlo) & (gm[3] == thi));
        u32x2 ga = {gm[0], gm[1]};
        PIN(ga);
        bf16x8 pa[2][4], wh[2];
        {
            u32x4 ml = {0u, 0u, ~tlo, ~thi};
            const unsigned mlsrc = (unsigned)(WS_ML + (bq * 4 + g) * STR + nl * 512 + th * 16);
            unsigned long long done = 0ull;
            unsigned lm = L_MSK;
            PIN(lm);
#pragma unroll 1
            for (unsigned spins = 0; spins < kSpinCap; ++spins) {
                unsigned long long m;
                if (wv == 0) {
                    if ((ml[2] != tlo) | (ml[3] != thi)) ml = LD16(mlsrc, 0);
                    m = __ballot((ml[2] == tlo) & (ml[3] == thi));
                    asm volatile("ds_write_b64 %0, %1" :: "v"(lm), "v"(m) : "memory");
                } else {
                    unsigned long long t;
                    asm volatile("ds_read_b64 %0, %1\n\ts_waitcnt lgkmcnt(0)" : "=v"(t) : "v"(lm) : "memory");
                    m = ((unsigned long long)(unsigned)__builtin_amdgcn_readfirstlane((unsigned)(t >> 32)) << 32) | (unsigned)__builtin_amdgcn_readfirstlane((unsigned)t);
                }
                if (wv == 0 && m != ~0ull) m = 0ull;
                m &= m >> 4; m &= m >> 2; m &= m >> 1;
                m &= 0x0101010101010101ull & ~done;
                if (m) {
                    done |= m;
#define PLD(bit, off, v, base) "s_bitcmp1_b64 %0, " #bit "\n\ts_cbranch_scc0 1f\n\ts_add_u32 m0, %1, " #off "\n\ts_nop 0\n\tglobal_load_lds_dwordx4 " v ", " base " sc1\n1:\n\t"
                    asm volatile(PLD(0, 0, "%2", "%4") PLD(8, 1024, "%3", "%4") PLD(16, 2048, "%2", "%5") PLD(24, 3072, "%3", "%5")
                                 PLD(32, 4096, "%2", "%6") PLD(40, 5120, "%3", "%6") PLD(48, 6144, "%2", "%7") PLD(56, 7168, "%3", "%7")
                                 :: "s"(m), "s"(rgn), "v"(psrc), "v"(psrc1), "s"(ws), "s"(ws + 524288), "s"(ws + 1048576), "s"(ws + 1572864) : "memory", "m0", "scc");
                    if (done == 0x0101010101010101ull) break;
                } else if (wv == 0) __builtin_amdgcn_s_sleep(SLPM);
                else __builtin_amdgcn_s_sleep(1);
            }
            mlm = __uint_as_float(ml[0]); mll = __uint_as_float(ml[1]);
            STAMP(10);
            if (wv == 0) {
                float M = mlm;
                M = fmaxf(M, dppf<0xB1>(M)); M = fmaxf(M, dppf<0x4E>(M)); M = fmaxf(M, dppf<0x141>(M)); M = fmaxf(M, dppf<0x140>(M));
                const float e = __builtin_amdgcn_exp2f(mlm - M);
                const float w = e * __builtin_amdgcn_rcpf(sum16(e * mll));
                const unsigned short hi = f2bf(w);
                *LP(unsigned short, L_WGT + lane * 2) = hi;
                *LP(unsigned short, L_WGT + 128 + lane * 2) = f2bf(w - bf2f(hi));
            }
            wg_barrier();
            wh[0] = *LP(bf16x8, wa0); wh[1] = *LP(bf16x8, wa1);
            __builtin_amdgcn_sched_barrier(0);
            VMW(0);
#pragma unroll
            for (int ks = 0; ks < 2; ++ks)
#pragma unroll
                for (int t = 0; t < 4; ++t) pa[ks][t] = tr_frag(pfa[t] + ks * 4096, pfa[t] + ks * 4096 + 512);
        }
        STAMP(11);
        {
            f32x4 d[4];
#pragma unroll
            for (int t = 0; t < 4; ++t) d[t] = MFMA16(pa[0][t], wh[0], (f32x4{0.f, 0.f, 0.f, 0.f}));
#pragma unroll
            for (int t = 0; t < 4; ++t) d[t] = MFMA16(pa[1][t], wh[1], d[t]);
#pragma unroll
            for (int t = 0; t < 4; ++t)
#pragma unroll
                for (int e = 0; e < 4; ++e) { float v = d[t][e] + dppf<0x124>(d[t][e]); asm("" : "+v"(v)); d[t][e] = v; }
            const u32x4 o0 = {pk2(d[0][0], d[0][1]), pk2(d[0][2], d[0][3]), pk2(d[1][0], d[1][1]), pk2(d[1][2], d[1][3])};
            const u32x4 o1 = {pk2(d[2][0], d[2][1]), pk2(d[2][2], d[2][3]), pk2(d[3][0], d[3][1]), pk2(d[3][2], d[3][3])};
            f32x4 y[4];
#pragma unroll
            for (int t = 0; t < 4; ++t) y[t] = MFMA16(fuv[0][t], BF8(o0), (f32x4{0.f, 0.f, 0.f, 0.f}));
#pragma unroll
            for (int t = 0; t < 4; ++t) y[t] = MFMA16(fuv[1][t], BF8(o1), y[t]);
            if (nl < 4) {
#pragma unroll
                for (int t = 0; t < 4; ++t) *LP(f32x4, rw2 + t * 64) = y[t];
            }
        }
        wg_barrier();
        {
            f32x4 cs[8];
#pragma unroll
            for (int w2 = 0; w2 < 8; ++w2) cs[w2] = *LP(f32x4, r2r + w2 * 1024);
            const f32x4 c4 = ((cs[0] + cs[1]) + (cs[2] + cs[3])) + ((cs[4] + cs[5]) + (cs[6] + cs[7]));
            *LP(u32x2, cw) = u32x2{pk2(c4[0], c4[1]), pk2(c4[2], c4[3])};
            asm volatile("s_waitcnt lgkmcnt(0)" ::: "memory");
            const u32x4 cx0 = *LP(u32x4, cr), cx1 = *LP(u32x4, cr + 64);
            f32x4 o[4];
#pragma unroll
            for (int q = 0; q < 4; ++q) o[q] = MFMA16(fwo[0][q], BF8(cx0), (f32x4{0.f, 0.f, 0.f, 0.f}));
#pragma unroll
            for (int q = 0; q < 4; ++q) o[q] = MFMA16(fwo[1][q], BF8(cx1), o[q]);
            const f32x4 sel = (nl & 8) ? ((nl & 4) ? o[3] : o[2]) : ((nl & 4) ? o[1] : o[0]);
            u32x4 sv = __builtin_bit_cast(u32x4, sel);
            if (__builtin_expect(mx4(sv, 0u) == SENT, 0)) sv = u32x4{f32s(sel[0]), f32s(sel[1]), f32s(sel[2]), f32s(sel[3])};
            ST16(sv, oo, 0);
        }
        STAMP(12);
        wg_barrier();
        if (wv == 7) {
            if (!early) {
                flags_wait(WS_FB + x * STR + lane * 16);
                ga = mod_get(WS_MOD + rb * STR + rcol * 8);
                PIN(ga);
            }
            u32x4 op[8];
            for (unsigned spins = 0;;) {
#pragma unroll
                for (int k = 0; k < 8; ++k) op[k] = LD16(osrc + (k & 3) * 1024, (k >> 2) * 4096);
                unsigned m = 0u;
#pragma unroll
                for (int k = 0; k < 8; ++k) m = mx4(op[k], m);
                if (__all(m != SENT)) break;
                if (++spins > kSpinCap) break;
                asm volatile("" ::: "memory");
            }
            STAMP(13);
            const f32x4 v = ((__builtin_bit_cast(f32x4, op[0]) + __builtin_bit_cast(f32x4, op[1])) + (__builtin_bit_cast(f32x4, op[2]) + __builtin_bit_cast(f32x4, op[3]))) +
                            ((__builtin_bit_cast(f32x4, op[4]) + __builtin_bit_cast(f32x4, op[5])) + (__builtin_bit_cast(f32x4, op[6]) + __builtin_bit_cast(f32x4, op[7])));
            float w[2];
#pragma unroll
            for (int q = 0; q < 2; ++q) {
                auto t = __builtin_amdgcn_permlane32_swap(__float_as_uint(v[q]), __float_as_uint(v[2 + q]), false, false);
                w[q] = __uint_as_float(t[0]) + __uint_as_float(t[1]);
                auto u = __builtin_amdgcn_permlane16_swap(__float_as_uint(w[q]), __float_as_uint(w[q]), false, false);
                w[q] = __uint_as_float(u[0]) + __uint_as_float(u[1]);
                w[q] += dppf<0x128>(w[q]);
            }
            unsigned t = pk2(w[0], w[1]);
            t = pk2(__uint_as_float(ga[0]) * lo16(t), __uint_as_float(ga[1]) * hi16(t));
            t = pk2s(lo16(xg) + lo16(t), hi16(xg) + hi16(t));
            __builtin_amdgcn_raw_buffer_store_b32(t, rw, xdst, 0, 16);
            __builtin_amdgcn_raw_buffer_store_b32(t, rw, xdst, 4 * STR, 16);
            STAMP(14);
        }
        STAMP_FLUSH();
        return;
    }

    const int s = j - 16;
    if (wv == 0) cnt_load();
    {
        const unsigned ln = (unsigned)(s * 64 + lane);
        if ((PFM & 1) && wv == 0) pf4<NTM & 1>((ln & 511u) * 6144u + (x * 2 + (ln >> 9)) * 128u, p.w_mod, L_SCR);
        if ((PFM & 2) && wv == 1) {
            const unsigned short* w = s < 8 ? p.w_uk : p.w_q;
            pf4<NTM & 1>((ln & 511u) * 8192u + x * 1024u + ((3u - ((unsigned)(size_t)w >> 7)) & 7u) * 128u, w, L_SCR);
        }
        if ((PFM & 48) == 48) {
            const unsigned r0 = (unsigned)(s * 4 + (wv & 3)), row = PFS == 0 ? r0 * 8 + (lane >> 3) : PFS == 1 ? (lane >> 3) * 64 + r0 : (r0 >> 5) * 256 + (lane >> 3) * 32 + (r0 & 31);
            pf4<NTM & 1>(row * 8192u + x * 1024u + (lane & 7u) * 128u, wv < 4 ? p.w_uk : p.w_q, L_SCR);
        }
        else if ((PFM & 16) && wv >= 4) { const unsigned l2 = (unsigned)((s * 4 + (wv & 3)) * 64 + lane); pf4<NTM & 1>((l2 >> 3) * 8192u + x * 1024u + (l2 & 7u) * 128u, p.w_q, L_SCR); }
        else if ((PFM & 32) && wv >= 4) { const unsigned l2 = (unsigned)((s * 4 + (wv & 3)) * 64 + lane); pf4<NTM & 1>((l2 >> 3) * 8192u + x * 1024u + (l2 & 7u) * 128u, p.w_uk, L_SCR); }
        if ((PFM & 4) && (wv & 6) == 2) { const unsigned l2 = (unsigned)((s * 2 + (wv & 1)) * 64 + lane); pf4<NTM & 1>((l2 & 511u) * 8192u + (x * 4 + (l2 >> 9)) * 256u, p.w_q, L_SCR); }
    }
    const int ug = x * 7 + s;
    const int wp = wv & 3, ch = wv >> 2, kb = wp * 2 + ch;
    int head = lane & 31;
    unsigned h2 = lane >> 5;
    const unsigned vb = (unsigned)(g * 1024 + ((nl ^ (g << 2)) << 4)), lb = (unsigned)((wv >> 1) * 32768 + (wv & 1) * 4096);
    const char* ck = (const char*)p.ckv + ((size_t)x * SK + s * 256 + wv * 16) * 1024;
    const unsigned rot = ROTC ? (unsigned)s & 3u : 0u;
    const unsigned vbr = vb ^ (rot << 8);
    const char* ck2 = ck + 131072;
    auto ck_dma = [&](unsigned i) { dma16x<NTM & 16>(vbr, ((i & 3u) << 4) | (((i >> 2) & 3u) << 8) | ((i & 3u) << 12), (i >> 4) ? ck2 : ck, lb + ((i >> 2) & 3u) * 8192u + (i & 3u) * 1024u); };
    bf16x8 ka[16];
    s16x4 va[8][4];
    unsigned kad[4], vad[4], vad2[4];
    auto ka_reads = [&](int cb) {
#pragma unroll
        for (int u = 0; u < 4; ++u) ka[cb * 4 + u] = *LP(bf16x8, kad[u] + cb * 8192);
    };
    auto va_reads = [&]() {
#pragma unroll
        for (int kk = 0; kk < 8; ++kk)
#pragma unroll
            for (int q = 0; q < 4; ++q) va[kk][q] = tr4((kk < 4 ? vad[q] : vad2[q]) + ((kk >> 1) & 1) * 32768 + (kk & 1) * 4096);
    };
    auto va_pin = [&]() {
#pragma unroll
        for (int q = 0; q < 8; ++q) PIN4(va[q][0], va[q][1], va[q][2], va[q][3]);
    };
    u32x4 fa = {0u, 0u, 0u, 0u};
    const unsigned fao = (unsigned)(WS_FI + x * STR + (lane & 7) * 16);
    constexpr int LAST = GI + 4 > 19 ? GI + 4 : 19;
    const unsigned qsrc = (unsigned)(WS_Q + (wv & 1) * 4 * STR + (x * 4 + (wv >> 1)) * 1024) + (g ^ rot) * 256 + ((nl ^ ((wv * 4) & 15)) << 4);
    auto q_dma = [&](int k) {
        unsigned t;
        asm volatile("s_mov_b32 m0, %3\n\tv_xor_b32 %0, %4, %1\n\tglobal_load_lds_dwordx4 %0, %2 sc1" : "=&v"(t) : "v"(qsrc), "s"(ws), "s"(L_SCR + (wv * 4 + k) * 1024), "i"(k * (STR + 16)) : "memory", "m0");
    };
#pragma unroll
    for (int i = 0; i <= LAST; ++i) {
        ck_dma(i);
        if (i == 3) {
#pragma unroll
            for (int u = 0; u < 4; ++u) {
                kad[u] = tile_off(wp * 32 + (head ^ (ch * 16)), ch * 8 + 2 * u + h2);
                vad[u] = tile_off(4 * h2 + (nl >> 2) + 8 * (u & 1), wv * 8 + 4 * (u >> 1) + 2 * (g & 1) + ((lane & 3) >> 1)) + 8 * (lane & 1);
                vad2[u] = vad[u] + 65536;
                PIN(kad[u]); PIN(vad[u]); PIN(vad2[u]);
            }
        }
        if (i > GI && i < GI + 4) VMW(8); else if (i < NTH) { if (i >= W0) VMW(W0); } else if (i >= 4) VMW(4);
        if (i == 15 && wv == 0) {
            PIN(fa);
            tag_take();
            if (!__all(flags_ok(fa))) flags_wait(fao);
        }
        if ((i & 3) == 3 && i >= 7 && i <= 19) {
            const int cb = (i - 7) >> 2;
            if (cb > 0) { PIN4(ka[cb * 4 - 4], ka[cb * 4 - 3], ka[cb * 4 - 2], ka[cb * 4 - 1]); va_pin(); }
            wg_barrier();
            ka_reads(cb);
            if ((wv >> 1) == cb) va_reads();
            STAMP(16 + cb);
        }
        if (i == 10 && wv == 0) asm volatile("buffer_load_dwordx4 %0, %1, %2, 0 offen sc1" : "+v"(fa) : "v"(fao), "s"(rw) : "memory");
        if (i == GI) { q_dma(0); q_dma(1); q_dma(2); q_dma(3); }
    }
    const unsigned L_X = L_SCR, L_P = L_SCR + 16384, L_EXM = L_SCR + 24576, L_EXS = L_SCR + 25600, L_BIG = L_SCR + 27648;
    unsigned nx = LAST + 1;
    unsigned qad[4];
    {
        unsigned qck = L_SCR + wv * 4096 + lane * 16;
        u32x4 qb[4];
#pragma unroll
        for (int k = 0; k < 4; ++k) qb[k] = *LP(u32x4, qck + k * 1024);
#pragma unroll
        for (int k = 0; k < 4; ++k) { qad[k] = L_SCR + head * 1024 + (((ch * 8 + 2 * k + h2) ^ (head & 15)) << 4); PIN(qad[k]); }
        STAMP(20);
        if (!__all(mx4(qb[0], mx4(qb[1], mx4(qb[2], mx4(qb[3], 0u)))) != SENT)) {
            for (unsigned spins = 0;;) {
#pragma unroll
                for (int k = 0; k < 4; ++k)
                    if (!__all(mx4(qb[k], 0u) != SENT)) q_dma(k);
                if (nx <= 25) {
#pragma unroll
                    for (int i = 0; i < 3; ++i) ck_dma(nx + i);
                    nx += 3;
                    VMW(3);
                } else {
                    for (; nx < 28; ++nx) ck_dma(nx);
                    VMW(0);
                }
#pragma unroll
                for (int k = 0; k < 4; ++k) qb[k] = *LP(u32x4, qck + k * 1024);
                if (__all(mx4(qb[0], mx4(qb[1], mx4(qb[2], mx4(qb[3], 0u)))) != SENT)) break;
                if (++spins > kSpinCap) break;
            }
        }
    }
    auto slot = [&](int k) { if (LAST + 1 + k < 32) { if (nx <= (unsigned)(LAST + 1 + k)) ck_dma(LAST + 1 + k); } };
    unsigned xw = L_X + wv * 2048 + lane * 16, xr = xw ^ 8192u, exw = L_EXM + (head * 8 + wv) * 4, exr = L_EXM + head * 32;
    unsigned pwr = L_P + kb * 1024 + lane * 16, prd = L_P + lane * 16, esw = L_EXS + (head * 16 + h2 * 8 + wv) * 4;
    PIN4(xw, xr, exw, exr); PIN(pwr); PIN(prd); PIN(esw);
    PIN4(ka[12], ka[13], ka[14], ka[15]); va_pin();
    wg_barrier();
    STAMP(21);
    bf16x8 qf[16];
#pragma unroll
    for (int ks = 0; ks < 16; ++ks) qf[ks] = *LP(bf16x8, qad[ks & 3] + (ks >> 2) * 256);
    __builtin_amdgcn_sched_barrier(0);
#pragma unroll
    for (int k = 0; k < SL0; ++k) slot(k);
    __builtin_amdgcn_sched_barrier(0);
    float lsum, mrun;
    f32x16 o[2];
    auto meet = [&](const f32x16& sc, float (&t)[8]) {
        *LP(f32x4, xw) = f32x4{sc[8], sc[9], sc[10], sc[11]}; *LP(f32x4, xw + 1024) = f32x4{sc[12], sc[13], sc[14], sc[15]};
        wg_barrier();
        const f32x4 x0 = *LP(f32x4, xr), x1 = *LP(f32x4, xr + 1024);
#pragma unroll
        for (int r = 0; r < 4; ++r) { t[r] = sc[r] + x0[r]; t[4 + r] = sc[4 + r] + x1[r]; }
    };
    {
        f32x16 sc;
#pragma unroll
        for (int i = 0; i < 16; ++i) sc[i] = 0.f;
#pragma unroll
        for (int ks = 0; ks < 16; ++ks) {
            sc = MFMA32(ka[ks], qf[ks], sc);
            if ((ks & 3) == 3) slot(SL0 + (ks >> 2));
            __builtin_amdgcn_sched_barrier(0);
        }
        wg_barrier();
        float t[8];
        meet(sc, t);
        {
            const float m = fmax3(fmax3(t[0], t[1], t[2]), fmax3(t[3], t[4], t[5]), fmax3(t[6], t[7], t[7]));
            auto u = __builtin_amdgcn_permlane32_swap(__float_as_uint(m), __float_as_uint(m), false, false);
            *LP(float, exw) = fmax3(__uint_as_float(u[0]), __uint_as_float(u[1]), __uint_as_float(u[1]));
        }
        slot(SL0 + 4);
        wg_barrier();
        const f32x4 a0 = *LP(f32x4, exr), a1 = *LP(f32x4, exr + 16);
        mrun = fmax3(fmax3(a0[0], a0[1], a0[2]), fmax3(a0[3], a1[0], a1[1]), fmax3(a1[2], a1[3], a1[3])) * SCL;
        float e[8];
#pragma unroll
        for (int r = 0; r < 8; ++r) e[r] = __builtin_amdgcn_exp2f(fmaf(t[r], SCL, -mrun));
        *LP(u32x4, pwr) = u32x4{pk2(e[0], e[1]), pk2(e[2], e[3]), pk2(e[4], e[5]), pk2(e[6], e[7])};
        lsum = ((e[0] + e[1]) + (e[2] + e[3])) + ((e[4] + e[5]) + (e[6] + e[7]));
        if (wv == 0) *LP(unsigned, L_BIG) = 0u;
        slot(SL0 + 5);
        wg_barrier();
        STAMP(22);
        bf16x8 pf[8];
#pragma unroll
        for (int kk = 0; kk < 8; ++kk) pf[kk] = *LP(bf16x8, prd + kk * 1024);
#pragma unroll
        for (int i = 0; i < 16; ++i) { o[0][i] = 0.f; o[1][i] = 0.f; }
        __builtin_amdgcn_sched_barrier(0);
#pragma unroll
        for (int kk = 0; kk < 8; ++kk) {
            o[0] = MFMA32(cat8(va[kk][0], va[kk][1]), pf[kk], o[0]);
            o[1] = MFMA32(cat8(va[kk][2], va[kk][3]), pf[kk], o[1]);
            if (kk < 6) slot(SL0 + 6 + kk);
            __builtin_amdgcn_sched_barrier(0);
        }
    }
    STAMP(23);
    rederive();
    head = lane & 31; h2 = lane >> 5;
    unsigned sw[4], srd = lb + (lane >> 3) * 128 + (((lane & 7) ^ (lane >> 3)) << 4);
#pragma unroll
    for (int q = 0; q < 4; ++q) sw[q] = lb + head * 128 + (((2 * q + h2) ^ (head & 7)) << 4);
    PIN4(sw[0], sw[1], sw[2], sw[3]);
    unsigned po = (unsigned)(WS_PART + ((x * 32 + (lane >> 3)) * 8 + (wv ^ (rot * 2))) * 2048 + (s ^ (lane >> 3)) * 128 + (lane & 7) * 16);
    unsigned mlo = (unsigned)(WS_ML + x * STR + s * 512 + head * 16), esr = L_EXS + head * 64;
    PIN4(esr, srd, po, mlo);
    {
        f32x16 sc;
#pragma unroll
        for (int i = 0; i < 16; ++i) sc[i] = 0.f;
        VMW(8);
        wg_barrier();
        ka_reads(0); ka_reads(1);
        if (wv < 4) va_reads();
        __builtin_amdgcn_sched_barrier(0);
#pragma unroll
        for (int ks = 0; ks < 8; ++ks) sc = MFMA32(ka[ks], qf[ks], sc);
        __builtin_amdgcn_sched_barrier(0);
        VMW(4);
        wg_barrier();
        ka_reads(2);
        if ((wv >> 1) == 2) va_reads();
        __builtin_amdgcn_sched_barrier(0);
#pragma unroll
        for (int ks = 8; ks < 12; ++ks) sc = MFMA32(ka[ks], qf[ks], sc);
        __builtin_amdgcn_sched_barrier(0);
        VMW(0);
        wg_barrier();
        STAMP(24);
        ka_reads(3);
        __builtin_amdgcn_sched_barrier(0);
#pragma unroll
        for (int ks = 12; ks < 16; ++ks) sc = MFMA32(ka[ks], qf[ks], sc);
        float t[8];
        meet(sc, t);
        float e[8];
        const float mloc = fmax3(fmax3(t[0], t[1], t[2]), fmax3(t[3], t[4], t[5]), fmax3(t[6], t[7], t[7]));
#pragma unroll
        for (int r = 0; r < 8; ++r) e[r] = __builtin_amdgcn_exp2f(fmaf(t[r], SCL, -mrun));
        *LP(u32x4, pwr) = u32x4{pk2(e[0], e[1]), pk2(e[2], e[3]), pk2(e[4], e[5]), pk2(e[6], e[7])};
        *LP(float, esw) = lsum + (((e[0] + e[1]) + (e[2] + e[3])) + ((e[4] + e[5]) + (e[6] + e[7])));
        if (__builtin_expect(!(fmaf(mloc, SCL, -mrun) <= BIGD), 0)) *LP(unsigned, L_BIG) = 1u;
        va_pin();
        wg_barrier();
        STAMP(25);
        unsigned big = *LP(unsigned, L_BIG);
        if ((wv >> 1) == 3) va_reads();
        bf16x8 pf[8];
#pragma unroll
        for (int kk = 0; kk < 8; ++kk) pf[kk] = *LP(bf16x8, prd + kk * 1024);
        __builtin_amdgcn_sched_barrier(0);
        if (__builtin_expect(__builtin_amdgcn_readfirstlane(big) != 0, 0)) {
            auto u = __builtin_amdgcn_permlane32_swap(__float_as_uint(mloc), __float_as_uint(mloc), false, false);
            *LP(float, exw) = fmaxf(__uint_as_float(u[0]), __uint_as_float(u[1]));
#pragma unroll
            for (int kk = 0; kk < 8; kk += 4) PIN4(pf[kk], pf[kk + 1], pf[kk + 2], pf[kk + 3]);
            wg_barrier();
            const f32x4 a0 = *LP(f32x4, exr), a1 = *LP(f32x4, exr + 16);
            const float mnew = fmaxf(fmaxf(fmaxf(fmaxf(a0[0], a0[1]), fmaxf(a0[2], a0[3])), fmaxf(fmaxf(a1[0], a1[1]), fmaxf(a1[2], a1[3]))) * SCL, mrun);
#pragma unroll
            for (int r = 0; r < 8; ++r) e[r] = __builtin_amdgcn_exp2f(fmaf(t[r], SCL, -mnew));
            *LP(u32x4, pwr) = u32x4{pk2(e[0], e[1]), pk2(e[2], e[3]), pk2(e[4], e[5]), pk2(e[6], e[7])};
            const float al = __builtin_amdgcn_exp2f(mrun - mnew);
            *LP(float, esw) = lsum * al + (((e[0] + e[1]) + (e[2] + e[3])) + ((e[4] + e[5]) + (e[6] + e[7])));
            mrun = mnew;
            o[0] *= al; o[1] *= al;
            wg_barrier();
#pragma unroll
            for (int kk = 0; kk < 8; ++kk) pf[kk] = *LP(bf16x8, prd + kk * 1024);
        }
        auto stage = [&](int t2, int gp) {
            const unsigned lo0 = pk2(o[t2][8 * gp + 0], o[t2][8 * gp + 1]), lo1 = pk2(o[t2][8 * gp + 2], o[t2][8 * gp + 3]);
            const unsigned hi0 = pk2(o[t2][8 * gp + 4], o[t2][8 * gp + 5]), hi1 = pk2(o[t2][8 * gp + 6], o[t2][8 * gp + 7]);
            auto r0 = __builtin_amdgcn_permlane32_swap(lo0, hi0, false, false);
            auto r1 = __builtin_amdgcn_permlane32_swap(lo1, hi1, false, false);
            *LP(u32x4, sw[t2 * 2 + gp]) = u32x4{r0[0], r1[0], r0[1], r1[1]};
        };
#pragma unroll
        for (int kk = 0; kk < 8; ++kk) o[0] = MFMA32(cat8(va[kk][0], va[kk][1]), pf[kk], o[0]);
        __builtin_amdgcn_sched_barrier(0);
#pragma unroll
        for (int kk = 0; kk < 8; ++kk) {
            o[1] = MFMA32(cat8(va[kk][2], va[kk][3]), pf[kk], o[1]);
            if (kk == 2) stage(0, 0);
            if (kk == 4) stage(0, 1);
            __builtin_amdgcn_sched_barrier(0);
        }
        stage(1, 0); stage(1, 1);
        asm volatile("s_waitcnt lgkmcnt(0)" ::: "memory");
        u32x4 w[4];
#pragma unroll
        for (int i = 0; i < 4; ++i) w[i] = *LP(u32x4, srd + i * 1024);
#pragma unroll
        for (int i = 0; i < 4; ++i) ST16(w[i], po, i * 131072);
        __builtin_amdgcn_sched_barrier(0);
        STAMP(26);
        u32x4 mlrec;
        {
            const f32x4 b0 = *LP(f32x4, esr), b1 = *LP(f32x4, esr + 16), b2 = *LP(f32x4, esr + 32), b3 = *LP(f32x4, esr + 48);
            const f32x4 bs = (b0 + b1) + (b2 + b3);
            mlrec = u32x4{__float_as_uint(mrun), __float_as_uint((bs[0] + bs[1]) + (bs[2] + bs[3])), tlo, thi};
            PIN(mlrec);
        }
        VMW(0);
        wg_barrier();
        if (wv == 0 && lane < 32) ST16(mlrec, mlo, 0);
        STAMP(27);
        wg_barrier();
    }
    const unsigned long long t_att = __builtin_amdgcn_s_memrealtime();
    rederive();
    while (__builtin_amdgcn_s_memrealtime() < t_att + (s >= 11 ? MD0 : MD1)) __builtin_amdgcn_s_sleep(2);
    if (s >= 11) {
        const unsigned n = (unsigned)((s - 11) * 8 + wv);
        if (n < 28) pf4<NTM & 32>((n * 64 + lane) * 1024u + x * 128u, p.w2, 0);
        { const unsigned l2 = n * 64 + lane; pf4<NTM & 32>((l2 / 5u) * (DFF * 2) + min(((unsigned)x * 448u & ~127u) + (l2 % 5u) * 128u, DFF * 2 - 4u), p.w1, 0); }
        if (wv == 0) flag_put(WS_FA, id - 128);
        STAMP_FLUSH();
        return;
    }
    const unsigned L_H2S = L_SCR, L_REDM = L_SCR + 16384;
    if (s < 7) {
        {
            const char* w1s = (const char*)(p.w1 + (size_t)wv * 64 * DFF + ug * 32);
#pragma unroll
            for (int i = 0; i < 4; ++i) dma16<NTM & 32>((unsigned)((lane >> 2) * DFF * 2 + (lane & 3) * 16), w1s + (size_t)i * 16 * DFF * 2, wv * 4096 + i * 1024);
        }
        if (wv == 0) {
            if (lane < 32) ST16(s4, (unsigned)(WS_U + x * STR + s * 512 + lane * 16), 0);
            *LP(u32x2, L_REDM) = u32x2{tlo, thi};
            flags_wait(WS_FB + x * STR + lane * 16);
        }
        VMW(0);
        if (wv == 0) flag_put(WS_FA, id - 128);
        wg_barrier();
        const int b = wv;
        u32x4 md[4];
        {
            const u32x2 tg = *LP(u32x2, L_REDM);
            tlo = __builtin_amdgcn_readfirstlane(tg[0]); thi = __builtin_amdgcn_readfirstlane(tg[1]);
            u32x4 mv[8];
            for (unsigned spins = 0;;) {
                unsigned bad = 0u;
#pragma unroll
                for (int k = 0; k < 8; ++k) mv[k] = LD16((unsigned)(WS_MOD + b * STR + (256 + (k >> 2) * 256 + lane * 4 + (k & 3)) * 16), 0);
#pragma unroll
                for (int k = 0; k < 8; ++k) bad |= (mv[k][2] ^ tlo) | (mv[k][3] ^ thi);
                if (__all(bad == 0u)) break;
                __builtin_amdgcn_s_sleep(16);
                if (++spins > kSpinCap) break;
                asm volatile("" ::: "memory");
            }
#pragma unroll
            for (int k = 0; k < 4; ++k) md[k] = u32x4{mv[2 * k][0], mv[2 * k][1], mv[2 * k + 1][0], mv[2 * k + 1][1]};
        }
        bf16x8 wfr[16];
        if (wv >= 6) {
#pragma unroll
            for (int ks = 0; ks < 16; ++ks) {
                const unsigned a = (unsigned)(ks * 2048 + (g * 8 + (nl >> 2)) * 64 + (wv & 1) * 32 + (lane & 3) * 8);
                wfr[ks] = tr_frag(a, a + 256);
            }
#pragma unroll
            for (int ks = 0; ks < 16; ks += 4) PIN4(wfr[ks], wfr[ks + 1], wfr[ks + 2], wfr[ks + 3]);
        }
        float scv[8];
#pragma unroll
        for (int q = 0; q < 8; ++q) { scv[q] = rbf(1.f + __uint_as_float(q < 4 ? md[2][q] : md[3][q - 4])); PIN(scv[q]); }
        unsigned har = L_H2S + ((lane & 7) * HS_LD + g * 8) * 2, haw = L_H2S + (b * HS_LD + lane * 8) * 2;
        unsigned udst = (unsigned)(WS_U + x * STR + (s * 2 + (wv & 1)) * 256 + (nl * 16 + g * 4) * 2), xsrc = (unsigned)(WS_X1 + x * STR + (b * 512 + lane * 8) * 2);
        PIN4(har, haw, udst, xsrc); PIN(md[0]); PIN(md[1]);
        STAMP(28);
        while (__builtin_amdgcn_s_memrealtime() < t_att + XGATE) __builtin_amdgcn_s_sleep(2);
        u32x4 xv;
        for (unsigned spins = 0;;) {
            xv = LD16(xsrc, 0);
            if (__all(mx4(xv, 0u) != SENT)) break;
            __builtin_amdgcn_s_sleep(SLPX);
            if (++spins > kSpinCap) break;
            asm volatile("" ::: "memory");
        }
        STAMP(29);
        {
            f32x2_t v[4];
#pragma unroll
            for (int q = 0; q < 4; ++q) v[q] = f32x2_t{lo16(xv[q]), hi16(xv[q])};
            const f32x2_t p1 = (v[0] + v[1]) + (v[2] + v[3]), p2 = (v[0] * v[0] + v[1] * v[1]) + (v[2] * v[2] + v[3] * v[3]);
            float mb, rden;
            ln_stats(p1[0] + p1[1], p2[0] + p2[1], mb, rden);
            u32x4 hr;
#pragma unroll
            for (int q = 0; q < 4; ++q) {
                const f32x2_t h = ((v[q] - mb) * rden) * f32x2_t{scv[2 * q], scv[2 * q + 1]} + f32x2_t{__uint_as_float(md[q >> 1][(q & 1) * 2]), __uint_as_float(md[q >> 1][(q & 1) * 2 + 1])};
                hr[q] = pk2(h[0], h[1]);
            }
            *LP(u32x4, haw) = hr;
        }
        wg_barrier();
        if (wv >= 6) {
            bf16x8 af[16];
#pragma unroll
            for (int ks = 0; ks < 16; ++ks) af[ks] = *LP(bf16x8, har + ks * 64);
            __builtin_amdgcn_sched_barrier(0);
            f32x4 d0 = {0.f, 0.f, 0.f, 0.f}, d1 = {0.f, 0.f, 0.f, 0.f};
#pragma unroll
            for (int ks = 0; ks < 16; ks += 2) { d0 = MFMA16(wfr[ks], af[ks], d0); d1 = MFMA16(wfr[ks + 1], af[ks + 1], d1); }
            d0 += d1;
            if (nl < 8) __builtin_amdgcn_raw_buffer_store_b64(u32x2{pk2s(fmaxf(d0[0], 0.f), fmaxf(d0[1], 0.f)), pk2s(fmaxf(d0[2], 0.f), fmaxf(d0[3], 0.f))}, rw, udst, 0, 16);
            STAMP(30);
        }
        STAMP_FLUSH();
        return;
    }
    {
        const int n0 = (x * 4 + (s - 7)) * 16;
        const char* mws = (const char*)(p.w2 + (size_t)wv * 224 * 512 + n0);
#pragma unroll
        for (int i = 0; i < 7; ++i) dma16<NTM & 32>((unsigned)((lane >> 1) * 1024 + (lane & 1) * 16), mws + i * 32768, rgn + i * 1024);
        const int fb = lane >> 3, fc = (lane & 7) * 2;
        u32x2 gb = {0u, 0u};
        if (wv == 0) {
            flag_put(WS_FA, id - 128);
            flags_wait(WS_FB + x * STR + lane * 16);
            flags_wait(WS_FA + x * STR + lane * 16);
            if (id == 23 * 8 && lane == 0) __builtin_amdgcn_raw_buffer_store_b64(u32x2{tlo, thi}, rw, WS_EPOCH, 0, 16);
            gb = mod_get(WS_MOD + fb * STR + (1536 + n0 + fc) * 8);
            PIN(gb);
        }
        VMW(0);
        bf16x8 wfr[7];
        const unsigned trl = (unsigned)(g * 8 + (nl >> 2)) * 32u + (unsigned)(lane & 3) * 8u;
#pragma unroll
        for (int ks = 0; ks < 7; ++ks) { wfr[ks] = tr_frag(rgn + ks * 1024 + trl, rgn + ks * 1024 + trl + 128); PIN(wfr[ks]); }
        unsigned usrc = (unsigned)(WS_U + wv * STR + (g >> 1) * 256 + (nl & 7) * 32 + (g & 1) * 16);
        unsigned rdw = L_REDM + wv * 512 + (nl & 7) * 64 + g * 16, rdr = L_REDM + lane * 8;
        unsigned short* outp = p.out + fb * 512 + n0 + fc;
        PIN(usrc); PIN(rdw); PIN(rdr); PIN(outp);
        STAMP(28);
        unsigned xg = 0u;
        if (wv == 0) {
            while (__builtin_amdgcn_s_memrealtime() < t_att + XGATE) __builtin_amdgcn_s_sleep(2);
            for (unsigned spins = 0;;) {
                xg = __builtin_amdgcn_raw_buffer_load_b32(rw, (unsigned)(WS_X1 + x * STR + (fb * 512 + n0 + fc) * 2), 0, 16);
                if (__all(xg != SENT)) break;
                __builtin_amdgcn_s_sleep(SLPD);
                if (++spins > kSpinCap) break;
                asm volatile("" ::: "memory");
            }
            __builtin_amdgcn_s_sleep(SLPG);
        }
        wg_barrier();
        u32x4 uf[7];
        for (unsigned spins = 0;;) {
#pragma unroll
            for (int ks = 0; ks < 7; ++ks) uf[ks] = LD16(usrc + ks * 512, 0);
            unsigned m = 0u;
#pragma unroll
            for (int ks = 0; ks < 7; ++ks) m = mx4(uf[ks], m);
            if (__all(m != SENT)) break;
            if (++spins > kSpinCap) break;
            asm volatile("" ::: "memory");
        }
        STAMP(29);
        f32x4 d = {0.f, 0.f, 0.f, 0.f}, d2 = {0.f, 0.f, 0.f, 0.f};
#pragma unroll
        for (int ks = 0; ks < 7; ++ks) {
            if (ks & 1) d2 = MFMA16(wfr[ks], BF8(uf[ks]), d2);
            else d = MFMA16(wfr[ks], BF8(uf[ks]), d);
        }
        *LP(f32x4, rdw) = d + d2;
        wg_barrier();
        if (wv == 0) {
            f32x2_t c[8];
#pragma unroll
            for (int w2 = 0; w2 < 8; ++w2) c[w2] = *LP(f32x2_t, rdr + w2 * 512);
            const f32x2_t sm = ((c[0] + c[1]) + (c[2] + c[3])) + ((c[4] + c[5]) + (c[6] + c[7]));
            unsigned t = pk2(sm[0], sm[1]);
            t = pk2(__uint_as_float(gb[0]) * lo16(t), __uint_as_float(gb[1]) * hi16(t));
            *(GAS unsigned*)outp = pk2(lo16(xg) + lo16(t), hi16(xg) + hi16(t));
            STAMP(31);
        }
        STAMP_FLUSH();
    }
}

extern "C" void kernel_launch(void* const* d_in, const int* in_sizes, int n_in,
                              void* d_out, int out_size, void* d_ws, size_t ws_size,
                              hipStream_t stream) {
    Params p;
    p.x = (const unsigned short*)d_in[0];
    p.ckv = (const unsigned short*)d_in[1];
    p.w_q = (const unsigned short*)d_in[2];
    p.w_uk = (const unsigned short*)d_in[3];
    p.w_uv = (const unsigned short*)d_in[4];
    p.w_o = (const unsigned short*)d_in[5];
    p.w1 = (const unsigned short*)d_in[7];
    p.w2 = (const unsigned short*)d_in[8];
    p.c = (const unsigned short*)d_in[9];
    p.w_mod = (const unsigned short*)d_in[10];
    p.ws = (char*)d_ws;
    p.out = (unsigned short*)d_out;
    (void)hipFuncSetAttribute((const void*)k_main, hipFuncAttributeMaxDynamicSharedMemorySize, 163840);
    k_main<<<256, 512, 163840, stream>>>(p);
}
```

```cpp
#define GI 18
#define TWD 650
#define MEAS 4
#define AVG 0
#include <hip/hip_runtime.h>
#include <hip/hip_bf16.h>
#include <stdint.h>

#define NH 32
#define SK 4096
#define DFF 1792
#define ROTC 1
#define PFS 2
#define XNO 0
#define CPJ 7
#define NTM 16
#define QHELP 4
#define HP 13
#define NTH 0
#define W0 2
#define SL0 1
#define LMPF 0
#define SLPB 20
#define MD0 40
#define MD1 80
#define NEARP 1
#define NCAL 2
#define LMD 250
#define PFM 57
#define CPFE 34200
#define SLPM 0
#define SLPD 12
#define SLPG 8
#define SLPX 1
#define SLPH 4

using u16x8 = __attribute__((ext_vector_type(8))) unsigned short;
using s16x4 = __attribute__((ext_vector_type(4))) short;
using bf16x8 = __attribute__((ext_vector_type(8))) short;
using f32x16 = __attribute__((ext_vector_type(16))) float;
using f32x4 = __attribute__((ext_vector_type(4))) float;
using u32x4 = __attribute__((ext_vector_type(4))) unsigned;
using u32x2 = __attribute__((ext_vector_type(2))) unsigned;

#define GAS __attribute__((address_space(1)))
#define LAS __attribute__((address_space(3)))
typedef GAS unsigned long long gu64;
#define RLX_AGENT __ATOMIC_RELAXED, __HIP_MEMORY_SCOPE_AGENT
#define SENT 0xffffffffu
#define TAGC 0x9e3779b9u

__device__ __forceinline__ float bf2f(unsigned short u) { return __uint_as_float(((unsigned)u) << 16); }
typedef __attribute__((ext_vector_type(2))) __bf16 bf16x2_t;
typedef __attribute__((ext_vector_type(2))) float f32x2_t;
__device__ __forceinline__ unsigned pk2(float a, float b) {
    f32x2_t v = {a, b};
    bf16x2_t r = __builtin_convertvector(v, bf16x2_t);
    return __builtin_bit_cast(unsigned, r);
}
__device__ __forceinline__ unsigned pk2s(float a, float b) {
    const unsigned r = pk2(a, b);
    return r == SENT ? 0x7fc07fc0u : r;
}
__device__ __forceinline__ unsigned f32s(float a) {
    const unsigned r = __float_as_uint(a);
    return r == SENT ? 0x7fc00000u : r;
}
__device__ __forceinline__ unsigned short f2bf(float f) { return (unsigned short)(pk2(f, 0.f) & 0xffffu); }
__device__ __forceinline__ float rbf(float f) { return __uint_as_float(pk2(f, 0.f) << 16); }
__device__ __forceinline__ float lo16(unsigned u) { return __uint_as_float(u << 16); }
__device__ __forceinline__ float hi16(unsigned u) { return __uint_as_float(u & 0xffff0000u); }
__device__ __forceinline__ unsigned umax3(unsigned a, unsigned b, unsigned c) { return max(max(a, b), c); }
__device__ __forceinline__ unsigned mx4(const u32x4& v, unsigned m) { return umax3(v[2], v[3], umax3(v[0], v[1], m)); }
__device__ __forceinline__ float fmax3(float a, float b, float c) { float r; asm("v_max3_f32 %0, %1, %2, %3" : "=v"(r) : "v"(a), "v"(b), "v"(c)); return r; }
template <int C> __device__ __forceinline__ float dppf(float v) { return __int_as_float(__builtin_amdgcn_update_dpp(0, __float_as_int(v), C, 0xf, 0xf, true)); }
__device__ __forceinline__ float sum16(float v) { v += dppf<0xB1>(v); v += dppf<0x4E>(v); v += dppf<0x141>(v); v += dppf<0x140>(v); return v; }

__device__ __forceinline__ void ln_stats(float p1, float p2, float& mb, float& rden) {
    auto t = __builtin_amdgcn_permlane32_swap(__float_as_uint(p1), __float_as_uint(p2), false, false);
    float a = sum16(__uint_as_float(t[0]) + __uint_as_float(t[1]));
    auto t2 = __builtin_amdgcn_permlane16_swap(__float_as_uint(a), __float_as_uint(a), false, false);
    a = (__uint_as_float(t2[0]) + __uint_as_float(t2[1])) * (1.f / 512.f);
    auto t3 = __builtin_amdgcn_permlane32_swap(__float_as_uint(a), __float_as_uint(a), false, false);
    const float m = __uint_as_float(t3[0]);
    const float var = rbf(fmaxf(fmaf(-m, m, __uint_as_float(t3[1])), 0.f));
    mb = rbf(m);
    rden = __builtin_amdgcn_rcpf(rbf(__builtin_amdgcn_sqrtf(rbf(var + 1e-5f))));
}
#define STR 262144
#define WS_Q 0
#define WS_H 32768
#define WS_X1 65536
#define WS_ML 81920
#define WS_MOD 98304
#define WS_U 114688
#define WS_FI 131072
#define WS_FA 132096
#define WS_FB 133120
#define WS_NP 134144
#define WS_EPOCH 135168
#define WS_CAL 147456
#define WS_OUTP 2097152
#define WS_PART 4194304
#define WS_END 12582912

#define LP(T, off) ((LAS T*)(size_t)(unsigned)(off))
#define STAMP(k) do { if (((k) == MEAS || ((k) == 0 && (blockIdx.x >> 4) == 7 && wv == 7)) && (threadIdx.x & 63) == 0) __hip_atomic_store((gu64*)(p.ws + (128u << 20)) + ((k) == 0 ? 0 : 2048) + blockIdx.x * 8 + wv, (unsigned long long)__builtin_amdgcn_s_memrealtime(), RLX_AGENT); \
    if ((k) == 31) { \
        const unsigned long long t1 = __builtin_amdgcn_s_memrealtime(); \
        unsigned long long m = ~0ull, mx = 0ull; \
        for (int q = 0; q < 32; ++q) { const unsigned long long v = __hip_atomic_load((gu64*)(p.ws + (128u << 20)) + q * 64 + (threadIdx.x & 63), RLX_AGENT); if (t1 - v < 10000ull) m = v < m ? v : m; } \
        for (int o = 1; o < 64; o <<= 1) { const unsigned long long v = __shfl_xor(m, o); m = v < m ? v : m; } \
        for (int q = 0; q < 32; ++q) { const unsigned long long v = __hip_atomic_load((gu64*)(p.ws + (128u << 20)) + 2048 + q * 64 + (threadIdx.x & 63), RLX_AGENT); if (v - m < 10000ull) mx = v > mx ? v : mx; } \
        for (int o = 1; o < 64; o <<= 1) { const unsigned long long v = __shfl_xor(mx, o); mx = v > mx ? v : mx; } \
        if (AVG) { unsigned sm = 0, cn = 0; \
            for (int q = 0; q < 32; ++q) { const unsigned long long v = __hip_atomic_load((gu64*)(p.ws + (128u << 20)) + 2048 + q * 64 + (threadIdx.x & 63), RLX_AGENT); if (v - m < 10000ull) { sm += (unsigned)(v - m); ++cn; } } \
            for (int o = 1; o < 64; o <<= 1) { sm += __shfl_xor(sm, o); cn += __shfl_xor(cn, o); } \
            mx = m + sm / max(cn, 1u); } \
        if (MEAS == 31) mx = t1; \
        const unsigned dl = MEAS == 99 ? 0u : min(__builtin_amdgcn_readfirstlane((unsigned)(mx - m)), 5000u) * 4u; \
        while (__builtin_amdgcn_s_memrealtime() < t1 + dl) __builtin_amdgcn_s_sleep(1); \
    } } while (0)
#define STAMP_FLUSH() do {} while (0)
__device__ __forceinline__ s16x4 tr4(unsigned a) { return __builtin_amdgcn_ds_read_tr16_b64_v4i16(LP(s16x4, a)); }
__device__ __forceinline__ bf16x8 cat8(s16x4 a, s16x4 b) { return __builtin_shufflevector(a, b, 0, 1, 2, 3, 4, 5, 6, 7); }
__device__ __forceinline__ bf16x8 tr_frag(unsigned a0, unsigned a1) { return cat8(tr4(a0), tr4(a1)); }
__device__ __forceinline__ unsigned swz(unsigned row) { return ((row & 3) << 2) | ((row >> 2) & 3); }
__device__ __forceinline__ unsigned off_b(unsigned row, unsigned ch) { return 256u * row + 16u * (ch ^ swz(row)); }
__device__ __forceinline__ unsigned tile_off(unsigned key, unsigned chunk) { return ((key >> 5) * 4 + (chunk >> 4)) * 8192u + off_b(key & 31, chunk & 15); }
__device__ __forceinline__ void wg_barrier() { asm volatile("s_waitcnt lgkmcnt(0)\n\ts_barrier" ::: "memory"); }
template <int NT> __device__ __forceinline__ void dma16(unsigned voff, const void* sbase, unsigned lds_dst) {
    if (NT) asm volatile("s_mov_b32 m0, %2\n\ts_nop 0\n\tglobal_load_lds_dwordx4 %0, %1 nt" :: "v"(voff), "s"(sbase), "s"(lds_dst) : "memory", "m0");
    else asm volatile("s_mov_b32 m0, %2\n\ts_nop 0\n\tglobal_load_lds_dwordx4 %0, %1" :: "v"(voff), "s"(sbase), "s"(lds_dst) : "memory", "m0");
}
template <int NT> __device__ __forceinline__ void dma16x(unsigned voff, unsigned vx, const void* sbase, unsigned lds_dst) {
    unsigned t;
#define DMAX(pol, c) asm volatile("s_mov_b32 m0, %3\n\tv_xor_b32 %0, %4, %1\n\tglobal_load_lds_dwordx4 %0, %2" pol : "=&v"(t) : "v"(voff), "s"(sbase), "s"(lds_dst), c(vx) : "memory", "m0")
    if (__builtin_constant_p(vx)) { if (NT) DMAX(" nt", "i"); else DMAX("", "i"); }
    else { if (NT) DMAX(" nt", "s"); else DMAX("", "s"); }
}
template <int NT> __device__ __forceinline__ void pf4(unsigned voff, const void* sbase, unsigned lds_dst) {
    if (NT) asm volatile("s_mov_b32 m0, %2\n\ts_nop 0\n\tglobal_load_lds_dword %0, %1 nt" :: "v"(voff), "s"(sbase), "s"(lds_dst) : "memory", "m0");
    else asm volatile("s_mov_b32 m0, %2\n\ts_nop 0\n\tglobal_load_lds_dword %0, %1" :: "v"(voff), "s"(sbase), "s"(lds_dst) : "memory", "m0");
}
#define XGATE 300
#define SPINCAP (1u << 20)
constexpr unsigned kSpinCap = SPINCAP;
struct Params {
    const unsigned short* x;
    const unsigned short* ckv;
    const unsigned short* w_q;
    const unsigned short* w_uk;
    const unsigned short* w_uv;
    const unsigned short* w_o;
    const unsigned short* w1;
    const unsigned short* w2;
    const unsigned short* c;
    const unsigned short* w_mod;
    char* ws;
    unsigned short* out;
};
#define VMW(n) asm volatile("s_waitcnt vmcnt(%0)" :: "n"(n) : "memory")
#define PIN(x) asm volatile("" : "+v"(x))
#define PIN4(a, b, c, d) asm volatile("" : "+v"(a), "+v"(b), "+v"(c), "+v"(d) :: "memory")
#define MFMA16(a, b, c) __builtin_amdgcn_mfma_f32_16x16x32_bf16(a, b, c, 0, 0, 0)
#define MFMA32(a, b, c) __builtin_amdgcn_mfma_f32_32x32x16_bf16(a, b, c, 0, 0, 0)
#define BF8(x) __builtin_bit_cast(bf16x8, x)
#define ST16(v, off, soff) __builtin_amdgcn_raw_buffer_store_b128(v, rw, off, soff, 16)
#define LD16(off, soff) __builtin_amdgcn_raw_buffer_load_b128(rw, off, soff, 16)
#define SCL (0.08838834764831845f * 1.4426950408889634f)
#define BIGD 60.f
#define L_SCR 131072
#define HS_LD 520

__global__ __launch_bounds__(512, 2) void k_main(Params p) {
    unsigned long long pc;
    asm volatile("s_getpc_b64 %0" : "=s"(pc));
    const int id = blockIdx.x, tid = threadIdx.x;
    int lane = tid & 63;
    const int wv = __builtin_amdgcn_readfirstlane(tid >> 6);
    int g = lane >> 4, nl = lane & 15;
    auto rederive = [&]() {
        asm volatile("v_mbcnt_lo_u32_b32 %0, -1, 0\n\tv_mbcnt_hi_u32_b32 %0, -1, %0" : "=v"(lane));
        g = lane >> 4; nl = lane & 15;
    };
    const int x = id & 7, j = id >> 3;
    if (wv < 5 && j == 15) {
        asm volatile("s_mov_b32 m0, %2\n\ts_nop 0\n\tglobal_load_lds_dword %0, %1" :: "v"(min((unsigned)((wv * 64 + lane) * 128), (unsigned)CPFE)), "s"(pc), "s"(L_SCR + 31744) : "memory", "m0");
    }
    if ((j >> 2) == 3) {
        dma16<0>((unsigned)(((nl & 7) * 512 + (nl >> 3) * 32 + g * 8) * 2 + wv * 128), p.c, wv * 16384 + 8192);
        dma16<0>((unsigned)tid * 16u, p.x, wv * 16384 + 9216);
    }
    char* ws = p.ws;
    STAMP(0);
    const __amdgpu_buffer_rsrc_t rw = __builtin_amdgcn_make_buffer_rsrc(ws, 0, WS_END, 0x00020000);
    const u32x4 s4 = {SENT, SENT, SENT, SENT};
    unsigned long long cnt = 0ull;
    auto cnt_load = [&]() { asm volatile("global_load_dwordx2 %0, %1, %2 sc1" : "+v"(cnt) : "v"((unsigned)WS_EPOCH), "s"(ws) : "memory"); };
    unsigned tlo = 0u, thi = 0u;
    auto tag_take = [&]() {
        asm volatile("" : "+v"(cnt) :: "memory");
        const unsigned long long t = cnt + 1ull;
        tlo = __builtin_amdgcn_readfirstlane((unsigned)t); thi = __builtin_amdgcn_readfirstlane((unsigned)(t >> 32));
    };
    auto flag_put = [&](unsigned base, unsigned idx) {
        if (lane < 8) __builtin_amdgcn_raw_buffer_store_b64(u32x2{tlo, thi}, rw, base + lane * STR + idx * 8, 0, 16);
    };
    auto mod_get = [&](unsigned off) {
        u32x4 v;
        for (unsigned spins = 0;;) {
            v = LD16(off, 0);
            if (__all((v[2] == tlo) & (v[3] == thi))) break;
            __builtin_amdgcn_s_sleep(16);
            if (++spins > kSpinCap) break;
            asm volatile("" ::: "memory");
        }
        return u32x2{v[0], v[1]};
    };
    auto np_mask = [&](u32x4 v) {
        for (unsigned spins = 0; !__all((v[2] == tlo) & (v[3] == thi)) && spins < kSpinCap; ++spins) {
            __builtin_amdgcn_s_sleep(8);
            asm volatile("buffer_load_dwordx4 %0, %1, %2, 0 offen sc1\n\ts_waitcnt vmcnt(0)" : "=v"(v) : "v"((unsigned)(WS_NP + x * STR + (lane & 7) * 16)), "s"(rw) : "memory");
        }
        const int d = (int)v[0];
        int t = (lane & 4) ? -d : d;
        t += __builtin_amdgcn_update_dpp(0, t, 0xB1, 0xf, 0xf, true);
        t += __builtin_amdgcn_update_dpp(0, t, 0x4E, 0xf, 0xf, true);
        t += __builtin_amdgcn_update_dpp(0, t, 0x141, 0xf, 0xf, true);
        const bool n1 = abs(d) > 160 ? d < 0 : (t < 0) != ((lane & 4) != 0);
        return NEARP ? (unsigned)__ballot(n1) & 0xffu : 0u;
    };
    auto flags_ok = [&](const u32x4& f) { return (f[0] == tlo) & (f[1] == thi) & (f[2] == tlo) & (f[3] == thi); };
    auto flags_wait = [&](unsigned off) {
        for (unsigned spins = 0;;) {
            u32x4 f;
            asm volatile("buffer_load_dwordx4 %0, %1, %2, 0 offen sc1\n\ts_waitcnt vmcnt(0)" : "=v"(f) : "v"(off), "s"(rw) : "memory");
            if (__all(flags_ok(f))) break;
            __builtin_amdgcn_s_sleep(16);
            if (++spins > kSpinCap) break;
            asm volatile("" ::: "memory");
        }
    };
    const unsigned pcl = (unsigned)(((((lane & 7) >> 1) ^ ((lane >> 4) & 1)) * 32) + (lane & 1) * 16);
    auto dma_piece = [&](const unsigned short* src, unsigned ldb, unsigned dst, int n, int nt) {
        const unsigned v0 = (lane >> 3) * ldb + pcl, v1 = v0 ^ 64u;
#pragma unroll
        for (int i = 0; i < n; ++i) {
            if (nt) dma16<1>((i & 1) ? v1 : v0, (const char*)src + (size_t)i * 8 * ldb, dst + i * 1024);
            else dma16<0>((i & 1) ? v1 : v0, (const char*)src + (size_t)i * 8 * ldb, dst + i * 1024);
        }
    };
    const unsigned frl = (unsigned)((g * 8 + (nl >> 2)) * 128 + (lane & 3) * 8), frs = (unsigned)((nl >> 3) | ((g & 1) << 1));
    auto piece_frag = [&](unsigned base, int ks, int t) {
        const unsigned a = base + frl + ((t ^ frs) << 5) + ks * 4096;
        return tr_frag(a, a + 512);
    };
    const unsigned rgn = (unsigned)(wv * 16384);

    if (j < 16) {
        const unsigned long long t_start = __builtin_amdgcn_s_memrealtime();
        const int th = x * 4 + (j & 3);
        if (j < 12) {
            const int qt = j >> 2, rot = qt * 5;
            const bool pub = wv < (qt < 2 ? 3 : 2);
            const unsigned qo = (unsigned)(WS_Q + (th & 7) * STR + ((nl & 7) * 4 + (th >> 3)) * 1024 + (qt * 192 + wv * 64 + g * 16 + (nl >> 3) * 8) * 2);
            const unsigned vb = (unsigned)(g * 8192 + ((nl ^ (g << 2)) << 4));
            const char* wuk = (const char*)(p.w_uk + th * 128) + (size_t)qt * 192 * 8192;
            const char* wq = (const char*)(p.w_q + th * 128) + (size_t)wv * 32768;
            const unsigned L_U = 114688;
            {
                const int k0 = qt < 2 ? wv * 6 : wv * 4;
#pragma unroll
                for (int i = 0; i < 6; ++i) {
                    if (i < 4 || qt < 2) dma16<NTM & 2>(vb ^ (((k0 + i) & 3) << 4), wuk + (size_t)(k0 + i) * 32768, L_U + (k0 + i) * 1024);
                    if (i == 4) VMW(4);
                }
            }
            const unsigned vq = vb ^ ((wv & 3) << 4);
            auto wq_dma = [&](int ps) { dma16<NTM & 2>(vq, wq + (size_t)((ps + rot) & 15) * 262144, ps * 8192 + wv * 1024); };
            bf16x8 uk[4][4], aq[16];
            u32x4 fi, hv[4];
            unsigned fio = (unsigned)(WS_FI + x * STR + (lane & 7) * 16), hsrc, hsrc2, ta0, ta1, tb0, tb1, qo2, hw0, hw1, hr, hr2, qw, thx = 0u;
            const unsigned L_HI = L_SCR, L_QI = L_SCR + 8192;
            auto h_load = [&]() {
                asm volatile("buffer_load_dwordx4 %0, %4, %6, 0 offen sc1\n\tbuffer_load_dwordx4 %1, %4, %6, 0 offen offset:1024 sc1\n\t"
                             "buffer_load_dwordx4 %2, %5, %6, 0 offen sc1\n\tbuffer_load_dwordx4 %3, %5, %6, 0 offen offset:1024 sc1"
                             : "+v"(hv[0]), "+v"(hv[1]), "+v"(hv[2]), "+v"(hv[3]) : "v"(hsrc), "v"(hsrc2), "s"(rw) : "memory");
            };
            auto h_bad = [&]() {
                PIN4(hv[0], hv[1], hv[2], hv[3]);
                unsigned bad = (hv[0][1] ^ tlo) | (hv[0][3] ^ thx) | (hv[1][1] ^ tlo) | (hv[1][3] ^ thx) | (hv[2][1] ^ tlo) | (hv[2][3] ^ thx) | (hv[3][1] ^ tlo) | (hv[3][3] ^ thx);
                PIN(bad);
                return bad;
            };
#pragma unroll
            for (int ps = 0; ps < 16; ++ps) {
                wq_dma(ps);
                if (ps == 4) cnt_load();
                if (ps == HP) { h_load(); asm volatile("buffer_load_dwordx4 %0, %1, %2, 0 offen sc1" : "=v"(fi) : "v"(fio), "s"(rw) : "memory"); }
                if (ps >= 4 && ps < 8) VMW(5); else if (ps >= HP && ps < HP + 4) VMW(9); else VMW(4);
                if (ps == 3) {
                    asm volatile("s_barrier" ::: "memory");
                    if (pub) {
#pragma unroll
                        for (int t = 0; t < 4; ++t)
#pragma unroll
                            for (int s = 0; s < 4; ++s) uk[t][s] = *LP(bf16x8, L_U + off_b(wv * 64 + (nl >> 2) * 16 + t * 4 + (nl & 3), s * 4 + g));
                    }
                    STAMP(1);
                }
                if (ps == 5) {
                    hsrc = (unsigned)(WS_H + x * STR + wv * 2048 + lane * 16); hsrc2 = hsrc + 16384;
                    ta0 = off_b(8 * g + (nl >> 2), 2 * wv + ((lane & 3) >> 1)) + 8 * (lane & 1); ta1 = off_b(8 * g + 4 + (nl >> 2), 2 * wv + ((lane & 3) >> 1)) + 8 * (lane & 1);
                    tb0 = ta0 + 65536; tb1 = ta1 + 65536; qo2 = qo;
                    hw0 = L_HI + ((2 * wv - rot) & 15) * 512 + (lane >> 4) * 128 + (lane & 7) * 16 + ((lane >> 3) & 1) * 8;
                    hw1 = L_HI + ((2 * wv + 1 - rot) & 15) * 512 + (lane >> 4) * 128 + (lane & 7) * 16 + ((lane >> 3) & 1) * 8;
                    hr2 = L_HI + g * 128 + (nl & 7) * 16 + (nl >> 3) * 512;
                    PIN(hr2);
                    hr = L_HI + g * 128 + (nl & 7) * 16; qw = L_QI + (wv >> 1) * 512 + ((wv & 1) * 2 + (g >> 1)) * 128 + (nl & 7) * 16 + (g & 1) * 8;
                    PIN4(hsrc, hsrc2, ta0, ta1); PIN4(tb0, tb1, qo2, hw0); PIN(hw1); PIN(hr); PIN(qw);
                }
                if (ps == 8) { tag_take(); thx = tlo ^ thi ^ TAGC; }
                if (ps == 9) {
#pragma unroll
                    for (int t = 0; t < 4; ++t) PIN4(uk[t][0], uk[t][1], uk[t][2], uk[t][3]);
                    wg_barrier();
#pragma unroll
                    for (int q = 0; q < 6; ++q) aq[q] = tr_frag(ta0 + q * 8192, ta1 + q * 8192);
                }
                if (ps == 13) {
                    asm volatile("s_barrier" ::: "memory");
#pragma unroll
                    for (int q = 6; q < 10; ++q) aq[q] = q < 8 ? tr_frag(ta0 + q * 8192, ta1 + q * 8192) : tr_frag(tb0 + (q - 8) * 8192, tb1 + (q - 8) * 8192);
                }
            }
            STAMP(15);
            if (HP + 4 > 15) VMW(HP + 4 > 15 ? 15 - HP : 0);
            if (!__all(h_bad() == 0u)) {
                for (unsigned spins = 0;;) {
                    h_load();
                    VMW(0);
                    if (__all(h_bad() == 0u)) break;
                    __builtin_amdgcn_s_sleep(SLPH);
                    if (++spins > kSpinCap) break;
                }
            }
            {
                unsigned hh[4];
#pragma unroll
                for (int q = 0; q < 4; ++q) {
                    const unsigned t = hv[2 + (q >> 1)][(q & 1) * 2], sh = hv[q >> 1][(q & 1) * 2];
                    hh[q] = pk2(lo16(t) + lo16(sh), hi16(t) + hi16(sh));
                }
                *LP(u32x2, hw0) = u32x2{hh[0], hh[1]};
                *LP(u32x2, hw1) = u32x2{hh[2], hh[3]};
            }
            STAMP(3);
            asm volatile("s_waitcnt vmcnt(0) lgkmcnt(0)\n\ts_barrier" : "+v"(fi) :: "memory");
            STAMP(2);
            {
                bf16x8 hb[8];
#pragma unroll
                for (int q = 0; q < 5; ++q) hb[q] = *LP(bf16x8, hr2 + q * 1024);
#pragma unroll
                for (int ps = 10; ps < 16; ++ps) {
                    if (!(ps & 1)) hb[ps >> 1] = *LP(bf16x8, hr2 + (ps >> 1) * 1024);
                    aq[ps] = tr_frag(tb0 + (ps - 8) * 8192, tb1 + (ps - 8) * 8192);
                }
                __builtin_amdgcn_sched_barrier(0);
                f32x4 d0 = {0.f, 0.f, 0.f, 0.f}, d1 = {0.f, 0.f, 0.f, 0.f};
#pragma unroll
                for (int ps = 0; ps < 16; ps += 2) {
                    d0 = MFMA16(aq[ps], hb[ps >> 1], d0); d1 = MFMA16(aq[ps + 1], hb[ps >> 1], d1);
                    if (ps == 6) { unsigned bad = (fi[0] ^ tlo) | (fi[1] ^ thi) | (fi[2] ^ tlo) | (fi[3] ^ thi); PIN(bad); if (!__all(bad == 0u)) flags_wait(fio); }
                }
#pragma unroll
                for (int e = 0; e < 4; ++e) { float v = d0[e] + dppf<0x128>(d1[e]); asm("" : "+v"(v)); d0[e] = v; }
                if (nl < 8) *LP(u32x2, qw) = u32x2{pk2(d0[0], d0[1]), pk2(d0[2], d0[3])};
            }
            wg_barrier();
            if (pub) {
                bf16x8 qb[4];
#pragma unroll
                for (int s = 0; s < 4; ++s) qb[s] = *LP(bf16x8, hr + 8192 + s * 512);
                __builtin_amdgcn_sched_barrier(0);
                f32x4 d[4];
#pragma unroll
                for (int t = 0; t < 4; ++t) d[t] = MFMA16(uk[t][0], qb[0], (f32x4{0.f, 0.f, 0.f, 0.f}));
#pragma unroll
                for (int s = 1; s < 4; ++s)
#pragma unroll
                    for (int t = 0; t < 4; ++t) d[t] = MFMA16(uk[t][s], qb[s], d[t]);
                const f32x4 a = nl & 8 ? d[2] : d[0], b = nl & 8 ? d[3] : d[1];
                u32x4 qv = {pk2(a[0], a[1]), pk2(a[2], a[3]), pk2(b[0], b[1]), pk2(b[2], b[3])};
                if (__builtin_expect(mx4(qv, 0u) == SENT, 0)) qv = u32x4{pk2s(a[0], a[1]), pk2s(a[2], a[3]), pk2s(b[0], b[1]), pk2s(b[2], b[3])};
                ST16(qv, qo2, 0);
            }
            STAMP(4);
            wg_barrier();
            if (LMPF && wv >= 3 && wv < 6) {
                const unsigned n = (unsigned)(j * 3 + wv - 3);
                if (n < 32) pf4<0>(((n & 7u) * 64 + lane) * 6144u + 2048u + (x * 4 + (n >> 3)) * 128u, p.w_mod, L_SCR + 31744);
            }
            if (QHELP && wv < 3 && j + 12 * wv < 32) {
                const int hh = j + 12 * wv;
#pragma unroll 1
                for (unsigned spins = 0; spins < 64; ++spins) {
                    __builtin_amdgcn_s_sleep(QHELP);
                    const u32x4 v = LD16((unsigned)(WS_Q + (hh & 7) * STR + (x * 4 + (hh >> 3)) * 1024 + lane * 16), 0);
                    if (__all(mx4(v, 0u) != SENT)) break;
                    asm volatile("" ::: "memory");
                }
                STAMP(6);
            }
        } else {
            const int cg = x * 2 + (j & 1);
            if (j < 14) {
                dma_piece(p.w_mod + (size_t)wv * 64 * 3072 + cg * 64, 6144, rgn, 8, NTM & 2);
                cnt_load();
                VMW(9);
            } else {
                cnt_load();
                ST16(s4, (unsigned)(WS_Q + wv * STR + cg * 2048 + lane * 16), 0); ST16(s4, (unsigned)(WS_Q + wv * STR + cg * 2048 + lane * 16), 1024);
                VMW(0);
                tag_take();
                wg_barrier();
                if (wv == 7) flag_put(WS_FI, cg);
                STAMP(15);
                if (j == 14) {
                    u32x2 best = {~0u, ~0u};
#pragma unroll
                    for (int r = 0; r < 2 * NCAL; ++r) {
                        const unsigned hf = (r ^ wv) & 1;
                        VMW(0);
                        const unsigned long long t0 = __builtin_amdgcn_s_memtime();
                        if (lane < 8) ST16(s4, WS_CAL + x * STR + hf * 8192 + (wv * 8 + r) * 128 + lane * 16, 0);
                        VMW(0);
                        const unsigned dt = (unsigned)(__builtin_amdgcn_s_memtime() - t0);
                        if (hf) best[1] = min(best[1], dt); else best[0] = min(best[0], dt);
                    }
                    *LP(u32x2, 16384 * 7 + 14336 + wv * 8) = best;
                    wg_barrier();
                    if (wv == 0 && lane < 8) {
                        u32x2 m = *LP(u32x2, 16384 * 7 + 14336 + lane * 8);
#pragma unroll
                        for (int o = 1; o < 8; o <<= 1) { m[0] = min(m[0], (unsigned)__shfl_xor(m[0], o)); m[1] = min(m[1], (unsigned)__shfl_xor(m[1], o)); }
                        ST16((u32x4{(unsigned)max(-1000, min(1000, (int)m[1] - (int)m[0])), 0u, tlo, thi}), (unsigned)(WS_NP + lane * STR + x * 16), 0);
                    }
                    STAMP(9);
                }
            }
            const u16x8 cf = *LP(u16x8, rgn + 8192 + lane * 16), xa = *LP(u16x8, rgn + 9216 + lane * 16);
            u32x4 af[2];
#pragma unroll
            for (int q = 0; q < 4; ++q) {
                const float a = bf2f(cf[2 * q]), b = bf2f(cf[2 * q + 1]);
                af[0][q] = pk2(a * __builtin_amdgcn_rcpf(1.f + __expf(-a)), b * __builtin_amdgcn_rcpf(1.f + __expf(-b)));
                af[1][q] = (unsigned)__builtin_amdgcn_update_dpp(0, (int)af[0][q], 0x128, 0xf, 0xf, true);
            }
            const unsigned L_MS = L_SCR;
            unsigned wa[4], fr[4];
#pragma unroll
            for (int t = 0; t < 4; ++t) { wa[t] = L_MS + wv * 2048 + (nl & 7) * 256 + (((t * 4 + g) ^ (nl & 7)) << 4) + (nl >> 3) * 16384; fr[t] = rgn + frl + ((t ^ frs) << 5); }
            PIN4(wa[0], wa[1], wa[2], wa[3]); PIN4(fr[0], fr[1], fr[2], fr[3]); PIN(af[0]); PIN(af[1]);
            const unsigned L_XN = 12288;
            auto mod_mm = [&](bool ln) {
                VMW(0);
                bf16x8 f[2][4];
#pragma unroll
                for (int ks = 0; ks < 2; ++ks)
#pragma unroll
                    for (int t = 0; t < 4; ++t) f[ks][t] = tr_frag(fr[t] + ks * 4096, fr[t] + ks * 4096 + 512);
                __builtin_amdgcn_sched_barrier(0);
                f32x4 d[4];
#pragma unroll
                for (int t = 0; t < 4; ++t) d[t] = MFMA16(f[0][t], BF8(af[0]), (f32x4{0.f, 0.f, 0.f, 0.f}));
#pragma unroll
                for (int t = 0; t < 4; ++t) d[t] = MFMA16(f[1][t], BF8(af[1]), d[t]);
#pragma unroll
                for (int t = 0; t < 4; ++t) *LP(f32x4, wa[t]) = d[t];
                if (ln) {
                    float v[8];
#pragma unroll
                    for (int q = 0; q < 8; ++q) v[q] = bf2f(xa[q]);
                    const float p1 = ((v[0] + v[1]) + (v[2] + v[3])) + ((v[4] + v[5]) + (v[6] + v[7]));
                    const float p2 = ((v[0] * v[0] + v[1] * v[1]) + (v[2] * v[2] + v[3] * v[3])) + ((v[4] * v[4] + v[5] * v[5]) + (v[6] * v[6] + v[7] * v[7]));
                    float mb, rden;
                    ln_stats(p1, p2, mb, rden);
                    u32x4 r;
#pragma unroll
                    for (int q = 0; q < 4; ++q) { const unsigned d = pk2(v[2 * q] - mb, v[2 * q + 1] - mb); r[q] = pk2(lo16(d) * rden, hi16(d) * rden); }
                    *LP(u32x4, L_XN + wv * 16384 + lane * 16) = r;
                }
                wg_barrier();
            };
            auto sum8 = [&](unsigned a) {
                f32x4 u[8];
#pragma unroll
                for (int w2 = 0; w2 < 8; ++w2) u[w2] = *LP(f32x4, a + w2 * 2048);
                __builtin_amdgcn_sched_barrier(0);
                return ((u[0] + u[1]) + (u[2] + u[3])) + ((u[4] + u[5]) + (u[6] + u[7]));
            };
            if (j < 14) {
                const int hb = lane & 7, c4 = 2 * wv + (nl >> 3);
                unsigned hoff = (unsigned)(WS_H + g * STR + cg * 2048 + wv * 256 + nl * 16);
                unsigned sa = L_MS + hb * 256 + ((c4 ^ hb) << 4);
                unsigned xa2 = L_XN + hb * 16384 + (cg & 7) * 128 + c4 * 8;
                PIN(hoff); PIN(sa); PIN(xa2);
                STAMP(1);
                mod_mm(cg >= 8);
                STAMP(2);
                const u32x2 xn = *LP(u32x2, xa2);
                const f32x4 v = sum8(sa);
                tag_take();
                const unsigned thx = tlo ^ thi ^ TAGC;
                u32x2 r = {pk2(v[0], v[1]), pk2(v[2], v[3])};
                if (cg >= 8) {
#pragma unroll
                    for (int q = 0; q < 2; ++q) { const unsigned sc = pk2(1.f + lo16(r[q]), 1.f + hi16(r[q])); r[q] = pk2(lo16(xn[q]) * lo16(sc), hi16(xn[q]) * hi16(sc)); }
                }
                ST16((u32x4{r[0], tlo, r[1], thx}), hoff, 0);
                ST16((u32x4{r[0], tlo, r[1], thx}), hoff, 4 * STR);
                STAMP(5);
            }
            wg_barrier();
            while (__builtin_amdgcn_s_memrealtime() < t_start + LMD) __builtin_amdgcn_s_sleep(2);
            const int lg = x * 4 + (j & 3);
            dma_piece(p.w_mod + (size_t)wv * 64 * 3072 + 1024 + lg * 64, 6144, rgn, 8, NTM & 4);
            mod_mm(false);
            if (wv < 4) {
                const int b = wv * 2 + (lane >> 5), cp = lane & 31;
                f32x2_t u[8];
#pragma unroll
                for (int w2 = 0; w2 < 8; ++w2) u[w2] = *LP(f32x2_t, L_MS + w2 * 2048 + b * 256 + (((cp >> 1) ^ b) << 4) + (cp & 1) * 8);
                const f32x2_t v = ((u[0] + u[1]) + (u[2] + u[3])) + ((u[4] + u[5]) + (u[6] + u[7]));
                ST16((u32x4{__float_as_uint(rbf(v[0])), __float_as_uint(rbf(v[1])), tlo, thi}), (unsigned)(WS_MOD + b * STR + (lg * 32 + cp) * 16), 0);
            }
            STAMP(6);
        }
        const int bq = (j >> 2) & 1, dh = j >> 3, src = th * 2 + dh, rb = bq * 4 + (src >> 4), rc = src & 15;
        wg_barrier();
        while (__builtin_amdgcn_s_memrealtime() < t_start + TWD - 60) __builtin_amdgcn_s_sleep(2);
        const unsigned npm = np_mask(LD16((unsigned)(WS_NP + x * STR + (lane & 7) * 16), 0)), np = (npm >> x) & 1u;
        unsigned oo = (unsigned)(WS_OUTP + ((bq * 4 + (nl & 3)) * 16 + wv * 2 + (nl >> 3)) * 16384 + (src ^ wv) * 128 + ((nl >> 2) & 1) * 64 + g * 16) + ((~npm >> ((nl & 3) * 2 + (wv >> 2))) & 1u) * 8192u;
        const int rcol = rc * 32 + (lane & 7) * 4 + (lane >> 5) * 2;
        unsigned xdst = (unsigned)(WS_X1 + ((lane >> 3) & 3) * STR + (rb * 512 + rcol) * 2), xdst2 = xdst + 4 * STR + ((npm >> (4 + ((lane >> 3) & 3))) & 1u) * 8192u;
        xdst += ((npm >> ((lane >> 3) & 3)) & 1u) * 8192u;
        while (__builtin_amdgcn_s_memrealtime() < t_start + TWD) __builtin_amdgcn_s_sleep(2);
        STAMP(7);
        if ((PFM & 8) && wv == 0 && lane < 32) pf4<NTM & 8>((unsigned)((j * 32 + lane) * 8192 + x * 1024 + ((3u - ((unsigned)(size_t)p.w_uv >> 7)) & 7u) * 128), p.w_uv, L_SCR + 31744);
        ST16(s4, oo, 0);
        if (wv == 7) { __builtin_amdgcn_raw_buffer_store_b32(SENT, rw, xdst, 0, 16); __builtin_amdgcn_raw_buffer_store_b32(SENT, rw, xdst2, 0, 16); }
        if (bq) dma_piece(p.w_o + (size_t)(th * 128 + dh * 64) * 512 + wv * 64, 1024, rgn + 8192, 8, NTM & 8);
        dma_piece(p.w_uv + ((size_t)wv * 64 * NH + th) * 128 + dh * 64, 8192, rgn, 8, NTM & 8);
        if (!bq) dma_piece(p.w_o + (size_t)(th * 128 + dh * 64) * 512 + wv * 64, 1024, rgn + 8192, 8, NTM & 8);
        VMW(16);
        asm volatile("s_barrier" ::: "memory");
        if (wv == 0) flag_put(WS_FB, id);
        u32x4 fb, gm;
        unsigned xg;
        VMW(4);
        if (wv == 7)
            asm volatile("buffer_load_dwordx4 %0, %3, %5, 0 offen sc1\n\tbuffer_load_dwordx4 %1, %4, %5, 0 offen sc1\n\tglobal_load_dword %2, %6, %7" : "=&v"(fb), "=&v"(gm), "=&v"(xg)
                         : "v"((unsigned)(WS_FB + x * STR + lane * 16)), "v"((unsigned)(WS_MOD + rb * STR + rcol * 8)), "s"(rw), "v"((unsigned)(rb * 512 + rcol) * 2u), "s"(p.x) : "memory");
        VMW(0);
        asm volatile("" : "+v"(fb), "+v"(gm), "+v"(xg));
        STAMP(8);
        bf16x8 fuv[2][4], fwo[2][4];
        unsigned pfa[4];
#pragma unroll
        for (int t = 0; t < 4; ++t) { pfa[t] = rgn + frl + ((t ^ frs) << 5); PIN(pfa[t]); }
#pragma unroll
        for (int ks = 0; ks < 2; ++ks)
#pragma unroll
            for (int t = 0; t < 4; ++t) {
                const unsigned a = rgn + (unsigned)((g * 4 + (nl >> 2)) * 128 + (lane & 3) * 8) + ((t ^ (unsigned)((nl >> 3) | (g & 2))) << 5) + ks * 4096;
                fuv[ks][t] = tr_frag(a, a + 2048);
                fwo[ks][t] = tr_frag(pfa[t] + 8192 + ks * 4096, pfa[t] + 8192 + ks * 4096 + 512);
            }
#pragma unroll
        for (int ks = 0; ks < 2; ++ks) { PIN4(fuv[ks][0], fuv[ks][1], fuv[ks][2], fuv[ks][3]); PIN4(fwo[ks][0], fwo[ks][1], fwo[ks][2], fwo[ks][3]); }
        const unsigned L_WGT = L_SCR, L_MSK = L_SCR + 256, L_RED = L_SCR + 1024;
        unsigned psrc = (unsigned)(WS_PART + (bq * 4 * 32 + th) * 32768 + (wv >> 2) * 16384 + (wv & 3) * 2048 + ((lane >> 3) ^ (th & 7)) * 128) + np * 8192u + pcl, psrc1 = (psrc ^ 64u) + 1024;
        unsigned psrc2 = psrc ^ 8192u, psrc3 = psrc1 ^ 8192u;
        PIN(psrc2); PIN(psrc3); PIN(xdst2);
        unsigned rw2 = L_RED + wv * 1024 + (nl & 3) * 256 + g * 16;
        unsigned wa0 = (nl & 3) == (g >> 1) ? L_WGT + (nl & 4) * 32 + g * 16 : L_WGT + 272, wa1 = (nl & 3) == 2 + (g >> 1) ? L_WGT + (nl & 4) * 32 + 64 + g * 16 : L_WGT + 272;
        unsigned cr = rgn + (nl & 3) * 128 + g * 16, r2r = L_RED + lane * 16, cw = rgn + lane * 8;
        unsigned osrc = (unsigned)(WS_OUTP + (rb * 16 + rc) * 16384 + lane * 16) + (np ^ 1u) * 8192u;
        PIN(psrc); PIN(psrc1); PIN(rw2); PIN4(wa0, wa1, cr, r2r); PIN4(cw, oo, osrc, xdst);
        float mlm = 0.f, mll = 0.f;
        if (wv == 0) *LP(unsigned, L_WGT + 256 + lane * 4) = 0u;
        wg_barrier();
        const bool early = __all(flags_ok(fb) & (gm[2] == tlo) & (gm[3] == thi));
        u32x2 ga = {gm[0], gm[1]};
        PIN(ga);
        bf16x8 pa[2][4], wh[2];
        {
            u32x4 ml = {0u, 0u, ~tlo, ~thi};
            const unsigned mlsrc = (unsigned)(WS_ML + (bq * 4 + g) * STR + nl * 512 + th * 16) + ((npm >> (x & 6)) & 1u) * 8192u;
            unsigned long long done = 0ull;
            unsigned lm = L_MSK;
            PIN(lm);
#pragma unroll 1
            for (unsigned spins = 0; spins < kSpinCap; ++spins) {
                unsigned long long m;
                if (wv == 0) {
                    if ((ml[2] != tlo) | (ml[3] != thi)) ml = LD16(mlsrc, 0);
                    m = __ballot((ml[2] == tlo) & (ml[3] == thi));
                    unsigned long long f = m & (m >> 4);
                    f &= f >> 2; f &= f >> 1;
                    asm volatile("ds_write_b64 %0, %1" :: "v"(lm), "v"(f) : "memory");
                    m = m == ~0ull ? 0x0101010101010101ull : 0ull;
                } else {
                    unsigned long long t;
                    asm volatile("ds_read_b64 %0, %1\n\ts_waitcnt lgkmcnt(0)" : "=v"(t) : "v"(lm) : "memory");
                    m = ((unsigned long long)(unsigned)__builtin_amdgcn_readfirstlane((unsigned)(t >> 32)) << 32) | (unsigned)__builtin_amdgcn_readfirstlane((unsigned)t);
                    m &= 0x0101010101010101ull & ~done;
                }
                if (m) {
                    done |= m;
#define PLD(bit, off, v, base) "s_bitcmp1_b64 %0, " #bit "\n\ts_cbranch_scc0 1f\n\ts_add_u32 m0, %1, " #off "\n\ts_nop 0\n\tglobal_load_lds_dwordx4 " v ", " base " sc1\n1:\n\t"
                    asm volatile(PLD(0, 0, "%2", "%4") PLD(8, 1024, "%3", "%4") PLD(16, 2048, "%2", "%5") PLD(24, 3072, "%3", "%5")
                                 PLD(32, 4096, "%8", "%6") PLD(40, 5120, "%9", "%6") PLD(48, 6144, "%8", "%7") PLD(56, 7168, "%9", "%7")
                                 :: "s"(m), "s"(rgn), "v"(psrc), "v"(psrc1), "s"(ws), "s"(ws + 1048576), "s"(ws + 2097152), "s"(ws + 3145728), "v"(psrc2), "v"(psrc3) : "memory", "m0", "scc");
                    if (done == 0x0101010101010101ull) break;
                } else if (SLPM > 0 && wv == 0) __builtin_amdgcn_s_sleep(SLPM);
            }
            mlm = __uint_as_float(ml[0]); mll = __uint_as_float(ml[1]);
            STAMP(10);
            if (wv == 0) {
                float M = mlm;
                M = fmaxf(M, dppf<0xB1>(M)); M = fmaxf(M, dppf<0x4E>(M)); M = fmaxf(M, dppf<0x141>(M)); M = fmaxf(M, dppf<0x140>(M));
                const float e = __builtin_amdgcn_exp2f(mlm - M);
                const float w = e * __builtin_amdgcn_rcpf(sum16(e * mll));
                const unsigned short hi = f2bf(w);
                *LP(unsigned short, L_WGT + lane * 2) = hi;
                *LP(unsigned short, L_WGT + 128 + lane * 2) = f2bf(w - bf2f(hi));
            }
            wg_barrier();
            wh[0] = *LP(bf16x8, wa0); wh[1] = *LP(bf16x8, wa1);
            __builtin_amdgcn_sched_barrier(0);
            VMW(0);
#pragma unroll
            for (int ks = 0; ks < 2; ++ks)
#pragma unroll
                for (int t = 0; t < 4; ++t) pa[ks][t] = tr_frag(pfa[t] + ks * 4096, pfa[t] + ks * 4096 + 512);
        }
        STAMP(11);
        {
            f32x4 d[4];
#pragma unroll
            for (int t = 0; t < 4; ++t) d[t] = MFMA16(pa[0][t], wh[0], (f32x4{0.f, 0.f, 0.f, 0.f}));
#pragma unroll
            for (int t = 0; t < 4; ++t) d[t] = MFMA16(pa[1][t], wh[1], d[t]);
#pragma unroll
            for (int t = 0; t < 4; ++t)
#pragma unroll
                for (int e = 0; e < 4; ++e) { float v = d[t][e] + dppf<0x124>(d[t][e]); asm("" : "+v"(v)); d[t][e] = v; }
            const u32x4 o0 = {pk2(d[0][0], d[0][1]), pk2(d[0][2], d[0][3]), pk2(d[1][0], d[1][1]), pk2(d[1][2], d[1][3])};
            const u32x4 o1 = {pk2(d[2][0], d[2][1]), pk2(d[2][2], d[2][3]), pk2(d[3][0], d[3][1]), pk2(d[3][2], d[3][3])};
            f32x4 y[4];
#pragma unroll
            for (int t = 0; t < 4; ++t) y[t] = MFMA16(fuv[0][t], BF8(o0), (f32x4{0.f, 0.f, 0.f, 0.f}));
#pragma unroll
            for (int t = 0; t < 4; ++t) y[t] = MFMA16(fuv[1][t], BF8(o1), y[t]);
            if (nl < 4) {
#pragma unroll
                for (int t = 0; t < 4; ++t) *LP(f32x4, rw2 + t * 64) = y[t];
            }
        }
        wg_barrier();
        {
            f32x4 cs[8];
#pragma unroll
            for (int w2 = 0; w2 < 8; ++w2) cs[w2] = *LP(f32x4, r2r + w2 * 1024);
            const f32x4 c4 = ((cs[0] + cs[1]) + (cs[2] + cs[3])) + ((cs[4] + cs[5]) + (cs[6] + cs[7]));
            *LP(u32x2, cw) = u32x2{pk2(c4[0], c4[1]), pk2(c4[2], c4[3])};
            asm volatile("s_waitcnt lgkmcnt(0)" ::: "memory");
            const u32x4 cx0 = *LP(u32x4, cr), cx1 = *LP(u32x4, cr + 64);
            f32x4 o[4];
#pragma unroll
            for (int q = 0; q < 4; ++q) o[q] = MFMA16(fwo[0][q], BF8(cx0), (f32x4{0.f, 0.f, 0.f, 0.f}));
#pragma unroll
            for (int q = 0; q < 4; ++q) o[q] = MFMA16(fwo[1][q], BF8(cx1), o[q]);
            const f32x4 sel = (nl & 8) ? ((nl & 4) ? o[3] : o[2]) : ((nl & 4) ? o[1] : o[0]);
            u32x4 sv = __builtin_bit_cast(u32x4, sel);
            if (__builtin_expect(mx4(sv, 0u) == SENT, 0)) sv = u32x4{f32s(sel[0]), f32s(sel[1]), f32s(sel[2]), f32s(sel[3])};
            ST16(sv, oo, 0);
        }
        STAMP(12);
        wg_barrier();
        if (wv == 7) {
            if (!early) {
                flags_wait(WS_FB + x * STR + lane * 16);
                ga = mod_get(WS_MOD + rb * STR + rcol * 8);
                PIN(ga);
            }
            u32x4 op[8];
            for (unsigned spins = 0;;) {
#pragma unroll
                for (int k = 0; k < 8; ++k) op[k] = LD16(osrc + (k & 3) * 1024, (k >> 2) * 4096);
                unsigned m = 0u;
#pragma unroll
                for (int k = 0; k < 8; ++k) m = mx4(op[k], m);
                if (__all(m != SENT)) break;
                if (++spins > kSpinCap) break;
                asm volatile("" ::: "memory");
            }
            STAMP(13);
            const f32x4 v = ((__builtin_bit_cast(f32x4, op[0]) + __builtin_bit_cast(f32x4, op[1])) + (__builtin_bit_cast(f32x4, op[2]) + __builtin_bit_cast(f32x4, op[3]))) +
                            ((__builtin_bit_cast(f32x4, op[4]) + __builtin_bit_cast(f32x4, op[5])) + (__builtin_bit_cast(f32x4, op[6]) + __builtin_bit_cast(f32x4, op[7])));
            float w[2];
#pragma unroll
            for (int q = 0; q < 2; ++q) {
                auto t = __builtin_amdgcn_permlane32_swap(__float_as_uint(v[q]), __float_as_uint(v[2 + q]), false, false);
                w[q] = __uint_as_float(t[0]) + __uint_as_float(t[1]);
                auto u = __builtin_amdgcn_permlane16_swap(__float_as_uint(w[q]), __float_as_uint(w[q]), false, false);
                w[q] = __uint_as_float(u[0]) + __uint_as_float(u[1]);
                w[q] += dppf<0x128>(w[q]);
            }
            unsigned t = pk2(w[0], w[1]);
            t = pk2(__uint_as_float(ga[0]) * lo16(t), __uint_as_float(ga[1]) * hi16(t));
            t = pk2s(lo16(xg) + lo16(t), hi16(xg) + hi16(t));
            __builtin_amdgcn_raw_buffer_store_b32(t, rw, xdst, 0, 16);
            __builtin_amdgcn_raw_buffer_store_b32(t, rw, xdst2, 0, 16);
            STAMP(14);
        }
        STAMP_FLUSH();
        return;
    }

    const int s = j - 16;
    if (wv == 0) cnt_load();
    {
        const unsigned ln = (unsigned)(s * 64 + lane);
        if ((PFM & 1) && wv == 0) pf4<NTM & 1>((ln & 511u) * 6144u + (x * 2 + (ln >> 9)) * 128u, p.w_mod, L_SCR);
        if ((PFM & 2) && wv == 1) {
            const unsigned short* w = s < 8 ? p.w_uk : p.w_q;
            pf4<NTM & 1>((ln & 511u) * 8192u + x * 1024u + ((3u - ((unsigned)(size_t)w >> 7)) & 7u) * 128u, w, L_SCR);
        }
        if ((PFM & 48) == 48) {
            const unsigned r0 = (unsigned)(s * 4 + (wv & 3)), row = PFS == 0 ? r0 * 8 + (lane >> 3) : PFS == 1 ? (lane >> 3) * 64 + r0 : (r0 >> 5) * 256 + (lane >> 3) * 32 + (r0 & 31);
            pf4<NTM & 1>(row * 8192u + x * 1024u + (lane & 7u) * 128u, wv < 4 ? p.w_uk : p.w_q, L_SCR);
        }
        else if ((PFM & 16) && wv >= 4) { const unsigned l2 = (unsigned)((s * 4 + (wv & 3)) * 64 + lane); pf4<NTM & 1>((l2 >> 3) * 8192u + x * 1024u + (l2 & 7u) * 128u, p.w_q, L_SCR); }
        else if ((PFM & 32) && wv >= 4) { const unsigned l2 = (unsigned)((s * 4 + (wv & 3)) * 64 + lane); pf4<NTM & 1>((l2 >> 3) * 8192u + x * 1024u + (l2 & 7u) * 128u, p.w_uk, L_SCR); }
        if ((PFM & 4) && (wv & 6) == 2) { const unsigned l2 = (unsigned)((s * 2 + (wv & 1)) * 64 + lane); pf4<NTM & 1>((l2 & 511u) * 8192u + (x * 4 + (l2 >> 9)) * 256u, p.w_q, L_SCR); }
    }
    const int ug = x * 7 + s;
    const int wp = wv & 3, ch = wv >> 2, kb = wp * 2 + ch;
    int head = lane & 31;
    unsigned h2 = lane >> 5;
    const unsigned vb = (unsigned)(g * 1024 + ((nl ^ (g << 2)) << 4)), lb = (unsigned)((wv >> 1) * 32768 + (wv & 1) * 4096);
    const char* ck = (const char*)p.ckv + ((size_t)x * SK + s * 256 + wv * 16) * 1024;
    const unsigned rot = ROTC ? (unsigned)s & 3u : 0u;
    const unsigned vbr = vb ^ (rot << 8);
    const char* ck2 = ck + 131072;
    auto ck_dma = [&](unsigned i) { dma16x<NTM & 16>(vbr, ((i & 3u) << 4) | (((i >> 2) & 3u) << 8) | ((i & 3u) << 12), (i >> 4) ? ck2 : ck, lb + ((i >> 2) & 3u) * 8192u + (i & 3u) * 1024u); };
    bf16x8 ka[16];
    s16x4 va[8][4];
    unsigned kad[4], vad[4], vad2[4];
    auto ka_reads = [&](int cb) {
#pragma unroll
        for (int u = 0; u < 4; ++u) ka[cb * 4 + u] = *LP(bf16x8, kad[u] + cb * 8192);
    };
    auto va_reads = [&]() {
#pragma unroll
        for (int kk = 0; kk < 8; ++kk)
#pragma unroll
            for (int q = 0; q < 4; ++q) va[kk][q] = tr4((kk < 4 ? vad[q] : vad2[q]) + ((kk >> 1) & 1) * 32768 + (kk & 1) * 4096);
    };
    auto va_pin = [&]() {
#pragma unroll
        for (int q = 0; q < 8; ++q) PIN4(va[q][0], va[q][1], va[q][2], va[q][3]);
    };
    u32x4 fa = {0u, 0u, 0u, 0u};
    const unsigned fao = (unsigned)(WS_FI + x * STR + (lane & 7) * 16);
    constexpr int LAST = GI + 4 > 19 ? GI + 4 : 19;
    const unsigned qsrc = (unsigned)(WS_Q + (wv & 1) * 4 * STR + (x * 4 + (wv >> 1)) * 1024) + (g ^ rot) * 256 + ((nl ^ ((wv * 4) & 15)) << 4);
    auto q_dma = [&](int k) {
        unsigned t;
        asm volatile("s_mov_b32 m0, %3\n\tv_xor_b32 %0, %4, %1\n\tglobal_load_lds_dwordx4 %0, %2 sc1" : "=&v"(t) : "v"(qsrc), "s"(ws), "s"(L_SCR + (wv * 4 + k) * 1024), "i"(k * (STR + 16)) : "memory", "m0");
    };
#pragma unroll
    for (int i = 0; i <= LAST; ++i) {
        ck_dma(i);
        if (i == 3) {
#pragma unroll
            for (int u = 0; u < 4; ++u) {
                kad[u] = tile_off(wp * 32 + (head ^ (ch * 16)), ch * 8 + 2 * u + h2);
                vad[u] = tile_off(4 * h2 + (nl >> 2) + 8 * (u & 1), wv * 8 + 4 * (u >> 1) + 2 * (g & 1) + ((lane & 3) >> 1)) + 8 * (lane & 1);
                vad2[u] = vad[u] + 65536;
                PIN(kad[u]); PIN(vad[u]); PIN(vad2[u]);
            }
        }
        if (i > GI && i < GI + 4) VMW(8); else if (i < NTH) { if (i >= W0) VMW(W0); } else if (i >= 4) VMW(4);
        if (i == 15 && wv == 0) {
            PIN(fa);
            tag_take();
            if (!__all(flags_ok(fa))) flags_wait(fao);
        }
        if ((i & 3) == 3 && i >= 7 && i <= 19) {
            const int cb = (i - 7) >> 2;
            if (cb > 0) { PIN4(ka[cb * 4 - 4], ka[cb * 4 - 3], ka[cb * 4 - 2], ka[cb * 4 - 1]); va_pin(); }
            wg_barrier();
            ka_reads(cb);
            if ((wv >> 1) == cb) va_reads();
            STAMP(16 + cb);
        }
        if (i == 10 && wv == 0) asm volatile("buffer_load_dwordx4 %0, %1, %2, 0 offen sc1" : "+v"(fa) : "v"(fao), "s"(rw) : "memory");
        if (i == GI && wv == 0) asm volatile("buffer_load_dwordx4 %0, %1, %2, 0 offen offset:%3 sc1" : "+v"(fa) : "v"(fao), "s"(rw), "n"(WS_NP - WS_FI) : "memory");
        if (i == GI) { q_dma(0); q_dma(1); q_dma(2); q_dma(3); }
    }
    const unsigned L_X = L_SCR, L_P = L_SCR + 16384, L_EXM = L_SCR + 24576, L_EXS = L_SCR + 25600, L_BIG = L_SCR + 27648;
    unsigned nx = LAST + 1, npm = 0u;
    unsigned qad[4];
    {
        unsigned qck = L_SCR + wv * 4096 + lane * 16;
        u32x4 qb[4];
#pragma unroll
        for (int k = 0; k < 4; ++k) qb[k] = *LP(u32x4, qck + k * 1024);
#pragma unroll
        for (int k = 0; k < 4; ++k) { qad[k] = L_SCR + head * 1024 + (((ch * 8 + 2 * k + h2) ^ (head & 15)) << 4); PIN(qad[k]); }
        STAMP(20);
        if (!__all(mx4(qb[0], mx4(qb[1], mx4(qb[2], mx4(qb[3], 0u)))) != SENT)) {
            for (unsigned spins = 0;;) {
#pragma unroll
                for (int k = 0; k < 4; ++k)
                    if (!__all(mx4(qb[k], 0u) != SENT)) q_dma(k);
                if (nx <= 25) {
#pragma unroll
                    for (int i = 0; i < 3; ++i) ck_dma(nx + i);
                    nx += 3;
                    VMW(3);
                } else {
                    for (; nx < 28; ++nx) ck_dma(nx);
                    VMW(0);
                }
#pragma unroll
                for (int k = 0; k < 4; ++k) qb[k] = *LP(u32x4, qck + k * 1024);
                if (__all(mx4(qb[0], mx4(qb[1], mx4(qb[2], mx4(qb[3], 0u)))) != SENT)) break;
                if (++spins > kSpinCap) break;
            }
        }
    }
    constexpr int NE = LAST + 1 + SL0 + 5 < 28 ? LAST + 1 + SL0 + 5 : 28;
    auto slot = [&](int k) { if (LAST + 1 + k < 28) { if (nx <= (unsigned)(LAST + 1 + k)) ck_dma(LAST + 1 + k); } };
    auto slot2 = [&](int k) { if (NE + k < 28) { if (nx <= (unsigned)(NE + k)) ck_dma(NE + k); } else if (NE + k < 32) ck_dma(NE + k); };
    unsigned xw = wp * 32768 + 24576 + ch * 2048 + lane * 16, xr = xw ^ 2048u, exw = L_EXM + (head * 8 + wv) * 4, exr = L_EXM + head * 32;
    unsigned pwr = L_P + kb * 1024 + lane * 16, prd = L_P + lane * 16, esw = L_EXS + (head * 16 + h2 * 8 + wv) * 4;
    PIN4(xw, xr, exw, exr); PIN(pwr); PIN(prd); PIN(esw);
    PIN4(ka[12], ka[13], ka[14], ka[15]); va_pin();
    wg_barrier();
    STAMP(21);
    bf16x8 qf[16];
#pragma unroll
    for (int ks = 0; ks < 16; ++ks) qf[ks] = *LP(bf16x8, qad[ks & 3] + (ks >> 2) * 256);
    __builtin_amdgcn_sched_barrier(0);
#pragma unroll
    for (int k = 0; k < SL0; ++k) slot(k);
    __builtin_amdgcn_sched_barrier(0);
    float lsum, mrun;
    f32x16 o[2], sc2;
    auto meet = [&](const f32x16& sc, float (&t)[8]) {
        *LP(f32x4, xw) = f32x4{sc[8], sc[9], sc[10], sc[11]}; *LP(f32x4, xw + 1024) = f32x4{sc[12], sc[13], sc[14], sc[15]};
        wg_barrier();
        const f32x4 x0 = *LP(f32x4, xr), x1 = *LP(f32x4, xr + 1024);
#pragma unroll
        for (int r = 0; r < 4; ++r) { t[r] = sc[r] + x0[r]; t[4 + r] = sc[4 + r] + x1[r]; }
    };
    {
        f32x16 sc;
#pragma unroll
        for (int i = 0; i < 16; ++i) sc[i] = 0.f;
#pragma unroll
        for (int ks = 0; ks < 16; ++ks) {
            sc = MFMA32(ka[ks], qf[ks], sc);
            if ((ks & 3) == 3) slot(SL0 + (ks >> 2));
            __builtin_amdgcn_sched_barrier(0);
        }
        float t[8];
        meet(sc, t);
        {
            const float m = fmax3(fmax3(t[0], t[1], t[2]), fmax3(t[3], t[4], t[5]), fmax3(t[6], t[7], t[7]));
            auto u = __builtin_amdgcn_permlane32_swap(__float_as_uint(m), __float_as_uint(m), false, false);
            *LP(float, exw) = fmax3(__uint_as_float(u[0]), __uint_as_float(u[1]), __uint_as_float(u[1]));
        }
        slot(SL0 + 4);
        VMW(NE > 24 ? NE - 24 : 0);
        wg_barrier();
        const f32x4 a0 = *LP(f32x4, exr), a1 = *LP(f32x4, exr + 16);
        ka_reads(0); ka_reads(1);
        __builtin_amdgcn_sched_barrier(0);
        slot2(0);
        mrun = fmax3(fmax3(a0[0], a0[1], a0[2]), fmax3(a0[3], a1[0], a1[1]), fmax3(a1[2], a1[3], a1[3])) * SCL;
        float e[8];
#pragma unroll
        for (int r = 0; r < 8; ++r) e[r] = __builtin_amdgcn_exp2f(fmaf(t[r], SCL, -mrun));
        *LP(u32x4, pwr) = u32x4{pk2(e[0], e[1]), pk2(e[2], e[3]), pk2(e[4], e[5]), pk2(e[6], e[7])};
        lsum = ((e[0] + e[1]) + (e[2] + e[3])) + ((e[4] + e[5]) + (e[6] + e[7]));
        if (wv == 0) { PIN(fa); *LP(u32x4, L_BIG) = u32x4{0u, np_mask(fa), tlo, thi}; }
        slot2(1);
        wg_barrier();
        STAMP(22);
        bf16x8 pf[8];
#pragma unroll
        for (int kk = 0; kk < 8; ++kk) pf[kk] = *LP(bf16x8, prd + kk * 1024);
#pragma unroll
        for (int i = 0; i < 16; ++i) { o[0][i] = 0.f; o[1][i] = 0.f; sc2[i] = 0.f; }
        __builtin_amdgcn_sched_barrier(0);
#pragma unroll
        for (int ks = 0; ks < 8; ++ks) {
            sc2 = MFMA32(ka[ks], qf[ks], sc2);
            if (ks & 1) slot2(2 + (ks >> 1));
            __builtin_amdgcn_sched_barrier(0);
        }
#pragma unroll
        for (int kk = 0; kk < 8; ++kk) {
            o[0] = MFMA32(cat8(va[kk][0], va[kk][1]), pf[kk], o[0]);
            o[1] = MFMA32(cat8(va[kk][2], va[kk][3]), pf[kk], o[1]);
            if (kk < 4) slot2(6 + kk);
            __builtin_amdgcn_sched_barrier(0);
        }
    }
    STAMP(23);
    rederive();
    head = lane & 31; h2 = lane >> 5;
    unsigned sw[4], srd = lb + (lane >> 3) * 128 + (((lane & 7) ^ (lane >> 3)) << 4);
#pragma unroll
    for (int q = 0; q < 4; ++q) sw[q] = lb + head * 128 + (((2 * q + h2) ^ (head & 7)) << 4);
    PIN4(sw[0], sw[1], sw[2], sw[3]);
    xw = L_X + wv * 2048 + lane * 16; xr = xw ^ 8192u;
    PIN(xw); PIN(xr);
    {
        const u32x4 q = *LP(u32x4, L_BIG);
        npm = __builtin_amdgcn_readfirstlane(q[1]); tlo = __builtin_amdgcn_readfirstlane(q[2]); thi = __builtin_amdgcn_readfirstlane(q[3]);
    }
    unsigned po[4];
    {
        const unsigned cw = wv ^ (rot * 2), p0 = (unsigned)(WS_PART + (x * 32 + (lane >> 3)) * 32768 + (s ^ (lane >> 3)) * 128 + (lane & 7) * 16) + (cw >> 2) * 16384 + (cw & 3) * 2048;
#pragma unroll
        for (int i = 0; i < 4; ++i) po[i] = p0 + i * 262144 + (((npm >> (2 * i + (lane >> 5))) ^ (x >> 1)) & 1u) * 8192u;
    }
    unsigned mlo = (unsigned)(WS_ML + x * STR + s * 512 + head * 16) + ((npm >> ((head >> 3) * 2)) & 1u) * 8192u, esr = L_EXS + head * 64;
    PIN4(po[0], po[1], po[2], po[3]); PIN(esr); PIN(srd); PIN(mlo);
    {
        f32x16& sc = sc2;
        if (wv < 4) va_reads();
        VMW(4);
        wg_barrier();
        ka_reads(2);
        if ((wv >> 1) == 2) va_reads();
        __builtin_amdgcn_sched_barrier(0);
#pragma unroll
        for (int ks = 8; ks < 12; ++ks) sc = MFMA32(ka[ks], qf[ks], sc);
        __builtin_amdgcn_sched_barrier(0);
        VMW(0);
        wg_barrier();
        STAMP(24);
        ka_reads(3);
        __builtin_amdgcn_sched_barrier(0);
#pragma unroll
        for (int ks = 12; ks < 16; ++ks) sc = MFMA32(ka[ks], qf[ks], sc);
        float t[8];
        meet(sc, t);
        float e[8];
        const float mloc = fmax3(fmax3(t[0], t[1], t[2]), fmax3(t[3], t[4], t[5]), fmax3(t[6], t[7], t[7]));
#pragma unroll
        for (int r = 0; r < 8; ++r) e[r] = __builtin_amdgcn_exp2f(fmaf(t[r], SCL, -mrun));
        *LP(u32x4, pwr) = u32x4{pk2(e[0], e[1]), pk2(e[2], e[3]), pk2(e[4], e[5]), pk2(e[6], e[7])};
        *LP(float, esw) = lsum + (((e[0] + e[1]) + (e[2] + e[3])) + ((e[4] + e[5]) + (e[6] + e[7])));
        if (__builtin_expect(!(fmaf(mloc, SCL, -mrun) <= BIGD), 0)) *LP(unsigned, L_BIG) = 1u;
        va_pin();
        wg_barrier();
        STAMP(25);
        unsigned big = *LP(unsigned, L_BIG);
        if ((wv >> 1) == 3) va_reads();
        bf16x8 pf[8];
#pragma unroll
        for (int kk = 0; kk < 8; ++kk) pf[kk] = *LP(bf16x8, prd + kk * 1024);
        __builtin_amdgcn_sched_barrier(0);
        if (__builtin_expect(__builtin_amdgcn_readfirstlane(big) != 0, 0)) {
            auto u = __builtin_amdgcn_permlane32_swap(__float_as_uint(mloc), __float_as_uint(mloc), false, false);
            *LP(float, exw) = fmaxf(__uint_as_float(u[0]), __uint_as_float(u[1]));
#pragma unroll
            for (int kk = 0; kk < 8; kk += 4) PIN4(pf[kk], pf[kk + 1], pf[kk + 2], pf[kk + 3]);
            wg_barrier();
            const f32x4 a0 = *LP(f32x4, exr), a1 = *LP(f32x4, exr + 16);
            const float mnew = fmaxf(fmaxf(fmaxf(fmaxf(a0[0], a0[1]), fmaxf(a0[2], a0[3])), fmaxf(fmaxf(a1[0], a1[1]), fmaxf(a1[2], a1[3]))) * SCL, mrun);
#pragma unroll
            for (int r = 0; r < 8; ++r) e[r] = __builtin_amdgcn_exp2f(fmaf(t[r], SCL, -mnew));
            *LP(u32x4, pwr) = u32x4{pk2(e[0], e[1]), pk2(e[2], e[3]), pk2(e[4], e[5]), pk2(e[6], e[7])};
            const float al = __builtin_amdgcn_exp2f(mrun - mnew);
            *LP(float, esw) = lsum * al + (((e[0] + e[1]) + (e[2] + e[3])) + ((e[4] + e[5]) + (e[6] + e[7])));
            mrun = mnew;
            o[0] *= al; o[1] *= al;
            wg_barrier();
#pragma unroll
            for (int kk = 0; kk < 8; ++kk) pf[kk] = *LP(bf16x8, prd + kk * 1024);
        }
        auto stage = [&](int t2, int gp) {
            const unsigned lo0 = pk2(o[t2][8 * gp + 0], o[t2][8 * gp + 1]), lo1 = pk2(o[t2][8 * gp + 2], o[t2][8 * gp + 3]);
            const unsigned hi0 = pk2(o[t2][8 * gp + 4], o[t2][8 * gp + 5]), hi1 = pk2(o[t2][8 * gp + 6], o[t2][8 * gp + 7]);
            auto r0 = __builtin_amdgcn_permlane32_swap(lo0, hi0, false, false);
            auto r1 = __builtin_amdgcn_permlane32_swap(lo1, hi1, false, false);
            *LP(u32x4, sw[t2 * 2 + gp]) = u32x4{r0[0], r1[0], r0[1], r1[1]};
        };
#pragma unroll
        for (int kk = 0; kk < 8; ++kk) o[0] = MFMA32(cat8(va[kk][0], va[kk][1]), pf[kk], o[0]);
        __builtin_amdgcn_sched_barrier(0);
#pragma unroll
        for (int kk = 0; kk < 8; ++kk) {
            o[1] = MFMA32(cat8(va[kk][2], va[kk][3]), pf[kk], o[1]);
            if (kk == 2) { PIN(o[0]); stage(0, 0); }
            if (kk == 4) stage(0, 1);
            __builtin_amdgcn_sched_barrier(0);
        }
        stage(1, 0); stage(1, 1);
        asm volatile("s_waitcnt lgkmcnt(0)" ::: "memory");
        u32x4 w[4];
#pragma unroll
        for (int i = 0; i < 4; ++i) w[i] = *LP(u32x4, srd + i * 1024);
#pragma unroll
        for (int i = 0; i < 4; ++i) ST16(w[i], po[i], 0);
        __builtin_amdgcn_sched_barrier(0);
        STAMP(26);
        u32x4 mlrec;
        {
            const f32x4 b0 = *LP(f32x4, esr), b1 = *LP(f32x4, esr + 16), b2 = *LP(f32x4, esr + 32), b3 = *LP(f32x4, esr + 48);
            const f32x4 bs = (b0 + b1) + (b2 + b3);
            mlrec = u32x4{__float_as_uint(mrun), __float_as_uint((bs[0] + bs[1]) + (bs[2] + bs[3])), tlo, thi};
            PIN(mlrec);
        }
        VMW(0);
        wg_barrier();
        if (wv == 0 && lane < 32) ST16(mlrec, mlo, 0);
        STAMP(27);
        wg_barrier();
    }
    const unsigned long long t_att = __builtin_amdgcn_s_memrealtime();
    rederive();
    while (__builtin_amdgcn_s_memrealtime() < t_att + (s >= 11 ? MD0 : MD1)) __builtin_amdgcn_s_sleep(2);
    if (s >= 11) {
        const unsigned n = (unsigned)((s - 11) * 8 + wv);
        if (n < 28) pf4<NTM & 32>((n * 64 + lane) * 1024u + x * 128u, p.w2, 0);
        { const unsigned l2 = n * 64 + lane; pf4<NTM & 32>((l2 / 5u) * (DFF * 2) + min(((unsigned)x * 448u & ~127u) + (l2 % 5u) * 128u, DFF * 2 - 4u), p.w1, 0); }
        if (wv == 0) flag_put(WS_FA, id - 128);
        STAMP_FLUSH();
        return;
    }
    const unsigned L_H2S = L_SCR, L_REDM = L_SCR + 16384;
    if (s < 7) {
        {
            const char* w1s = (const char*)(p.w1 + (size_t)wv * 64 * DFF + ug * 32);
#pragma unroll
            for (int i = 0; i < 4; ++i) dma16<NTM & 32>((unsigned)((lane >> 2) * DFF * 2 + (lane & 3) * 16), w1s + (size_t)i * 16 * DFF * 2, wv * 4096 + i * 1024);
        }
        const int b = wv;
        u32x4 md[4];
        {
            if (wv == 0) ST16(s4, (unsigned)(WS_U + x * STR + (lane >> 5) * 8192 + s * 512 + (lane & 31) * 16), 0);
            u32x4 mv[8], fb;
            for (unsigned spins = 0;;) {
                fb = LD16((unsigned)(WS_FB + x * STR + lane * 16), 0);
#pragma unroll
                for (int k = 0; k < 8; ++k) mv[k] = LD16((unsigned)(WS_MOD + b * STR + (256 + (k >> 2) * 256 + lane * 4 + (k & 3)) * 16), 0);
                unsigned bad = (fb[0] ^ tlo) | (fb[1] ^ thi) | (fb[2] ^ tlo) | (fb[3] ^ thi);
#pragma unroll
                for (int k = 0; k < 8; ++k) bad |= (mv[k][2] ^ tlo) | (mv[k][3] ^ thi);
                if (__all(bad == 0u)) break;
                __builtin_amdgcn_s_sleep(16);
                if (++spins > kSpinCap) break;
                asm volatile("" ::: "memory");
            }
#pragma unroll
            for (int k = 0; k < 4; ++k) md[k] = u32x4{mv[2 * k][0], mv[2 * k][1], mv[2 * k + 1][0], mv[2 * k + 1][1]};
        }
        VMW(0);
        if (wv == 0) flag_put(WS_FA, id - 128);
        wg_barrier();
        bf16x8 wk0[8], wk1[8];
        if (wv >= 6) {
#pragma unroll
            for (int q = 0; q < 8; ++q) {
                const unsigned a = (unsigned)(q * 4096 + (g * 8 + (nl >> 2)) * 64 + (wv & 1) * 32 + (lane & 3) * 8);
                wk0[q] = tr_frag(a, a + 256); wk1[q] = tr_frag(a + 2048, a + 2304);
            }
#pragma unroll
            for (int q = 0; q < 8; q += 2) PIN4(wk0[q], wk0[q + 1], wk1[q], wk1[q + 1]);
        }
        float scv[8];
#pragma unroll
        for (int q = 0; q < 8; ++q) { scv[q] = rbf(1.f + __uint_as_float(q < 4 ? md[2][q] : md[3][q - 4])); PIN(scv[q]); }
        unsigned har = L_H2S + ((lane & 7) * HS_LD + g * 8 + (nl >> 3) * 32) * 2, haw = L_H2S + (b * HS_LD + lane * 8) * 2;
        unsigned udst = (unsigned)(WS_U + x * STR + (nl >> 3) * 8192 + (s * 2 + (wv & 1)) * 256 + ((nl & 7) * 16 + g * 4) * 2), xsrc = (unsigned)(WS_X1 + x * STR + (b * 512 + lane * 8) * 2) + ((npm >> x) & 1u) * 8192u;
        PIN4(har, haw, udst, xsrc); PIN(md[0]); PIN(md[1]);
        STAMP(28);
        while (__builtin_amdgcn_s_memrealtime() < t_att + XGATE) __builtin_amdgcn_s_sleep(2);
        u32x4 xv;
        for (unsigned spins = 0;;) {
            xv = LD16(xsrc, 0);
            if (__all(mx4(xv, 0u) != SENT)) break;
            __builtin_amdgcn_s_sleep(SLPX);
            if (++spins > kSpinCap) break;
            asm volatile("" ::: "memory");
        }
        STAMP(29);
        {
            f32x2_t v[4];
#pragma unroll
            for (int q = 0; q < 4; ++q) v[q] = f32x2_t{lo16(xv[q]), hi16(xv[q])};
            const f32x2_t p1 = (v[0] + v[1]) + (v[2] + v[3]), p2 = (v[0] * v[0] + v[1] * v[1]) + (v[2] * v[2] + v[3] * v[3]);
            float mb, rden;
            ln_stats(p1[0] + p1[1], p2[0] + p2[1], mb, rden);
            u32x4 hr;
#pragma unroll
            for (int q = 0; q < 4; ++q) {
                const f32x2_t h = ((v[q] - mb) * rden) * f32x2_t{scv[2 * q], scv[2 * q + 1]} + f32x2_t{__uint_as_float(md[q >> 1][(q & 1) * 2]), __uint_as_float(md[q >> 1][(q & 1) * 2 + 1])};
                hr[q] = pk2(h[0], h[1]);
            }
            *LP(u32x4, haw) = hr;
        }
        wg_barrier();
        if (wv >= 6) {
            bf16x8 af[8];
#pragma unroll
            for (int q = 0; q < 8; ++q) af[q] = *LP(bf16x8, har + q * 128);
            __builtin_amdgcn_sched_barrier(0);
            f32x4 d0 = {0.f, 0.f, 0.f, 0.f}, d1 = {0.f, 0.f, 0.f, 0.f};
#pragma unroll
            for (int q = 0; q < 8; ++q) { d0 = MFMA16(wk0[q], af[q], d0); d1 = MFMA16(wk1[q], af[q], d1); }
            const f32x4 own = nl < 8 ? d0 : d1;
#pragma unroll
            for (int e = 0; e < 4; ++e) { float v = own[e] + dppf<0x128>(own[e]); asm("" : "+v"(v)); d0[e] = fmaxf(v, 0.f); }
            u32x2 uv = {pk2(d0[0], d0[1]), pk2(d0[2], d0[3])};
            if (__builtin_expect(max(uv[0], uv[1]) == SENT, 0)) uv = u32x2{pk2s(d0[0], d0[1]), pk2s(d0[2], d0[3])};
            __builtin_amdgcn_raw_buffer_store_b64(uv, rw, udst, 0, 16);
            STAMP(30);
        }
        STAMP_FLUSH();
        return;
    }
    {
        const int n0 = (x * 4 + (s - 7)) * 16;
        const char* mws = (const char*)(p.w2 + (size_t)wv * 224 * 512 + n0);
#pragma unroll
        for (int i = 0; i < 7; ++i) dma16<NTM & 32>((unsigned)((lane >> 1) * 1024 + (lane & 1) * 16), mws + i * 32768, rgn + i * 1024);
        const int fb = lane >> 3, fc = (lane & 7) * 2;
        u32x2 gb = {0u, 0u};
        if (wv == 0) {
            flag_put(WS_FA, id - 128);
            gb = mod_get(WS_MOD + fb * STR + (1536 + n0 + fc) * 8);
            flags_wait(WS_FB + x * STR + lane * 16);
            flags_wait(WS_FA + x * STR + lane * 16);
            if (id == 23 * 8 && lane == 0) __builtin_amdgcn_raw_buffer_store_b64(u32x2{tlo, thi}, rw, WS_EPOCH, 0, 16);
            PIN(gb);
        }
        VMW(0);
        bf16x8 wfr[7];
        const unsigned trl = (unsigned)(g * 8 + (nl >> 2)) * 32u + (unsigned)(lane & 3) * 8u;
#pragma unroll
        for (int ks = 0; ks < 7; ++ks) { wfr[ks] = tr_frag(rgn + ks * 1024 + trl, rgn + ks * 1024 + trl + 128); PIN(wfr[ks]); }
        unsigned usrc = (unsigned)(WS_U + wv * STR + (g >> 1) * 256 + (nl & 7) * 32 + (g & 1) * 16) + ((npm >> x) & 1u) * 8192u;
        unsigned rdw = L_REDM + wv * 512 + (nl & 7) * 64 + g * 16, rdr = L_REDM + lane * 8;
        unsigned short* outp = p.out + fb * 512 + n0 + fc;
        PIN(usrc); PIN(rdw); PIN(rdr); PIN(outp);
        STAMP(28);
        unsigned xg = 0u;
        if (wv == 0) {
            while (__builtin_amdgcn_s_memrealtime() < t_att + XGATE) __builtin_amdgcn_s_sleep(2);
            for (unsigned spins = 0;;) {
                xg = __builtin_amdgcn_raw_buffer_load_b32(rw, (unsigned)(WS_X1 + x * STR + (fb * 512 + n0 + fc) * 2) + ((npm >> x) & 1u) * 8192u, 0, 16);
                if (__all(xg != SENT)) break;
                __builtin_amdgcn_s_sleep(SLPD);
                if (++spins > kSpinCap) break;
                asm volatile("" ::: "memory");
            }
            __builtin_amdgcn_s_sleep(SLPG);
        }
        wg_barrier();
        u32x4 uf[7];
        for (unsigned spins = 0;;) {
#pragma unroll
            for (int ks = 0; ks < 7; ++ks) uf[ks] = LD16(usrc + ks * 512, 0);
            unsigned m = 0u;
#pragma unroll
            for (int ks = 0; ks < 7; ++ks) m = mx4(uf[ks], m);
            if (__all(m != SENT)) break;
            if (++spins > kSpinCap) break;
            asm volatile("" ::: "memory");
        }
        STAMP(29);
        f32x4 d = {0.f, 0.f, 0.f, 0.f}, d2 = {0.f, 0.f, 0.f, 0.f};
#pragma unroll
        for (int ks = 0; ks < 7; ++ks) {
            if (ks & 1) d2 = MFMA16(wfr[ks], BF8(uf[ks]), d2);
            else d = MFMA16(wfr[ks], BF8(uf[ks]), d);
        }
        *LP(f32x4, rdw) = d + d2;
        wg_barrier();
        if (wv == 0) {
            f32x2_t c[8];
#pragma unroll
            for (int w2 = 0; w2 < 8; ++w2) c[w2] = *LP(f32x2_t, rdr + w2 * 512);
            const f32x2_t sm = ((c[0] + c[1]) + (c[2] + c[3])) + ((c[4] + c[5]) + (c[6] + c[7]));
            unsigned t = pk2(sm[0], sm[1]);
            t = pk2(__uint_as_float(gb[0]) * lo16(t), __uint_as_float(gb[1]) * hi16(t));
            *(GAS unsigned*)outp = pk2(lo16(xg) + lo16(t), hi16(xg) + hi16(t));
            STAMP(31);
        }
        STAMP_FLUSH();
    }
}

extern "C" void kernel_launch(void* const* d_in, const int* in_sizes, int n_in,
                              void* d_out, int out_size, void* d_ws, size_t ws_size,
                              hipStream_t stream) {
    Params p;
    p.x = (const unsigned short*)d_in[0];
    p.ckv = (const unsigned short*)d_in[1];
    p.w_q = (const unsigned short*)d_in[2];
    p.w_uk = (const unsigned short*)d_in[3];
    p.w_uv = (const unsigned short*)d_in[4];
    p.w_o = (const unsigned short*)d_in[5];
    p.w1 = (const unsigned short*)d_in[7];
    p.w2 = (const unsigned short*)d_in[8];
    p.c = (const unsigned short*)d_in[9];
    p.w_mod = (const unsigned short*)d_in[10];
    p.ws = (char*)d_ws;
    p.out = (unsigned short*)d_out;
    (void)hipFuncSetAttribute((const void*)k_main, hipFuncAttributeMaxDynamicSharedMemorySize, 163840);
    k_main<<<256, 512, 163840, stream>>>(p);
}
```

```cpp
#include <hip/hip_runtime.h>
#include <hip/hip_bf16.h>
#include <stdint.h>

#define NH 32
#define SK 4096
#define DFF 1792
#define GI 19
#define ROTC 1
#define PFS 2
#define XNO 0
#define CPJ 7
#define NTM 16
#define QHELP 4
#define HP 13
#define NTH 0
#define W0 2
#define SL0 1
#define LMPF 0
#define SLPB 20
#define MD0 0
#define MD1 0
#define NEARP 1
#define NCAL 2
#define TLBW 0
#define TWD 680
#define LMD 250
#define PFM 57
#define CPFE 34200
#define SLPM 0
#define SLPD 12
#define SLPG 8
#define SLPX 1
#define SLPH 4

using u16x8 = __attribute__((ext_vector_type(8))) unsigned short;
using s16x4 = __attribute__((ext_vector_type(4))) short;
using bf16x8 = __attribute__((ext_vector_type(8))) short;
using f32x16 = __attribute__((ext_vector_type(16))) float;
using f32x4 = __attribute__((ext_vector_type(4))) float;
using u32x4 = __attribute__((ext_vector_type(4))) unsigned;
using u32x2 = __attribute__((ext_vector_type(2))) unsigned;

#define GAS __attribute__((address_space(1)))
#define LAS __attribute__((address_space(3)))
typedef GAS unsigned long long gu64;
#define RLX_AGENT __ATOMIC_RELAXED, __HIP_MEMORY_SCOPE_AGENT
#define SENT 0xffffffffu
#define TAGC 0x9e3779b9u

__device__ __forceinline__ float bf2f(unsigned short u) { return __uint_as_float(((unsigned)u) << 16); }
typedef __attribute__((ext_vector_type(2))) __bf16 bf16x2_t;
typedef __attribute__((ext_vector_type(2))) float f32x2_t;
__device__ __forceinline__ unsigned pk2(float a, float b) {
    f32x2_t v = {a, b};
    bf16x2_t r = __builtin_convertvector(v, bf16x2_t);
    return __builtin_bit_cast(unsigned, r);
}
__device__ __forceinline__ unsigned pk2s(float a, float b) {
    const unsigned r = pk2(a, b);
    return r == SENT ? 0x7fc07fc0u : r;
}
__device__ __forceinline__ unsigned f32s(float a) {
    const unsigned r = __float_as_uint(a);
    return r == SENT ? 0x7fc00000u : r;
}
__device__ __forceinline__ unsigned short f2bf(float f) { return (unsigned short)(pk2(f, 0.f) & 0xffffu); }
__device__ __forceinline__ float rbf(float f) { return __uint_as_float(pk2(f, 0.f) << 16); }
__device__ __forceinline__ float lo16(unsigned u) { return __uint_as_float(u << 16); }
__device__ __forceinline__ float hi16(unsigned u) { return __uint_as_float(u & 0xffff0000u); }
__device__ __forceinline__ unsigned umax3(unsigned a, unsigned b, unsigned c) { return max(max(a, b), c); }
__device__ __forceinline__ unsigned mx4(const u32x4& v, unsigned m) { return umax3(v[2], v[3], umax3(v[0], v[1], m)); }
__device__ __forceinline__ float fmax3(float a, float b, float c) { float r; asm("v_max3_f32 %0, %1, %2, %3" : "=v"(r) : "v"(a), "v"(b), "v"(c)); return r; }
template <int C> __device__ __forceinline__ float dppf(float v) { return __int_as_float(__builtin_amdgcn_update_dpp(0, __float_as_int(v), C, 0xf, 0xf, true)); }
__device__ __forceinline__ float sum16(float v) { v += dppf<0xB1>(v); v += dppf<0x4E>(v); v += dppf<0x141>(v); v += dppf<0x140>(v); return v; }

__device__ __forceinline__ void ln_stats(float p1, float p2, float& mb, float& rden) {
    auto t = __builtin_amdgcn_permlane32_swap(__float_as_uint(p1), __float_as_uint(p2), false, false);
    float a = sum16(__uint_as_float(t[0]) + __uint_as_float(t[1]));
    auto t2 = __builtin_amdgcn_permlane16_swap(__float_as_uint(a), __float_as_uint(a), false, false);
    a = (__uint_as_float(t2[0]) + __uint_as_float(t2[1])) * (1.f / 512.f);
    auto t3 = __builtin_amdgcn_permlane32_swap(__float_as_uint(a), __float_as_uint(a), false, false);
    const float m = __uint_as_float(t3[0]);
    const float var = rbf(fmaxf(fmaf(-m, m, __uint_as_float(t3[1])), 0.f));
    mb = rbf(m);
    rden = __builtin_amdgcn_rcpf(rbf(__builtin_amdgcn_sqrtf(rbf(var + 1e-5f))));
}
#define STR 262144
#define WS_Q 0
#define WS_H 32768
#define WS_X1 65536
#define WS_ML 81920
#define WS_MOD 98304
#define WS_MB 139264
#define WS_U 114688
#define WS_FI 131072
#define WS_FA 132096
#define WS_FB 133120
#define WS_NP 134144
#define WS_EPOCH 135168
#define WS_CAL 147456
#define WS_OUTP 2097152
#define WS_PART 4194304
#define WS_END 12582912

#define LP(T, off) ((LAS T*)(size_t)(unsigned)(off))
#define STAMP(k) do {} while (0)
#define STAMP_FLUSH() do {} while (0)
__device__ __forceinline__ s16x4 tr4(unsigned a) { return __builtin_amdgcn_ds_read_tr16_b64_v4i16(LP(s16x4, a)); }
__device__ __forceinline__ bf16x8 cat8(s16x4 a, s16x4 b) { return __builtin_shufflevector(a, b, 0, 1, 2, 3, 4, 5, 6, 7); }
__device__ __forceinline__ bf16x8 tr_frag(unsigned a0, unsigned a1) { return cat8(tr4(a0), tr4(a1)); }
__device__ __forceinline__ unsigned swz(unsigned row) { return ((row & 3) << 2) | ((row >> 2) & 3); }
__device__ __forceinline__ unsigned off_b(unsigned row, unsigned ch) { return 256u * row + 16u * (ch ^ swz(row)); }
__device__ __forceinline__ unsigned tile_off(unsigned key, unsigned chunk) { return ((key >> 5) * 4 + (chunk >> 4)) * 8192u + off_b(key & 31, chunk & 15); }
__device__ __forceinline__ void wg_barrier() { asm volatile("s_waitcnt lgkmcnt(0)\n\ts_barrier" ::: "memory"); }
template <int NT> __device__ __forceinline__ void dma16(unsigned voff, const void* sbase, unsigned lds_dst) {
    if (NT) asm volatile("s_mov_b32 m0, %2\n\ts_nop 0\n\tglobal_load_lds_dwordx4 %0, %1 nt" :: "v"(voff), "s"(sbase), "s"(lds_dst) : "memory", "m0");
    else asm volatile("s_mov_b32 m0, %2\n\ts_nop 0\n\tglobal_load_lds_dwordx4 %0, %1" :: "v"(voff), "s"(sbase), "s"(lds_dst) : "memory", "m0");
}
template <int NT> __device__ __forceinline__ void dma16x(unsigned voff, unsigned vx, const void* sbase, unsigned lds_dst) {
    unsigned t;
#define DMAX(pol, c) asm volatile("s_mov_b32 m0, %3\n\tv_xor_b32 %0, %4, %1\n\tglobal_load_lds_dwordx4 %0, %2" pol : "=&v"(t) : "v"(voff), "s"(sbase), "s"(lds_dst), c(vx) : "memory", "m0")
    if (__builtin_constant_p(vx)) { if (NT) DMAX(" nt", "i"); else DMAX("", "i"); }
    else { if (NT) DMAX(" nt", "s"); else DMAX("", "s"); }
}
template <int NT> __device__ __forceinline__ void pf4(unsigned voff, const void* sbase, unsigned lds_dst) {
    if (NT) asm volatile("s_mov_b32 m0, %2\n\ts_nop 0\n\tglobal_load_lds_dword %0, %1 nt" :: "v"(voff), "s"(sbase), "s"(lds_dst) : "memory", "m0");
    else asm volatile("s_mov_b32 m0, %2\n\ts_nop 0\n\tglobal_load_lds_dword %0, %1" :: "v"(voff), "s"(sbase), "s"(lds_dst) : "memory", "m0");
}
#define XGATE 300
#define SPINCAP (1u << 20)
constexpr unsigned kSpinCap = SPINCAP;
struct Params {
    const unsigned short* x;
    const unsigned short* ckv;
    const unsigned short* w_q;
    const unsigned short* w_uk;
    const unsigned short* w_uv;
    const unsigned short* w_o;
    const unsigned short* w1;
    const unsigned short* w2;
    const unsigned short* c;
    const unsigned short* w_mod;
    char* ws;
    unsigned short* out;
};
#define VMW(n) asm volatile("s_waitcnt vmcnt(%0)" :: "n"(n) : "memory")
#define PIN(x) asm volatile("" : "+v"(x))
#define PIN4(a, b, c, d) asm volatile("" : "+v"(a), "+v"(b), "+v"(c), "+v"(d) :: "memory")
#define MFMA16(a, b, c) __builtin_amdgcn_mfma_f32_16x16x32_bf16(a, b, c, 0, 0, 0)
#define MFMA32(a, b, c) __builtin_amdgcn_mfma_f32_32x32x16_bf16(a, b, c, 0, 0, 0)
#define BF8(x) __builtin_bit_cast(bf16x8, x)
#define ST16(v, off, soff) __builtin_amdgcn_raw_buffer_store_b128(v, rw, off, soff, 16)
#define LD16(off, soff) __builtin_amdgcn_raw_buffer_load_b128(rw, off, soff, 16)
#define SCL (0.08838834764831845f * 1.4426950408889634f)
#define BIGD 60.f
#define L_SCR 131072
#define HS_LD 520

__global__ __launch_bounds__(512, 2) void k_main(Params p) {
    unsigned long long pc;
    asm volatile("s_getpc_b64 %0" : "=s"(pc));
    const int id = blockIdx.x, tid = threadIdx.x;
    int lane = tid & 63;
    const int wv = __builtin_amdgcn_readfirstlane(tid >> 6);
    int g = lane >> 4, nl = lane & 15;
    auto rederive = [&]() {
        asm volatile("v_mbcnt_lo_u32_b32 %0, -1, 0\n\tv_mbcnt_hi_u32_b32 %0, -1, %0" : "=v"(lane));
        g = lane >> 4; nl = lane & 15;
    };
    const int x = id & 7, j = id >> 3;
    if (wv < 5 && j == 15) {
        asm volatile("s_mov_b32 m0, %2\n\ts_nop 0\n\tglobal_load_lds_dword %0, %1" :: "v"(min((unsigned)((wv * 64 + lane) * 128), (unsigned)CPFE)), "s"(pc), "s"(L_SCR + 31744) : "memory", "m0");
    }
    if ((j >> 2) == 3) {
        dma16<0>((unsigned)(((nl & 7) * 512 + (nl >> 3) * 32 + g * 8) * 2 + wv * 128), p.c, wv * 16384 + 8192);
        dma16<0>((unsigned)tid * 16u, p.x, wv * 16384 + 9216);
    }
    char* ws = p.ws;
    STAMP(0);
    const __amdgpu_buffer_rsrc_t rw = __builtin_amdgcn_make_buffer_rsrc(ws, 0, WS_END, 0x00020000);
    const u32x4 s4 = {SENT, SENT, SENT, SENT};
    unsigned long long cnt = 0ull;
    auto cnt_load = [&]() { asm volatile("global_load_dwordx2 %0, %1, %2 sc1" : "+v"(cnt) : "v"((unsigned)WS_EPOCH), "s"(ws) : "memory"); };
    unsigned tlo = 0u, thi = 0u;
    auto tag_take = [&]() {
        asm volatile("" : "+v"(cnt) :: "memory");
        const unsigned long long t = cnt + 1ull;
        tlo = __builtin_amdgcn_readfirstlane((unsigned)t); thi = __builtin_amdgcn_readfirstlane((unsigned)(t >> 32));
    };
    auto flag_put = [&](unsigned base, unsigned idx) {
        if (lane < 8) __builtin_amdgcn_raw_buffer_store_b64(u32x2{tlo, thi}, rw, base + lane * STR + idx * 8, 0, 16);
    };
    auto mod_get = [&](unsigned off) {
        u32x4 v;
        for (unsigned spins = 0;;) {
            v = LD16(off, 0);
            if (__all((v[2] == tlo) & (v[3] == thi))) break;
            __builtin_amdgcn_s_sleep(16);
            if (++spins > kSpinCap) break;
            asm volatile("" ::: "memory");
        }
        return u32x2{v[0], v[1]};
    };
    auto np_mask = [&](u32x4 v) {
        for (unsigned spins = 0; !__all((v[2] == tlo) & (v[3] == thi)) && spins < kSpinCap; ++spins) {
            __builtin_amdgcn_s_sleep(8);
            asm volatile("buffer_load_dwordx4 %0, %1, %2, 0 offen sc1\n\ts_waitcnt vmcnt(0)" : "=v"(v) : "v"((unsigned)(WS_NP + x * STR + (lane & 7) * 16)), "s"(rw) : "memory");
        }
        const int d = (int)v[0];
        int t = (lane & 4) ? -d : d;
        t += __builtin_amdgcn_update_dpp(0, t, 0xB1, 0xf, 0xf, true);
        t += __builtin_amdgcn_update_dpp(0, t, 0x4E, 0xf, 0xf, true);
        t += __builtin_amdgcn_update_dpp(0, t, 0x141, 0xf, 0xf, true);
        const bool n1 = abs(d) > 160 ? d < 0 : (t < 0) != ((lane & 4) != 0);
        return NEARP ? (unsigned)__ballot(n1) & 0xffu : 0u;
    };
    auto flags_ok = [&](const u32x4& f) { return (f[0] == tlo) & (f[1] == thi) & (f[2] == tlo) & (f[3] == thi); };
    auto flags_wait = [&](unsigned off) {
        for (unsigned spins = 0;;) {
            u32x4 f;
            asm volatile("buffer_load_dwordx4 %0, %1, %2, 0 offen sc1\n\ts_waitcnt vmcnt(0)" : "=v"(f) : "v"(off), "s"(rw) : "memory");
            if (__all(flags_ok(f))) break;
            __builtin_amdgcn_s_sleep(16);
            if (++spins > kSpinCap) break;
            asm volatile("" ::: "memory");
        }
    };
    const unsigned pcl = (unsigned)(((((lane & 7) >> 1) ^ ((lane >> 4) & 1)) * 32) + (lane & 1) * 16);
    auto dma_piece = [&](const unsigned short* src, unsigned ldb, unsigned dst, int n, int nt) {
        const unsigned v0 = (lane >> 3) * ldb + pcl, v1 = v0 ^ 64u;
#pragma unroll
        for (int i = 0; i < n; ++i) {
            if (nt) dma16<1>((i & 1) ? v1 : v0, (const char*)src + (size_t)i * 8 * ldb, dst + i * 1024);
            else dma16<0>((i & 1) ? v1 : v0, (const char*)src + (size_t)i * 8 * ldb, dst + i * 1024);
        }
    };
    const unsigned frl = (unsigned)((g * 8 + (nl >> 2)) * 128 + (lane & 3) * 8), frs = (unsigned)((nl >> 3) | ((g & 1) << 1));
    auto piece_frag = [&](unsigned base, int ks, int t) {
        const unsigned a = base + frl + ((t ^ frs) << 5) + ks * 4096;
        return tr_frag(a, a + 512);
    };
    const unsigned rgn = (unsigned)(wv * 16384);

    if (j < 16) {
        const unsigned long long t_start = __builtin_amdgcn_s_memrealtime();
        const int th = x * 4 + (j & 3);
        if (j < 12) {
            const int qt = j >> 2, rot = qt * 5;
            const bool pub = wv < (qt < 2 ? 3 : 2);
            const unsigned qo = (unsigned)(WS_Q + (th & 7) * STR + ((nl & 7) * 4 + (th >> 3)) * 1024 + (qt * 192 + wv * 64 + g * 16 + (nl >> 3) * 8) * 2);
            const unsigned vb = (unsigned)(g * 8192 + ((nl ^ (g << 2)) << 4));
            const char* wuk = (const char*)(p.w_uk + th * 128) + (size_t)qt * 192 * 8192;
            const char* wq = (const char*)(p.w_q + th * 128) + (size_t)wv * 32768;
            const unsigned L_U = 114688;
            {
                const int k0 = qt < 2 ? wv * 6 : wv * 4;
#pragma unroll
                for (int i = 0; i < 6; ++i) {
                    if (i < 4 || qt < 2) dma16<NTM & 2>(vb ^ (((k0 + i) & 3) << 4), wuk + (size_t)(k0 + i) * 32768, L_U + (k0 + i) * 1024);
                    if (i == 4) VMW(4);
                }
            }
            const unsigned vq = vb ^ ((wv & 3) << 4);
            auto wq_dma = [&](int ps) { dma16<NTM & 2>(vq, wq + (size_t)((ps + rot) & 15) * 262144, ps * 8192 + wv * 1024); };
            bf16x8 uk[4][4], aq[16];
            u32x4 fi, hv[4];
            unsigned fio = (unsigned)(WS_FI + x * STR + (lane & 7) * 16), hsrc, hsrc2, ta0, ta1, tb0, tb1, qo2, hw0, hw1, hr, hr2, qw, thx = 0u;
            const unsigned L_HI = L_SCR, L_QI = L_SCR + 8192;
            auto h_load = [&]() {
                asm volatile("buffer_load_dwordx4 %0, %4, %6, 0 offen sc1\n\tbuffer_load_dwordx4 %1, %4, %6, 0 offen offset:1024 sc1\n\t"
                             "buffer_load_dwordx4 %2, %5, %6, 0 offen sc1\n\tbuffer_load_dwordx4 %3, %5, %6, 0 offen offset:1024 sc1"
                             : "+v"(hv[0]), "+v"(hv[1]), "+v"(hv[2]), "+v"(hv[3]) : "v"(hsrc), "v"(hsrc2), "s"(rw) : "memory");
            };
            auto h_bad = [&]() {
                PIN4(hv[0], hv[1], hv[2], hv[3]);
                unsigned bad = (hv[0][1] ^ tlo) | (hv[0][3] ^ thx) | (hv[1][1] ^ tlo) | (hv[1][3] ^ thx) | (hv[2][1] ^ tlo) | (hv[2][3] ^ thx) | (hv[3][1] ^ tlo) | (hv[3][3] ^ thx);
                PIN(bad);
                return bad;
            };
#pragma unroll
            for (int ps = 0; ps < 16; ++ps) {
                wq_dma(ps);
                if (ps == 4) cnt_load();
                if (ps == HP) { h_load(); asm volatile("buffer_load_dwordx4 %0, %1, %2, 0 offen sc1" : "=v"(fi) : "v"(fio), "s"(rw) : "memory"); }
                if (ps >= 4 && ps < 8) VMW(5); else if (ps >= HP && ps < HP + 4) VMW(9); else VMW(4);
                if (ps == 3) {
                    asm volatile("s_barrier" ::: "memory");
                    if (pub) {
#pragma unroll
                        for (int t = 0; t < 4; ++t)
#pragma unroll
                            for (int s = 0; s < 4; ++s) uk[t][s] = *LP(bf16x8, L_U + off_b(wv * 64 + (nl >> 2) * 16 + t * 4 + (nl & 3), s * 4 + g));
                    }
                    STAMP(1);
                }
                if (ps == 5) {
                    hsrc = (unsigned)(WS_H + x * STR + wv * 2048 + lane * 16); hsrc2 = hsrc + 16384;
                    ta0 = off_b(8 * g + (nl >> 2), 2 * wv + ((lane & 3) >> 1)) + 8 * (lane & 1); ta1 = off_b(8 * g + 4 + (nl >> 2), 2 * wv + ((lane & 3) >> 1)) + 8 * (lane & 1);
                    tb0 = ta0 + 65536; tb1 = ta1 + 65536; qo2 = qo;
                    hw0 = L_HI + ((2 * wv - rot) & 15) * 512 + (lane >> 4) * 128 + (lane & 7) * 16 + ((lane >> 3) & 1) * 8;
                    hw1 = L_HI + ((2 * wv + 1 - rot) & 15) * 512 + (lane >> 4) * 128 + (lane & 7) * 16 + ((lane >> 3) & 1) * 8;
                    hr2 = L_HI + g * 128 + (nl & 7) * 16 + (nl >> 3) * 512;
                    PIN(hr2);
                    hr = L_HI + g * 128 + (nl & 7) * 16; qw = L_QI + (wv >> 1) * 512 + ((wv & 1) * 2 + (g >> 1)) * 128 + (nl & 7) * 16 + (g & 1) * 8;
                    PIN4(hsrc, hsrc2, ta0, ta1); PIN4(tb0, tb1, qo2, hw0); PIN(hw1); PIN(hr); PIN(qw);
                }
                if (ps == 8) { tag_take(); thx = tlo ^ thi ^ TAGC; }
                if (ps == 9) {
#pragma unroll
                    for (int t = 0; t < 4; ++t) PIN4(uk[t][0], uk[t][1], uk[t][2], uk[t][3]);
                    wg_barrier();
#pragma unroll
                    for (int q = 0; q < 6; ++q) aq[q] = tr_frag(ta0 + q * 8192, ta1 + q * 8192);
                }
                if (ps == 13) {
                    asm volatile("s_barrier" ::: "memory");
#pragma unroll
                    for (int q = 6; q < 10; ++q) aq[q] = q < 8 ? tr_frag(ta0 + q * 8192, ta1 + q * 8192) : tr_frag(tb0 + (q - 8) * 8192, tb1 + (q - 8) * 8192);
                }
            }
            STAMP(15);
            if (HP + 4 > 15) VMW(HP + 4 > 15 ? 15 - HP : 0);
            if (!__all(h_bad() == 0u)) {
                for (unsigned spins = 0;;) {
                    h_load();
                    VMW(0);
                    if (__all(h_bad() == 0u)) break;
                    __builtin_amdgcn_s_sleep(SLPH);
                    if (++spins > kSpinCap) break;
                }
            }
            {
                unsigned hh[4];
#pragma unroll
                for (int q = 0; q < 4; ++q) {
                    const unsigned t = hv[2 + (q >> 1)][(q & 1) * 2], sh = hv[q >> 1][(q & 1) * 2];
                    hh[q] = pk2(lo16(t) + lo16(sh), hi16(t) + hi16(sh));
                }
                *LP(u32x2, hw0) = u32x2{hh[0], hh[1]};
                *LP(u32x2, hw1) = u32x2{hh[2], hh[3]};
            }
            STAMP(3);
            asm volatile("s_waitcnt vmcnt(0) lgkmcnt(0)\n\ts_barrier" : "+v"(fi) :: "memory");
            STAMP(2);
            {
                bf16x8 hb[8];
#pragma unroll
                for (int q = 0; q < 5; ++q) hb[q] = *LP(bf16x8, hr2 + q * 1024);
#pragma unroll
                for (int ps = 10; ps < 16; ++ps) {
                    if (!(ps & 1)) hb[ps >> 1] = *LP(bf16x8, hr2 + (ps >> 1) * 1024);
                    aq[ps] = tr_frag(tb0 + (ps - 8) * 8192, tb1 + (ps - 8) * 8192);
                }
                __builtin_amdgcn_sched_barrier(0);
                f32x4 d0 = {0.f, 0.f, 0.f, 0.f}, d1 = {0.f, 0.f, 0.f, 0.f};
#pragma unroll
                for (int ps = 0; ps < 16; ps += 2) {
                    d0 = MFMA16(aq[ps], hb[ps >> 1], d0); d1 = MFMA16(aq[ps + 1], hb[ps >> 1], d1);
                    if (ps == 6) { unsigned bad = (fi[0] ^ tlo) | (fi[1] ^ thi) | (fi[2] ^ tlo) | (fi[3] ^ thi); PIN(bad); if (!__all(bad == 0u)) flags_wait(fio); }
                }
#pragma unroll
                for (int e = 0; e < 4; ++e) { float v = d0[e] + dppf<0x128>(d1[e]); asm("" : "+v"(v)); d0[e] = v; }
                if (nl < 8) *LP(u32x2, qw) = u32x2{pk2(d0[0], d0[1]), pk2(d0[2], d0[3])};
            }
            wg_barrier();
            if (pub) {
                bf16x8 qb[4];
#pragma unroll
                for (int s = 0; s < 4; ++s) qb[s] = *LP(bf16x8, hr + 8192 + s * 512);
                __builtin_amdgcn_sched_barrier(0);
                f32x4 d[4];
#pragma unroll
                for (int t = 0; t < 4; ++t) d[t] = MFMA16(uk[t][0], qb[0], (f32x4{0.f, 0.f, 0.f, 0.f}));
#pragma unroll
                for (int s = 1; s < 4; ++s)
#pragma unroll
                    for (int t = 0; t < 4; ++t) d[t] = MFMA16(uk[t][s], qb[s], d[t]);
                const f32x4 a = nl & 8 ? d[2] : d[0], b = nl & 8 ? d[3] : d[1];
                u32x4 qv = {pk2(a[0], a[1]), pk2(a[2], a[3]), pk2(b[0], b[1]), pk2(b[2], b[3])};
                if (__builtin_expect(mx4(qv, 0u) == SENT, 0)) qv = u32x4{pk2s(a[0], a[1]), pk2s(a[2], a[3]), pk2s(b[0], b[1]), pk2s(b[2], b[3])};
                ST16(qv, qo2, 0);
            }
            STAMP(4);
            wg_barrier();
            if (LMPF && wv >= 3 && wv < 6) {
                const unsigned n = (unsigned)(j * 3 + wv - 3);
                if (n < 32) pf4<0>(((n & 7u) * 64 + lane) * 6144u + 2048u + (x * 4 + (n >> 3)) * 128u, p.w_mod, L_SCR + 31744);
            }
            if (QHELP && wv < 3 && j + 12 * wv < 32) {
                const int hh = j + 12 * wv;
#pragma unroll 1
                for (unsigned spins = 0; spins < 64; ++spins) {
                    __builtin_amdgcn_s_sleep(QHELP);
                    const u32x4 v = LD16((unsigned)(WS_Q + (hh & 7) * STR + (x * 4 + (hh >> 3)) * 1024 + lane * 16), 0);
                    if (__all(mx4(v, 0u) != SENT)) break;
                    asm volatile("" ::: "memory");
                }
                STAMP(6);
            }
        } else {
            const int cg = x * 2 + (j & 1);
            if (j < 14) {
                dma_piece(p.w_mod + (size_t)wv * 64 * 3072 + cg * 64, 6144, rgn, 8, NTM & 2);
                cnt_load();
                VMW(9);
            } else {
                cnt_load();
                if (TLBW && j == 14) {
                    const char* base = wv == 0 ? (const char*)p.w_uv : wv == 1 ? (const char*)p.w_o : wv == 2 ? (const char*)p.w1 : wv == 3 ? (const char*)p.w2 : wv == 4 ? (const char*)p.out : ws + (wv - 5) * 4194304;
                    pf4<0>(min((unsigned)lane * 65536u, wv == 4 ? 0u : (wv & 6) == 2 ? 27u * 65536u : ~0u) + (wv > 4 ? 61440u : 0u), base, L_SCR + 31744);
                }
                ST16(s4, (unsigned)(WS_Q + wv * STR + cg * 2048 + lane * 16), 0); ST16(s4, (unsigned)(WS_Q + wv * STR + cg * 2048 + lane * 16), 1024);
                VMW(0);
                tag_take();
                wg_barrier();
                if (wv == 7) flag_put(WS_FI, cg);
                STAMP(15);
                if (j == 14) {
                    u32x2 best = {~0u, ~0u};
#pragma unroll
                    for (int r = 0; r < 2 * NCAL; ++r) {
                        const unsigned hf = (r ^ wv) & 1;
                        VMW(0);
                        const unsigned long long t0 = __builtin_amdgcn_s_memtime();
                        if (lane < 8) ST16(s4, WS_CAL + x * STR + hf * 8192 + (wv * 8 + r) * 128 + lane * 16, 0);
                        VMW(0);
                        const unsigned dt = (unsigned)(__builtin_amdgcn_s_memtime() - t0);
                        if (hf) best[1] = min(best[1], dt); else best[0] = min(best[0], dt);
                    }
                    *LP(u32x2, 16384 * 7 + 14336 + wv * 8) = best;
                    wg_barrier();
                    if (wv == 0 && lane < 8) {
                        u32x2 m = *LP(u32x2, 16384 * 7 + 14336 + lane * 8);
#pragma unroll
                        for (int o = 1; o < 8; o <<= 1) { m[0] = min(m[0], (unsigned)__shfl_xor(m[0], o)); m[1] = min(m[1], (unsigned)__shfl_xor(m[1], o)); }
                        ST16((u32x4{(unsigned)max(-1000, min(1000, (int)m[1] - (int)m[0])), 0u, tlo, thi}), (unsigned)(WS_NP + lane * STR + x * 16), 0);
                    }
                    STAMP(9);
                }
            }
            const u16x8 cf = *LP(u16x8, rgn + 8192 + lane * 16), xa = *LP(u16x8, rgn + 9216 + lane * 16);
            u32x4 af[2];
#pragma unroll
            for (int q = 0; q < 4; ++q) {
                const float a = bf2f(cf[2 * q]), b = bf2f(cf[2 * q + 1]);
                af[0][q] = pk2(a * __builtin_amdgcn_rcpf(1.f + __expf(-a)), b * __builtin_amdgcn_rcpf(1.f + __expf(-b)));
                af[1][q] = (unsigned)__builtin_amdgcn_update_dpp(0, (int)af[0][q], 0x128, 0xf, 0xf, true);
            }
            const unsigned L_MS = L_SCR;
            unsigned wa[4], fr[4];
#pragma unroll
            for (int t = 0; t < 4; ++t) { wa[t] = L_MS + wv * 2048 + (nl & 7) * 256 + (((t * 4 + g) ^ (nl & 7)) << 4) + (nl >> 3) * 16384; fr[t] = rgn + frl + ((t ^ frs) << 5); }
            PIN4(wa[0], wa[1], wa[2], wa[3]); PIN4(fr[0], fr[1], fr[2], fr[3]); PIN(af[0]); PIN(af[1]);
            const unsigned L_XN = 12288;
            auto mod_mm = [&](bool ln) {
                VMW(0);
                bf16x8 f[2][4];
#pragma unroll
                for (int ks = 0; ks < 2; ++ks)
#pragma unroll
                    for (int t = 0; t < 4; ++t) f[ks][t] = tr_frag(fr[t] + ks * 4096, fr[t] + ks * 4096 + 512);
                __builtin_amdgcn_sched_barrier(0);
                f32x4 d[4];
#pragma unroll
                for (int t = 0; t < 4; ++t) d[t] = MFMA16(f[0][t], BF8(af[0]), (f32x4{0.f, 0.f, 0.f, 0.f}));
#pragma unroll
                for (int t = 0; t < 4; ++t) d[t] = MFMA16(f[1][t], BF8(af[1]), d[t]);
#pragma unroll
                for (int t = 0; t < 4; ++t) *LP(f32x4, wa[t]) = d[t];
                if (ln) {
                    float v[8];
#pragma unroll
                    for (int q = 0; q < 8; ++q) v[q] = bf2f(xa[q]);
                    const float p1 = ((v[0] + v[1]) + (v[2] + v[3])) + ((v[4] + v[5]) + (v[6] + v[7]));
                    const float p2 = ((v[0] * v[0] + v[1] * v[1]) + (v[2] * v[2] + v[3] * v[3])) + ((v[4] * v[4] + v[5] * v[5]) + (v[6] * v[6] + v[7] * v[7]));
                    float mb, rden;
                    ln_stats(p1, p2, mb, rden);
                    u32x4 r;
#pragma unroll
                    for (int q = 0; q < 4; ++q) { const unsigned d = pk2(v[2 * q] - mb, v[2 * q + 1] - mb); r[q] = pk2(lo16(d) * rden, hi16(d) * rden); }
                    *LP(u32x4, L_XN + wv * 16384 + lane * 16) = r;
                }
                wg_barrier();
            };
            auto sum8 = [&](unsigned a) {
                f32x4 u[8];
#pragma unroll
                for (int w2 = 0; w2 < 8; ++w2) u[w2] = *LP(f32x4, a + w2 * 2048);
                __builtin_amdgcn_sched_barrier(0);
                return ((u[0] + u[1]) + (u[2] + u[3])) + ((u[4] + u[5]) + (u[6] + u[7]));
            };
            if (j < 14) {
                const int hb = lane & 7, c4 = 2 * wv + (nl >> 3);
                unsigned hoff = (unsigned)(WS_H + g * STR + cg * 2048 + wv * 256 + nl * 16);
                unsigned sa = L_MS + hb * 256 + ((c4 ^ hb) << 4);
                unsigned xa2 = L_XN + hb * 16384 + (cg & 7) * 128 + c4 * 8;
                PIN(hoff); PIN(sa); PIN(xa2);
                STAMP(1);
                mod_mm(cg >= 8);
                STAMP(2);
                const u32x2 xn = *LP(u32x2, xa2);
                const f32x4 v = sum8(sa);
                tag_take();
                const unsigned thx = tlo ^ thi ^ TAGC;
                u32x2 r = {pk2(v[0], v[1]), pk2(v[2], v[3])};
                if (cg >= 8) {
#pragma unroll
                    for (int q = 0; q < 2; ++q) { const unsigned sc = pk2(1.f + lo16(r[q]), 1.f + hi16(r[q])); r[q] = pk2(lo16(xn[q]) * lo16(sc), hi16(xn[q]) * hi16(sc)); }
                }
                ST16((u32x4{r[0], tlo, r[1], thx}), hoff, 0);
                ST16((u32x4{r[0], tlo, r[1], thx}), hoff, 4 * STR);
                STAMP(5);
            }
            wg_barrier();
            while (__builtin_amdgcn_s_memrealtime() < t_start + LMD) __builtin_amdgcn_s_sleep(2);
            const int lg = x * 4 + (j & 3);
            dma_piece(p.w_mod + (size_t)wv * 64 * 3072 + 1024 + lg * 64, 6144, rgn, 8, NTM & 4);
            mod_mm(false);
            if (lg >= 8 && lg < 24) {
                if (wv == 0) {
                    const int b = lane >> 3, c8 = lane & 7;
                    const f32x4 v0 = sum8(L_MS + b * 256 + (((2 * c8) ^ b) << 4)), v1 = sum8(L_MS + b * 256 + (((2 * c8 + 1) ^ b) << 4));
                    ST16((u32x4{pk2(v0[0], v0[1]), pk2(v0[2], v0[3]), pk2(v1[0], v1[1]), pk2(v1[2], v1[3])}), (unsigned)(WS_MB + b * STR + (lg - 8) * 128 + c8 * 16), 0);
                }
            } else if (wv < 4) {
                const int b = wv * 2 + (lane >> 5), cp = lane & 31;
                f32x2_t u[8];
#pragma unroll
                for (int w2 = 0; w2 < 8; ++w2) u[w2] = *LP(f32x2_t, L_MS + w2 * 2048 + b * 256 + (((cp >> 1) ^ b) << 4) + (cp & 1) * 8);
                const f32x2_t v = ((u[0] + u[1]) + (u[2] + u[3])) + ((u[4] + u[5]) + (u[6] + u[7]));
                ST16((u32x4{__float_as_uint(rbf(v[0])), __float_as_uint(rbf(v[1])), tlo, thi}), (unsigned)(WS_MOD + b * STR + (lg * 32 + cp) * 16), 0);
            }
            STAMP(6);
        }
        const int bq = (j >> 2) & 1, dh = j >> 3, src = th * 2 + dh, rb = bq * 4 + (src >> 4), rc = src & 15;
        wg_barrier();
        while (__builtin_amdgcn_s_memrealtime() < t_start + TWD - 110) __builtin_amdgcn_s_sleep(2);
        const unsigned npm = np_mask(LD16((unsigned)(WS_NP + x * STR + (lane & 7) * 16), 0)), np = (npm >> x) & 1u;
        unsigned oo = (unsigned)(WS_OUTP + ((bq * 4 + (nl & 3)) * 16 + wv * 2 + (nl >> 3)) * 16384 + (src ^ wv) * 128 + ((nl >> 2) & 1) * 64 + g * 16) + ((~npm >> ((nl & 3) * 2 + (wv >> 2))) & 1u) * 8192u;
        const int rcol = rc * 32 + (lane & 7) * 4 + (lane >> 5) * 2;
        unsigned xdst = (unsigned)(WS_X1 + ((lane >> 3) & 3) * STR + (rb * 512 + rcol) * 2), xdst2 = xdst + 4 * STR + ((npm >> (4 + ((lane >> 3) & 3))) & 1u) * 8192u;
        xdst += ((npm >> ((lane >> 3) & 3)) & 1u) * 8192u;
        ST16(s4, oo, 0);
        if (wv == 7) { __builtin_amdgcn_raw_buffer_store_b32(SENT, rw, xdst, 0, 16); __builtin_amdgcn_raw_buffer_store_b32(SENT, rw, xdst2, 0, 16); }
        VMW(0);
        wg_barrier();
        if (wv == 0) flag_put(WS_FB, id);
        while (__builtin_amdgcn_s_memrealtime() < t_start + TWD) __builtin_amdgcn_s_sleep(2);
        asm volatile("s_barrier" ::: "memory");
        STAMP(7);
        if ((PFM & 8) && wv == 0 && lane < 32) pf4<NTM & 8>((unsigned)((j * 32 + lane) * 8192 + x * 1024 + ((3u - ((unsigned)(size_t)p.w_uv >> 7)) & 7u) * 128), p.w_uv, L_SCR + 31744);
        if (bq) dma_piece(p.w_o + (size_t)(th * 128 + dh * 64) * 512 + wv * 64, 1024, rgn + 8192, 8, NTM & 8);
        dma_piece(p.w_uv + ((size_t)wv * 64 * NH + th) * 128 + dh * 64, 8192, rgn, 8, NTM & 8);
        if (!bq) dma_piece(p.w_o + (size_t)(th * 128 + dh * 64) * 512 + wv * 64, 1024, rgn + 8192, 8, NTM & 8);
        u32x4 fb, gm;
        unsigned xg;
        VMW(4);
        if (wv == 7)
            asm volatile("buffer_load_dwordx4 %0, %3, %5, 0 offen sc1\n\tbuffer_load_dwordx4 %1, %4, %5, 0 offen sc1\n\tglobal_load_dword %2, %6, %7" : "=&v"(fb), "=&v"(gm), "=&v"(xg)
                         : "v"((unsigned)(WS_FB + x * STR + lane * 16)), "v"((unsigned)(WS_MOD + rb * STR + rcol * 8)), "s"(rw), "v"((unsigned)(rb * 512 + rcol) * 2u), "s"(p.x) : "memory");
        VMW(0);
        asm volatile("" : "+v"(fb), "+v"(gm), "+v"(xg));
        STAMP(8);
        bf16x8 fuv[2][4], fwo[2][4];
        unsigned pfa[4];
#pragma unroll
        for (int t = 0; t < 4; ++t) { pfa[t] = rgn + frl + ((t ^ frs) << 5); PIN(pfa[t]); }
#pragma unroll
        for (int ks = 0; ks < 2; ++ks)
#pragma unroll
            for (int t = 0; t < 4; ++t) {
                const unsigned a = rgn + (unsigned)((g * 4 + (nl >> 2)) * 128 + (lane & 3) * 8) + ((t ^ (unsigned)((nl >> 3) | (g & 2))) << 5) + ks * 4096;
                fuv[ks][t] = tr_frag(a, a + 2048);
                fwo[ks][t] = tr_frag(pfa[t] + 8192 + ks * 4096, pfa[t] + 8192 + ks * 4096 + 512);
            }
#pragma unroll
        for (int ks = 0; ks < 2; ++ks) { PIN4(fuv[ks][0], fuv[ks][1], fuv[ks][2], fuv[ks][3]); PIN4(fwo[ks][0], fwo[ks][1], fwo[ks][2], fwo[ks][3]); }
        const unsigned L_WGT = L_SCR, L_MSK = L_SCR + 256, L_RED = L_SCR + 1024;
        unsigned psrc = (unsigned)(WS_PART + (bq * 4 * 32 + th) * 32768 + (wv >> 2) * 16384 + (wv & 3) * 2048 + ((lane >> 3) ^ (th & 7)) * 128) + np * 8192u + pcl, psrc1 = (psrc ^ 64u) + 1024;
        unsigned psrc2 = psrc ^ 8192u, psrc3 = psrc1 ^ 8192u;
        PIN(psrc2); PIN(psrc3); PIN(xdst2);
        unsigned rw2 = L_RED + wv * 1024 + (nl & 3) * 256 + g * 16;
        unsigned wa0 = (nl & 3) == (g >> 1) ? L_WGT + (nl & 4) * 32 + g * 16 : L_WGT + 272, wa1 = (nl & 3) == 2 + (g >> 1) ? L_WGT + (nl & 4) * 32 + 64 + g * 16 : L_WGT + 272;
        unsigned cr = rgn + (nl & 3) * 128 + g * 16, r2r = L_RED + lane * 16, cw = rgn + lane * 8;
        unsigned osrc = (unsigned)(WS_OUTP + (rb * 16 + rc) * 16384 + lane * 16) + (np ^ 1u) * 8192u;
        PIN(psrc); PIN(psrc1); PIN(rw2); PIN4(wa0, wa1, cr, r2r); PIN4(cw, oo, osrc, xdst);
        float mlm = 0.f, mll = 0.f;
        if (wv == 0) *LP(unsigned, L_WGT + 256 + lane * 4) = 0u;
        wg_barrier();
        const bool early = __all(flags_ok(fb) & (gm[2] == tlo) & (gm[3] == thi));
        u32x2 ga = {gm[0], gm[1]};
        PIN(ga);
        bf16x8 pa[2][4], wh[2];
        {
            u32x4 ml = {0u, 0u, ~tlo, ~thi};
            const unsigned mlsrc = (unsigned)(WS_ML + (bq * 4 + g) * STR + nl * 512 + th * 16) + ((npm >> (x & 6)) & 1u) * 8192u;
            unsigned long long done = 0ull;
            unsigned lm = L_MSK;
            PIN(lm);
#pragma unroll 1
            for (unsigned spins = 0; spins < kSpinCap; ++spins) {
                unsigned long long m;
                if (wv == 0) {
                    if ((ml[2] != tlo) | (ml[3] != thi)) ml = LD16(mlsrc, 0);
                    m = __ballot((ml[2] == tlo) & (ml[3] == thi));
                    unsigned long long f = m & (m >> 4);
                    f &= f >> 2; f &= f >> 1;
                    asm volatile("ds_write_b64 %0, %1" :: "v"(lm), "v"(f) : "memory");
                    m = m == ~0ull ? 0x0101010101010101ull : 0ull;
                } else {
                    unsigned long long t;
                    asm volatile("ds_read_b64 %0, %1\n\ts_waitcnt lgkmcnt(0)" : "=v"(t) : "v"(lm) : "memory");
                    m = ((unsigned long long)(unsigned)__builtin_amdgcn_readfirstlane((unsigned)(t >> 32)) << 32) | (unsigned)__builtin_amdgcn_readfirstlane((unsigned)t);
                    m &= 0x0101010101010101ull & ~done;
                }
                if (m) {
                    done |= m;
#define PLD(bit, off, v, base) "s_bitcmp1_b64 %0, " #bit "\n\ts_cbranch_scc0 1f\n\ts_add_u32 m0, %1, " #off "\n\ts_nop 0\n\tglobal_load_lds_dwordx4 " v ", " base " sc1\n1:\n\t"
                    asm volatile(PLD(0, 0, "%2", "%4") PLD(8, 1024, "%3", "%4") PLD(16, 2048, "%2", "%5") PLD(24, 3072, "%3", "%5")
                                 PLD(32, 4096, "%8", "%6") PLD(40, 5120, "%9", "%6") PLD(48, 6144, "%8", "%7") PLD(56, 7168, "%9", "%7")
                                 :: "s"(m), "s"(rgn), "v"(psrc), "v"(psrc1), "s"(ws), "s"(ws + 1048576), "s"(ws + 2097152), "s"(ws + 3145728), "v"(psrc2), "v"(psrc3) : "memory", "m0", "scc");
                    if (done == 0x0101010101010101ull) break;
                } else if (SLPM > 0 && wv == 0) __builtin_amdgcn_s_sleep(SLPM);
            }
            mlm = __uint_as_float(ml[0]); mll = __uint_as_float(ml[1]);
            STAMP(10);
            if (wv == 0) {
                float M = mlm;
                M = fmaxf(M, dppf<0xB1>(M)); M = fmaxf(M, dppf<0x4E>(M)); M = fmaxf(M, dppf<0x141>(M)); M = fmaxf(M, dppf<0x140>(M));
                const float e = __builtin_amdgcn_exp2f(mlm - M);
                const float w = e * __builtin_amdgcn_rcpf(sum16(e * mll));
                const unsigned short hi = f2bf(w);
                *LP(unsigned short, L_WGT + lane * 2) = hi;
                *LP(unsigned short, L_WGT + 128 + lane * 2) = f2bf(w - bf2f(hi));
            }
            wg_barrier();
            wh[0] = *LP(bf16x8, wa0); wh[1] = *LP(bf16x8, wa1);
            __builtin_amdgcn_sched_barrier(0);
            VMW(0);
#pragma unroll
            for (int ks = 0; ks < 2; ++ks)
#pragma unroll
                for (int t = 0; t < 4; ++t) pa[ks][t] = tr_frag(pfa[t] + ks * 4096, pfa[t] + ks * 4096 + 512);
        }
        STAMP(11);
        {
            f32x4 d[4];
#pragma unroll
            for (int t = 0; t < 4; ++t) d[t] = MFMA16(pa[0][t], wh[0], (f32x4{0.f, 0.f, 0.f, 0.f}));
#pragma unroll
            for (int t = 0; t < 4; ++t) d[t] = MFMA16(pa[1][t], wh[1], d[t]);
#pragma unroll
            for (int t = 0; t < 4; ++t)
#pragma unroll
                for (int e = 0; e < 4; ++e) { float v = d[t][e] + dppf<0x124>(d[t][e]); asm("" : "+v"(v)); d[t][e] = v; }
            const u32x4 o0 = {pk2(d[0][0], d[0][1]), pk2(d[0][2], d[0][3]), pk2(d[1][0], d[1][1]), pk2(d[1][2], d[1][3])};
            const u32x4 o1 = {pk2(d[2][0], d[2][1]), pk2(d[2][2], d[2][3]), pk2(d[3][0], d[3][1]), pk2(d[3][2], d[3][3])};
            f32x4 y[4];
#pragma unroll
            for (int t = 0; t < 4; ++t) y[t] = MFMA16(fuv[0][t], BF8(o0), (f32x4{0.f, 0.f, 0.f, 0.f}));
#pragma unroll
            for (int t = 0; t < 4; ++t) y[t] = MFMA16(fuv[1][t], BF8(o1), y[t]);
            if (nl < 4) {
#pragma unroll
                for (int t = 0; t < 4; ++t) *LP(f32x4, rw2 + t * 64) = y[t];
            }
        }
        wg_barrier();
        {
            f32x4 cs[8];
#pragma unroll
            for (int w2 = 0; w2 < 8; ++w2) cs[w2] = *LP(f32x4, r2r + w2 * 1024);
            const f32x4 c4 = ((cs[0] + cs[1]) + (cs[2] + cs[3])) + ((cs[4] + cs[5]) + (cs[6] + cs[7]));
            *LP(u32x2, cw) = u32x2{pk2(c4[0], c4[1]), pk2(c4[2], c4[3])};
            asm volatile("s_waitcnt lgkmcnt(0)" ::: "memory");
            const u32x4 cx0 = *LP(u32x4, cr), cx1 = *LP(u32x4, cr + 64);
            f32x4 o[4];
#pragma unroll
            for (int q = 0; q < 4; ++q) o[q] = MFMA16(fwo[0][q], BF8(cx0), (f32x4{0.f, 0.f, 0.f, 0.f}));
#pragma unroll
            for (int q = 0; q < 4; ++q) o[q] = MFMA16(fwo[1][q], BF8(cx1), o[q]);
            const f32x4 sel = (nl & 8) ? ((nl & 4) ? o[3] : o[2]) : ((nl & 4) ? o[1] : o[0]);
            u32x4 sv = __builtin_bit_cast(u32x4, sel);
            if (__builtin_expect(mx4(sv, 0u) == SENT, 0)) sv = u32x4{f32s(sel[0]), f32s(sel[1]), f32s(sel[2]), f32s(sel[3])};
            ST16(sv, oo, 0);
        }
        STAMP(12);
        wg_barrier();
        if (wv == 7) {
            if (!early) {
                flags_wait(WS_FB + x * STR + lane * 16);
                ga = mod_get(WS_MOD + rb * STR + rcol * 8);
                PIN(ga);
            }
            u32x4 op[8];
            for (unsigned spins = 0;;) {
#pragma unroll
                for (int k = 0; k < 8; ++k) op[k] = LD16(osrc + (k & 3) * 1024, (k >> 2) * 4096);
                unsigned m = 0u;
#pragma unroll
                for (int k = 0; k < 8; ++k) m = mx4(op[k], m);
                if (__all(m != SENT)) break;
                if (++spins > kSpinCap) break;
                asm volatile("" ::: "memory");
            }
            STAMP(13);
            const f32x4 v = ((__builtin_bit_cast(f32x4, op[0]) + __builtin_bit_cast(f32x4, op[1])) + (__builtin_bit_cast(f32x4, op[2]) + __builtin_bit_cast(f32x4, op[3]))) +
                            ((__builtin_bit_cast(f32x4, op[4]) + __builtin_bit_cast(f32x4, op[5])) + (__builtin_bit_cast(f32x4, op[6]) + __builtin_bit_cast(f32x4, op[7])));
            float w[2];
#pragma unroll
            for (int q = 0; q < 2; ++q) {
                auto t = __builtin_amdgcn_permlane32_swap(__float_as_uint(v[q]), __float_as_uint(v[2 + q]), false, false);
                w[q] = __uint_as_float(t[0]) + __uint_as_float(t[1]);
                auto u = __builtin_amdgcn_permlane16_swap(__float_as_uint(w[q]), __float_as_uint(w[q]), false, false);
                w[q] = __uint_as_float(u[0]) + __uint_as_float(u[1]);
                w[q] += dppf<0x128>(w[q]);
            }
            unsigned t = pk2(w[0], w[1]);
            t = pk2(__uint_as_float(ga[0]) * lo16(t), __uint_as_float(ga[1]) * hi16(t));
            t = pk2s(lo16(xg) + lo16(t), hi16(xg) + hi16(t));
            __builtin_amdgcn_raw_buffer_store_b32(t, rw, xdst, 0, 16);
            __builtin_amdgcn_raw_buffer_store_b32(t, rw, xdst2, 0, 16);
            STAMP(14);
        }
        STAMP_FLUSH();
        return;
    }

    const int s = j - 16;
    if (wv == 0) cnt_load();
    {
        const unsigned ln = (unsigned)(s * 64 + lane);
        if ((PFM & 1) && wv == 0) pf4<NTM & 1>((ln & 511u) * 6144u + (x * 2 + (ln >> 9)) * 128u, p.w_mod, L_SCR);
        if ((PFM & 2) && wv == 1) {
            const unsigned short* w = s < 8 ? p.w_uk : p.w_q;
            pf4<NTM & 1>((ln & 511u) * 8192u + x * 1024u + ((3u - ((unsigned)(size_t)w >> 7)) & 7u) * 128u, w, L_SCR);
        }
        if ((PFM & 48) == 48) {
            const unsigned r0 = (unsigned)(s * 4 + (wv & 3)), row = PFS == 0 ? r0 * 8 + (lane >> 3) : PFS == 1 ? (lane >> 3) * 64 + r0 : (r0 >> 5) * 256 + (lane >> 3) * 32 + (r0 & 31);
            pf4<NTM & 1>(row * 8192u + x * 1024u + (lane & 7u) * 128u, wv < 4 ? p.w_uk : p.w_q, L_SCR);
        }
        else if ((PFM & 16) && wv >= 4) { const unsigned l2 = (unsigned)((s * 4 + (wv & 3)) * 64 + lane); pf4<NTM & 1>((l2 >> 3) * 8192u + x * 1024u + (l2 & 7u) * 128u, p.w_q, L_SCR); }
        else if ((PFM & 32) && wv >= 4) { const unsigned l2 = (unsigned)((s * 4 + (wv & 3)) * 64 + lane); pf4<NTM & 1>((l2 >> 3) * 8192u + x * 1024u + (l2 & 7u) * 128u, p.w_uk, L_SCR); }
        if ((PFM & 4) && (wv & 6) == 2) { const unsigned l2 = (unsigned)((s * 2 + (wv & 1)) * 64 + lane); pf4<NTM & 1>((l2 & 511u) * 8192u + (x * 4 + (l2 >> 9)) * 256u, p.w_q, L_SCR); }
    }
    const int ug = x * 7 + s;
    const int wp = wv & 3, ch = wv >> 2, kb = wp * 2 + ch;
    int head = lane & 31;
    unsigned h2 = lane >> 5;
    const unsigned vb = (unsigned)(g * 1024 + ((nl ^ (g << 2)) << 4)), lb = (unsigned)((wv >> 1) * 32768 + (wv & 1) * 4096);
    const char* ck = (const char*)p.ckv + ((size_t)x * SK + s * 256 + wv * 16) * 1024;
    const unsigned rot = ROTC ? (unsigned)s & 3u : 0u;
    const unsigned vbr = vb ^ (rot << 8);
    const char* ck2 = ck + 131072;
    auto ck_dma = [&](unsigned i) { dma16x<NTM & 16>(vbr, ((i & 3u) << 4) | (((i >> 2) & 3u) << 8) | ((i & 3u) << 12), (i >> 4) ? ck2 : ck, lb + ((i >> 2) & 3u) * 8192u + (i & 3u) * 1024u); };
    bf16x8 ka[16];
    s16x4 va[8][4];
    unsigned kad[4], vad[4], vad2[4];
    auto ka_reads = [&](int cb) {
#pragma unroll
        for (int u = 0; u < 4; ++u) ka[cb * 4 + u] = *LP(bf16x8, kad[u] + cb * 8192);
    };
    auto va_reads = [&]() {
#pragma unroll
        for (int kk = 0; kk < 8; ++kk)
#pragma unroll
            for (int q = 0; q < 4; ++q) va[kk][q] = tr4((kk < 4 ? vad[q] : vad2[q]) + ((kk >> 1) & 1) * 32768 + (kk & 1) * 4096);
    };
    auto va_pin = [&]() {
#pragma unroll
        for (int q = 0; q < 8; ++q) PIN4(va[q][0], va[q][1], va[q][2], va[q][3]);
    };
    u32x4 fa = {0u, 0u, 0u, 0u};
    const unsigned fao = (unsigned)(WS_FI + x * STR + (lane & 7) * 16);
    constexpr int LAST = GI + 4 > 19 ? GI + 4 : 19;
    const unsigned qsrc = (unsigned)(WS_Q + (wv & 1) * 4 * STR + (x * 4 + (wv >> 1)) * 1024) + (g ^ rot) * 256 + ((nl ^ ((wv * 4) & 15)) << 4);
    auto q_dma = [&](int k) {
        unsigned t;
        asm volatile("s_mov_b32 m0, %3\n\tv_xor_b32 %0, %4, %1\n\tglobal_load_lds_dwordx4 %0, %2 sc1" : "=&v"(t) : "v"(qsrc), "s"(ws), "s"(L_SCR + (wv * 4 + k) * 1024), "i"(k * (STR + 16)) : "memory", "m0");
    };
#pragma unroll
    for (int i = 0; i <= LAST; ++i) {
        ck_dma(i);
        if (i == 3) {
#pragma unroll
            for (int u = 0; u < 4; ++u) {
                kad[u] = tile_off(wp * 32 + (head ^ (ch * 16)), ch * 8 + 2 * u + h2);
                vad[u] = tile_off(4 * h2 + (nl >> 2) + 8 * (u & 1), wv * 8 + 4 * (u >> 1) + 2 * (g & 1) + ((lane & 3) >> 1)) + 8 * (lane & 1);
                vad2[u] = vad[u] + 65536;
                PIN(kad[u]); PIN(vad[u]); PIN(vad2[u]);
            }
        }
        if (i > GI && i < GI + 4) VMW(8); else if (i < NTH) { if (i >= W0) VMW(W0); } else if (i >= 4) VMW(4);
        if (i == 15 && wv == 0) {
            PIN(fa);
            tag_take();
            if (!__all(flags_ok(fa))) flags_wait(fao);
        }
        if ((i & 3) == 3 && i >= 7 && i <= 19) {
            const int cb = (i - 7) >> 2;
            if (cb > 0) { PIN4(ka[cb * 4 - 4], ka[cb * 4 - 3], ka[cb * 4 - 2], ka[cb * 4 - 1]); va_pin(); }
            wg_barrier();
            ka_reads(cb);
            if ((wv >> 1) == cb) va_reads();
            STAMP(16 + cb);
        }
        if (i == 10 && wv == 0) asm volatile("buffer_load_dwordx4 %0, %1, %2, 0 offen sc1" : "+v"(fa) : "v"(fao), "s"(rw) : "memory");
        if (i == GI) { q_dma(0); q_dma(1); q_dma(2); q_dma(3); }
    }
    const unsigned L_X = L_SCR, L_P = L_SCR + 16384, L_EXM = L_SCR + 24576, L_EXS = L_SCR + 25600, L_BIG = L_SCR + 27648;
    unsigned nx = LAST + 1, npm = 0u;
    unsigned qad[4];
    {
        unsigned qck = L_SCR + wv * 4096 + lane * 16;
        u32x4 qb[4];
#pragma unroll
        for (int k = 0; k < 4; ++k) qb[k] = *LP(u32x4, qck + k * 1024);
#pragma unroll
        for (int k = 0; k < 4; ++k) { qad[k] = L_SCR + head * 1024 + (((ch * 8 + 2 * k + h2) ^ (head & 15)) << 4); PIN(qad[k]); }
        STAMP(20);
        if (!__all(mx4(qb[0], mx4(qb[1], mx4(qb[2], mx4(qb[3], 0u)))) != SENT)) {
            for (unsigned spins = 0;;) {
#pragma unroll
                for (int k = 0; k < 4; ++k)
                    if (!__all(mx4(qb[k], 0u) != SENT)) q_dma(k);
                if (nx <= 25) {
#pragma unroll
                    for (int i = 0; i < 3; ++i) ck_dma(nx + i);
                    nx += 3;
                    VMW(3);
                } else {
                    for (; nx < 28; ++nx) ck_dma(nx);
                    VMW(0);
                }
#pragma unroll
                for (int k = 0; k < 4; ++k) qb[k] = *LP(u32x4, qck + k * 1024);
                if (__all(mx4(qb[0], mx4(qb[1], mx4(qb[2], mx4(qb[3], 0u)))) != SENT)) break;
                if (++spins > kSpinCap) break;
            }
        }
    }
    constexpr int NE = LAST + 1 + SL0 + 5 < 28 ? LAST + 1 + SL0 + 5 : 28;
    auto slot = [&](int k) { if (LAST + 1 + k < 28) { if (nx <= (unsigned)(LAST + 1 + k)) ck_dma(LAST + 1 + k); } };
    auto slot2 = [&](int k) { if (NE + k < 28) { if (nx <= (unsigned)(NE + k)) ck_dma(NE + k); } else if (NE + k < 32) ck_dma(NE + k); };
    unsigned xw = wp * 32768 + 24576 + ch * 2048 + lane * 16, xr = xw ^ 2048u, exw = L_EXM + (head * 8 + wv) * 4, exr = L_EXM + head * 32;
    unsigned pwr = L_P + kb * 1024 + lane * 16, prd = L_P + lane * 16, esw = L_EXS + (head * 16 + h2 * 8 + wv) * 4;
    PIN4(xw, xr, exw, exr); PIN(pwr); PIN(prd); PIN(esw);
    PIN4(ka[12], ka[13], ka[14], ka[15]); va_pin();
    wg_barrier();
    STAMP(21);
    bf16x8 qf[16];
#pragma unroll
    for (int ks = 0; ks < 16; ++ks) qf[ks] = *LP(bf16x8, qad[ks & 3] + (ks >> 2) * 256);
    __builtin_amdgcn_sched_barrier(0);
#pragma unroll
    for (int k = 0; k < SL0; ++k) slot(k);
    __builtin_amdgcn_sched_barrier(0);
    float lsum, mrun;
    f32x16 o[2], sc2;
    auto meet = [&](const f32x16& sc, float (&t)[8]) {
        *LP(f32x4, xw) = f32x4{sc[8], sc[9], sc[10], sc[11]}; *LP(f32x4, xw + 1024) = f32x4{sc[12], sc[13], sc[14], sc[15]};
        wg_barrier();
        const f32x4 x0 = *LP(f32x4, xr), x1 = *LP(f32x4, xr + 1024);
#pragma unroll
        for (int r = 0; r < 4; ++r) { t[r] = sc[r] + x0[r]; t[4 + r] = sc[4 + r] + x1[r]; }
    };
    {
        f32x16 sc;
#pragma unroll
        for (int i = 0; i < 16; ++i) sc[i] = 0.f;
#pragma unroll
        for (int ks = 0; ks < 16; ++ks) {
            sc = MFMA32(ka[ks], qf[ks], sc);
            if ((ks & 3) == 3) slot(SL0 + (ks >> 2));
            __builtin_amdgcn_sched_barrier(0);
        }
        float t[8];
        meet(sc, t);
        {
            const float m = fmax3(fmax3(t[0], t[1], t[2]), fmax3(t[3], t[4], t[5]), fmax3(t[6], t[7], t[7]));
            auto u = __builtin_amdgcn_permlane32_swap(__float_as_uint(m), __float_as_uint(m), false, false);
            *LP(float, exw) = fmax3(__uint_as_float(u[0]), __uint_as_float(u[1]), __uint_as_float(u[1]));
        }
        slot(SL0 + 4);
        VMW(NE > 24 ? NE - 24 : 0);
        wg_barrier();
        const f32x4 a0 = *LP(f32x4, exr), a1 = *LP(f32x4, exr + 16);
        ka_reads(0); ka_reads(1);
        __builtin_amdgcn_sched_barrier(0);
        slot2(0);
        mrun = fmax3(fmax3(a0[0], a0[1], a0[2]), fmax3(a0[3], a1[0], a1[1]), fmax3(a1[2], a1[3], a1[3])) * SCL;
        float e[8];
#pragma unroll
        for (int r = 0; r < 8; ++r) e[r] = __builtin_amdgcn_exp2f(fmaf(t[r], SCL, -mrun));
        *LP(u32x4, pwr) = u32x4{pk2(e[0], e[1]), pk2(e[2], e[3]), pk2(e[4], e[5]), pk2(e[6], e[7])};
        lsum = ((e[0] + e[1]) + (e[2] + e[3])) + ((e[4] + e[5]) + (e[6] + e[7]));
        if (wv == 0) *LP(u32x4, L_BIG) = u32x4{0u, 0u, tlo, thi};
        slot2(1);
        wg_barrier();
        STAMP(22);
        bf16x8 pf[8];
#pragma unroll
        for (int kk = 0; kk < 8; ++kk) pf[kk] = *LP(bf16x8, prd + kk * 1024);
#pragma unroll
        for (int i = 0; i < 16; ++i) { o[0][i] = 0.f; o[1][i] = 0.f; sc2[i] = 0.f; }
        __builtin_amdgcn_sched_barrier(0);
#pragma unroll
        for (int ks = 0; ks < 8; ++ks) {
            sc2 = MFMA32(ka[ks], qf[ks], sc2);
            if (ks & 1) slot2(2 + (ks >> 1));
            __builtin_amdgcn_sched_barrier(0);
        }
#pragma unroll
        for (int kk = 0; kk < 8; ++kk) {
            o[0] = MFMA32(cat8(va[kk][0], va[kk][1]), pf[kk], o[0]);
            o[1] = MFMA32(cat8(va[kk][2], va[kk][3]), pf[kk], o[1]);
            if (kk < 4) slot2(6 + kk);
            __builtin_amdgcn_sched_barrier(0);
        }
    }
    STAMP(23);
    rederive();
    head = lane & 31; h2 = lane >> 5;
    unsigned sw[4], srd = lb + (lane >> 3) * 128 + (((lane & 7) ^ (lane >> 3)) << 4);
#pragma unroll
    for (int q = 0; q < 4; ++q) sw[q] = lb + head * 128 + (((2 * q + h2) ^ (head & 7)) << 4);
    PIN4(sw[0], sw[1], sw[2], sw[3]);
    xw = L_X + wv * 2048 + lane * 16; xr = xw ^ 8192u;
    PIN(xw); PIN(xr);
    {
        const u32x4 q = *LP(u32x4, L_BIG);
        tlo = __builtin_amdgcn_readfirstlane(q[2]); thi = __builtin_amdgcn_readfirstlane(q[3]);
    }
    unsigned po[4];
    const unsigned cw = wv ^ (rot * 2);
    unsigned p0 = (unsigned)(WS_PART + (x * 32 + (lane >> 3)) * 32768 + (s ^ (lane >> 3)) * 128 + (lane & 7) * 16) + (cw >> 2) * 16384 + (cw & 3) * 2048;
    unsigned mlo = (unsigned)(WS_ML + x * STR + s * 512 + head * 16), esr = L_EXS + head * 64;
    unsigned fbo = (unsigned)(WS_FB + x * STR + lane * 16), npo = (unsigned)(WS_NP + x * STR + (lane & 7) * 16);
    PIN(p0); PIN(npo); PIN4(esr, srd, mlo, fbo);
    u32x4 nq;
    {
        f32x16& sc = sc2;
        if (wv < 4) va_reads();
        VMW(4);
        wg_barrier();
        ka_reads(2);
        if ((wv >> 1) == 2) va_reads();
        __builtin_amdgcn_sched_barrier(0);
#pragma unroll
        for (int ks = 8; ks < 12; ++ks) sc = MFMA32(ka[ks], qf[ks], sc);
        __builtin_amdgcn_sched_barrier(0);
        VMW(0);
        wg_barrier();
        STAMP(24);
        asm volatile("buffer_load_dwordx4 %0, %1, %2, 0 offen sc1" : "=v"(nq) : "v"(npo), "s"(rw) : "memory");
        if (wv == 0 && s < 11) asm volatile("buffer_load_dwordx4 %0, %1, %2, 0 offen sc1" : "=v"(fa) : "v"(fbo), "s"(rw) : "memory");
        ka_reads(3);
        __builtin_amdgcn_sched_barrier(0);
#pragma unroll
        for (int ks = 12; ks < 16; ++ks) sc = MFMA32(ka[ks], qf[ks], sc);
        float t[8];
        meet(sc, t);
        float e[8];
        const float mloc = fmax3(fmax3(t[0], t[1], t[2]), fmax3(t[3], t[4], t[5]), fmax3(t[6], t[7], t[7]));
#pragma unroll
        for (int r = 0; r < 8; ++r) e[r] = __builtin_amdgcn_exp2f(fmaf(t[r], SCL, -mrun));
        *LP(u32x4, pwr) = u32x4{pk2(e[0], e[1]), pk2(e[2], e[3]), pk2(e[4], e[5]), pk2(e[6], e[7])};
        *LP(float, esw) = lsum + (((e[0] + e[1]) + (e[2] + e[3])) + ((e[4] + e[5]) + (e[6] + e[7])));
        if (__builtin_expect(!(fmaf(mloc, SCL, -mrun) <= BIGD), 0)) *LP(unsigned, L_BIG) = 1u;
        va_pin();
        wg_barrier();
        STAMP(25);
        unsigned big = *LP(unsigned, L_BIG);
        if ((wv >> 1) == 3) va_reads();
        bf16x8 pf[8];
#pragma unroll
        for (int kk = 0; kk < 8; ++kk) pf[kk] = *LP(bf16x8, prd + kk * 1024);
        __builtin_amdgcn_sched_barrier(0);
        if (__builtin_expect(__builtin_amdgcn_readfirstlane(big) != 0, 0)) {
            auto u = __builtin_amdgcn_permlane32_swap(__float_as_uint(mloc), __float_as_uint(mloc), false, false);
            *LP(float, exw) = fmaxf(__uint_as_float(u[0]), __uint_as_float(u[1]));
#pragma unroll
            for (int kk = 0; kk < 8; kk += 4) PIN4(pf[kk], pf[kk + 1], pf[kk + 2], pf[kk + 3]);
            wg_barrier();
            const f32x4 a0 = *LP(f32x4, exr), a1 = *LP(f32x4, exr + 16);
            const float mnew = fmaxf(fmaxf(fmaxf(fmaxf(a0[0], a0[1]), fmaxf(a0[2], a0[3])), fmaxf(fmaxf(a1[0], a1[1]), fmaxf(a1[2], a1[3]))) * SCL, mrun);
#pragma unroll
            for (int r = 0; r < 8; ++r) e[r] = __builtin_amdgcn_exp2f(fmaf(t[r], SCL, -mnew));
            *LP(u32x4, pwr) = u32x4{pk2(e[0], e[1]), pk2(e[2], e[3]), pk2(e[4], e[5]), pk2(e[6], e[7])};
            const float al = __builtin_amdgcn_exp2f(mrun - mnew);
            *LP(float, esw) = lsum * al + (((e[0] + e[1]) + (e[2] + e[3])) + ((e[4] + e[5]) + (e[6] + e[7])));
            mrun = mnew;
            o[0] *= al; o[1] *= al;
            wg_barrier();
#pragma unroll
            for (int kk = 0; kk < 8; ++kk) pf[kk] = *LP(bf16x8, prd + kk * 1024);
        }
        auto stage = [&](int t2, int gp) {
            const unsigned lo0 = pk2(o[t2][8 * gp + 0], o[t2][8 * gp + 1]), lo1 = pk2(o[t2][8 * gp + 2], o[t2][8 * gp + 3]);
            const unsigned hi0 = pk2(o[t2][8 * gp + 4], o[t2][8 * gp + 5]), hi1 = pk2(o[t2][8 * gp + 6], o[t2][8 * gp + 7]);
            auto r0 = __builtin_amdgcn_permlane32_swap(lo0, hi0, false, false);
            auto r1 = __builtin_amdgcn_permlane32_swap(lo1, hi1, false, false);
            *LP(u32x4, sw[t2 * 2 + gp]) = u32x4{r0[0], r1[0], r0[1], r1[1]};
        };
#pragma unroll
        for (int kk = 0; kk < 8; ++kk) {
            o[0] = MFMA32(cat8(va[kk][0], va[kk][1]), pf[kk], o[0]);
            if (kk == 3) {
                asm volatile("s_waitcnt vmcnt(0)" : "+v"(nq) :: "memory");
                npm = np_mask(nq);
            }
            if (kk == 5) {
#pragma unroll
                for (int i = 0; i < 4; ++i) po[i] = p0 + i * 262144 + (((npm >> (2 * i + (lane >> 5))) ^ (x >> 1)) & 1u) * 8192u;
                mlo += ((npm >> ((head >> 3) * 2)) & 1u) * 8192u;
                PIN4(po[0], po[1], po[2], po[3]); PIN(mlo);
            }
            __builtin_amdgcn_sched_barrier(0);
        }
#pragma unroll
        for (int kk = 0; kk < 8; ++kk) {
            o[1] = MFMA32(cat8(va[kk][2], va[kk][3]), pf[kk], o[1]);
            if (kk == 2) { PIN(o[0]); stage(0, 0); }
            if (kk == 4) stage(0, 1);
            __builtin_amdgcn_sched_barrier(0);
        }
        stage(1, 0); stage(1, 1);
        asm volatile("s_waitcnt lgkmcnt(0)" ::: "memory");
        u32x4 w[4];
#pragma unroll
        for (int i = 0; i < 4; ++i) w[i] = *LP(u32x4, srd + i * 1024);
#pragma unroll
        for (int i = 0; i < 4; ++i) ST16(w[i], po[i], 0);
        __builtin_amdgcn_sched_barrier(0);
        STAMP(26);
        u32x4 mlrec;
        {
            const f32x4 b0 = *LP(f32x4, esr), b1 = *LP(f32x4, esr + 16), b2 = *LP(f32x4, esr + 32), b3 = *LP(f32x4, esr + 48);
            const f32x4 bs = (b0 + b1) + (b2 + b3);
            mlrec = u32x4{__float_as_uint(mrun), __float_as_uint((bs[0] + bs[1]) + (bs[2] + bs[3])), tlo, thi};
            PIN(mlrec);
        }
        VMW(0);
        wg_barrier();
        if (wv == 0) {
            if (lane < 32) ST16(mlrec, mlo, 0);
            PIN(fa);
            if (s < 11 && !__all(flags_ok(fa))) flags_wait(fbo);
        }
        STAMP(27);
        wg_barrier();
    }
    const unsigned long long t_att = __builtin_amdgcn_s_memrealtime();
    rederive();
    while (__builtin_amdgcn_s_memrealtime() < t_att + (s >= 11 ? MD0 : MD1)) __builtin_amdgcn_s_sleep(2);
    if (s >= 11) {
        const unsigned n = (unsigned)((s - 11) * 8 + wv);
        if (n < 28) pf4<NTM & 32>((n * 64 + lane) * 1024u + x * 128u, p.w2, 0);
        { const unsigned l2 = n * 64 + lane; pf4<NTM & 32>((l2 / 5u) * (DFF * 2) + min(((unsigned)x * 448u & ~127u) + (l2 % 5u) * 128u, DFF * 2 - 4u), p.w1, 0); }
        if (wv == 0) flag_put(WS_FA, id - 128);
        STAMP_FLUSH();
        return;
    }
    const unsigned L_H2S = L_SCR, L_REDM = L_SCR + 16384;
    if (s < 7) {
        {
            const char* w1s = (const char*)(p.w1 + (size_t)wv * 64 * DFF + ug * 32);
#pragma unroll
            for (int i = 0; i < 4; ++i) dma16<NTM & 32>((unsigned)((lane >> 2) * DFF * 2 + (lane & 3) * 16), w1s + (size_t)i * 16 * DFF * 2, wv * 4096 + i * 1024);
        }
        const int b = wv;
        if (wv == 0) ST16(s4, (unsigned)(WS_U + x * STR + (lane >> 5) * 8192 + s * 512 + (lane & 31) * 16), 0);
        const u32x4 shf = LD16((unsigned)(WS_MB + b * STR + lane * 16), 0), scl = LD16((unsigned)(WS_MB + b * STR + 1024 + lane * 16), 0);
        VMW(0);
        if (wv == 0) flag_put(WS_FA, id - 128);
        wg_barrier();
        bf16x8 wk0[8], wk1[8];
        if (wv >= 6) {
#pragma unroll
            for (int q = 0; q < 8; ++q) {
                const unsigned a = (unsigned)(q * 4096 + (g * 8 + (nl >> 2)) * 64 + (wv & 1) * 32 + (lane & 3) * 8);
                wk0[q] = tr_frag(a, a + 256); wk1[q] = tr_frag(a + 2048, a + 2304);
            }
#pragma unroll
            for (int q = 0; q < 8; q += 2) PIN4(wk0[q], wk0[q + 1], wk1[q], wk1[q + 1]);
        }
        float scv[8], shv[8];
#pragma unroll
        for (int q = 0; q < 4; ++q) {
            scv[2 * q] = rbf(1.f + lo16(scl[q])); scv[2 * q + 1] = rbf(1.f + hi16(scl[q])); shv[2 * q] = lo16(shf[q]); shv[2 * q + 1] = hi16(shf[q]);
            PIN4(scv[2 * q], scv[2 * q + 1], shv[2 * q], shv[2 * q + 1]);
        }
        unsigned har = L_H2S + ((lane & 7) * HS_LD + g * 8 + (nl >> 3) * 32) * 2, haw = L_H2S + (b * HS_LD + lane * 8) * 2;
        unsigned udst = (unsigned)(WS_U + x * STR + (nl >> 3) * 8192 + (s * 2 + (wv & 1)) * 256 + ((nl & 7) * 16 + g * 4) * 2), xsrc = (unsigned)(WS_X1 + x * STR + (b * 512 + lane * 8) * 2) + ((npm >> x) & 1u) * 8192u;
        PIN4(har, haw, udst, xsrc);
        STAMP(28);
        while (__builtin_amdgcn_s_memrealtime() < t_att + XGATE) __builtin_amdgcn_s_sleep(2);
        u32x4 xv;
        for (unsigned spins = 0;;) {
            xv = LD16(xsrc, 0);
            if (__all(mx4(xv, 0u) != SENT)) break;
            __builtin_amdgcn_s_sleep(SLPX);
            if (++spins > kSpinCap) break;
            asm volatile("" ::: "memory");
        }
        STAMP(29);
        {
            f32x2_t v[4];
#pragma unroll
            for (int q = 0; q < 4; ++q) v[q] = f32x2_t{lo16(xv[q]), hi16(xv[q])};
            const f32x2_t p1 = (v[0] + v[1]) + (v[2] + v[3]), p2 = (v[0] * v[0] + v[1] * v[1]) + (v[2] * v[2] + v[3] * v[3]);
            float mb, rden;
            ln_stats(p1[0] + p1[1], p2[0] + p2[1], mb, rden);
            u32x4 hr;
#pragma unroll
            for (int q = 0; q < 4; ++q) {
                const f32x2_t h = ((v[q] - mb) * rden) * f32x2_t{scv[2 * q], scv[2 * q + 1]} + f32x2_t{shv[2 * q], shv[2 * q + 1]};
                hr[q] = pk2(h[0], h[1]);
            }
            *LP(u32x4, haw) = hr;
        }
        wg_barrier();
        if (wv >= 6) {
            bf16x8 af[8];
#pragma unroll
            for (int q = 0; q < 8; ++q) af[q] = *LP(bf16x8, har + q * 128);
            __builtin_amdgcn_sched_barrier(0);
            f32x4 d0 = {0.f, 0.f, 0.f, 0.f}, d1 = {0.f, 0.f, 0.f, 0.f};
#pragma unroll
            for (int q = 0; q < 8; ++q) { d0 = MFMA16(wk0[q], af[q], d0); d1 = MFMA16(wk1[q], af[q], d1); }
            const f32x4 own = nl < 8 ? d0 : d1;
#pragma unroll
            for (int e = 0; e < 4; ++e) { float v = own[e] + dppf<0x128>(own[e]); asm("" : "+v"(v)); d0[e] = fmaxf(v, 0.f); }
            u32x2 uv = {pk2(d0[0], d0[1]), pk2(d0[2], d0[3])};
            if (__builtin_expect(max(uv[0], uv[1]) == SENT, 0)) uv = u32x2{pk2s(d0[0], d0[1]), pk2s(d0[2], d0[3])};
            __builtin_amdgcn_raw_buffer_store_b64(uv, rw, udst, 0, 16);
            STAMP(30);
        }
        STAMP_FLUSH();
        return;
    }
    {
        const int n0 = (x * 4 + (s - 7)) * 16;
        const char* mws = (const char*)(p.w2 + (size_t)wv * 224 * 512 + n0);
#pragma unroll
        for (int i = 0; i < 7; ++i) dma16<NTM & 32>((unsigned)((lane >> 1) * 1024 + (lane & 1) * 16), mws + i * 32768, rgn + i * 1024);
        const int fb = lane >> 3, fc = (lane & 7) * 2;
        u32x2 gb = {0u, 0u};
        if (wv == 0) {
            flag_put(WS_FA, id - 128);
            gb = mod_get(WS_MOD + fb * STR + (1536 + n0 + fc) * 8);
            flags_wait(WS_FA + x * STR + lane * 16);
            if (id == 23 * 8 && lane == 0) __builtin_amdgcn_raw_buffer_store_b64(u32x2{tlo, thi}, rw, WS_EPOCH, 0, 16);
            PIN(gb);
        }
        VMW(0);
        bf16x8 wfr[7];
        const unsigned trl = (unsigned)(g * 8 + (nl >> 2)) * 32u + (unsigned)(lane & 3) * 8u;
#pragma unroll
        for (int ks = 0; ks < 7; ++ks) { wfr[ks] = tr_frag(rgn + ks * 1024 + trl, rgn + ks * 1024 + trl + 128); PIN(wfr[ks]); }
        unsigned usrc = (unsigned)(WS_U + wv * STR + (g >> 1) * 256 + (nl & 7) * 32 + (g & 1) * 16) + ((npm >> x) & 1u) * 8192u;
        unsigned rdw = L_REDM + wv * 512 + (nl & 7) * 64 + g * 16, rdr = L_REDM + lane * 8;
        unsigned short* outp = p.out + fb * 512 + n0 + fc;
        PIN(usrc); PIN(rdw); PIN(rdr); PIN(outp);
        STAMP(28);
        unsigned xg = 0u;
        if (wv == 0) {
            while (__builtin_amdgcn_s_memrealtime() < t_att + XGATE) __builtin_amdgcn_s_sleep(2);
            for (unsigned spins = 0;;) {
                xg = __builtin_amdgcn_raw_buffer_load_b32(rw, (unsigned)(WS_X1 + x * STR + (fb * 512 + n0 + fc) * 2) + ((npm >> x) & 1u) * 8192u, 0, 16);
                if (__all(xg != SENT)) break;
                __builtin_amdgcn_s_sleep(SLPD);
                if (++spins > kSpinCap) break;
                asm volatile("" ::: "memory");
            }
            __builtin_amdgcn_s_sleep(SLPG);
        }
        wg_barrier();
        u32x4 uf[7];
        for (unsigned spins = 0;;) {
#pragma unroll
            for (int ks = 0; ks < 7; ++ks) uf[ks] = LD16(usrc + ks * 512, 0);
            unsigned m = 0u;
#pragma unroll
            for (int ks = 0; ks < 7; ++ks) m = mx4(uf[ks], m);
            if (__all(m != SENT)) break;
            if (++spins > kSpinCap) break;
            asm volatile("" ::: "memory");
        }
        STAMP(29);
        f32x4 d = {0.f, 0.f, 0.f, 0.f}, d2 = {0.f, 0.f, 0.f, 0.f};
#pragma unroll
        for (int ks = 0; ks < 7; ++ks) {
            if (ks & 1) d2 = MFMA16(wfr[ks], BF8(uf[ks]), d2);
            else d = MFMA16(wfr[ks], BF8(uf[ks]), d);
        }
        *LP(f32x4, rdw) = d + d2;
        wg_barrier();
        if (wv == 0) {
            f32x2_t c[8];
#pragma unroll
            for (int w2 = 0; w2 < 8; ++w2) c[w2] = *LP(f32x2_t, rdr + w2 * 512);
            const f32x2_t sm = ((c[0] + c[1]) + (c[2] + c[3])) + ((c[4] + c[5]) + (c[6] + c[7]));
            unsigned t = pk2(sm[0], sm[1]);
            t = pk2(__uint_as_float(gb[0]) * lo16(t), __uint_as_float(gb[1]) * hi16(t));
            *(GAS unsigned*)outp = pk2(lo16(xg) + lo16(t), hi16(xg) + hi16(t));
            STAMP(31);
        }
        STAMP_FLUSH();
    }
}

extern "C" void kernel_launch(void* const* d_in, const int* in_sizes, int n_in,
                              void* d_out, int out_size, void* d_ws, size_t ws_size,
                              hipStream_t stream) {
    Params p;
    p.x = (const unsigned short*)d_in[0];
    p.ckv = (const unsigned short*)d_in[1];
    p.w_q = (const unsigned short*)d_in[2];
    p.w_uk = (const unsigned short*)d_in[3];
    p.w_uv = (const unsigned short*)d_in[4];
    p.w_o = (const unsigned short*)d_in[5];
    p.w1 = (const unsigned short*)d_in[7];
    p.w2 = (const unsigned short*)d_in[8];
    p.c = (const unsigned short*)d_in[9];
    p.w_mod = (const unsigned short*)d_in[10];
    p.ws = (char*)d_ws;
    p.out = (unsigned short*)d_out;
    (void)hipFuncSetAttribute((const void*)k_main, hipFuncAttributeMaxDynamicSharedMemorySize, 163840);
    k_main<<<256, 512, 163840, stream>>>(p);
}
```

```cpp
#define MEAS 4
#define AVG 0
#include <hip/hip_runtime.h>
#include <hip/hip_bf16.h>
#include <stdint.h>

#define NH 32
#define SK 4096
#define DFF 1792
#define GI 19
#define ROTC 1
#define PFS 2
#define XNO 0
#define CPJ 7
#define NTM 16
#define QHELP 4
#define HP 13
#define NTH 0
#define W0 2
#define SL0 1
#define LMPF 0
#define SLPB 20
#define MD0 0
#define MD1 0
#define NEARP 1
#define NCAL 2
#define TLBW 0
#define TWD 680
#define LMD 250
#define PFM 57
#define CPFE 34200
#define SLPM 0
#define SLPD 12
#define SLPG 8
#define SLPX 1
#define SLPH 4

using u16x8 = __attribute__((ext_vector_type(8))) unsigned short;
using s16x4 = __attribute__((ext_vector_type(4))) short;
using bf16x8 = __attribute__((ext_vector_type(8))) short;
using f32x16 = __attribute__((ext_vector_type(16))) float;
using f32x4 = __attribute__((ext_vector_type(4))) float;
using u32x4 = __attribute__((ext_vector_type(4))) unsigned;
using u32x2 = __attribute__((ext_vector_type(2))) unsigned;

#define GAS __attribute__((address_space(1)))
#define LAS __attribute__((address_space(3)))
typedef GAS unsigned long long gu64;
#define RLX_AGENT __ATOMIC_RELAXED, __HIP_MEMORY_SCOPE_AGENT
#define SENT 0xffffffffu
#define TAGC 0x9e3779b9u

__device__ __forceinline__ float bf2f(unsigned short u) { return __uint_as_float(((unsigned)u) << 16); }
typedef __attribute__((ext_vector_type(2))) __bf16 bf16x2_t;
typedef __attribute__((ext_vector_type(2))) float f32x2_t;
__device__ __forceinline__ unsigned pk2(float a, float b) {
    f32x2_t v = {a, b};
    bf16x2_t r = __builtin_convertvector(v, bf16x2_t);
    return __builtin_bit_cast(unsigned, r);
}
__device__ __forceinline__ unsigned pk2s(float a, float b) {
    const unsigned r = pk2(a, b);
    return r == SENT ? 0x7fc07fc0u : r;
}
__device__ __forceinline__ unsigned f32s(float a) {
    const unsigned r = __float_as_uint(a);
    return r == SENT ? 0x7fc00000u : r;
}
__device__ __forceinline__ unsigned short f2bf(float f) { return (unsigned short)(pk2(f, 0.f) & 0xffffu); }
__device__ __forceinline__ float rbf(float f) { return __uint_as_float(pk2(f, 0.f) << 16); }
__device__ __forceinline__ float lo16(unsigned u) { return __uint_as_float(u << 16); }
__device__ __forceinline__ float hi16(unsigned u) { return __uint_as_float(u & 0xffff0000u); }
__device__ __forceinline__ unsigned umax3(unsigned a, unsigned b, unsigned c) { return max(max(a, b), c); }
__device__ __forceinline__ unsigned mx4(const u32x4& v, unsigned m) { return umax3(v[2], v[3], umax3(v[0], v[1], m)); }
__device__ __forceinline__ float fmax3(float a, float b, float c) { float r; asm("v_max3_f32 %0, %1, %2, %3" : "=v"(r) : "v"(a), "v"(b), "v"(c)); return r; }
template <int C> __device__ __forceinline__ float dppf(float v) { return __int_as_float(__builtin_amdgcn_update_dpp(0, __float_as_int(v), C, 0xf, 0xf, true)); }
__device__ __forceinline__ float sum16(float v) { v += dppf<0xB1>(v); v += dppf<0x4E>(v); v += dppf<0x141>(v); v += dppf<0x140>(v); return v; }

__device__ __forceinline__ void ln_stats(float p1, float p2, float& mb, float& rden) {
    auto t = __builtin_amdgcn_permlane32_swap(__float_as_uint(p1), __float_as_uint(p2), false, false);
    float a = sum16(__uint_as_float(t[0]) + __uint_as_float(t[1]));
    auto t2 = __builtin_amdgcn_permlane16_swap(__float_as_uint(a), __float_as_uint(a), false, false);
    a = (__uint_as_float(t2[0]) + __uint_as_float(t2[1])) * (1.f / 512.f);
    auto t3 = __builtin_amdgcn_permlane32_swap(__float_as_uint(a), __float_as_uint(a), false, false);
    const float m = __uint_as_float(t3[0]);
    const float var = rbf(fmaxf(fmaf(-m, m, __uint_as_float(t3[1])), 0.f));
    mb = rbf(m);
    rden = __builtin_amdgcn_rcpf(rbf(__builtin_amdgcn_sqrtf(rbf(var + 1e-5f))));
}
#define STR 262144
#define WS_Q 0
#define WS_H 32768
#define WS_X1 65536
#define WS_ML 81920
#define WS_MOD 98304
#define WS_MB 139264
#define WS_U 114688
#define WS_FI 131072
#define WS_FA 132096
#define WS_FB 133120
#define WS_NP 134144
#define WS_EPOCH 135168
#define WS_CAL 147456
#define WS_OUTP 2097152
#define WS_PART 4194304
#define WS_END 12582912

#define LP(T, off) ((LAS T*)(size_t)(unsigned)(off))
#define STAMP(k) do { if (((k) == MEAS || ((k) == 0 && (blockIdx.x >> 4) == 7 && wv == 7)) && (threadIdx.x & 63) == 0) __hip_atomic_store((gu64*)(p.ws + (128u << 20)) + ((k) == 0 ? 0 : 2048) + blockIdx.x * 8 + wv, (unsigned long long)__builtin_amdgcn_s_memrealtime(), RLX_AGENT); \
    if ((k) == 31) { \
        const unsigned long long t1 = __builtin_amdgcn_s_memrealtime(); \
        unsigned long long m = ~0ull, mx = 0ull; \
        for (int q = 0; q < 32; ++q) { const unsigned long long v = __hip_atomic_load((gu64*)(p.ws + (128u << 20)) + q * 64 + (threadIdx.x & 63), RLX_AGENT); if (t1 - v < 10000ull) m = v < m ? v : m; } \
        for (int o = 1; o < 64; o <<= 1) { const unsigned long long v = __shfl_xor(m, o); m = v < m ? v : m; } \
        for (int q = 0; q < 32; ++q) { const unsigned long long v = __hip_atomic_load((gu64*)(p.ws + (128u << 20)) + 2048 + q * 64 + (threadIdx.x & 63), RLX_AGENT); if (v - m < 10000ull) mx = v > mx ? v : mx; } \
        for (int o = 1; o < 64; o <<= 1) { const unsigned long long v = __shfl_xor(mx, o); mx = v > mx ? v : mx; } \
        if (AVG) { unsigned sm = 0, cn = 0; \
            for (int q = 0; q < 32; ++q) { const unsigned long long v = __hip_atomic_load((gu64*)(p.ws + (128u << 20)) + 2048 + q * 64 + (threadIdx.x & 63), RLX_AGENT); if (v - m < 10000ull) { sm += (unsigned)(v - m); ++cn; } } \
            for (int o = 1; o < 64; o <<= 1) { sm += __shfl_xor(sm, o); cn += __shfl_xor(cn, o); } \
            mx = m + sm / max(cn, 1u); } \
        if (MEAS == 31) mx = t1; \
        const unsigned dl = MEAS == 99 ? 0u : min(__builtin_amdgcn_readfirstlane((unsigned)(mx - m)), 5000u) * 4u; \
        while (__builtin_amdgcn_s_memrealtime() < t1 + dl) __builtin_amdgcn_s_sleep(1); \
    } } while (0)
#define STAMP_FLUSH() do {} while (0)
__device__ __forceinline__ s16x4 tr4(unsigned a) { return __builtin_amdgcn_ds_read_tr16_b64_v4i16(LP(s16x4, a)); }
__device__ __forceinline__ bf16x8 cat8(s16x4 a, s16x4 b) { return __builtin_shufflevector(a, b, 0, 1, 2, 3, 4, 5, 6, 7); }
__device__ __forceinline__ bf16x8 tr_frag(unsigned a0, unsigned a1) { return cat8(tr4(a0), tr4(a1)); }
__device__ __forceinline__ unsigned swz(unsigned row) { return ((row & 3) << 2) | ((row >> 2) & 3); }
__device__ __forceinline__ unsigned off_b(unsigned row, unsigned ch) { return 256u * row + 16u * (ch ^ swz(row)); }
__device__ __forceinline__ unsigned tile_off(unsigned key, unsigned chunk) { return ((key >> 5) * 4 + (chunk >> 4)) * 8192u + off_b(key & 31, chunk & 15); }
__device__ __forceinline__ void wg_barrier() { asm volatile("s_waitcnt lgkmcnt(0)\n\ts_barrier" ::: "memory"); }
template <int NT> __device__ __forceinline__ void dma16(unsigned voff, const void* sbase, unsigned lds_dst) {
    if (NT) asm volatile("s_mov_b32 m0, %2\n\ts_nop 0\n\tglobal_load_lds_dwordx4 %0, %1 nt" :: "v"(voff), "s"(sbase), "s"(lds_dst) : "memory", "m0");
    else asm volatile("s_mov_b32 m0, %2\n\ts_nop 0\n\tglobal_load_lds_dwordx4 %0, %1" :: "v"(voff), "s"(sbase), "s"(lds_dst) : "memory", "m0");
}
template <int NT> __device__ __forceinline__ void dma16x(unsigned voff, unsigned vx, const void* sbase, unsigned lds_dst) {
    unsigned t;
#define DMAX(pol, c) asm volatile("s_mov_b32 m0, %3\n\tv_xor_b32 %0, %4, %1\n\tglobal_load_lds_dwordx4 %0, %2" pol : "=&v"(t) : "v"(voff), "s"(sbase), "s"(lds_dst), c(vx) : "memory", "m0")
    if (__builtin_constant_p(vx)) { if (NT) DMAX(" nt", "i"); else DMAX("", "i"); }
    else { if (NT) DMAX(" nt", "s"); else DMAX("", "s"); }
}
template <int NT> __device__ __forceinline__ void pf4(unsigned voff, const void* sbase, unsigned lds_dst) {
    if (NT) asm volatile("s_mov_b32 m0, %2\n\ts_nop 0\n\tglobal_load_lds_dword %0, %1 nt" :: "v"(voff), "s"(sbase), "s"(lds_dst) : "memory", "m0");
    else asm volatile("s_mov_b32 m0, %2\n\ts_nop 0\n\tglobal_load_lds_dword %0, %1" :: "v"(voff), "s"(sbase), "s"(lds_dst) : "memory", "m0");
}
#define XGATE 300
#define SPINCAP (1u << 20)
constexpr unsigned kSpinCap = SPINCAP;
struct Params {
    const unsigned short* x;
    const unsigned short* ckv;
    const unsigned short* w_q;
    const unsigned short* w_uk;
    const unsigned short* w_uv;
    const unsigned short* w_o;
    const unsigned short* w1;
    const unsigned short* w2;
    const unsigned short* c;
    const unsigned short* w_mod;
    char* ws;
    unsigned short* out;
};
#define VMW(n) asm volatile("s_waitcnt vmcnt(%0)" :: "n"(n) : "memory")
#define PIN(x) asm volatile("" : "+v"(x))
#define PIN4(a, b, c, d) asm volatile("" : "+v"(a), "+v"(b), "+v"(c), "+v"(d) :: "memory")
#define MFMA16(a, b, c) __builtin_amdgcn_mfma_f32_16x16x32_bf16(a, b, c, 0, 0, 0)
#define MFMA32(a, b, c) __builtin_amdgcn_mfma_f32_32x32x16_bf16(a, b, c, 0, 0, 0)
#define BF8(x) __builtin_bit_cast(bf16x8, x)
#define ST16(v, off, soff) __builtin_amdgcn_raw_buffer_store_b128(v, rw, off, soff, 16)
#define LD16(off, soff) __builtin_amdgcn_raw_buffer_load_b128(rw, off, soff, 16)
#define SCL (0.08838834764831845f * 1.4426950408889634f)
#define BIGD 60.f
#define L_SCR 131072
#define HS_LD 520

__global__ __launch_bounds__(512, 2) void k_main(Params p) {
    unsigned long long pc;
    asm volatile("s_getpc_b64 %0" : "=s"(pc));
    const int id = blockIdx.x, tid = threadIdx.x;
    int lane = tid & 63;
    const int wv = __builtin_amdgcn_readfirstlane(tid >> 6);
    int g = lane >> 4, nl = lane & 15;
    auto rederive = [&]() {
        asm volatile("v_mbcnt_lo_u32_b32 %0, -1, 0\n\tv_mbcnt_hi_u32_b32 %0, -1, %0" : "=v"(lane));
        g = lane >> 4; nl = lane & 15;
    };
    const int x = id & 7, j = id >> 3;
    if (wv < 5 && j == 15) {
        asm volatile("s_mov_b32 m0, %2\n\ts_nop 0\n\tglobal_load_lds_dword %0, %1" :: "v"(min((unsigned)((wv * 64 + lane) * 128), (unsigned)CPFE)), "s"(pc), "s"(L_SCR + 31744) : "memory", "m0");
    }
    if ((j >> 2) == 3) {
        dma16<0>((unsigned)(((nl & 7) * 512 + (nl >> 3) * 32 + g * 8) * 2 + wv * 128), p.c, wv * 16384 + 8192);
        dma16<0>((unsigned)tid * 16u, p.x, wv * 16384 + 9216);
    }
    char* ws = p.ws;
    STAMP(0);
    const __amdgpu_buffer_rsrc_t rw = __builtin_amdgcn_make_buffer_rsrc(ws, 0, WS_END, 0x00020000);
    const u32x4 s4 = {SENT, SENT, SENT, SENT};
    unsigned long long cnt = 0ull;
    auto cnt_load = [&]() { asm volatile("global_load_dwordx2 %0, %1, %2 sc1" : "+v"(cnt) : "v"((unsigned)WS_EPOCH), "s"(ws) : "memory"); };
    unsigned tlo = 0u, thi = 0u;
    auto tag_take = [&]() {
        asm volatile("" : "+v"(cnt) :: "memory");
        const unsigned long long t = cnt + 1ull;
        tlo = __builtin_amdgcn_readfirstlane((unsigned)t); thi = __builtin_amdgcn_readfirstlane((unsigned)(t >> 32));
    };
    auto flag_put = [&](unsigned base, unsigned idx) {
        if (lane < 8) __builtin_amdgcn_raw_buffer_store_b64(u32x2{tlo, thi}, rw, base + lane * STR + idx * 8, 0, 16);
    };
    auto mod_get = [&](unsigned off) {
        u32x4 v;
        for (unsigned spins = 0;;) {
            v = LD16(off, 0);
            if (__all((v[2] == tlo) & (v[3] == thi))) break;
            __builtin_amdgcn_s_sleep(16);
            if (++spins > kSpinCap) break;
            asm volatile("" ::: "memory");
        }
        return u32x2{v[0], v[1]};
    };
    auto np_mask = [&](u32x4 v) {
        for (unsigned spins = 0; !__all((v[2] == tlo) & (v[3] == thi)) && spins < kSpinCap; ++spins) {
            __builtin_amdgcn_s_sleep(8);
            asm volatile("buffer_load_dwordx4 %0, %1, %2, 0 offen sc1\n\ts_waitcnt vmcnt(0)" : "=v"(v) : "v"((unsigned)(WS_NP + x * STR + (lane & 7) * 16)), "s"(rw) : "memory");
        }
        const int d = (int)v[0];
        int t = (lane & 4) ? -d : d;
        t += __builtin_amdgcn_update_dpp(0, t, 0xB1, 0xf, 0xf, true);
        t += __builtin_amdgcn_update_dpp(0, t, 0x4E, 0xf, 0xf, true);
        t += __builtin_amdgcn_update_dpp(0, t, 0x141, 0xf, 0xf, true);
        const bool n1 = abs(d) > 160 ? d < 0 : (t < 0) != ((lane & 4) != 0);
        return NEARP ? (unsigned)__ballot(n1) & 0xffu : 0u;
    };
    auto flags_ok = [&](const u32x4& f) { return (f[0] == tlo) & (f[1] == thi) & (f[2] == tlo) & (f[3] == thi); };
    auto flags_wait = [&](unsigned off) {
        for (unsigned spins = 0;;) {
            u32x4 f;
            asm volatile("buffer_load_dwordx4 %0, %1, %2, 0 offen sc1\n\ts_waitcnt vmcnt(0)" : "=v"(f) : "v"(off), "s"(rw) : "memory");
            if (__all(flags_ok(f))) break;
            __builtin_amdgcn_s_sleep(16);
            if (++spins > kSpinCap) break;
            asm volatile("" ::: "memory");
        }
    };
    const unsigned pcl = (unsigned)(((((lane & 7) >> 1) ^ ((lane >> 4) & 1)) * 32) + (lane & 1) * 16);
    auto dma_piece = [&](const unsigned short* src, unsigned ldb, unsigned dst, int n, int nt) {
        const unsigned v0 = (lane >> 3) * ldb + pcl, v1 = v0 ^ 64u;
#pragma unroll
        for (int i = 0; i < n; ++i) {
            if (nt) dma16<1>((i & 1) ? v1 : v0, (const char*)src + (size_t)i * 8 * ldb, dst + i * 1024);
            else dma16<0>((i & 1) ? v1 : v0, (const char*)src + (size_t)i * 8 * ldb, dst + i * 1024);
        }
    };
    const unsigned frl = (unsigned)((g * 8 + (nl >> 2)) * 128 + (lane & 3) * 8), frs = (unsigned)((nl >> 3) | ((g & 1) << 1));
    auto piece_frag = [&](unsigned base, int ks, int t) {
        const unsigned a = base + frl + ((t ^ frs) << 5) + ks * 4096;
        return tr_frag(a, a + 512);
    };
    const unsigned rgn = (unsigned)(wv * 16384);

    if (j < 16) {
        const unsigned long long t_start = __builtin_amdgcn_s_memrealtime();
        const int th = x * 4 + (j & 3);
        if (j < 12) {
            const int qt = j >> 2, rot = qt * 5;
            const bool pub = wv < (qt < 2 ? 3 : 2);
            const unsigned qo = (unsigned)(WS_Q + (th & 7) * STR + ((nl & 7) * 4 + (th >> 3)) * 1024 + (qt * 192 + wv * 64 + g * 16 + (nl >> 3) * 8) * 2);
            const unsigned vb = (unsigned)(g * 8192 + ((nl ^ (g << 2)) << 4));
            const char* wuk = (const char*)(p.w_uk + th * 128) + (size_t)qt * 192 * 8192;
            const char* wq = (const char*)(p.w_q + th * 128) + (size_t)wv * 32768;
            const unsigned L_U = 114688;
            {
                const int k0 = qt < 2 ? wv * 6 : wv * 4;
#pragma unroll
                for (int i = 0; i < 6; ++i) {
                    if (i < 4 || qt < 2) dma16<NTM & 2>(vb ^ (((k0 + i) & 3) << 4), wuk + (size_t)(k0 + i) * 32768, L_U + (k0 + i) * 1024);
                    if (i == 4) VMW(4);
                }
            }
            const unsigned vq = vb ^ ((wv & 3) << 4);
            auto wq_dma = [&](int ps) { dma16<NTM & 2>(vq, wq + (size_t)((ps + rot) & 15) * 262144, ps * 8192 + wv * 1024); };
            bf16x8 uk[4][4], aq[16];
            u32x4 fi, hv[4];
            unsigned fio = (unsigned)(WS_FI + x * STR + (lane & 7) * 16), hsrc, hsrc2, ta0, ta1, tb0, tb1, qo2, hw0, hw1, hr, hr2, qw, thx = 0u;
            const unsigned L_HI = L_SCR, L_QI = L_SCR + 8192;
            auto h_load = [&]() {
                asm volatile("buffer_load_dwordx4 %0, %4, %6, 0 offen sc1\n\tbuffer_load_dwordx4 %1, %4, %6, 0 offen offset:1024 sc1\n\t"
                             "buffer_load_dwordx4 %2, %5, %6, 0 offen sc1\n\tbuffer_load_dwordx4 %3, %5, %6, 0 offen offset:1024 sc1"
                             : "+v"(hv[0]), "+v"(hv[1]), "+v"(hv[2]), "+v"(hv[3]) : "v"(hsrc), "v"(hsrc2), "s"(rw) : "memory");
            };
            auto h_bad = [&]() {
                PIN4(hv[0], hv[1], hv[2], hv[3]);
                unsigned bad = (hv[0][1] ^ tlo) | (hv[0][3] ^ thx) | (hv[1][1] ^ tlo) | (hv[1][3] ^ thx) | (hv[2][1] ^ tlo) | (hv[2][3] ^ thx) | (hv[3][1] ^ tlo) | (hv[3][3] ^ thx);
                PIN(bad);
                return bad;
            };
#pragma unroll
            for (int ps = 0; ps < 16; ++ps) {
                wq_dma(ps);
                if (ps == 4) cnt_load();
                if (ps == HP) { h_load(); asm volatile("buffer_load_dwordx4 %0, %1, %2, 0 offen sc1" : "=v"(fi) : "v"(fio), "s"(rw) : "memory"); }
                if (ps >= 4 && ps < 8) VMW(5); else if (ps >= HP && ps < HP + 4) VMW(9); else VMW(4);
                if (ps == 3) {
                    asm volatile("s_barrier" ::: "memory");
                    if (pub) {
#pragma unroll
                        for (int t = 0; t < 4; ++t)
#pragma unroll
                            for (int s = 0; s < 4; ++s) uk[t][s] = *LP(bf16x8, L_U + off_b(wv * 64 + (nl >> 2) * 16 + t * 4 + (nl & 3), s * 4 + g));
                    }
                    STAMP(1);
                }
                if (ps == 5) {
                    hsrc = (unsigned)(WS_H + x * STR + wv * 2048 + lane * 16); hsrc2 = hsrc + 16384;
                    ta0 = off_b(8 * g + (nl >> 2), 2 * wv + ((lane & 3) >> 1)) + 8 * (lane & 1); ta1 = off_b(8 * g + 4 + (nl >> 2), 2 * wv + ((lane & 3) >> 1)) + 8 * (lane & 1);
                    tb0 = ta0 + 65536; tb1 = ta1 + 65536; qo2 = qo;
                    hw0 = L_HI + ((2 * wv - rot) & 15) * 512 + (lane >> 4) * 128 + (lane & 7) * 16 + ((lane >> 3) & 1) * 8;
                    hw1 = L_HI + ((2 * wv + 1 - rot) & 15) * 512 + (lane >> 4) * 128 + (lane & 7) * 16 + ((lane >> 3) & 1) * 8;
                    hr2 = L_HI + g * 128 + (nl & 7) * 16 + (nl >> 3) * 512;
                    PIN(hr2);
                    hr = L_HI + g * 128 + (nl & 7) * 16; qw = L_QI + (wv >> 1) * 512 + ((wv & 1) * 2 + (g >> 1)) * 128 + (nl & 7) * 16 + (g & 1) * 8;
                    PIN4(hsrc, hsrc2, ta0, ta1); PIN4(tb0, tb1, qo2, hw0); PIN(hw1); PIN(hr); PIN(qw);
                }
                if (ps == 8) { tag_take(); thx = tlo ^ thi ^ TAGC; }
                if (ps == 9) {
#pragma unroll
                    for (int t = 0; t < 4; ++t) PIN4(uk[t][0], uk[t][1], uk[t][2], uk[t][3]);
                    wg_barrier();
#pragma unroll
                    for (int q = 0; q < 6; ++q) aq[q] = tr_frag(ta0 + q * 8192, ta1 + q * 8192);
                }
                if (ps == 13) {
                    asm volatile("s_barrier" ::: "memory");
#pragma unroll
                    for (int q = 6; q < 10; ++q) aq[q] = q < 8 ? tr_frag(ta0 + q * 8192, ta1 + q * 8192) : tr_frag(tb0 + (q - 8) * 8192, tb1 + (q - 8) * 8192);
                }
            }
            STAMP(15);
            if (HP + 4 > 15) VMW(HP + 4 > 15 ? 15 - HP : 0);
            if (!__all(h_bad() == 0u)) {
                for (unsigned spins = 0;;) {
                    h_load();
                    VMW(0);
                    if (__all(h_bad() == 0u)) break;
                    __builtin_amdgcn_s_sleep(SLPH);
                    if (++spins > kSpinCap) break;
                }
            }
            {
                unsigned hh[4];
#pragma unroll
                for (int q = 0; q < 4; ++q) {
                    const unsigned t = hv[2 + (q >> 1)][(q & 1) * 2], sh = hv[q >> 1][(q & 1) * 2];
                    hh[q] = pk2(lo16(t) + lo16(sh), hi16(t) + hi16(sh));
                }
                *LP(u32x2, hw0) = u32x2{hh[0], hh[1]};
                *LP(u32x2, hw1) = u32x2{hh[2], hh[3]};
            }
            STAMP(3);
            asm volatile("s_waitcnt vmcnt(0) lgkmcnt(0)\n\ts_barrier" : "+v"(fi) :: "memory");
            STAMP(2);
            {
                bf16x8 hb[8];
#pragma unroll
                for (int q = 0; q < 5; ++q) hb[q] = *LP(bf16x8, hr2 + q * 1024);
#pragma unroll
                for (int ps = 10; ps < 16; ++ps) {
                    if (!(ps & 1)) hb[ps >> 1] = *LP(bf16x8, hr2 + (ps >> 1) * 1024);
                    aq[ps] = tr_frag(tb0 + (ps - 8) * 8192, tb1 + (ps - 8) * 8192);
                }
                __builtin_amdgcn_sched_barrier(0);
                f32x4 d0 = {0.f, 0.f, 0.f, 0.f}, d1 = {0.f, 0.f, 0.f, 0.f};
#pragma unroll
                for (int ps = 0; ps < 16; ps += 2) {
                    d0 = MFMA16(aq[ps], hb[ps >> 1], d0); d1 = MFMA16(aq[ps + 1], hb[ps >> 1], d1);
                    if (ps == 6) { unsigned bad = (fi[0] ^ tlo) | (fi[1] ^ thi) | (fi[2] ^ tlo) | (fi[3] ^ thi); PIN(bad); if (!__all(bad == 0u)) flags_wait(fio); }
                }
#pragma unroll
                for (int e = 0; e < 4; ++e) { float v = d0[e] + dppf<0x128>(d1[e]); asm("" : "+v"(v)); d0[e] = v; }
                if (nl < 8) *LP(u32x2, qw) = u32x2{pk2(d0[0], d0[1]), pk2(d0[2], d0[3])};
            }
            wg_barrier();
            if (pub) {
                bf16x8 qb[4];
#pragma unroll
                for (int s = 0; s < 4; ++s) qb[s] = *LP(bf16x8, hr + 8192 + s * 512);
                __builtin_amdgcn_sched_barrier(0);
                f32x4 d[4];
#pragma unroll
                for (int t = 0; t < 4; ++t) d[t] = MFMA16(uk[t][0], qb[0], (f32x4{0.f, 0.f, 0.f, 0.f}));
#pragma unroll
                for (int s = 1; s < 4; ++s)
#pragma unroll
                    for (int t = 0; t < 4; ++t) d[t] = MFMA16(uk[t][s], qb[s], d[t]);
                const f32x4 a = nl & 8 ? d[2] : d[0], b = nl & 8 ? d[3] : d[1];
                u32x4 qv = {pk2(a[0], a[1]), pk2(a[2], a[3]), pk2(b[0], b[1]), pk2(b[2], b[3])};
                if (__builtin_expect(mx4(qv, 0u) == SENT, 0)) qv = u32x4{pk2s(a[0], a[1]), pk2s(a[2], a[3]), pk2s(b[0], b[1]), pk2s(b[2], b[3])};
                ST16(qv, qo2, 0);
            }
            STAMP(4);
            wg_barrier();
            if (LMPF && wv >= 3 && wv < 6) {
                const unsigned n = (unsigned)(j * 3 + wv - 3);
                if (n < 32) pf4<0>(((n & 7u) * 64 + lane) * 6144u + 2048u + (x * 4 + (n >> 3)) * 128u, p.w_mod, L_SCR + 31744);
            }
            if (QHELP && wv < 3 && j + 12 * wv < 32) {
                const int hh = j + 12 * wv;
#pragma unroll 1
                for (unsigned spins = 0; spins < 64; ++spins) {
                    __builtin_amdgcn_s_sleep(QHELP);
                    const u32x4 v = LD16((unsigned)(WS_Q + (hh & 7) * STR + (x * 4 + (hh >> 3)) * 1024 + lane * 16), 0);
                    if (__all(mx4(v, 0u) != SENT)) break;
                    asm volatile("" ::: "memory");
                }
                STAMP(6);
            }
        } else {
            const int cg = x * 2 + (j & 1);
            if (j < 14) {
                dma_piece(p.w_mod + (size_t)wv * 64 * 3072 + cg * 64, 6144, rgn, 8, NTM & 2);
                cnt_load();
                VMW(9);
            } else {
                cnt_load();
                if (TLBW && j == 14) {
                    const char* base = wv == 0 ? (const char*)p.w_uv : wv == 1 ? (const char*)p.w_o : wv == 2 ? (const char*)p.w1 : wv == 3 ? (const char*)p.w2 : wv == 4 ? (const char*)p.out : ws + (wv - 5) * 4194304;
                    pf4<0>(min((unsigned)lane * 65536u, wv == 4 ? 0u : (wv & 6) == 2 ? 27u * 65536u : ~0u) + (wv > 4 ? 61440u : 0u), base, L_SCR + 31744);
                }
                ST16(s4, (unsigned)(WS_Q + wv * STR + cg * 2048 + lane * 16), 0); ST16(s4, (unsigned)(WS_Q + wv * STR + cg * 2048 + lane * 16), 1024);
                VMW(0);
                tag_take();
                wg_barrier();
                if (wv == 7) flag_put(WS_FI, cg);
                STAMP(15);
                if (j == 14) {
                    u32x2 best = {~0u, ~0u};
#pragma unroll
                    for (int r = 0; r < 2 * NCAL; ++r) {
                        const unsigned hf = (r ^ wv) & 1;
                        VMW(0);
                        const unsigned long long t0 = __builtin_amdgcn_s_memtime();
                        if (lane < 8) ST16(s4, WS_CAL + x * STR + hf * 8192 + (wv * 8 + r) * 128 + lane * 16, 0);
                        VMW(0);
                        const unsigned dt = (unsigned)(__builtin_amdgcn_s_memtime() - t0);
                        if (hf) best[1] = min(best[1], dt); else best[0] = min(best[0], dt);
                    }
                    *LP(u32x2, 16384 * 7 + 14336 + wv * 8) = best;
                    wg_barrier();
                    if (wv == 0 && lane < 8) {
                        u32x2 m = *LP(u32x2, 16384 * 7 + 14336 + lane * 8);
#pragma unroll
                        for (int o = 1; o < 8; o <<= 1) { m[0] = min(m[0], (unsigned)__shfl_xor(m[0], o)); m[1] = min(m[1], (unsigned)__shfl_xor(m[1], o)); }
                        ST16((u32x4{(unsigned)max(-1000, min(1000, (int)m[1] - (int)m[0])), 0u, tlo, thi}), (unsigned)(WS_NP + lane * STR + x * 16), 0);
                    }
                    STAMP(9);
                }
            }
            const u16x8 cf = *LP(u16x8, rgn + 8192 + lane * 16), xa = *LP(u16x8, rgn + 9216 + lane * 16);
            u32x4 af[2];
#pragma unroll
            for (int q = 0; q < 4; ++q) {
                const float a = bf2f(cf[2 * q]), b = bf2f(cf[2 * q + 1]);
                af[0][q] = pk2(a * __builtin_amdgcn_rcpf(1.f + __expf(-a)), b * __builtin_amdgcn_rcpf(1.f + __expf(-b)));
                af[1][q] = (unsigned)__builtin_amdgcn_update_dpp(0, (int)af[0][q], 0x128, 0xf, 0xf, true);
            }
            const unsigned L_MS = L_SCR;
            unsigned wa[4], fr[4];
#pragma unroll
            for (int t = 0; t < 4; ++t) { wa[t] = L_MS + wv * 2048 + (nl & 7) * 256 + (((t * 4 + g) ^ (nl & 7)) << 4) + (nl >> 3) * 16384; fr[t] = rgn + frl + ((t ^ frs) << 5); }
            PIN4(wa[0], wa[1], wa[2], wa[3]); PIN4(fr[0], fr[1], fr[2], fr[3]); PIN(af[0]); PIN(af[1]);
            const unsigned L_XN = 12288;
            auto mod_mm = [&](bool ln) {
                VMW(0);
                bf16x8 f[2][4];
#pragma unroll
                for (int ks = 0; ks < 2; ++ks)
#pragma unroll
                    for (int t = 0; t < 4; ++t) f[ks][t] = tr_frag(fr[t] + ks * 4096, fr[t] + ks * 4096 + 512);
                __builtin_amdgcn_sched_barrier(0);
                f32x4 d[4];
#pragma unroll
                for (int t = 0; t < 4; ++t) d[t] = MFMA16(f[0][t], BF8(af[0]), (f32x4{0.f, 0.f, 0.f, 0.f}));
#pragma unroll
                for (int t = 0; t < 4; ++t) d[t] = MFMA16(f[1][t], BF8(af[1]), d[t]);
#pragma unroll
                for (int t = 0; t < 4; ++t) *LP(f32x4, wa[t]) = d[t];
                if (ln) {
                    float v[8];
#pragma unroll
                    for (int q = 0; q < 8; ++q) v[q] = bf2f(xa[q]);
                    const float p1 = ((v[0] + v[1]) + (v[2] + v[3])) + ((v[4] + v[5]) + (v[6] + v[7]));
                    const float p2 = ((v[0] * v[0] + v[1] * v[1]) + (v[2] * v[2] + v[3] * v[3])) + ((v[4] * v[4] + v[5] * v[5]) + (v[6] * v[6] + v[7] * v[7]));
                    float mb, rden;
                    ln_stats(p1, p2, mb, rden);
                    u32x4 r;
#pragma unroll
                    for (int q = 0; q < 4; ++q) { const unsigned d = pk2(v[2 * q] - mb, v[2 * q + 1] - mb); r[q] = pk2(lo16(d) * rden, hi16(d) * rden); }
                    *LP(u32x4, L_XN + wv * 16384 + lane * 16) = r;
                }
                wg_barrier();
            };
            auto sum8 = [&](unsigned a) {
                f32x4 u[8];
#pragma unroll
                for (int w2 = 0; w2 < 8; ++w2) u[w2] = *LP(f32x4, a + w2 * 2048);
                __builtin_amdgcn_sched_barrier(0);
                return ((u[0] + u[1]) + (u[2] + u[3])) + ((u[4] + u[5]) + (u[6] + u[7]));
            };
            if (j < 14) {
                const int hb = lane & 7, c4 = 2 * wv + (nl >> 3);
                unsigned hoff = (unsigned)(WS_H + g * STR + cg * 2048 + wv * 256 + nl * 16);
                unsigned sa = L_MS + hb * 256 + ((c4 ^ hb) << 4);
                unsigned xa2 = L_XN + hb * 16384 + (cg & 7) * 128 + c4 * 8;
                PIN(hoff); PIN(sa); PIN(xa2);
                STAMP(1);
                mod_mm(cg >= 8);
                STAMP(2);
                const u32x2 xn = *LP(u32x2, xa2);
                const f32x4 v = sum8(sa);
                tag_take();
                const unsigned thx = tlo ^ thi ^ TAGC;
                u32x2 r = {pk2(v[0], v[1]), pk2(v[2], v[3])};
                if (cg >= 8) {
#pragma unroll
                    for (int q = 0; q < 2; ++q) { const unsigned sc = pk2(1.f + lo16(r[q]), 1.f + hi16(r[q])); r[q] = pk2(lo16(xn[q]) * lo16(sc), hi16(xn[q]) * hi16(sc)); }
                }
                ST16((u32x4{r[0], tlo, r[1], thx}), hoff, 0);
                ST16((u32x4{r[0], tlo, r[1], thx}), hoff, 4 * STR);
                STAMP(5);
            }
            wg_barrier();
            while (__builtin_amdgcn_s_memrealtime() < t_start + LMD) __builtin_amdgcn_s_sleep(2);
            const int lg = x * 4 + (j & 3);
            dma_piece(p.w_mod + (size_t)wv * 64 * 3072 + 1024 + lg * 64, 6144, rgn, 8, NTM & 4);
            mod_mm(false);
            if (lg >= 8 && lg < 24) {
                if (wv == 0) {
                    const int b = lane >> 3, c8 = lane & 7;
                    const f32x4 v0 = sum8(L_MS + b * 256 + (((2 * c8) ^ b) << 4)), v1 = sum8(L_MS + b * 256 + (((2 * c8 + 1) ^ b) << 4));
                    ST16((u32x4{pk2(v0[0], v0[1]), pk2(v0[2], v0[3]), pk2(v1[0], v1[1]), pk2(v1[2], v1[3])}), (unsigned)(WS_MB + b * STR + (lg - 8) * 128 + c8 * 16), 0);
                }
            } else if (wv < 4) {
                const int b = wv * 2 + (lane >> 5), cp = lane & 31;
                f32x2_t u[8];
#pragma unroll
                for (int w2 = 0; w2 < 8; ++w2) u[w2] = *LP(f32x2_t, L_MS + w2 * 2048 + b * 256 + (((cp >> 1) ^ b) << 4) + (cp & 1) * 8);
                const f32x2_t v = ((u[0] + u[1]) + (u[2] + u[3])) + ((u[4] + u[5]) + (u[6] + u[7]));
                ST16((u32x4{__float_as_uint(rbf(v[0])), __float_as_uint(rbf(v[1])), tlo, thi}), (unsigned)(WS_MOD + b * STR + (lg * 32 + cp) * 16), 0);
            }
            STAMP(6);
        }
        const int bq = (j >> 2) & 1, dh = j >> 3, src = th * 2 + dh, rb = bq * 4 + (src >> 4), rc = src & 15;
        wg_barrier();
        while (__builtin_amdgcn_s_memrealtime() < t_start + TWD - 110) __builtin_amdgcn_s_sleep(2);
        const unsigned npm = np_mask(LD16((unsigned)(WS_NP + x * STR + (lane & 7) * 16), 0)), np = (npm >> x) & 1u;
        unsigned oo = (unsigned)(WS_OUTP + ((bq * 4 + (nl & 3)) * 16 + wv * 2 + (nl >> 3)) * 16384 + (src ^ wv) * 128 + ((nl >> 2) & 1) * 64 + g * 16) + ((~npm >> ((nl & 3) * 2 + (wv >> 2))) & 1u) * 8192u;
        const int rcol = rc * 32 + (lane & 7) * 4 + (lane >> 5) * 2;
        unsigned xdst = (unsigned)(WS_X1 + ((lane >> 3) & 3) * STR + (rb * 512 + rcol) * 2), xdst2 = xdst + 4 * STR + ((npm >> (4 + ((lane >> 3) & 3))) & 1u) * 8192u;
        xdst += ((npm >> ((lane >> 3) & 3)) & 1u) * 8192u;
        ST16(s4, oo, 0);
        if (wv == 7) { __builtin_amdgcn_raw_buffer_store_b32(SENT, rw, xdst, 0, 16); __builtin_amdgcn_raw_buffer_store_b32(SENT, rw, xdst2, 0, 16); }
        VMW(0);
        wg_barrier();
        if (wv == 0) flag_put(WS_FB, id);
        while (__builtin_amdgcn_s_memrealtime() < t_start + TWD) __builtin_amdgcn_s_sleep(2);
        asm volatile("s_barrier" ::: "memory");
        STAMP(7);
        if ((PFM & 8) && wv == 0 && lane < 32) pf4<NTM & 8>((unsigned)((j * 32 + lane) * 8192 + x * 1024 + ((3u - ((unsigned)(size_t)p.w_uv >> 7)) & 7u) * 128), p.w_uv, L_SCR + 31744);
        if (bq) dma_piece(p.w_o + (size_t)(th * 128 + dh * 64) * 512 + wv * 64, 1024, rgn + 8192, 8, NTM & 8);
        dma_piece(p.w_uv + ((size_t)wv * 64 * NH + th) * 128 + dh * 64, 8192, rgn, 8, NTM & 8);
        if (!bq) dma_piece(p.w_o + (size_t)(th * 128 + dh * 64) * 512 + wv * 64, 1024, rgn + 8192, 8, NTM & 8);
        u32x4 fb, gm;
        unsigned xg;
        VMW(4);
        if (wv == 7)
            asm volatile("buffer_load_dwordx4 %0, %3, %5, 0 offen sc1\n\tbuffer_load_dwordx4 %1, %4, %5, 0 offen sc1\n\tglobal_load_dword %2, %6, %7" : "=&v"(fb), "=&v"(gm), "=&v"(xg)
                         : "v"((unsigned)(WS_FB + x * STR + lane * 16)), "v"((unsigned)(WS_MOD + rb * STR + rcol * 8)), "s"(rw), "v"((unsigned)(rb * 512 + rcol) * 2u), "s"(p.x) : "memory");
        VMW(0);
        asm volatile("" : "+v"(fb), "+v"(gm), "+v"(xg));
        STAMP(8);
        bf16x8 fuv[2][4], fwo[2][4];
        unsigned pfa[4];
#pragma unroll
        for (int t = 0; t < 4; ++t) { pfa[t] = rgn + frl + ((t ^ frs) << 5); PIN(pfa[t]); }
#pragma unroll
        for (int ks = 0; ks < 2; ++ks)
#pragma unroll
            for (int t = 0; t < 4; ++t) {
                const unsigned a = rgn + (unsigned)((g * 4 + (nl >> 2)) * 128 + (lane & 3) * 8) + ((t ^ (unsigned)((nl >> 3) | (g & 2))) << 5) + ks * 4096;
                fuv[ks][t] = tr_frag(a, a + 2048);
                fwo[ks][t] = tr_frag(pfa[t] + 8192 + ks * 4096, pfa[t] + 8192 + ks * 4096 + 512);
            }
#pragma unroll
        for (int ks = 0; ks < 2; ++ks) { PIN4(fuv[ks][0], fuv[ks][1], fuv[ks][2], fuv[ks][3]); PIN4(fwo[ks][0], fwo[ks][1], fwo[ks][2], fwo[ks][3]); }
        const unsigned L_WGT = L_SCR, L_MSK = L_SCR + 256, L_RED = L_SCR + 1024;
        unsigned psrc = (unsigned)(WS_PART + (bq * 4 * 32 + th) * 32768 + (wv >> 2) * 16384 + (wv & 3) * 2048 + ((lane >> 3) ^ (th & 7)) * 128) + np * 8192u + pcl, psrc1 = (psrc ^ 64u) + 1024;
        unsigned psrc2 = psrc ^ 8192u, psrc3 = psrc1 ^ 8192u;
        PIN(psrc2); PIN(psrc3); PIN(xdst2);
        unsigned rw2 = L_RED + wv * 1024 + (nl & 3) * 256 + g * 16;
        unsigned wa0 = (nl & 3) == (g >> 1) ? L_WGT + (nl & 4) * 32 + g * 16 : L_WGT + 272, wa1 = (nl & 3) == 2 + (g >> 1) ? L_WGT + (nl & 4) * 32 + 64 + g * 16 : L_WGT + 272;
        unsigned cr = rgn + (nl & 3) * 128 + g * 16, r2r = L_RED + lane * 16, cw = rgn + lane * 8;
        unsigned osrc = (unsigned)(WS_OUTP + (rb * 16 + rc) * 16384 + lane * 16) + (np ^ 1u) * 8192u;
        PIN(psrc); PIN(psrc1); PIN(rw2); PIN4(wa0, wa1, cr, r2r); PIN4(cw, oo, osrc, xdst);
        float mlm = 0.f, mll = 0.f;
        if (wv == 0) *LP(unsigned, L_WGT + 256 + lane * 4) = 0u;
        wg_barrier();
        const bool early = __all(flags_ok(fb) & (gm[2] == tlo) & (gm[3] == thi));
        u32x2 ga = {gm[0], gm[1]};
        PIN(ga);
        bf16x8 pa[2][4], wh[2];
        {
            u32x4 ml = {0u, 0u, ~tlo, ~thi};
            const unsigned mlsrc = (unsigned)(WS_ML + (bq * 4 + g) * STR + nl * 512 + th * 16) + ((npm >> (x & 6)) & 1u) * 8192u;
            unsigned long long done = 0ull;
            unsigned lm = L_MSK;
            PIN(lm);
#pragma unroll 1
            for (unsigned spins = 0; spins < kSpinCap; ++spins) {
                unsigned long long m;
                if (wv == 0) {
                    if ((ml[2] != tlo) | (ml[3] != thi)) ml = LD16(mlsrc, 0);
                    m = __ballot((ml[2] == tlo) & (ml[3] == thi));
                    unsigned long long f = m & (m >> 4);
                    f &= f >> 2; f &= f >> 1;
                    asm volatile("ds_write_b64 %0, %1" :: "v"(lm), "v"(f) : "memory");
                    m = m == ~0ull ? 0x0101010101010101ull : 0ull;
                } else {
                    unsigned long long t;
                    asm volatile("ds_read_b64 %0, %1\n\ts_waitcnt lgkmcnt(0)" : "=v"(t) : "v"(lm) : "memory");
                    m = ((unsigned long long)(unsigned)__builtin_amdgcn_readfirstlane((unsigned)(t >> 32)) << 32) | (unsigned)__builtin_amdgcn_readfirstlane((unsigned)t);
                    m &= 0x0101010101010101ull & ~done;
                }
                if (m) {
                    done |= m;
#define PLD(bit, off, v, base) "s_bitcmp1_b64 %0, " #bit "\n\ts_cbranch_scc0 1f\n\ts_add_u32 m0, %1, " #off "\n\ts_nop 0\n\tglobal_load_lds_dwordx4 " v ", " base " sc1\n1:\n\t"
                    asm volatile(PLD(0, 0, "%2", "%4") PLD(8, 1024, "%3", "%4") PLD(16, 2048, "%2", "%5") PLD(24, 3072, "%3", "%5")
                                 PLD(32, 4096, "%8", "%6") PLD(40, 5120, "%9", "%6") PLD(48, 6144, "%8", "%7") PLD(56, 7168, "%9", "%7")
                                 :: "s"(m), "s"(rgn), "v"(psrc), "v"(psrc1), "s"(ws), "s"(ws + 1048576), "s"(ws + 2097152), "s"(ws + 3145728), "v"(psrc2), "v"(psrc3) : "memory", "m0", "scc");
                    if (done == 0x0101010101010101ull) break;
                } else if (SLPM > 0 && wv == 0) __builtin_amdgcn_s_sleep(SLPM);
            }
            mlm = __uint_as_float(ml[0]); mll = __uint_as_float(ml[1]);
            STAMP(10);
            if (wv == 0) {
                float M = mlm;
                M = fmaxf(M, dppf<0xB1>(M)); M = fmaxf(M, dppf<0x4E>(M)); M = fmaxf(M, dppf<0x141>(M)); M = fmaxf(M, dppf<0x140>(M));
                const float e = __builtin_amdgcn_exp2f(mlm - M);
                const float w = e * __builtin_amdgcn_rcpf(sum16(e * mll));
                const unsigned short hi = f2bf(w);
                *LP(unsigned short, L_WGT + lane * 2) = hi;
                *LP(unsigned short, L_WGT + 128 + lane * 2) = f2bf(w - bf2f(hi));
            }
            wg_barrier();
            wh[0] = *LP(bf16x8, wa0); wh[1] = *LP(bf16x8, wa1);
            __builtin_amdgcn_sched_barrier(0);
            VMW(0);
#pragma unroll
            for (int ks = 0; ks < 2; ++ks)
#pragma unroll
                for (int t = 0; t < 4; ++t) pa[ks][t] = tr_frag(pfa[t] + ks * 4096, pfa[t] + ks * 4096 + 512);
        }
        STAMP(11);
        {
            f32x4 d[4];
#pragma unroll
            for (int t = 0; t < 4; ++t) d[t] = MFMA16(pa[0][t], wh[0], (f32x4{0.f, 0.f, 0.f, 0.f}));
#pragma unroll
            for (int t = 0; t < 4; ++t) d[t] = MFMA16(pa[1][t], wh[1], d[t]);
#pragma unroll
            for (int t = 0; t < 4; ++t)
#pragma unroll
                for (int e = 0; e < 4; ++e) { float v = d[t][e] + dppf<0x124>(d[t][e]); asm("" : "+v"(v)); d[t][e] = v; }
            const u32x4 o0 = {pk2(d[0][0], d[0][1]), pk2(d[0][2], d[0][3]), pk2(d[1][0], d[1][1]), pk2(d[1][2], d[1][3])};
            const u32x4 o1 = {pk2(d[2][0], d[2][1]), pk2(d[2][2], d[2][3]), pk2(d[3][0], d[3][1]), pk2(d[3][2], d[3][3])};
            f32x4 y[4];
#pragma unroll
            for (int t = 0; t < 4; ++t) y[t] = MFMA16(fuv[0][t], BF8(o0), (f32x4{0.f, 0.f, 0.f, 0.f}));
#pragma unroll
            for (int t = 0; t < 4; ++t) y[t] = MFMA16(fuv[1][t], BF8(o1), y[t]);
            if (nl < 4) {
#pragma unroll
                for (int t = 0; t < 4; ++t) *LP(f32x4, rw2 + t * 64) = y[t];
            }
        }
        wg_barrier();
        {
            f32x4 cs[8];
#pragma unroll
            for (int w2 = 0; w2 < 8; ++w2) cs[w2] = *LP(f32x4, r2r + w2 * 1024);
            const f32x4 c4 = ((cs[0] + cs[1]) + (cs[2] + cs[3])) + ((cs[4] + cs[5]) + (cs[6] + cs[7]));
            *LP(u32x2, cw) = u32x2{pk2(c4[0], c4[1]), pk2(c4[2], c4[3])};
            asm volatile("s_waitcnt lgkmcnt(0)" ::: "memory");
            const u32x4 cx0 = *LP(u32x4, cr), cx1 = *LP(u32x4, cr + 64);
            f32x4 o[4];
#pragma unroll
            for (int q = 0; q < 4; ++q) o[q] = MFMA16(fwo[0][q], BF8(cx0), (f32x4{0.f, 0.f, 0.f, 0.f}));
#pragma unroll
            for (int q = 0; q < 4; ++q) o[q] = MFMA16(fwo[1][q], BF8(cx1), o[q]);
            const f32x4 sel = (nl & 8) ? ((nl & 4) ? o[3] : o[2]) : ((nl & 4) ? o[1] : o[0]);
            u32x4 sv = __builtin_bit_cast(u32x4, sel);
            if (__builtin_expect(mx4(sv, 0u) == SENT, 0)) sv = u32x4{f32s(sel[0]), f32s(sel[1]), f32s(sel[2]), f32s(sel[3])};
            ST16(sv, oo, 0);
        }
        STAMP(12);
        wg_barrier();
        if (wv == 7) {
            if (!early) {
                flags_wait(WS_FB + x * STR + lane * 16);
                ga = mod_get(WS_MOD + rb * STR + rcol * 8);
                PIN(ga);
            }
            u32x4 op[8];
            for (unsigned spins = 0;;) {
#pragma unroll
                for (int k = 0; k < 8; ++k) op[k] = LD16(osrc + (k & 3) * 1024, (k >> 2) * 4096);
                unsigned m = 0u;
#pragma unroll
                for (int k = 0; k < 8; ++k) m = mx4(op[k], m);
                if (__all(m != SENT)) break;
                if (++spins > kSpinCap) break;
                asm volatile("" ::: "memory");
            }
            STAMP(13);
            const f32x4 v = ((__builtin_bit_cast(f32x4, op[0]) + __builtin_bit_cast(f32x4, op[1])) + (__builtin_bit_cast(f32x4, op[2]) + __builtin_bit_cast(f32x4, op[3]))) +
                            ((__builtin_bit_cast(f32x4, op[4]) + __builtin_bit_cast(f32x4, op[5])) + (__builtin_bit_cast(f32x4, op[6]) + __builtin_bit_cast(f32x4, op[7])));
            float w[2];
#pragma unroll
            for (int q = 0; q < 2; ++q) {
                auto t = __builtin_amdgcn_permlane32_swap(__float_as_uint(v[q]), __float_as_uint(v[2 + q]), false, false);
                w[q] = __uint_as_float(t[0]) + __uint_as_float(t[1]);
                auto u = __builtin_amdgcn_permlane16_swap(__float_as_uint(w[q]), __float_as_uint(w[q]), false, false);
                w[q] = __uint_as_float(u[0]) + __uint_as_float(u[1]);
                w[q] += dppf<0x128>(w[q]);
            }
            unsigned t = pk2(w[0], w[1]);
            t = pk2(__uint_as_float(ga[0]) * lo16(t), __uint_as_float(ga[1]) * hi16(t));
            t = pk2s(lo16(xg) + lo16(t), hi16(xg) + hi16(t));
            __builtin_amdgcn_raw_buffer_store_b32(t, rw, xdst, 0, 16);
            __builtin_amdgcn_raw_buffer_store_b32(t, rw, xdst2, 0, 16);
            STAMP(14);
        }
        STAMP_FLUSH();
        return;
    }

    const int s = j - 16;
    if (wv == 0) cnt_load();
    {
        const unsigned ln = (unsigned)(s * 64 + lane);
        if ((PFM & 1) && wv == 0) pf4<NTM & 1>((ln & 511u) * 6144u + (x * 2 + (ln >> 9)) * 128u, p.w_mod, L_SCR);
        if ((PFM & 2) && wv == 1) {
            const unsigned short* w = s < 8 ? p.w_uk : p.w_q;
            pf4<NTM & 1>((ln & 511u) * 8192u + x * 1024u + ((3u - ((unsigned)(size_t)w >> 7)) & 7u) * 128u, w, L_SCR);
        }
        if ((PFM & 48) == 48) {
            const unsigned r0 = (unsigned)(s * 4 + (wv & 3)), row = PFS == 0 ? r0 * 8 + (lane >> 3) : PFS == 1 ? (lane >> 3) * 64 + r0 : (r0 >> 5) * 256 + (lane >> 3) * 32 + (r0 & 31);
            pf4<NTM & 1>(row * 8192u + x * 1024u + (lane & 7u) * 128u, wv < 4 ? p.w_uk : p.w_q, L_SCR);
        }
        else if ((PFM & 16) && wv >= 4) { const unsigned l2 = (unsigned)((s * 4 + (wv & 3)) * 64 + lane); pf4<NTM & 1>((l2 >> 3) * 8192u + x * 1024u + (l2 & 7u) * 128u, p.w_q, L_SCR); }
        else if ((PFM & 32) && wv >= 4) { const unsigned l2 = (unsigned)((s * 4 + (wv & 3)) * 64 + lane); pf4<NTM & 1>((l2 >> 3) * 8192u + x * 1024u + (l2 & 7u) * 128u, p.w_uk, L_SCR); }
        if ((PFM & 4) && (wv & 6) == 2) { const unsigned l2 = (unsigned)((s * 2 + (wv & 1)) * 64 + lane); pf4<NTM & 1>((l2 & 511u) * 8192u + (x * 4 + (l2 >> 9)) * 256u, p.w_q, L_SCR); }
    }
    const int ug = x * 7 + s;
    const int wp = wv & 3, ch = wv >> 2, kb = wp * 2 + ch;
    int head = lane & 31;
    unsigned h2 = lane >> 5;
    const unsigned vb = (unsigned)(g * 1024 + ((nl ^ (g << 2)) << 4)), lb = (unsigned)((wv >> 1) * 32768 + (wv & 1) * 4096);
    const char* ck = (const char*)p.ckv + ((size_t)x * SK + s * 256 + wv * 16) * 1024;
    const unsigned rot = ROTC ? (unsigned)s & 3u : 0u;
    const unsigned vbr = vb ^ (rot << 8);
    const char* ck2 = ck + 131072;
    auto ck_dma = [&](unsigned i) { dma16x<NTM & 16>(vbr, ((i & 3u) << 4) | (((i >> 2) & 3u) << 8) | ((i & 3u) << 12), (i >> 4) ? ck2 : ck, lb + ((i >> 2) & 3u) * 8192u + (i & 3u) * 1024u); };
    bf16x8 ka[16];
    s16x4 va[8][4];
    unsigned kad[4], vad[4], vad2[4];
    auto ka_reads = [&](int cb) {
#pragma unroll
        for (int u = 0; u < 4; ++u) ka[cb * 4 + u] = *LP(bf16x8, kad[u] + cb * 8192);
    };
    auto va_reads = [&]() {
#pragma unroll
        for (int kk = 0; kk < 8; ++kk)
#pragma unroll
            for (int q = 0; q < 4; ++q) va[kk][q] = tr4((kk < 4 ? vad[q] : vad2[q]) + ((kk >> 1) & 1) * 32768 + (kk & 1) * 4096);
    };
    auto va_pin = [&]() {
#pragma unroll
        for (int q = 0; q < 8; ++q) PIN4(va[q][0], va[q][1], va[q][2], va[q][3]);
    };
    u32x4 fa = {0u, 0u, 0u, 0u};
    const unsigned fao = (unsigned)(WS_FI + x * STR + (lane & 7) * 16);
    constexpr int LAST = GI + 4 > 19 ? GI + 4 : 19;
    const unsigned qsrc = (unsigned)(WS_Q + (wv & 1) * 4 * STR + (x * 4 + (wv >> 1)) * 1024) + (g ^ rot) * 256 + ((nl ^ ((wv * 4) & 15)) << 4);
    auto q_dma = [&](int k) {
        unsigned t;
        asm volatile("s_mov_b32 m0, %3\n\tv_xor_b32 %0, %4, %1\n\tglobal_load_lds_dwordx4 %0, %2 sc1" : "=&v"(t) : "v"(qsrc), "s"(ws), "s"(L_SCR + (wv * 4 + k) * 1024), "i"(k * (STR + 16)) : "memory", "m0");
    };
#pragma unroll
    for (int i = 0; i <= LAST; ++i) {
        ck_dma(i);
        if (i == 3) {
#pragma unroll
            for (int u = 0; u < 4; ++u) {
                kad[u] = tile_off(wp * 32 + (head ^ (ch * 16)), ch * 8 + 2 * u + h2);
                vad[u] = tile_off(4 * h2 + (nl >> 2) + 8 * (u & 1), wv * 8 + 4 * (u >> 1) + 2 * (g & 1) + ((lane & 3) >> 1)) + 8 * (lane & 1);
                vad2[u] = vad[u] + 65536;
                PIN(kad[u]); PIN(vad[u]); PIN(vad2[u]);
            }
        }
        if (i > GI && i < GI + 4) VMW(8); else if (i < NTH) { if (i >= W0) VMW(W0); } else if (i >= 4) VMW(4);
        if (i == 15 && wv == 0) {
            PIN(fa);
            tag_take();
            if (!__all(flags_ok(fa))) flags_wait(fao);
        }
        if ((i & 3) == 3 && i >= 7 && i <= 19) {
            const int cb = (i - 7) >> 2;
            if (cb > 0) { PIN4(ka[cb * 4 - 4], ka[cb * 4 - 3], ka[cb * 4 - 2], ka[cb * 4 - 1]); va_pin(); }
            wg_barrier();
            ka_reads(cb);
            if ((wv >> 1) == cb) va_reads();
            STAMP(16 + cb);
        }
        if (i == 10 && wv == 0) asm volatile("buffer_load_dwordx4 %0, %1, %2, 0 offen sc1" : "+v"(fa) : "v"(fao), "s"(rw) : "memory");
        if (i == GI) { q_dma(0); q_dma(1); q_dma(2); q_dma(3); }
    }
    const unsigned L_X = L_SCR, L_P = L_SCR + 16384, L_EXM = L_SCR + 24576, L_EXS = L_SCR + 25600, L_BIG = L_SCR + 27648;
    unsigned nx = LAST + 1, npm = 0u;
    unsigned qad[4];
    {
        unsigned qck = L_SCR + wv * 4096 + lane * 16;
        u32x4 qb[4];
#pragma unroll
        for (int k = 0; k < 4; ++k) qb[k] = *LP(u32x4, qck + k * 1024);
#pragma unroll
        for (int k = 0; k < 4; ++k) { qad[k] = L_SCR + head * 1024 + (((ch * 8 + 2 * k + h2) ^ (head & 15)) << 4); PIN(qad[k]); }
        STAMP(20);
        if (!__all(mx4(qb[0], mx4(qb[1], mx4(qb[2], mx4(qb[3], 0u)))) != SENT)) {
            for (unsigned spins = 0;;) {
#pragma unroll
                for (int k = 0; k < 4; ++k)
                    if (!__all(mx4(qb[k], 0u) != SENT)) q_dma(k);
                if (nx <= 25) {
#pragma unroll
                    for (int i = 0; i < 3; ++i) ck_dma(nx + i);
                    nx += 3;
                    VMW(3);
                } else {
                    for (; nx < 28; ++nx) ck_dma(nx);
                    VMW(0);
                }
#pragma unroll
                for (int k = 0; k < 4; ++k) qb[k] = *LP(u32x4, qck + k * 1024);
                if (__all(mx4(qb[0], mx4(qb[1], mx4(qb[2], mx4(qb[3], 0u)))) != SENT)) break;
                if (++spins > kSpinCap) break;
            }
        }
    }
    constexpr int NE = LAST + 1 + SL0 + 5 < 28 ? LAST + 1 + SL0 + 5 : 28;
    auto slot = [&](int k) { if (LAST + 1 + k < 28) { if (nx <= (unsigned)(LAST + 1 + k)) ck_dma(LAST + 1 + k); } };
    auto slot2 = [&](int k) { if (NE + k < 28) { if (nx <= (unsigned)(NE + k)) ck_dma(NE + k); } else if (NE + k < 32) ck_dma(NE + k); };
    unsigned xw = wp * 32768 + 24576 + ch * 2048 + lane * 16, xr = xw ^ 2048u, exw = L_EXM + (head * 8 + wv) * 4, exr = L_EXM + head * 32;
    unsigned pwr = L_P + kb * 1024 + lane * 16, prd = L_P + lane * 16, esw = L_EXS + (head * 16 + h2 * 8 + wv) * 4;
    PIN4(xw, xr, exw, exr); PIN(pwr); PIN(prd); PIN(esw);
    PIN4(ka[12], ka[13], ka[14], ka[15]); va_pin();
    wg_barrier();
    STAMP(21);
    bf16x8 qf[16];
#pragma unroll
    for (int ks = 0; ks < 16; ++ks) qf[ks] = *LP(bf16x8, qad[ks & 3] + (ks >> 2) * 256);
    __builtin_amdgcn_sched_barrier(0);
#pragma unroll
    for (int k = 0; k < SL0; ++k) slot(k);
    __builtin_amdgcn_sched_barrier(0);
    float lsum, mrun;
    f32x16 o[2], sc2;
    auto meet = [&](const f32x16& sc, float (&t)[8]) {
        *LP(f32x4, xw) = f32x4{sc[8], sc[9], sc[10], sc[11]}; *LP(f32x4, xw + 1024) = f32x4{sc[12], sc[13], sc[14], sc[15]};
        wg_barrier();
        const f32x4 x0 = *LP(f32x4, xr), x1 = *LP(f32x4, xr + 1024);
#pragma unroll
        for (int r = 0; r < 4; ++r) { t[r] = sc[r] + x0[r]; t[4 + r] = sc[4 + r] + x1[r]; }
    };
    {
        f32x16 sc;
#pragma unroll
        for (int i = 0; i < 16; ++i) sc[i] = 0.f;
#pragma unroll
        for (int ks = 0; ks < 16; ++ks) {
            sc = MFMA32(ka[ks], qf[ks], sc);
            if ((ks & 3) == 3) slot(SL0 + (ks >> 2));
            __builtin_amdgcn_sched_barrier(0);
        }
        float t[8];
        meet(sc, t);
        {
            const float m = fmax3(fmax3(t[0], t[1], t[2]), fmax3(t[3], t[4], t[5]), fmax3(t[6], t[7], t[7]));
            auto u = __builtin_amdgcn_permlane32_swap(__float_as_uint(m), __float_as_uint(m), false, false);
            *LP(float, exw) = fmax3(__uint_as_float(u[0]), __uint_as_float(u[1]), __uint_as_float(u[1]));
        }
        slot(SL0 + 4);
        VMW(NE > 24 ? NE - 24 : 0);
        wg_barrier();
        const f32x4 a0 = *LP(f32x4, exr), a1 = *LP(f32x4, exr + 16);
        ka_reads(0); ka_reads(1);
        __builtin_amdgcn_sched_barrier(0);
        slot2(0);
        mrun = fmax3(fmax3(a0[0], a0[1], a0[2]), fmax3(a0[3], a1[0], a1[1]), fmax3(a1[2], a1[3], a1[3])) * SCL;
        float e[8];
#pragma unroll
        for (int r = 0; r < 8; ++r) e[r] = __builtin_amdgcn_exp2f(fmaf(t[r], SCL, -mrun));
        *LP(u32x4, pwr) = u32x4{pk2(e[0], e[1]), pk2(e[2], e[3]), pk2(e[4], e[5]), pk2(e[6], e[7])};
        lsum = ((e[0] + e[1]) + (e[2] + e[3])) + ((e[4] + e[5]) + (e[6] + e[7]));
        if (wv == 0) *LP(u32x4, L_BIG) = u32x4{0u, 0u, tlo, thi};
        slot2(1);
        wg_barrier();
        STAMP(22);
        bf16x8 pf[8];
#pragma unroll
        for (int kk = 0; kk < 8; ++kk) pf[kk] = *LP(bf16x8, prd + kk * 1024);
#pragma unroll
        for (int i = 0; i < 16; ++i) { o[0][i] = 0.f; o[1][i] = 0.f; sc2[i] = 0.f; }
        __builtin_amdgcn_sched_barrier(0);
#pragma unroll
        for (int ks = 0; ks < 8; ++ks) {
            sc2 = MFMA32(ka[ks], qf[ks], sc2);
            if (ks & 1) slot2(2 + (ks >> 1));
            __builtin_amdgcn_sched_barrier(0);
        }
#pragma unroll
        for (int kk = 0; kk < 8; ++kk) {
            o[0] = MFMA32(cat8(va[kk][0], va[kk][1]), pf[kk], o[0]);
            o[1] = MFMA32(cat8(va[kk][2], va[kk][3]), pf[kk], o[1]);
            if (kk < 4) slot2(6 + kk);
            __builtin_amdgcn_sched_barrier(0);
        }
    }
    STAMP(23);
    rederive();
    head = lane & 31; h2 = lane >> 5;
    unsigned sw[4], srd = lb + (lane >> 3) * 128 + (((lane & 7) ^ (lane >> 3)) << 4);
#pragma unroll
    for (int q = 0; q < 4; ++q) sw[q] = lb + head * 128 + (((2 * q + h2) ^ (head & 7)) << 4);
    PIN4(sw[0], sw[1], sw[2], sw[3]);
    xw = L_X + wv * 2048 + lane * 16; xr = xw ^ 8192u;
    PIN(xw); PIN(xr);
    {
        const u32x4 q = *LP(u32x4, L_BIG);
        tlo = __builtin_amdgcn_readfirstlane(q[2]); thi = __builtin_amdgcn_readfirstlane(q[3]);
    }
    unsigned po[4];
    const unsigned cw = wv ^ (rot * 2);
    unsigned p0 = (unsigned)(WS_PART + (x * 32 + (lane >> 3)) * 32768 + (s ^ (lane >> 3)) * 128 + (lane & 7) * 16) + (cw >> 2) * 16384 + (cw & 3) * 2048;
    unsigned mlo = (unsigned)(WS_ML + x * STR + s * 512 + head * 16), esr = L_EXS + head * 64;
    unsigned fbo = (unsigned)(WS_FB + x * STR + lane * 16), npo = (unsigned)(WS_NP + x * STR + (lane & 7) * 16);
    PIN(p0); PIN(npo); PIN4(esr, srd, mlo, fbo);
    u32x4 nq;
    {
        f32x16& sc = sc2;
        if (wv < 4) va_reads();
        VMW(4);
        wg_barrier();
        ka_reads(2);
        if ((wv >> 1) == 2) va_reads();
        __builtin_amdgcn_sched_barrier(0);
#pragma unroll
        for (int ks = 8; ks < 12; ++ks) sc = MFMA32(ka[ks], qf[ks], sc);
        __builtin_amdgcn_sched_barrier(0);
        VMW(0);
        wg_barrier();
        STAMP(24);
        asm volatile("buffer_load_dwordx4 %0, %1, %2, 0 offen sc1" : "=v"(nq) : "v"(npo), "s"(rw) : "memory");
        if (wv == 0 && s < 11) asm volatile("buffer_load_dwordx4 %0, %1, %2, 0 offen sc1" : "=v"(fa) : "v"(fbo), "s"(rw) : "memory");
        ka_reads(3);
        __builtin_amdgcn_sched_barrier(0);
#pragma unroll
        for (int ks = 12; ks < 16; ++ks) sc = MFMA32(ka[ks], qf[ks], sc);
        float t[8];
        meet(sc, t);
        float e[8];
        const float mloc = fmax3(fmax3(t[0], t[1], t[2]), fmax3(t[3], t[4], t[5]), fmax3(t[6], t[7], t[7]));
#pragma unroll
        for (int r = 0; r < 8; ++r) e[r] = __builtin_amdgcn_exp2f(fmaf(t[r], SCL, -mrun));
        *LP(u32x4, pwr) = u32x4{pk2(e[0], e[1]), pk2(e[2], e[3]), pk2(e[4], e[5]), pk2(e[6], e[7])};
        *LP(float, esw) = lsum + (((e[0] + e[1]) + (e[2] + e[3])) + ((e[4] + e[5]) + (e[6] + e[7])));
        if (__builtin_expect(!(fmaf(mloc, SCL, -mrun) <= BIGD), 0)) *LP(unsigned, L_BIG) = 1u;
        va_pin();
        wg_barrier();
        STAMP(25);
        unsigned big = *LP(unsigned, L_BIG);
        if ((wv >> 1) == 3) va_reads();
        bf16x8 pf[8];
#pragma unroll
        for (int kk = 0; kk < 8; ++kk) pf[kk] = *LP(bf16x8, prd + kk * 1024);
        __builtin_amdgcn_sched_barrier(0);
        if (__builtin_expect(__builtin_amdgcn_readfirstlane(big) != 0, 0)) {
            auto u = __builtin_amdgcn_permlane32_swap(__float_as_uint(mloc), __float_as_uint(mloc), false, false);
            *LP(float, exw) = fmaxf(__uint_as_float(u[0]), __uint_as_float(u[1]));
#pragma unroll
            for (int kk = 0; kk < 8; kk += 4) PIN4(pf[kk], pf[kk + 1], pf[kk + 2], pf[kk + 3]);
            wg_barrier();
            const f32x4 a0 = *LP(f32x4, exr), a1 = *LP(f32x4, exr + 16);
            const float mnew = fmaxf(fmaxf(fmaxf(fmaxf(a0[0], a0[1]), fmaxf(a0[2], a0[3])), fmaxf(fmaxf(a1[0], a1[1]), fmaxf(a1[2], a1[3]))) * SCL, mrun);
#pragma unroll
            for (int r = 0; r < 8; ++r) e[r] = __builtin_amdgcn_exp2f(fmaf(t[r], SCL, -mnew));
            *LP(u32x4, pwr) = u32x4{pk2(e[0], e[1]), pk2(e[2], e[3]), pk2(e[4], e[5]), pk2(e[6], e[7])};
            const float al = __builtin_amdgcn_exp2f(mrun - mnew);
            *LP(float, esw) = lsum * al + (((e[0] + e[1]) + (e[2] + e[3])) + ((e[4] + e[5]) + (e[6] + e[7])));
            mrun = mnew;
            o[0] *= al; o[1] *= al;
            wg_barrier();
#pragma unroll
            for (int kk = 0; kk < 8; ++kk) pf[kk] = *LP(bf16x8, prd + kk * 1024);
        }
        auto stage = [&](int t2, int gp) {
            const unsigned lo0 = pk2(o[t2][8 * gp + 0], o[t2][8 * gp + 1]), lo1 = pk2(o[t2][8 * gp + 2], o[t2][8 * gp + 3]);
            const unsigned hi0 = pk2(o[t2][8 * gp + 4], o[t2][8 * gp + 5]), hi1 = pk2(o[t2][8 * gp + 6], o[t2][8 * gp + 7]);
            auto r0 = __builtin_amdgcn_permlane32_swap(lo0, hi0, false, false);
            auto r1 = __builtin_amdgcn_permlane32_swap(lo1, hi1, false, false);
            *LP(u32x4, sw[t2 * 2 + gp]) = u32x4{r0[0], r1[0], r0[1], r1[1]};
        };
#pragma unroll
        for (int kk = 0; kk < 8; ++kk) {
            o[0] = MFMA32(cat8(va[kk][0], va[kk][1]), pf[kk], o[0]);
            if (kk == 3) {
                asm volatile("s_waitcnt vmcnt(0)" : "+v"(nq) :: "memory");
                npm = np_mask(nq);
            }
            if (kk == 5) {
#pragma unroll
                for (int i = 0; i < 4; ++i) po[i] = p0 + i * 262144 + (((npm >> (2 * i + (lane >> 5))) ^ (x >> 1)) & 1u) * 8192u;
                mlo += ((npm >> ((head >> 3) * 2)) & 1u) * 8192u;
                PIN4(po[0], po[1], po[2], po[3]); PIN(mlo);
            }
            __builtin_amdgcn_sched_barrier(0);
        }
#pragma unroll
        for (int kk = 0; kk < 8; ++kk) {
            o[1] = MFMA32(cat8(va[kk][2], va[kk][3]), pf[kk], o[1]);
            if (kk == 2) { PIN(o[0]); stage(0, 0); }
            if (kk == 4) stage(0, 1);
            __builtin_amdgcn_sched_barrier(0);
        }
        stage(1, 0); stage(1, 1);
        asm volatile("s_waitcnt lgkmcnt(0)" ::: "memory");
        u32x4 w[4];
#pragma unroll
        for (int i = 0; i < 4; ++i) w[i] = *LP(u32x4, srd + i * 1024);
#pragma unroll
        for (int i = 0; i < 4; ++i) ST16(w[i], po[i], 0);
        __builtin_amdgcn_sched_barrier(0);
        STAMP(26);
        u32x4 mlrec;
        {
            const f32x4 b0 = *LP(f32x4, esr), b1 = *LP(f32x4, esr + 16), b2 = *LP(f32x4, esr + 32), b3 = *LP(f32x4, esr + 48);
            const f32x4 bs = (b0 + b1) + (b2 + b3);
            mlrec = u32x4{__float_as_uint(mrun), __float_as_uint((bs[0] + bs[1]) + (bs[2] + bs[3])), tlo, thi};
            PIN(mlrec);
        }
        VMW(0);
        wg_barrier();
        if (wv == 0) {
            if (lane < 32) ST16(mlrec, mlo, 0);
            PIN(fa);
            if (s < 11 && !__all(flags_ok(fa))) flags_wait(fbo);
        }
        STAMP(27);
        wg_barrier();
    }
    const unsigned long long t_att = __builtin_amdgcn_s_memrealtime();
    rederive();
    while (__builtin_amdgcn_s_memrealtime() < t_att + (s >= 11 ? MD0 : MD1)) __builtin_amdgcn_s_sleep(2);
    if (s >= 11) {
        const unsigned n = (unsigned)((s - 11) * 8 + wv);
        if (n < 28) pf4<NTM & 32>((n * 64 + lane) * 1024u + x * 128u, p.w2, 0);
        { const unsigned l2 = n * 64 + lane; pf4<NTM & 32>((l2 / 5u) * (DFF * 2) + min(((unsigned)x * 448u & ~127u) + (l2 % 5u) * 128u, DFF * 2 - 4u), p.w1, 0); }
        if (wv == 0) flag_put(WS_FA, id - 128);
        STAMP_FLUSH();
        return;
    }
    const unsigned L_H2S = L_SCR, L_REDM = L_SCR + 16384;
    if (s < 7) {
        {
            const char* w1s = (const char*)(p.w1 + (size_t)wv * 64 * DFF + ug * 32);
#pragma unroll
            for (int i = 0; i < 4; ++i) dma16<NTM & 32>((unsigned)((lane >> 2) * DFF * 2 + (lane & 3) * 16), w1s + (size_t)i * 16 * DFF * 2, wv * 4096 + i * 1024);
        }
        const int b = wv;
        if (wv == 0) ST16(s4, (unsigned)(WS_U + x * STR + (lane >> 5) * 8192 + s * 512 + (lane & 31) * 16), 0);
        const u32x4 shf = LD16((unsigned)(WS_MB + b * STR + lane * 16), 0), scl = LD16((unsigned)(WS_MB + b * STR + 1024 + lane * 16), 0);
        VMW(0);
        if (wv == 0) flag_put(WS_FA, id - 128);
        wg_barrier();
        bf16x8 wk0[8], wk1[8];
        if (wv >= 6) {
#pragma unroll
            for (int q = 0; q < 8; ++q) {
                const unsigned a = (unsigned)(q * 4096 + (g * 8 + (nl >> 2)) * 64 + (wv & 1) * 32 + (lane & 3) * 8);
                wk0[q] = tr_frag(a, a + 256); wk1[q] = tr_frag(a + 2048, a + 2304);
            }
#pragma unroll
            for (int q = 0; q < 8; q += 2) PIN4(wk0[q], wk0[q + 1], wk1[q], wk1[q + 1]);
        }
        float scv[8], shv[8];
#pragma unroll
        for (int q = 0; q < 4; ++q) {
            scv[2 * q] = rbf(1.f + lo16(scl[q])); scv[2 * q + 1] = rbf(1.f + hi16(scl[q])); shv[2 * q] = lo16(shf[q]); shv[2 * q + 1] = hi16(shf[q]);
            PIN4(scv[2 * q], scv[2 * q + 1], shv[2 * q], shv[2 * q + 1]);
        }
        unsigned har = L_H2S + ((lane & 7) * HS_LD + g * 8 + (nl >> 3) * 32) * 2, haw = L_H2S + (b * HS_LD + lane * 8) * 2;
        unsigned udst = (unsigned)(WS_U + x * STR + (nl >> 3) * 8192 + (s * 2 + (wv & 1)) * 256 + ((nl & 7) * 16 + g * 4) * 2), xsrc = (unsigned)(WS_X1 + x * STR + (b * 512 + lane * 8) * 2) + ((npm >> x) & 1u) * 8192u;
        PIN4(har, haw, udst, xsrc);
        STAMP(28);
        while (__builtin_amdgcn_s_memrealtime() < t_att + XGATE) __builtin_amdgcn_s_sleep(2);
        u32x4 xv;
        for (unsigned spins = 0;;) {
            xv = LD16(xsrc, 0);
            if (__all(mx4(xv, 0u) != SENT)) break;
            __builtin_amdgcn_s_sleep(SLPX);
            if (++spins > kSpinCap) break;
            asm volatile("" ::: "memory");
        }
        STAMP(29);
        {
            f32x2_t v[4];
#pragma unroll
            for (int q = 0; q < 4; ++q) v[q] = f32x2_t{lo16(xv[q]), hi16(xv[q])};
            const f32x2_t p1 = (v[0] + v[1]) + (v[2] + v[3]), p2 = (v[0] * v[0] + v[1] * v[1]) + (v[2] * v[2] + v[3] * v[3]);
            float mb, rden;
            ln_stats(p1[0] + p1[1], p2[0] + p2[1], mb, rden);
            u32x4 hr;
#pragma unroll
            for (int q = 0; q < 4; ++q) {
                const f32x2_t h = ((v[q] - mb) * rden) * f32x2_t{scv[2 * q], scv[2 * q + 1]} + f32x2_t{shv[2 * q], shv[2 * q + 1]};
                hr[q] = pk2(h[0], h[1]);
            }
            *LP(u32x4, haw) = hr;
        }
        wg_barrier();
        if (wv >= 6) {
            bf16x8 af[8];
#pragma unroll
            for (int q = 0; q < 8; ++q) af[q] = *LP(bf16x8, har + q * 128);
            __builtin_amdgcn_sched_barrier(0);
            f32x4 d0 = {0.f, 0.f, 0.f, 0.f}, d1 = {0.f, 0.f, 0.f, 0.f};
#pragma unroll
            for (int q = 0; q < 8; ++q) { d0 = MFMA16(wk0[q], af[q], d0); d1 = MFMA16(wk1[q], af[q], d1); }
            const f32x4 own = nl < 8 ? d0 : d1;
#pragma unroll
            for (int e = 0; e < 4; ++e) { float v = own[e] + dppf<0x128>(own[e]); asm("" : "+v"(v)); d0[e] = fmaxf(v, 0.f); }
            u32x2 uv = {pk2(d0[0], d0[1]), pk2(d0[2], d0[3])};
            if (__builtin_expect(max(uv[0], uv[1]) == SENT, 0)) uv = u32x2{pk2s(d0[0], d0[1]), pk2s(d0[2], d0[3])};
            __builtin_amdgcn_raw_buffer_store_b64(uv, rw, udst, 0, 16);
            STAMP(30);
        }
        STAMP_FLUSH();
        return;
    }
    {
        const int n0 = (x * 4 + (s - 7)) * 16;
        const char* mws = (const char*)(p.w2 + (size_t)wv * 224 * 512 + n0);
#pragma unroll
        for (int i = 0; i < 7; ++i) dma16<NTM & 32>((unsigned)((lane >> 1) * 1024 + (lane & 1) * 16), mws + i * 32768, rgn + i * 1024);
        const int fb = lane >> 3, fc = (lane & 7) * 2;
        u32x2 gb = {0u, 0u};
        if (wv == 0) {
            flag_put(WS_FA, id - 128);
            gb = mod_get(WS_MOD + fb * STR + (1536 + n0 + fc) * 8);
            flags_wait(WS_FA + x * STR + lane * 16);
            if (id == 23 * 8 && lane == 0) __builtin_amdgcn_raw_buffer_store_b64(u32x2{tlo, thi}, rw, WS_EPOCH, 0, 16);
            PIN(gb);
        }
        VMW(0);
        bf16x8 wfr[7];
        const unsigned trl = (unsigned)(g * 8 + (nl >> 2)) * 32u + (unsigned)(lane & 3) * 8u;
#pragma unroll
        for (int ks = 0; ks < 7; ++ks) { wfr[ks] = tr_frag(rgn + ks * 1024 + trl, rgn + ks * 1024 + trl + 128); PIN(wfr[ks]); }
        unsigned usrc = (unsigned)(WS_U + wv * STR + (g >> 1) * 256 + (nl & 7) * 32 + (g & 1) * 16) + ((npm >> x) & 1u) * 8192u;
        unsigned rdw = L_REDM + wv * 512 + (nl & 7) * 64 + g * 16, rdr = L_REDM + lane * 8;
        unsigned short* outp = p.out + fb * 512 + n0 + fc;
        PIN(usrc); PIN(rdw); PIN(rdr); PIN(outp);
        STAMP(28);
        unsigned xg = 0u;
        if (wv == 0) {
            while (__builtin_amdgcn_s_memrealtime() < t_att + XGATE) __builtin_amdgcn_s_sleep(2);
            for (unsigned spins = 0;;) {
                xg = __builtin_amdgcn_raw_buffer_load_b32(rw, (unsigned)(WS_X1 + x * STR + (fb * 512 + n0 + fc) * 2) + ((npm >> x) & 1u) * 8192u, 0, 16);
                if (__all(xg != SENT)) break;
                __builtin_amdgcn_s_sleep(SLPD);
                if (++spins > kSpinCap) break;
                asm volatile("" ::: "memory");
            }
            __builtin_amdgcn_s_sleep(SLPG);
        }
        wg_barrier();
        u32x4 uf[7];
        for (unsigned spins = 0;;) {
#pragma unroll
            for (int ks = 0; ks < 7; ++ks) uf[ks] = LD16(usrc + ks * 512, 0);
            unsigned m = 0u;
#pragma unroll
            for (int ks = 0; ks < 7; ++ks) m = mx4(uf[ks], m);
            if (__all(m != SENT)) break;
            if (++spins > kSpinCap) break;
            asm volatile("" ::: "memory");
        }
        STAMP(29);
        f32x4 d = {0.f, 0.f, 0.f, 0.f}, d2 = {0.f, 0.f, 0.f, 0.f};
#pragma unroll
        for (int ks = 0; ks < 7; ++ks) {
            if (ks & 1) d2 = MFMA16(wfr[ks], BF8(uf[ks]), d2);
            else d = MFMA16(wfr[ks], BF8(uf[ks]), d);
        }
        *LP(f32x4, rdw) = d + d2;
        wg_barrier();
        if (wv == 0) {
            f32x2_t c[8];
#pragma unroll
            for (int w2 = 0; w2 < 8; ++w2) c[w2] = *LP(f32x2_t, rdr + w2 * 512);
            const f32x2_t sm = ((c[0] + c[1]) + (c[2] + c[3])) + ((c[4] + c[5]) + (c[6] + c[7]));
            unsigned t = pk2(sm[0], sm[1]);
            t = pk2(__uint_as_float(gb[0]) * lo16(t), __uint_as_float(gb[1]) * hi16(t));
            *(GAS unsigned*)outp = pk2(lo16(xg) + lo16(t), hi16(xg) + hi16(t));
            STAMP(31);
        }
        STAMP_FLUSH();
    }
}

extern "C" void kernel_launch(void* const* d_in, const int* in_sizes, int n_in,
                              void* d_out, int out_size, void* d_ws, size_t ws_size,
                              hipStream_t stream) {
    Params p;
    p.x = (const unsigned short*)d_in[0];
    p.ckv = (const unsigned short*)d_in[1];
    p.w_q = (const unsigned short*)d_in[2];
    p.w_uk = (const unsigned short*)d_in[3];
    p.w_uv = (const unsigned short*)d_in[4];
    p.w_o = (const unsigned short*)d_in[5];
    p.w1 = (const unsigned short*)d_in[7];
    p.w2 = (const unsigned short*)d_in[8];
    p.c = (const unsigned short*)d_in[9];
    p.w_mod = (const unsigned short*)d_in[10];
    p.ws = (char*)d_ws;
    p.out = (unsigned short*)d_out;
    (void)hipFuncSetAttribute((const void*)k_main, hipFuncAttributeMaxDynamicSharedMemorySize, 163840);
    k_main<<<256, 512, 163840, stream>>>(p);
}
```

```cpp
#include <hip/hip_runtime.h>
#include <hip/hip_bf16.h>
#include <stdint.h>

#define NH 32
#define SK 4096
#define DFF 1792
#define GI 19
#define ROTC 1
#define PFS 2
#define XNO 0
#define CPJ 7
#define NTM 16
#define QHELP 4
#define HP 13
#define NTH 0
#define W0 2
#define SL0 1
#define LMPF 0
#define SLPB 20
#define MD0 0
#define MD1 0
#define NEARP 1
#define NCAL 2
#define TLBW 0
#define TWD 680
#define LMD 250
#define PFM 57
#define CPFE 34200
#define SLPM 0
#define SLPD 12
#define SLPG 8
#define SLPX 1
#define SLPH 4

using u16x8 = __attribute__((ext_vector_type(8))) unsigned short;
using s16x4 = __attribute__((ext_vector_type(4))) short;
using bf16x8 = __attribute__((ext_vector_type(8))) short;
using f32x16 = __attribute__((ext_vector_type(16))) float;
using f32x4 = __attribute__((ext_vector_type(4))) float;
using u32x4 = __attribute__((ext_vector_type(4))) unsigned;
using u32x2 = __attribute__((ext_vector_type(2))) unsigned;

#define GAS __attribute__((address_space(1)))
#define LAS __attribute__((address_space(3)))
typedef GAS unsigned long long gu64;
#define RLX_AGENT __ATOMIC_RELAXED, __HIP_MEMORY_SCOPE_AGENT
#define SENT 0xffffffffu
#define TAGC 0x9e3779b9u

__device__ __forceinline__ float bf2f(unsigned short u) { return __uint_as_float(((unsigned)u) << 16); }
typedef __attribute__((ext_vector_type(2))) __bf16 bf16x2_t;
typedef __attribute__((ext_vector_type(2))) float f32x2_t;
__device__ __forceinline__ unsigned pk2(float a, float b) {
    f32x2_t v = {a, b};
    bf16x2_t r = __builtin_convertvector(v, bf16x2_t);
    return __builtin_bit_cast(unsigned, r);
}
__device__ __forceinline__ unsigned pk2s(float a, float b) {
    const unsigned r = pk2(a, b);
    return r == SENT ? 0x7fc07fc0u : r;
}
__device__ __forceinline__ unsigned f32s(float a) {
    const unsigned r = __float_as_uint(a);
    return r == SENT ? 0x7fc00000u : r;
}
__device__ __forceinline__ unsigned short f2bf(float f) { return (unsigned short)(pk2(f, 0.f) & 0xffffu); }
__device__ __forceinline__ float rbf(float f) { return __uint_as_float(pk2(f, 0.f) << 16); }
__device__ __forceinline__ float lo16(unsigned u) { return __uint_as_float(u << 16); }
__device__ __forceinline__ float hi16(unsigned u) { return __uint_as_float(u & 0xffff0000u); }
__device__ __forceinline__ unsigned umax3(unsigned a, unsigned b, unsigned c) { return max(max(a, b), c); }
__device__ __forceinline__ unsigned mx4(const u32x4& v, unsigned m) { return umax3(v[2], v[3], umax3(v[0], v[1], m)); }
__device__ __forceinline__ float fmax3(float a, float b, float c) { float r; asm("v_max3_f32 %0, %1, %2, %3" : "=v"(r) : "v"(a), "v"(b), "v"(c)); return r; }
template <int C> __device__ __forceinline__ float dppf(float v) { return __int_as_float(__builtin_amdgcn_update_dpp(0, __float_as_int(v), C, 0xf, 0xf, true)); }
__device__ __forceinline__ float sum16(float v) { v += dppf<0xB1>(v); v += dppf<0x4E>(v); v += dppf<0x141>(v); v += dppf<0x140>(v); return v; }

__device__ __forceinline__ void ln_stats(float p1, float p2, float& mb, float& rden) {
    auto t = __builtin_amdgcn_permlane32_swap(__float_as_uint(p1), __float_as_uint(p2), false, false);
    float a = sum16(__uint_as_float(t[0]) + __uint_as_float(t[1]));
    auto t2 = __builtin_amdgcn_permlane16_swap(__float_as_uint(a), __float_as_uint(a), false, false);
    a = (__uint_as_float(t2[0]) + __uint_as_float(t2[1])) * (1.f / 512.f);
    auto t3 = __builtin_amdgcn_permlane32_swap(__float_as_uint(a), __float_as_uint(a), false, false);
    const float m = __uint_as_float(t3[0]);
    const float var = rbf(fmaxf(fmaf(-m, m, __uint_as_float(t3[1])), 0.f));
    mb = rbf(m);
    rden = __builtin_amdgcn_rcpf(rbf(__builtin_amdgcn_sqrtf(rbf(var + 1e-5f))));
}
#define STR 262144
#define WS_Q 0
#define WS_H 32768
#define WS_X1 65536
#define WS_ML 81920
#define WS_MOD 98304
#define WS_MB 139264
#define WS_U 114688
#define WS_FI 131072
#define WS_FA 132096
#define WS_FB 133120
#define WS_NP 134144
#define WS_EPOCH 135168
#define WS_CAL 147456
#define WS_OUTP 2097152
#define WS_PART 4194304
#define WS_END 12582912

#define LP(T, off) ((LAS T*)(size_t)(unsigned)(off))
#define STAMP(k) do {} while (0)
#define STAMP_FLUSH() do {} while (0)
__device__ __forceinline__ s16x4 tr4(unsigned a) { return __builtin_amdgcn_ds_read_tr16_b64_v4i16(LP(s16x4, a)); }
__device__ __forceinline__ bf16x8 cat8(s16x4 a, s16x4 b) { return __builtin_shufflevector(a, b, 0, 1, 2, 3, 4, 5, 6, 7); }
__device__ __forceinline__ bf16x8 tr_frag(unsigned a0, unsigned a1) { return cat8(tr4(a0), tr4(a1)); }
__device__ __forceinline__ unsigned swz(unsigned row) { return ((row & 3) << 2) | ((row >> 2) & 3); }
__device__ __forceinline__ unsigned off_b(unsigned row, unsigned ch) { return 256u * row + 16u * (ch ^ swz(row)); }
__device__ __forceinline__ unsigned tile_off(unsigned key, unsigned chunk) { return ((key >> 5) * 4 + (chunk >> 4)) * 8192u + off_b(key & 31, chunk & 15); }
__device__ __forceinline__ void wg_barrier() { asm volatile("s_waitcnt lgkmcnt(0)\n\ts_barrier" ::: "memory"); }
template <int NT> __device__ __forceinline__ void dma16(unsigned voff, const void* sbase, unsigned lds_dst) {
    if (NT) asm volatile("s_mov_b32 m0, %2\n\ts_nop 0\n\tglobal_load_lds_dwordx4 %0, %1 nt" :: "v"(voff), "s"(sbase), "s"(lds_dst) : "memory", "m0");
    else asm volatile("s_mov_b32 m0, %2\n\ts_nop 0\n\tglobal_load_lds_dwordx4 %0, %1" :: "v"(voff), "s"(sbase), "s"(lds_dst) : "memory", "m0");
}
template <int NT> __device__ __forceinline__ void dma16x(unsigned voff, unsigned vx, const void* sbase, unsigned lds_dst) {
    unsigned t;
#define DMAX(pol, c) asm volatile("s_mov_b32 m0, %3\n\tv_xor_b32 %0, %4, %1\n\tglobal_load_lds_dwordx4 %0, %2" pol : "=&v"(t) : "v"(voff), "s"(sbase), "s"(lds_dst), c(vx) : "memory", "m0")
    if (__builtin_constant_p(vx)) { if (NT) DMAX(" nt", "i"); else DMAX("", "i"); }
    else { if (NT) DMAX(" nt", "s"); else DMAX("", "s"); }
}
template <int NT> __device__ __forceinline__ void pf4(unsigned voff, const void* sbase, unsigned lds_dst) {
    if (NT) asm volatile("s_mov_b32 m0, %2\n\ts_nop 0\n\tglobal_load_lds_dword %0, %1 nt" :: "v"(voff), "s"(sbase), "s"(lds_dst) : "memory", "m0");
    else asm volatile("s_mov_b32 m0, %2\n\ts_nop 0\n\tglobal_load_lds_dword %0, %1" :: "v"(voff), "s"(sbase), "s"(lds_dst) : "memory", "m0");
}
#define XGATE 300
#define SPINCAP (1u << 20)
constexpr unsigned kSpinCap = SPINCAP;
struct Params {
    const unsigned short* x;
    const unsigned short* ckv;
    const unsigned short* w_q;
    const unsigned short* w_uk;
    const unsigned short* w_uv;
    const unsigned short* w_o;
    const unsigned short* w1;
    const unsigned short* w2;
    const unsigned short* c;
    const unsigned short* w_mod;
    char* ws;
    unsigned short* out;
};
#define VMW(n) asm volatile("s_waitcnt vmcnt(%0)" :: "n"(n) : "memory")
#define PIN(x) asm volatile("" : "+v"(x))
#define PIN4(a, b, c, d) asm volatile("" : "+v"(a), "+v"(b), "+v"(c), "+v"(d) :: "memory")
#define MFMA16(a, b, c) __builtin_amdgcn_mfma_f32_16x16x32_bf16(a, b, c, 0, 0, 0)
#define MFMA32(a, b, c) __builtin_amdgcn_mfma_f32_32x32x16_bf16(a, b, c, 0, 0, 0)
#define BF8(x) __builtin_bit_cast(bf16x8, x)
#define ST16(v, off, soff) __builtin_amdgcn_raw_buffer_store_b128(v, rw, off, soff, 16)
#define LD16(off, soff) __builtin_amdgcn_raw_buffer_load_b128(rw, off, soff, 16)
#define SCL (0.08838834764831845f * 1.4426950408889634f)
#define BIGD 60.f
#define L_SCR 131072
#define HS_LD 520

__global__ __launch_bounds__(512, 2) void k_main(Params p) {
    unsigned long long t00; asm volatile("s_memrealtime %0" : "=s"(t00));
    unsigned long long pc;
    asm volatile("s_getpc_b64 %0" : "=s"(pc));
    const int id = blockIdx.x, tid = threadIdx.x;
    int lane = tid & 63;
    const int wv = __builtin_amdgcn_readfirstlane(tid >> 6);
    int g = lane >> 4, nl = lane & 15;
    auto rederive = [&]() {
        asm volatile("v_mbcnt_lo_u32_b32 %0, -1, 0\n\tv_mbcnt_hi_u32_b32 %0, -1, %0" : "=v"(lane));
        g = lane >> 4; nl = lane & 15;
    };
    const int x = id & 7, j = id >> 3;
    if (wv < 5 && j == 15) {
        asm volatile("s_mov_b32 m0, %2\n\ts_nop 0\n\tglobal_load_lds_dword %0, %1" :: "v"(min((unsigned)((wv * 64 + lane) * 128), (unsigned)CPFE)), "s"(pc), "s"(L_SCR + 31744) : "memory", "m0");
    }
    if ((j >> 2) == 3) {
        dma16<0>((unsigned)(((nl & 7) * 512 + (nl >> 3) * 32 + g * 8) * 2 + wv * 128), p.c, wv * 16384 + 8192);
        dma16<0>((unsigned)tid * 16u, p.x, wv * 16384 + 9216);
    }
    char* ws = p.ws;
    unsigned long long t01; asm volatile("s_memrealtime %0\n\ts_waitcnt lgkmcnt(0)" : "=s"(t01) : "s"(ws), "s"(p.ckv));
    STAMP(0);
    const __amdgpu_buffer_rsrc_t rw = __builtin_amdgcn_make_buffer_rsrc(ws, 0, WS_END, 0x00020000);
    const u32x4 s4 = {SENT, SENT, SENT, SENT};
    unsigned long long cnt = 0ull;
    auto cnt_load = [&]() { asm volatile("global_load_dwordx2 %0, %1, %2 sc1" : "+v"(cnt) : "v"((unsigned)WS_EPOCH), "s"(ws) : "memory"); };
    unsigned tlo = 0u, thi = 0u;
    auto tag_take = [&]() {
        asm volatile("" : "+v"(cnt) :: "memory");
        const unsigned long long t = cnt + 1ull;
        tlo = __builtin_amdgcn_readfirstlane((unsigned)t); thi = __builtin_amdgcn_readfirstlane((unsigned)(t >> 32));
    };
    auto flag_put = [&](unsigned base, unsigned idx) {
        if (lane < 8) __builtin_amdgcn_raw_buffer_store_b64(u32x2{tlo, thi}, rw, base + lane * STR + idx * 8, 0, 16);
    };
    auto mod_get = [&](unsigned off) {
        u32x4 v;
        for (unsigned spins = 0;;) {
            v = LD16(off, 0);
            if (__all((v[2] == tlo) & (v[3] == thi))) break;
            __builtin_amdgcn_s_sleep(16);
            if (++spins > kSpinCap) break;
            asm volatile("" ::: "memory");
        }
        return u32x2{v[0], v[1]};
    };
    auto np_mask = [&](u32x4 v) {
        for (unsigned spins = 0; !__all((v[2] == tlo) & (v[3] == thi)) && spins < kSpinCap; ++spins) {
            __builtin_amdgcn_s_sleep(8);
            asm volatile("buffer_load_dwordx4 %0, %1, %2, 0 offen sc1\n\ts_waitcnt vmcnt(0)" : "=v"(v) : "v"((unsigned)(WS_NP + x * STR + (lane & 7) * 16)), "s"(rw) : "memory");
        }
        const int d = (int)v[0];
        int t = (lane & 4) ? -d : d;
        t += __builtin_amdgcn_update_dpp(0, t, 0xB1, 0xf, 0xf, true);
        t += __builtin_amdgcn_update_dpp(0, t, 0x4E, 0xf, 0xf, true);
        t += __builtin_amdgcn_update_dpp(0, t, 0x141, 0xf, 0xf, true);
        const bool n1 = abs(d) > 160 ? d < 0 : (t < 0) != ((lane & 4) != 0);
        return NEARP ? (unsigned)__ballot(n1) & 0xffu : 0u;
    };
    auto flags_ok = [&](const u32x4& f) { return (f[0] == tlo) & (f[1] == thi) & (f[2] == tlo) & (f[3] == thi); };
    auto flags_wait = [&](unsigned off) {
        for (unsigned spins = 0;;) {
            u32x4 f;
            asm volatile("buffer_load_dwordx4 %0, %1, %2, 0 offen sc1\n\ts_waitcnt vmcnt(0)" : "=v"(f) : "v"(off), "s"(rw) : "memory");
            if (__all(flags_ok(f))) break;
            __builtin_amdgcn_s_sleep(16);
            if (++spins > kSpinCap) break;
            asm volatile("" ::: "memory");
        }
    };
    const unsigned pcl = (unsigned)(((((lane & 7) >> 1) ^ ((lane >> 4) & 1)) * 32) + (lane & 1) * 16);
    auto dma_piece = [&](const unsigned short* src, unsigned ldb, unsigned dst, int n, int nt) {
        const unsigned v0 = (lane >> 3) * ldb + pcl, v1 = v0 ^ 64u;
#pragma unroll
        for (int i = 0; i < n; ++i) {
            if (nt) dma16<1>((i & 1) ? v1 : v0, (const char*)src + (size_t)i * 8 * ldb, dst + i * 1024);
            else dma16<0>((i & 1) ? v1 : v0, (const char*)src + (size_t)i * 8 * ldb, dst + i * 1024);
        }
    };
    const unsigned frl = (unsigned)((g * 8 + (nl >> 2)) * 128 + (lane & 3) * 8), frs = (unsigned)((nl >> 3) | ((g & 1) << 1));
    auto piece_frag = [&](unsigned base, int ks, int t) {
        const unsigned a = base + frl + ((t ^ frs) << 5) + ks * 4096;
        return tr_frag(a, a + 512);
    };
    const unsigned rgn = (unsigned)(wv * 16384);

    if (j < 16) {
        const unsigned long long t_start = __builtin_amdgcn_s_memrealtime();
        const int th = x * 4 + (j & 3);
        if (j < 12) {
            const int qt = j >> 2, rot = qt * 5;
            const bool pub = wv < (qt < 2 ? 3 : 2);
            const unsigned qo = (unsigned)(WS_Q + (th & 7) * STR + ((nl & 7) * 4 + (th >> 3)) * 1024 + (qt * 192 + wv * 64 + g * 16 + (nl >> 3) * 8) * 2);
            const unsigned vb = (unsigned)(g * 8192 + ((nl ^ (g << 2)) << 4));
            const char* wuk = (const char*)(p.w_uk + th * 128) + (size_t)qt * 192 * 8192;
            const char* wq = (const char*)(p.w_q + th * 128) + (size_t)wv * 32768;
            const unsigned L_U = 114688;
            {
                const int k0 = qt < 2 ? wv * 6 : wv * 4;
#pragma unroll
                for (int i = 0; i < 6; ++i) {
                    if (i < 4 || qt < 2) dma16<NTM & 2>(vb ^ (((k0 + i) & 3) << 4), wuk + (size_t)(k0 + i) * 32768, L_U + (k0 + i) * 1024);
                    if (i == 4) VMW(4);
                }
            }
            const unsigned vq = vb ^ ((wv & 3) << 4);
            auto wq_dma = [&](int ps) { dma16<NTM & 2>(vq, wq + (size_t)((ps + rot) & 15) * 262144, ps * 8192 + wv * 1024); };
            bf16x8 uk[4][4], aq[16];
            u32x4 fi, hv[4];
            unsigned fio = (unsigned)(WS_FI + x * STR + (lane & 7) * 16), hsrc, hsrc2, ta0, ta1, tb0, tb1, qo2, hw0, hw1, hr, hr2, qw, thx = 0u;
            const unsigned L_HI = L_SCR, L_QI = L_SCR + 8192;
            auto h_load = [&]() {
                asm volatile("buffer_load_dwordx4 %0, %4, %6, 0 offen sc1\n\tbuffer_load_dwordx4 %1, %4, %6, 0 offen offset:1024 sc1\n\t"
                             "buffer_load_dwordx4 %2, %5, %6, 0 offen sc1\n\tbuffer_load_dwordx4 %3, %5, %6, 0 offen offset:1024 sc1"
                             : "+v"(hv[0]), "+v"(hv[1]), "+v"(hv[2]), "+v"(hv[3]) : "v"(hsrc), "v"(hsrc2), "s"(rw) : "memory");
            };
            auto h_bad = [&]() {
                PIN4(hv[0], hv[1], hv[2], hv[3]);
                unsigned bad = (hv[0][1] ^ tlo) | (hv[0][3] ^ thx) | (hv[1][1] ^ tlo) | (hv[1][3] ^ thx) | (hv[2][1] ^ tlo) | (hv[2][3] ^ thx) | (hv[3][1] ^ tlo) | (hv[3][3] ^ thx);
                PIN(bad);
                return bad;
            };
#pragma unroll
            for (int ps = 0; ps < 16; ++ps) {
                wq_dma(ps);
                if (ps == 4) cnt_load();
                if (ps == HP) { h_load(); asm volatile("buffer_load_dwordx4 %0, %1, %2, 0 offen sc1" : "=v"(fi) : "v"(fio), "s"(rw) : "memory"); }
                if (ps >= 4 && ps < 8) VMW(5); else if (ps >= HP && ps < HP + 4) VMW(9); else VMW(4);
                if (ps == 3) {
                    asm volatile("s_barrier" ::: "memory");
                    if (pub) {
#pragma unroll
                        for (int t = 0; t < 4; ++t)
#pragma unroll
                            for (int s = 0; s < 4; ++s) uk[t][s] = *LP(bf16x8, L_U + off_b(wv * 64 + (nl >> 2) * 16 + t * 4 + (nl & 3), s * 4 + g));
                    }
                    STAMP(1);
                }
                if (ps == 5) {
                    hsrc = (unsigned)(WS_H + x * STR + wv * 2048 + lane * 16); hsrc2 = hsrc + 16384;
                    ta0 = off_b(8 * g + (nl >> 2), 2 * wv + ((lane & 3) >> 1)) + 8 * (lane & 1); ta1 = off_b(8 * g + 4 + (nl >> 2), 2 * wv + ((lane & 3) >> 1)) + 8 * (lane & 1);
                    tb0 = ta0 + 65536; tb1 = ta1 + 65536; qo2 = qo;
                    hw0 = L_HI + ((2 * wv - rot) & 15) * 512 + (lane >> 4) * 128 + (lane & 7) * 16 + ((lane >> 3) & 1) * 8;
                    hw1 = L_HI + ((2 * wv + 1 - rot) & 15) * 512 + (lane >> 4) * 128 + (lane & 7) * 16 + ((lane >> 3) & 1) * 8;
                    hr2 = L_HI + g * 128 + (nl & 7) * 16 + (nl >> 3) * 512;
                    PIN(hr2);
                    hr = L_HI + g * 128 + (nl & 7) * 16; qw = L_QI + (wv >> 1) * 512 + ((wv & 1) * 2 + (g >> 1)) * 128 + (nl & 7) * 16 + (g & 1) * 8;
                    PIN4(hsrc, hsrc2, ta0, ta1); PIN4(tb0, tb1, qo2, hw0); PIN(hw1); PIN(hr); PIN(qw);
                }
                if (ps == 8) { tag_take(); thx = tlo ^ thi ^ TAGC; }
                if (ps == 9) {
#pragma unroll
                    for (int t = 0; t < 4; ++t) PIN4(uk[t][0], uk[t][1], uk[t][2], uk[t][3]);
                    wg_barrier();
#pragma unroll
                    for (int q = 0; q < 6; ++q) aq[q] = tr_frag(ta0 + q * 8192, ta1 + q * 8192);
                }
                if (ps == 13) {
                    asm volatile("s_barrier" ::: "memory");
#pragma unroll
                    for (int q = 6; q < 10; ++q) aq[q] = q < 8 ? tr_frag(ta0 + q * 8192, ta1 + q * 8192) : tr_frag(tb0 + (q - 8) * 8192, tb1 + (q - 8) * 8192);
                }
            }
            STAMP(15);
            if (HP + 4 > 15) VMW(HP + 4 > 15 ? 15 - HP : 0);
            if (!__all(h_bad() == 0u)) {
                for (unsigned spins = 0;;) {
                    h_load();
                    VMW(0);
                    if (__all(h_bad() == 0u)) break;
                    __builtin_amdgcn_s_sleep(SLPH);
                    if (++spins > kSpinCap) break;
                }
            }
            {
                unsigned hh[4];
#pragma unroll
                for (int q = 0; q < 4; ++q) {
                    const unsigned t = hv[2 + (q >> 1)][(q & 1) * 2], sh = hv[q >> 1][(q & 1) * 2];
                    hh[q] = pk2(lo16(t) + lo16(sh), hi16(t) + hi16(sh));
                }
                *LP(u32x2, hw0) = u32x2{hh[0], hh[1]};
                *LP(u32x2, hw1) = u32x2{hh[2], hh[3]};
            }
            STAMP(3);
            asm volatile("s_waitcnt vmcnt(0) lgkmcnt(0)\n\ts_barrier" : "+v"(fi) :: "memory");
            STAMP(2);
            {
                bf16x8 hb[8];
#pragma unroll
                for (int q = 0; q < 5; ++q) hb[q] = *LP(bf16x8, hr2 + q * 1024);
#pragma unroll
                for (int ps = 10; ps < 16; ++ps) {
                    if (!(ps & 1)) hb[ps >> 1] = *LP(bf16x8, hr2 + (ps >> 1) * 1024);
                    aq[ps] = tr_frag(tb0 + (ps - 8) * 8192, tb1 + (ps - 8) * 8192);
                }
                __builtin_amdgcn_sched_barrier(0);
                f32x4 d0 = {0.f, 0.f, 0.f, 0.f}, d1 = {0.f, 0.f, 0.f, 0.f};
#pragma unroll
                for (int ps = 0; ps < 16; ps += 2) {
                    d0 = MFMA16(aq[ps], hb[ps >> 1], d0); d1 = MFMA16(aq[ps + 1], hb[ps >> 1], d1);
                    if (ps == 6) { unsigned bad = (fi[0] ^ tlo) | (fi[1] ^ thi) | (fi[2] ^ tlo) | (fi[3] ^ thi); PIN(bad); if (!__all(bad == 0u)) flags_wait(fio); }
                }
#pragma unroll
                for (int e = 0; e < 4; ++e) { float v = d0[e] + dppf<0x128>(d1[e]); asm("" : "+v"(v)); d0[e] = v; }
                if (nl < 8) *LP(u32x2, qw) = u32x2{pk2(d0[0], d0[1]), pk2(d0[2], d0[3])};
            }
            wg_barrier();
            if (pub) {
                bf16x8 qb[4];
#pragma unroll
                for (int s = 0; s < 4; ++s) qb[s] = *LP(bf16x8, hr + 8192 + s * 512);
                __builtin_amdgcn_sched_barrier(0);
                f32x4 d[4];
#pragma unroll
                for (int t = 0; t < 4; ++t) d[t] = MFMA16(uk[t][0], qb[0], (f32x4{0.f, 0.f, 0.f, 0.f}));
#pragma unroll
                for (int s = 1; s < 4; ++s)
#pragma unroll
                    for (int t = 0; t < 4; ++t) d[t] = MFMA16(uk[t][s], qb[s], d[t]);
                const f32x4 a = nl & 8 ? d[2] : d[0], b = nl & 8 ? d[3] : d[1];
                u32x4 qv = {pk2(a[0], a[1]), pk2(a[2], a[3]), pk2(b[0], b[1]), pk2(b[2], b[3])};
                if (__builtin_expect(mx4(qv, 0u) == SENT, 0)) qv = u32x4{pk2s(a[0], a[1]), pk2s(a[2], a[3]), pk2s(b[0], b[1]), pk2s(b[2], b[3])};
                ST16(qv, qo2, 0);
            }
            STAMP(4);
            wg_barrier();
            if (LMPF && wv >= 3 && wv < 6) {
                const unsigned n = (unsigned)(j * 3 + wv - 3);
                if (n < 32) pf4<0>(((n & 7u) * 64 + lane) * 6144u + 2048u + (x * 4 + (n >> 3)) * 128u, p.w_mod, L_SCR + 31744);
            }
            if (QHELP && wv < 3 && j + 12 * wv < 32) {
                const int hh = j + 12 * wv;
#pragma unroll 1
                for (unsigned spins = 0; spins < 64; ++spins) {
                    __builtin_amdgcn_s_sleep(QHELP);
                    const u32x4 v = LD16((unsigned)(WS_Q + (hh & 7) * STR + (x * 4 + (hh >> 3)) * 1024 + lane * 16), 0);
                    if (__all(mx4(v, 0u) != SENT)) break;
                    asm volatile("" ::: "memory");
                }
                STAMP(6);
            }
        } else {
            const int cg = x * 2 + (j & 1);
            if (j < 14) {
                dma_piece(p.w_mod + (size_t)wv * 64 * 3072 + cg * 64, 6144, rgn, 8, NTM & 2);
                cnt_load();
                VMW(9);
            } else {
                cnt_load();
                if (TLBW && j == 14) {
                    const char* base = wv == 0 ? (const char*)p.w_uv : wv == 1 ? (const char*)p.w_o : wv == 2 ? (const char*)p.w1 : wv == 3 ? (const char*)p.w2 : wv == 4 ? (const char*)p.out : ws + (wv - 5) * 4194304;
                    pf4<0>(min((unsigned)lane * 65536u, wv == 4 ? 0u : (wv & 6) == 2 ? 27u * 65536u : ~0u) + (wv > 4 ? 61440u : 0u), base, L_SCR + 31744);
                }
                ST16(s4, (unsigned)(WS_Q + wv * STR + cg * 2048 + lane * 16), 0); ST16(s4, (unsigned)(WS_Q + wv * STR + cg * 2048 + lane * 16), 1024);
                VMW(0);
                tag_take();
                wg_barrier();
                if (wv == 7) flag_put(WS_FI, cg);
                STAMP(15);
                if (j == 14) {
                    u32x2 best = {~0u, ~0u};
#pragma unroll
                    for (int r = 0; r < 2 * NCAL; ++r) {
                        const unsigned hf = (r ^ wv) & 1;
                        VMW(0);
                        const unsigned long long t0 = __builtin_amdgcn_s_memtime();
                        if (lane < 8) ST16(s4, WS_CAL + x * STR + hf * 8192 + (wv * 8 + r) * 128 + lane * 16, 0);
                        VMW(0);
                        const unsigned dt = (unsigned)(__builtin_amdgcn_s_memtime() - t0);
                        if (hf) best[1] = min(best[1], dt); else best[0] = min(best[0], dt);
                    }
                    *LP(u32x2, 16384 * 7 + 14336 + wv * 8) = best;
                    wg_barrier();
                    if (wv == 0 && lane < 8) {
                        u32x2 m = *LP(u32x2, 16384 * 7 + 14336 + lane * 8);
#pragma unroll
                        for (int o = 1; o < 8; o <<= 1) { m[0] = min(m[0], (unsigned)__shfl_xor(m[0], o)); m[1] = min(m[1], (unsigned)__shfl_xor(m[1], o)); }
                        ST16((u32x4{(unsigned)max(-1000, min(1000, (int)m[1] - (int)m[0])), 0u, tlo, thi}), (unsigned)(WS_NP + lane * STR + x * 16), 0);
                    }
                    STAMP(9);
                }
            }
            const u16x8 cf = *LP(u16x8, rgn + 8192 + lane * 16), xa = *LP(u16x8, rgn + 9216 + lane * 16);
            u32x4 af[2];
#pragma unroll
            for (int q = 0; q < 4; ++q) {
                const float a = bf2f(cf[2 * q]), b = bf2f(cf[2 * q + 1]);
                af[0][q] = pk2(a * __builtin_amdgcn_rcpf(1.f + __expf(-a)), b * __builtin_amdgcn_rcpf(1.f + __expf(-b)));
                af[1][q] = (unsigned)__builtin_amdgcn_update_dpp(0, (int)af[0][q], 0x128, 0xf, 0xf, true);
            }
            const unsigned L_MS = L_SCR;
            unsigned wa[4], fr[4];
#pragma unroll
            for (int t = 0; t < 4; ++t) { wa[t] = L_MS + wv * 2048 + (nl & 7) * 256 + (((t * 4 + g) ^ (nl & 7)) << 4) + (nl >> 3) * 16384; fr[t] = rgn + frl + ((t ^ frs) << 5); }
            PIN4(wa[0], wa[1], wa[2], wa[3]); PIN4(fr[0], fr[1], fr[2], fr[3]); PIN(af[0]); PIN(af[1]);
            const unsigned L_XN = 12288;
            auto mod_mm = [&](bool ln) {
                VMW(0);
                bf16x8 f[2][4];
#pragma unroll
                for (int ks = 0; ks < 2; ++ks)
#pragma unroll
                    for (int t = 0; t < 4; ++t) f[ks][t] = tr_frag(fr[t] + ks * 4096, fr[t] + ks * 4096 + 512);
                __builtin_amdgcn_sched_barrier(0);
                f32x4 d[4];
#pragma unroll
                for (int t = 0; t < 4; ++t) d[t] = MFMA16(f[0][t], BF8(af[0]), (f32x4{0.f, 0.f, 0.f, 0.f}));
#pragma unroll
                for (int t = 0; t < 4; ++t) d[t] = MFMA16(f[1][t], BF8(af[1]), d[t]);
#pragma unroll
                for (int t = 0; t < 4; ++t) *LP(f32x4, wa[t]) = d[t];
                if (ln) {
                    float v[8];
#pragma unroll
                    for (int q = 0; q < 8; ++q) v[q] = bf2f(xa[q]);
                    const float p1 = ((v[0] + v[1]) + (v[2] + v[3])) + ((v[4] + v[5]) + (v[6] + v[7]));
                    const float p2 = ((v[0] * v[0] + v[1] * v[1]) + (v[2] * v[2] + v[3] * v[3])) + ((v[4] * v[4] + v[5] * v[5]) + (v[6] * v[6] + v[7] * v[7]));
                    float mb, rden;
                    ln_stats(p1, p2, mb, rden);
                    u32x4 r;
#pragma unroll
                    for (int q = 0; q < 4; ++q) { const unsigned d = pk2(v[2 * q] - mb, v[2 * q + 1] - mb); r[q] = pk2(lo16(d) * rden, hi16(d) * rden); }
                    *LP(u32x4, L_XN + wv * 16384 + lane * 16) = r;
                }
                wg_barrier();
            };
            auto sum8 = [&](unsigned a) {
                f32x4 u[8];
#pragma unroll
                for (int w2 = 0; w2 < 8; ++w2) u[w2] = *LP(f32x4, a + w2 * 2048);
                __builtin_amdgcn_sched_barrier(0);
                return ((u[0] + u[1]) + (u[2] + u[3])) + ((u[4] + u[5]) + (u[6] + u[7]));
            };
            if (j < 14) {
                const int hb = lane & 7, c4 = 2 * wv + (nl >> 3);
                unsigned hoff = (unsigned)(WS_H + g * STR + cg * 2048 + wv * 256 + nl * 16);
                unsigned sa = L_MS + hb * 256 + ((c4 ^ hb) << 4);
                unsigned xa2 = L_XN + hb * 16384 + (cg & 7) * 128 + c4 * 8;
                PIN(hoff); PIN(sa); PIN(xa2);
                STAMP(1);
                mod_mm(cg >= 8);
                STAMP(2);
                const u32x2 xn = *LP(u32x2, xa2);
                const f32x4 v = sum8(sa);
                tag_take();
                const unsigned thx = tlo ^ thi ^ TAGC;
                u32x2 r = {pk2(v[0], v[1]), pk2(v[2], v[3])};
                if (cg >= 8) {
#pragma unroll
                    for (int q = 0; q < 2; ++q) { const unsigned sc = pk2(1.f + lo16(r[q]), 1.f + hi16(r[q])); r[q] = pk2(lo16(xn[q]) * lo16(sc), hi16(xn[q]) * hi16(sc)); }
                }
                ST16((u32x4{r[0], tlo, r[1], thx}), hoff, 0);
                ST16((u32x4{r[0], tlo, r[1], thx}), hoff, 4 * STR);
                STAMP(5);
            }
            wg_barrier();
            while (__builtin_amdgcn_s_memrealtime() < t_start + LMD) __builtin_amdgcn_s_sleep(2);
            const int lg = x * 4 + (j & 3);
            dma_piece(p.w_mod + (size_t)wv * 64 * 3072 + 1024 + lg * 64, 6144, rgn, 8, NTM & 4);
            mod_mm(false);
            if (lg >= 8 && lg < 24) {
                if (wv == 0) {
                    const int b = lane >> 3, c8 = lane & 7;
                    const f32x4 v0 = sum8(L_MS + b * 256 + (((2 * c8) ^ b) << 4)), v1 = sum8(L_MS + b * 256 + (((2 * c8 + 1) ^ b) << 4));
                    ST16((u32x4{pk2(v0[0], v0[1]), pk2(v0[2], v0[3]), pk2(v1[0], v1[1]), pk2(v1[2], v1[3])}), (unsigned)(WS_MB + b * STR + (lg - 8) * 128 + c8 * 16), 0);
                }
            } else if (wv < 4) {
                const int b = wv * 2 + (lane >> 5), cp = lane & 31;
                f32x2_t u[8];
#pragma unroll
                for (int w2 = 0; w2 < 8; ++w2) u[w2] = *LP(f32x2_t, L_MS + w2 * 2048 + b * 256 + (((cp >> 1) ^ b) << 4) + (cp & 1) * 8);
                const f32x2_t v = ((u[0] + u[1]) + (u[2] + u[3])) + ((u[4] + u[5]) + (u[6] + u[7]));
                ST16((u32x4{__float_as_uint(rbf(v[0])), __float_as_uint(rbf(v[1])), tlo, thi}), (unsigned)(WS_MOD + b * STR + (lg * 32 + cp) * 16), 0);
            }
            STAMP(6);
        }
        const int bq = (j >> 2) & 1, dh = j >> 3, src = th * 2 + dh, rb = bq * 4 + (src >> 4), rc = src & 15;
        wg_barrier();
        while (__builtin_amdgcn_s_memrealtime() < t_start + TWD - 110) __builtin_amdgcn_s_sleep(2);
        const unsigned npm = np_mask(LD16((unsigned)(WS_NP + x * STR + (lane & 7) * 16), 0)), np = (npm >> x) & 1u;
        unsigned oo = (unsigned)(WS_OUTP + ((bq * 4 + (nl & 3)) * 16 + wv * 2 + (nl >> 3)) * 16384 + (src ^ wv) * 128 + ((nl >> 2) & 1) * 64 + g * 16) + ((~npm >> ((nl & 3) * 2 + (wv >> 2))) & 1u) * 8192u;
        const int rcol = rc * 32 + (lane & 7) * 4 + (lane >> 5) * 2;
        unsigned xdst = (unsigned)(WS_X1 + ((lane >> 3) & 3) * STR + (rb * 512 + rcol) * 2), xdst2 = xdst + 4 * STR + ((npm >> (4 + ((lane >> 3) & 3))) & 1u) * 8192u;
        xdst += ((npm >> ((lane >> 3) & 3)) & 1u) * 8192u;
        ST16(s4, oo, 0);
        if (wv == 7) { __builtin_amdgcn_raw_buffer_store_b32(SENT, rw, xdst, 0, 16); __builtin_amdgcn_raw_buffer_store_b32(SENT, rw, xdst2, 0, 16); }
        VMW(0);
        wg_barrier();
        if (wv == 0) flag_put(WS_FB, id);
        while (__builtin_amdgcn_s_memrealtime() < t_start + TWD) __builtin_amdgcn_s_sleep(2);
        asm volatile("s_barrier" ::: "memory");
        STAMP(7);
        if ((PFM & 8) && wv == 0 && lane < 32) pf4<NTM & 8>((unsigned)((j * 32 + lane) * 8192 + x * 1024 + ((3u - ((unsigned)(size_t)p.w_uv >> 7)) & 7u) * 128), p.w_uv, L_SCR + 31744);
        if (bq) dma_piece(p.w_o + (size_t)(th * 128 + dh * 64) * 512 + wv * 64, 1024, rgn + 8192, 8, NTM & 8);
        dma_piece(p.w_uv + ((size_t)wv * 64 * NH + th) * 128 + dh * 64, 8192, rgn, 8, NTM & 8);
        if (!bq) dma_piece(p.w_o + (size_t)(th * 128 + dh * 64) * 512 + wv * 64, 1024, rgn + 8192, 8, NTM & 8);
        u32x4 fb, gm;
        unsigned xg;
        VMW(4);
        if (wv == 7)
            asm volatile("buffer_load_dwordx4 %0, %3, %5, 0 offen sc1\n\tbuffer_load_dwordx4 %1, %4, %5, 0 offen sc1\n\tglobal_load_dword %2, %6, %7" : "=&v"(fb), "=&v"(gm), "=&v"(xg)
                         : "v"((unsigned)(WS_FB + x * STR + lane * 16)), "v"((unsigned)(WS_MOD + rb * STR + rcol * 8)), "s"(rw), "v"((unsigned)(rb * 512 + rcol) * 2u), "s"(p.x) : "memory");
        VMW(0);
        asm volatile("" : "+v"(fb), "+v"(gm), "+v"(xg));
        STAMP(8);
        bf16x8 fuv[2][4], fwo[2][4];
        unsigned pfa[4];
#pragma unroll
        for (int t = 0; t < 4; ++t) { pfa[t] = rgn + frl + ((t ^ frs) << 5); PIN(pfa[t]); }
#pragma unroll
        for (int ks = 0; ks < 2; ++ks)
#pragma unroll
            for (int t = 0; t < 4; ++t) {
                const unsigned a = rgn + (unsigned)((g * 4 + (nl >> 2)) * 128 + (lane & 3) * 8) + ((t ^ (unsigned)((nl >> 3) | (g & 2))) << 5) + ks * 4096;
                fuv[ks][t] = tr_frag(a, a + 2048);
                fwo[ks][t] = tr_frag(pfa[t] + 8192 + ks * 4096, pfa[t] + 8192 + ks * 4096 + 512);
            }
#pragma unroll
        for (int ks = 0; ks < 2; ++ks) { PIN4(fuv[ks][0], fuv[ks][1], fuv[ks][2], fuv[ks][3]); PIN4(fwo[ks][0], fwo[ks][1], fwo[ks][2], fwo[ks][3]); }
        const unsigned L_WGT = L_SCR, L_MSK = L_SCR + 256, L_RED = L_SCR + 1024;
        unsigned psrc = (unsigned)(WS_PART + (bq * 4 * 32 + th) * 32768 + (wv >> 2) * 16384 + (wv & 3) * 2048 + ((lane >> 3) ^ (th & 7)) * 128) + np * 8192u + pcl, psrc1 = (psrc ^ 64u) + 1024;
        unsigned psrc2 = psrc ^ 8192u, psrc3 = psrc1 ^ 8192u;
        PIN(psrc2); PIN(psrc3); PIN(xdst2);
        unsigned rw2 = L_RED + wv * 1024 + (nl & 3) * 256 + g * 16;
        unsigned wa0 = (nl & 3) == (g >> 1) ? L_WGT + (nl & 4) * 32 + g * 16 : L_WGT + 272, wa1 = (nl & 3) == 2 + (g >> 1) ? L_WGT + (nl & 4) * 32 + 64 + g * 16 : L_WGT + 272;
        unsigned cr = rgn + (nl & 3) * 128 + g * 16, r2r = L_RED + lane * 16, cw = rgn + lane * 8;
        unsigned osrc = (unsigned)(WS_OUTP + (rb * 16 + rc) * 16384 + lane * 16) + (np ^ 1u) * 8192u;
        PIN(psrc); PIN(psrc1); PIN(rw2); PIN4(wa0, wa1, cr, r2r); PIN4(cw, oo, osrc, xdst);
        float mlm = 0.f, mll = 0.f;
        if (wv == 0) *LP(unsigned, L_WGT + 256 + lane * 4) = 0u;
        wg_barrier();
        const bool early = __all(flags_ok(fb) & (gm[2] == tlo) & (gm[3] == thi));
        u32x2 ga = {gm[0], gm[1]};
        PIN(ga);
        bf16x8 pa[2][4], wh[2];
        {
            u32x4 ml = {0u, 0u, ~tlo, ~thi};
            const unsigned mlsrc = (unsigned)(WS_ML + (bq * 4 + g) * STR + nl * 512 + th * 16) + ((npm >> (x & 6)) & 1u) * 8192u;
            unsigned long long done = 0ull;
            unsigned lm = L_MSK;
            PIN(lm);
#pragma unroll 1
            for (unsigned spins = 0; spins < kSpinCap; ++spins) {
                unsigned long long m;
                if (wv == 0) {
                    if ((ml[2] != tlo) | (ml[3] != thi)) ml = LD16(mlsrc, 0);
                    m = __ballot((ml[2] == tlo) & (ml[3] == thi));
                    unsigned long long f = m & (m >> 4);
                    f &= f >> 2; f &= f >> 1;
                    asm volatile("ds_write_b64 %0, %1" :: "v"(lm), "v"(f) : "memory");
                    m = m == ~0ull ? 0x0101010101010101ull : 0ull;
                } else {
                    unsigned long long t;
                    asm volatile("ds_read_b64 %0, %1\n\ts_waitcnt lgkmcnt(0)" : "=v"(t) : "v"(lm) : "memory");
                    m = ((unsigned long long)(unsigned)__builtin_amdgcn_readfirstlane((unsigned)(t >> 32)) << 32) | (unsigned)__builtin_amdgcn_readfirstlane((unsigned)t);
                    m &= 0x0101010101010101ull & ~done;
                }
                if (m) {
                    done |= m;
#define PLD(bit, off, v, base) "s_bitcmp1_b64 %0, " #bit "\n\ts_cbranch_scc0 1f\n\ts_add_u32 m0, %1, " #off "\n\ts_nop 0\n\tglobal_load_lds_dwordx4 " v ", " base " sc1\n1:\n\t"
                    asm volatile(PLD(0, 0, "%2", "%4") PLD(8, 1024, "%3", "%4") PLD(16, 2048, "%2", "%5") PLD(24, 3072, "%3", "%5")
                                 PLD(32, 4096, "%8", "%6") PLD(40, 5120, "%9", "%6") PLD(48, 6144, "%8", "%7") PLD(56, 7168, "%9", "%7")
                                 :: "s"(m), "s"(rgn), "v"(psrc), "v"(psrc1), "s"(ws), "s"(ws + 1048576), "s"(ws + 2097152), "s"(ws + 3145728), "v"(psrc2), "v"(psrc3) : "memory", "m0", "scc");
                    if (done == 0x0101010101010101ull) break;
                } else if (SLPM > 0 && wv == 0) __builtin_amdgcn_s_sleep(SLPM);
            }
            mlm = __uint_as_float(ml[0]); mll = __uint_as_float(ml[1]);
            STAMP(10);
            if (wv == 0) {
                float M = mlm;
                M = fmaxf(M, dppf<0xB1>(M)); M = fmaxf(M, dppf<0x4E>(M)); M = fmaxf(M, dppf<0x141>(M)); M = fmaxf(M, dppf<0x140>(M));
                const float e = __builtin_amdgcn_exp2f(mlm - M);
                const float w = e * __builtin_amdgcn_rcpf(sum16(e * mll));
                const unsigned short hi = f2bf(w);
                *LP(unsigned short, L_WGT + lane * 2) = hi;
                *LP(unsigned short, L_WGT + 128 + lane * 2) = f2bf(w - bf2f(hi));
            }
            wg_barrier();
            wh[0] = *LP(bf16x8, wa0); wh[1] = *LP(bf16x8, wa1);
            __builtin_amdgcn_sched_barrier(0);
            VMW(0);
#pragma unroll
            for (int ks = 0; ks < 2; ++ks)
#pragma unroll
                for (int t = 0; t < 4; ++t) pa[ks][t] = tr_frag(pfa[t] + ks * 4096, pfa[t] + ks * 4096 + 512);
        }
        STAMP(11);
        {
            f32x4 d[4];
#pragma unroll
            for (int t = 0; t < 4; ++t) d[t] = MFMA16(pa[0][t], wh[0], (f32x4{0.f, 0.f, 0.f, 0.f}));
#pragma unroll
            for (int t = 0; t < 4; ++t) d[t] = MFMA16(pa[1][t], wh[1], d[t]);
#pragma unroll
            for (int t = 0; t < 4; ++t)
#pragma unroll
                for (int e = 0; e < 4; ++e) { float v = d[t][e] + dppf<0x124>(d[t][e]); asm("" : "+v"(v)); d[t][e] = v; }
            const u32x4 o0 = {pk2(d[0][0], d[0][1]), pk2(d[0][2], d[0][3]), pk2(d[1][0], d[1][1]), pk2(d[1][2], d[1][3])};
            const u32x4 o1 = {pk2(d[2][0], d[2][1]), pk2(d[2][2], d[2][3]), pk2(d[3][0], d[3][1]), pk2(d[3][2], d[3][3])};
            f32x4 y[4];
#pragma unroll
            for (int t = 0; t < 4; ++t) y[t] = MFMA16(fuv[0][t], BF8(o0), (f32x4{0.f, 0.f, 0.f, 0.f}));
#pragma unroll
            for (int t = 0; t < 4; ++t) y[t] = MFMA16(fuv[1][t], BF8(o1), y[t]);
            if (nl < 4) {
#pragma unroll
                for (int t = 0; t < 4; ++t) *LP(f32x4, rw2 + t * 64) = y[t];
            }
        }
        wg_barrier();
        {
            f32x4 cs[8];
#pragma unroll
            for (int w2 = 0; w2 < 8; ++w2) cs[w2] = *LP(f32x4, r2r + w2 * 1024);
            const f32x4 c4 = ((cs[0] + cs[1]) + (cs[2] + cs[3])) + ((cs[4] + cs[5]) + (cs[6] + cs[7]));
            *LP(u32x2, cw) = u32x2{pk2(c4[0], c4[1]), pk2(c4[2], c4[3])};
            asm volatile("s_waitcnt lgkmcnt(0)" ::: "memory");
            const u32x4 cx0 = *LP(u32x4, cr), cx1 = *LP(u32x4, cr + 64);
            f32x4 o[4];
#pragma unroll
            for (int q = 0; q < 4; ++q) o[q] = MFMA16(fwo[0][q], BF8(cx0), (f32x4{0.f, 0.f, 0.f, 0.f}));
#pragma unroll
            for (int q = 0; q < 4; ++q) o[q] = MFMA16(fwo[1][q], BF8(cx1), o[q]);
            const f32x4 sel = (nl & 8) ? ((nl & 4) ? o[3] : o[2]) : ((nl & 4) ? o[1] : o[0]);
            u32x4 sv = __builtin_bit_cast(u32x4, sel);
            if (__builtin_expect(mx4(sv, 0u) == SENT, 0)) sv = u32x4{f32s(sel[0]), f32s(sel[1]), f32s(sel[2]), f32s(sel[3])};
            ST16(sv, oo, 0);
        }
        STAMP(12);
        wg_barrier();
        if (wv == 7) {
            if (!early) {
                flags_wait(WS_FB + x * STR + lane * 16);
                ga = mod_get(WS_MOD + rb * STR + rcol * 8);
                PIN(ga);
            }
            u32x4 op[8];
            for (unsigned spins = 0;;) {
#pragma unroll
                for (int k = 0; k < 8; ++k) op[k] = LD16(osrc + (k & 3) * 1024, (k >> 2) * 4096);
                unsigned m = 0u;
#pragma unroll
                for (int k = 0; k < 8; ++k) m = mx4(op[k], m);
                if (__all(m != SENT)) break;
                if (++spins > kSpinCap) break;
                asm volatile("" ::: "memory");
            }
            STAMP(13);
            const f32x4 v = ((__builtin_bit_cast(f32x4, op[0]) + __builtin_bit_cast(f32x4, op[1])) + (__builtin_bit_cast(f32x4, op[2]) + __builtin_bit_cast(f32x4, op[3]))) +
                            ((__builtin_bit_cast(f32x4, op[4]) + __builtin_bit_cast(f32x4, op[5])) + (__builtin_bit_cast(f32x4, op[6]) + __builtin_bit_cast(f32x4, op[7])));
            float w[2];
#pragma unroll
            for (int q = 0; q < 2; ++q) {
                auto t = __builtin_amdgcn_permlane32_swap(__float_as_uint(v[q]), __float_as_uint(v[2 + q]), false, false);
                w[q] = __uint_as_float(t[0]) + __uint_as_float(t[1]);
                auto u = __builtin_amdgcn_permlane16_swap(__float_as_uint(w[q]), __float_as_uint(w[q]), false, false);
                w[q] = __uint_as_float(u[0]) + __uint_as_float(u[1]);
                w[q] += dppf<0x128>(w[q]);
            }
            unsigned t = pk2(w[0], w[1]);
            t = pk2(__uint_as_float(ga[0]) * lo16(t), __uint_as_float(ga[1]) * hi16(t));
            t = pk2s(lo16(xg) + lo16(t), hi16(xg) + hi16(t));
            __builtin_amdgcn_raw_buffer_store_b32(t, rw, xdst, 0, 16);
            __builtin_amdgcn_raw_buffer_store_b32(t, rw, xdst2, 0, 16);
            STAMP(14);
        }
        STAMP_FLUSH();
        return;
    }

    const int s = j - 16;
    if (wv == 0) cnt_load();
    {
        const unsigned ln = (unsigned)(s * 64 + lane);
        if ((PFM & 1) && wv == 0) pf4<NTM & 1>((ln & 511u) * 6144u + (x * 2 + (ln >> 9)) * 128u, p.w_mod, L_SCR);
        if ((PFM & 2) && wv == 1) {
            const unsigned short* w = s < 8 ? p.w_uk : p.w_q;
            pf4<NTM & 1>((ln & 511u) * 8192u + x * 1024u + ((3u - ((unsigned)(size_t)w >> 7)) & 7u) * 128u, w, L_SCR);
        }
        if ((PFM & 48) == 48) {
            const unsigned r0 = (unsigned)(s * 4 + (wv & 3)), row = PFS == 0 ? r0 * 8 + (lane >> 3) : PFS == 1 ? (lane >> 3) * 64 + r0 : (r0 >> 5) * 256 + (lane >> 3) * 32 + (r0 & 31);
            pf4<NTM & 1>(row * 8192u + x * 1024u + (lane & 7u) * 128u, wv < 4 ? p.w_uk : p.w_q, L_SCR);
        }
        else if ((PFM & 16) && wv >= 4) { const unsigned l2 = (unsigned)((s * 4 + (wv & 3)) * 64 + lane); pf4<NTM & 1>((l2 >> 3) * 8192u + x * 1024u + (l2 & 7u) * 128u, p.w_q, L_SCR); }
        else if ((PFM & 32) && wv >= 4) { const unsigned l2 = (unsigned)((s * 4 + (wv & 3)) * 64 + lane); pf4<NTM & 1>((l2 >> 3) * 8192u + x * 1024u + (l2 & 7u) * 128u, p.w_uk, L_SCR); }
        if ((PFM & 4) && (wv & 6) == 2) { const unsigned l2 = (unsigned)((s * 2 + (wv & 1)) * 64 + lane); pf4<NTM & 1>((l2 & 511u) * 8192u + (x * 4 + (l2 >> 9)) * 256u, p.w_q, L_SCR); }
    }
    const int ug = x * 7 + s;
    const int wp = wv & 3, ch = wv >> 2, kb = wp * 2 + ch;
    int head = lane & 31;
    unsigned h2 = lane >> 5;
    const unsigned vb = (unsigned)(g * 1024 + ((nl ^ (g << 2)) << 4)), lb = (unsigned)((wv >> 1) * 32768 + (wv & 1) * 4096);
    const char* ck = (const char*)p.ckv + ((size_t)x * SK + s * 256 + wv * 16) * 1024;
    const unsigned rot = ROTC ? (unsigned)s & 3u : 0u;
    const unsigned vbr = vb ^ (rot << 8);
    const char* ck2 = ck + 131072;
    auto ck_dma = [&](unsigned i) { dma16x<NTM & 16>(vbr, ((i & 3u) << 4) | (((i >> 2) & 3u) << 8) | ((i & 3u) << 12), (i >> 4) ? ck2 : ck, lb + ((i >> 2) & 3u) * 8192u + (i & 3u) * 1024u); };
    bf16x8 ka[16];
    s16x4 va[8][4];
    unsigned kad[4], vad[4], vad2[4];
    auto ka_reads = [&](int cb) {
#pragma unroll
        for (int u = 0; u < 4; ++u) ka[cb * 4 + u] = *LP(bf16x8, kad[u] + cb * 8192);
    };
    auto va_reads = [&]() {
#pragma unroll
        for (int kk = 0; kk < 8; ++kk)
#pragma unroll
            for (int q = 0; q < 4; ++q) va[kk][q] = tr4((kk < 4 ? vad[q] : vad2[q]) + ((kk >> 1) & 1) * 32768 + (kk & 1) * 4096);
    };
    auto va_pin = [&]() {
#pragma unroll
        for (int q = 0; q < 8; ++q) PIN4(va[q][0], va[q][1], va[q][2], va[q][3]);
    };
    u32x4 fa = {0u, 0u, 0u, 0u};
    const unsigned fao = (unsigned)(WS_FI + x * STR + (lane & 7) * 16);
    constexpr int LAST = GI + 4 > 19 ? GI + 4 : 19;
    const unsigned qsrc = (unsigned)(WS_Q + (wv & 1) * 4 * STR + (x * 4 + (wv >> 1)) * 1024) + (g ^ rot) * 256 + ((nl ^ ((wv * 4) & 15)) << 4);
    auto q_dma = [&](int k) {
        unsigned t;
        asm volatile("s_mov_b32 m0, %3\n\tv_xor_b32 %0, %4, %1\n\tglobal_load_lds_dwordx4 %0, %2 sc1" : "=&v"(t) : "v"(qsrc), "s"(ws), "s"(L_SCR + (wv * 4 + k) * 1024), "i"(k * (STR + 16)) : "memory", "m0");
    };
#pragma unroll
    for (int i = 0; i <= LAST; ++i) {
        ck_dma(i);
        if (i == 3) {
#pragma unroll
            for (int u = 0; u < 4; ++u) {
                kad[u] = tile_off(wp * 32 + (head ^ (ch * 16)), ch * 8 + 2 * u + h2);
                vad[u] = tile_off(4 * h2 + (nl >> 2) + 8 * (u & 1), wv * 8 + 4 * (u >> 1) + 2 * (g & 1) + ((lane & 3) >> 1)) + 8 * (lane & 1);
                vad2[u] = vad[u] + 65536;
                PIN(kad[u]); PIN(vad[u]); PIN(vad2[u]);
            }
        }
        if (i > GI && i < GI + 4) VMW(8); else if (i < NTH) { if (i >= W0) VMW(W0); } else if (i >= 4) VMW(4);
        if (i == 15 && wv == 0) {
            PIN(fa);
            tag_take();
            if (!__all(flags_ok(fa))) flags_wait(fao);
        }
        if ((i & 3) == 3 && i >= 7 && i <= 19) {
            const int cb = (i - 7) >> 2;
            if (cb > 0) { PIN4(ka[cb * 4 - 4], ka[cb * 4 - 3], ka[cb * 4 - 2], ka[cb * 4 - 1]); va_pin(); }
            wg_barrier();
            ka_reads(cb);
            if ((wv >> 1) == cb) va_reads();
            STAMP(16 + cb);
        }
        if (i == 10 && wv == 0) asm volatile("buffer_load_dwordx4 %0, %1, %2, 0 offen sc1" : "+v"(fa) : "v"(fao), "s"(rw) : "memory");
        if (i == GI) { q_dma(0); q_dma(1); q_dma(2); q_dma(3); }
    }
    const unsigned L_X = L_SCR, L_P = L_SCR + 16384, L_EXM = L_SCR + 24576, L_EXS = L_SCR + 25600, L_BIG = L_SCR + 27648;
    unsigned nx = LAST + 1, npm = 0u;
    unsigned qad[4];
    {
        unsigned qck = L_SCR + wv * 4096 + lane * 16;
        u32x4 qb[4];
#pragma unroll
        for (int k = 0; k < 4; ++k) qb[k] = *LP(u32x4, qck + k * 1024);
#pragma unroll
        for (int k = 0; k < 4; ++k) { qad[k] = L_SCR + head * 1024 + (((ch * 8 + 2 * k + h2) ^ (head & 15)) << 4); PIN(qad[k]); }
        STAMP(20);
        if (!__all(mx4(qb[0], mx4(qb[1], mx4(qb[2], mx4(qb[3], 0u)))) != SENT)) {
            for (unsigned spins = 0;;) {
#pragma unroll
                for (int k = 0; k < 4; ++k)
                    if (!__all(mx4(qb[k], 0u) != SENT)) q_dma(k);
                if (nx <= 25) {
#pragma unroll
                    for (int i = 0; i < 3; ++i) ck_dma(nx + i);
                    nx += 3;
                    VMW(3);
                } else {
                    for (; nx < 28; ++nx) ck_dma(nx);
                    VMW(0);
                }
#pragma unroll
                for (int k = 0; k < 4; ++k) qb[k] = *LP(u32x4, qck + k * 1024);
                if (__all(mx4(qb[0], mx4(qb[1], mx4(qb[2], mx4(qb[3], 0u)))) != SENT)) break;
                if (++spins > kSpinCap) break;
            }
        }
    }
    constexpr int NE = LAST + 1 + SL0 + 5 < 28 ? LAST + 1 + SL0 + 5 : 28;
    auto slot = [&](int k) { if (LAST + 1 + k < 28) { if (nx <= (unsigned)(LAST + 1 + k)) ck_dma(LAST + 1 + k); } };
    auto slot2 = [&](int k) { if (NE + k < 28) { if (nx <= (unsigned)(NE + k)) ck_dma(NE + k); } else if (NE + k < 32) ck_dma(NE + k); };
    unsigned xw = wp * 32768 + 24576 + ch * 2048 + lane * 16, xr = xw ^ 2048u, exw = L_EXM + (head * 8 + wv) * 4, exr = L_EXM + head * 32;
    unsigned pwr = L_P + kb * 1024 + lane * 16, prd = L_P + lane * 16, esw = L_EXS + (head * 16 + h2 * 8 + wv) * 4;
    PIN4(xw, xr, exw, exr); PIN(pwr); PIN(prd); PIN(esw);
    PIN4(ka[12], ka[13], ka[14], ka[15]); va_pin();
    wg_barrier();
    STAMP(21);
    bf16x8 qf[16];
#pragma unroll
    for (int ks = 0; ks < 16; ++ks) qf[ks] = *LP(bf16x8, qad[ks & 3] + (ks >> 2) * 256);
    __builtin_amdgcn_sched_barrier(0);
#pragma unroll
    for (int k = 0; k < SL0; ++k) slot(k);
    __builtin_amdgcn_sched_barrier(0);
    float lsum, mrun;
    f32x16 o[2], sc2;
    auto meet = [&](const f32x16& sc, float (&t)[8]) {
        *LP(f32x4, xw) = f32x4{sc[8], sc[9], sc[10], sc[11]}; *LP(f32x4, xw + 1024) = f32x4{sc[12], sc[13], sc[14], sc[15]};
        wg_barrier();
        const f32x4 x0 = *LP(f32x4, xr), x1 = *LP(f32x4, xr + 1024);
#pragma unroll
        for (int r = 0; r < 4; ++r) { t[r] = sc[r] + x0[r]; t[4 + r] = sc[4 + r] + x1[r]; }
    };
    {
        f32x16 sc;
#pragma unroll
        for (int i = 0; i < 16; ++i) sc[i] = 0.f;
#pragma unroll
        for (int ks = 0; ks < 16; ++ks) {
            sc = MFMA32(ka[ks], qf[ks], sc);
            if ((ks & 3) == 3) slot(SL0 + (ks >> 2));
            __builtin_amdgcn_sched_barrier(0);
        }
        float t[8];
        meet(sc, t);
        {
            const float m = fmax3(fmax3(t[0], t[1], t[2]), fmax3(t[3], t[4], t[5]), fmax3(t[6], t[7], t[7]));
            auto u = __builtin_amdgcn_permlane32_swap(__float_as_uint(m), __float_as_uint(m), false, false);
            *LP(float, exw) = fmax3(__uint_as_float(u[0]), __uint_as_float(u[1]), __uint_as_float(u[1]));
        }
        slot(SL0 + 4);
        VMW(NE > 24 ? NE - 24 : 0);
        wg_barrier();
        const f32x4 a0 = *LP(f32x4, exr), a1 = *LP(f32x4, exr + 16);
        ka_reads(0); ka_reads(1);
        __builtin_amdgcn_sched_barrier(0);
        slot2(0);
        mrun = fmax3(fmax3(a0[0], a0[1], a0[2]), fmax3(a0[3], a1[0], a1[1]), fmax3(a1[2], a1[3], a1[3])) * SCL;
        float e[8];
#pragma unroll
        for (int r = 0; r < 8; ++r) e[r] = __builtin_amdgcn_exp2f(fmaf(t[r], SCL, -mrun));
        *LP(u32x4, pwr) = u32x4{pk2(e[0], e[1]), pk2(e[2], e[3]), pk2(e[4], e[5]), pk2(e[6], e[7])};
        lsum = ((e[0] + e[1]) + (e[2] + e[3])) + ((e[4] + e[5]) + (e[6] + e[7]));
        if (wv == 0) *LP(u32x4, L_BIG) = u32x4{0u, 0u, tlo, thi};
        slot2(1);
        wg_barrier();
        STAMP(22);
        bf16x8 pf[8];
#pragma unroll
        for (int kk = 0; kk < 8; ++kk) pf[kk] = *LP(bf16x8, prd + kk * 1024);
#pragma unroll
        for (int i = 0; i < 16; ++i) { o[0][i] = 0.f; o[1][i] = 0.f; sc2[i] = 0.f; }
        __builtin_amdgcn_sched_barrier(0);
#pragma unroll
        for (int ks = 0; ks < 8; ++ks) {
            sc2 = MFMA32(ka[ks], qf[ks], sc2);
            if (ks & 1) slot2(2 + (ks >> 1));
            __builtin_amdgcn_sched_barrier(0);
        }
#pragma unroll
        for (int kk = 0; kk < 8; ++kk) {
            o[0] = MFMA32(cat8(va[kk][0], va[kk][1]), pf[kk], o[0]);
            o[1] = MFMA32(cat8(va[kk][2], va[kk][3]), pf[kk], o[1]);
            if (kk < 4) slot2(6 + kk);
            __builtin_amdgcn_sched_barrier(0);
        }
    }
    STAMP(23);
    rederive();
    head = lane & 31; h2 = lane >> 5;
    unsigned sw[4], srd = lb + (lane >> 3) * 128 + (((lane & 7) ^ (lane >> 3)) << 4);
#pragma unroll
    for (int q = 0; q < 4; ++q) sw[q] = lb + head * 128 + (((2 * q + h2) ^ (head & 7)) << 4);
    PIN4(sw[0], sw[1], sw[2], sw[3]);
    xw = L_X + wv * 2048 + lane * 16; xr = xw ^ 8192u;
    PIN(xw); PIN(xr);
    {
        const u32x4 q = *LP(u32x4, L_BIG);
        tlo = __builtin_amdgcn_readfirstlane(q[2]); thi = __builtin_amdgcn_readfirstlane(q[3]);
    }
    unsigned po[4];
    const unsigned cw = wv ^ (rot * 2);
    unsigned p0 = (unsigned)(WS_PART + (x * 32 + (lane >> 3)) * 32768 + (s ^ (lane >> 3)) * 128 + (lane & 7) * 16) + (cw >> 2) * 16384 + (cw & 3) * 2048;
    unsigned mlo = (unsigned)(WS_ML + x * STR + s * 512 + head * 16), esr = L_EXS + head * 64;
    unsigned fbo = (unsigned)(WS_FB + x * STR + lane * 16), npo = (unsigned)(WS_NP + x * STR + (lane & 7) * 16);
    PIN(p0); PIN(npo); PIN4(esr, srd, mlo, fbo);
    u32x4 nq;
    {
        f32x16& sc = sc2;
        if (wv < 4) va_reads();
        VMW(4);
        wg_barrier();
        ka_reads(2);
        if ((wv >> 1) == 2) va_reads();
        __builtin_amdgcn_sched_barrier(0);
#pragma unroll
        for (int ks = 8; ks < 12; ++ks) sc = MFMA32(ka[ks], qf[ks], sc);
        __builtin_amdgcn_sched_barrier(0);
        VMW(0);
        wg_barrier();
        STAMP(24);
        asm volatile("buffer_load_dwordx4 %0, %1, %2, 0 offen sc1" : "=v"(nq) : "v"(npo), "s"(rw) : "memory");
        if (wv == 0 && s < 11) asm volatile("buffer_load_dwordx4 %0, %1, %2, 0 offen sc1" : "=v"(fa) : "v"(fbo), "s"(rw) : "memory");
        ka_reads(3);
        __builtin_amdgcn_sched_barrier(0);
#pragma unroll
        for (int ks = 12; ks < 16; ++ks) sc = MFMA32(ka[ks], qf[ks], sc);
        float t[8];
        meet(sc, t);
        float e[8];
        const float mloc = fmax3(fmax3(t[0], t[1], t[2]), fmax3(t[3], t[4], t[5]), fmax3(t[6], t[7], t[7]));
#pragma unroll
        for (int r = 0; r < 8; ++r) e[r] = __builtin_amdgcn_exp2f(fmaf(t[r], SCL, -mrun));
        *LP(u32x4, pwr) = u32x4{pk2(e[0], e[1]), pk2(e[2], e[3]), pk2(e[4], e[5]), pk2(e[6], e[7])};
        *LP(float, esw) = lsum + (((e[0] + e[1]) + (e[2] + e[3])) + ((e[4] + e[5]) + (e[6] + e[7])));
        if (__builtin_expect(!(fmaf(mloc, SCL, -mrun) <= BIGD), 0)) *LP(unsigned, L_BIG) = 1u;
        va_pin();
        wg_barrier();
        STAMP(25);
        unsigned big = *LP(unsigned, L_BIG);
        if ((wv >> 1) == 3) va_reads();
        bf16x8 pf[8];
#pragma unroll
        for (int kk = 0; kk < 8; ++kk) pf[kk] = *LP(bf16x8, prd + kk * 1024);
        __builtin_amdgcn_sched_barrier(0);
        if (__builtin_expect(__builtin_amdgcn_readfirstlane(big) != 0, 0)) {
            auto u = __builtin_amdgcn_permlane32_swap(__float_as_uint(mloc), __float_as_uint(mloc), false, false);
            *LP(float, exw) = fmaxf(__uint_as_float(u[0]), __uint_as_float(u[1]));
#pragma unroll
            for (int kk = 0; kk < 8; kk += 4) PIN4(pf[kk], pf[kk + 1], pf[kk + 2], pf[kk + 3]);
            wg_barrier();
            const f32x4 a0 = *LP(f32x4, exr), a1 = *LP(f32x4, exr + 16);
            const float mnew = fmaxf(fmaxf(fmaxf(fmaxf(a0[0], a0[1]), fmaxf(a0[2], a0[3])), fmaxf(fmaxf(a1[0], a1[1]), fmaxf(a1[2], a1[3]))) * SCL, mrun);
#pragma unroll
            for (int r = 0; r < 8; ++r) e[r] = __builtin_amdgcn_exp2f(fmaf(t[r], SCL, -mnew));
            *LP(u32x4, pwr) = u32x4{pk2(e[0], e[1]), pk2(e[2], e[3]), pk2(e[4], e[5]), pk2(e[6], e[7])};
            const float al = __builtin_amdgcn_exp2f(mrun - mnew);
            *LP(float, esw) = lsum * al + (((e[0] + e[1]) + (e[2] + e[3])) + ((e[4] + e[5]) + (e[6] + e[7])));
            mrun = mnew;
            o[0] *= al; o[1] *= al;
            wg_barrier();
#pragma unroll
            for (int kk = 0; kk < 8; ++kk) pf[kk] = *LP(bf16x8, prd + kk * 1024);
        }
        auto stage = [&](int t2, int gp) {
            const unsigned lo0 = pk2(o[t2][8 * gp + 0], o[t2][8 * gp + 1]), lo1 = pk2(o[t2][8 * gp + 2], o[t2][8 * gp + 3]);
            const unsigned hi0 = pk2(o[t2][8 * gp + 4], o[t2][8 * gp + 5]), hi1 = pk2(o[t2][8 * gp + 6], o[t2][8 * gp + 7]);
            auto r0 = __builtin_amdgcn_permlane32_swap(lo0, hi0, false, false);
            auto r1 = __builtin_amdgcn_permlane32_swap(lo1, hi1, false, false);
            *LP(u32x4, sw[t2 * 2 + gp]) = u32x4{r0[0], r1[0], r0[1], r1[1]};
        };
#pragma unroll
        for (int kk = 0; kk < 8; ++kk) {
            o[0] = MFMA32(cat8(va[kk][0], va[kk][1]), pf[kk], o[0]);
            if (kk == 3) {
                asm volatile("s_waitcnt vmcnt(0)" : "+v"(nq) :: "memory");
                npm = np_mask(nq);
            }
            if (kk == 5) {
#pragma unroll
                for (int i = 0; i < 4; ++i) po[i] = p0 + i * 262144 + (((npm >> (2 * i + (lane >> 5))) ^ (x >> 1)) & 1u) * 8192u;
                mlo += ((npm >> ((head >> 3) * 2)) & 1u) * 8192u;
                PIN4(po[0], po[1], po[2], po[3]); PIN(mlo);
            }
            __builtin_amdgcn_sched_barrier(0);
        }
#pragma unroll
        for (int kk = 0; kk < 8; ++kk) {
            o[1] = MFMA32(cat8(va[kk][2], va[kk][3]), pf[kk], o[1]);
            if (kk == 2) { PIN(o[0]); stage(0, 0); }
            if (kk == 4) stage(0, 1);
            __builtin_amdgcn_sched_barrier(0);
        }
        stage(1, 0); stage(1, 1);
        asm volatile("s_waitcnt lgkmcnt(0)" ::: "memory");
        u32x4 w[4];
#pragma unroll
        for (int i = 0; i < 4; ++i) w[i] = *LP(u32x4, srd + i * 1024);
#pragma unroll
        for (int i = 0; i < 4; ++i) ST16(w[i], po[i], 0);
        __builtin_amdgcn_sched_barrier(0);
        STAMP(26);
        u32x4 mlrec;
        {
            const f32x4 b0 = *LP(f32x4, esr), b1 = *LP(f32x4, esr + 16), b2 = *LP(f32x4, esr + 32), b3 = *LP(f32x4, esr + 48);
            const f32x4 bs = (b0 + b1) + (b2 + b3);
            mlrec = u32x4{__float_as_uint(mrun), __float_as_uint((bs[0] + bs[1]) + (bs[2] + bs[3])), tlo, thi};
            PIN(mlrec);
        }
        VMW(0);
        wg_barrier();
        if (wv == 0) {
            if (lane < 32) ST16(mlrec, mlo, 0);
            PIN(fa);
            if (s < 11 && !__all(flags_ok(fa))) flags_wait(fbo);
        }
        STAMP(27);
        wg_barrier();
    }
    const unsigned long long t_att = __builtin_amdgcn_s_memrealtime();
    rederive();
    while (__builtin_amdgcn_s_memrealtime() < t_att + (s >= 11 ? MD0 : MD1)) __builtin_amdgcn_s_sleep(2);
    if (s >= 11) {
        const unsigned n = (unsigned)((s - 11) * 8 + wv);
        if (n < 28) pf4<NTM & 32>((n * 64 + lane) * 1024u + x * 128u, p.w2, 0);
        { const unsigned l2 = n * 64 + lane; pf4<NTM & 32>((l2 / 5u) * (DFF * 2) + min(((unsigned)x * 448u & ~127u) + (l2 % 5u) * 128u, DFF * 2 - 4u), p.w1, 0); }
        if (wv == 0) flag_put(WS_FA, id - 128);
        STAMP_FLUSH();
        return;
    }
    const unsigned L_H2S = L_SCR, L_REDM = L_SCR + 16384;
    if (s < 7) {
        {
            const char* w1s = (const char*)(p.w1 + (size_t)wv * 64 * DFF + ug * 32);
#pragma unroll
            for (int i = 0; i < 4; ++i) dma16<NTM & 32>((unsigned)((lane >> 2) * DFF * 2 + (lane & 3) * 16), w1s + (size_t)i * 16 * DFF * 2, wv * 4096 + i * 1024);
        }
        const int b = wv;
        if (wv == 0) ST16(s4, (unsigned)(WS_U + x * STR + (lane >> 5) * 8192 + s * 512 + (lane & 31) * 16), 0);
        const u32x4 shf = LD16((unsigned)(WS_MB + b * STR + lane * 16), 0), scl = LD16((unsigned)(WS_MB + b * STR + 1024 + lane * 16), 0);
        VMW(0);
        if (wv == 0) flag_put(WS_FA, id - 128);
        wg_barrier();
        bf16x8 wk0[8], wk1[8];
        if (wv >= 6) {
#pragma unroll
            for (int q = 0; q < 8; ++q) {
                const unsigned a = (unsigned)(q * 4096 + (g * 8 + (nl >> 2)) * 64 + (wv & 1) * 32 + (lane & 3) * 8);
                wk0[q] = tr_frag(a, a + 256); wk1[q] = tr_frag(a + 2048, a + 2304);
            }
#pragma unroll
            for (int q = 0; q < 8; q += 2) PIN4(wk0[q], wk0[q + 1], wk1[q], wk1[q + 1]);
        }
        float scv[8], shv[8];
#pragma unroll
        for (int q = 0; q < 4; ++q) {
            scv[2 * q] = rbf(1.f + lo16(scl[q])); scv[2 * q + 1] = rbf(1.f + hi16(scl[q])); shv[2 * q] = lo16(shf[q]); shv[2 * q + 1] = hi16(shf[q]);
            PIN4(scv[2 * q], scv[2 * q + 1], shv[2 * q], shv[2 * q + 1]);
        }
        unsigned har = L_H2S + ((lane & 7) * HS_LD + g * 8 + (nl >> 3) * 32) * 2, haw = L_H2S + (b * HS_LD + lane * 8) * 2;
        unsigned udst = (unsigned)(WS_U + x * STR + (nl >> 3) * 8192 + (s * 2 + (wv & 1)) * 256 + ((nl & 7) * 16 + g * 4) * 2), xsrc = (unsigned)(WS_X1 + x * STR + (b * 512 + lane * 8) * 2) + ((npm >> x) & 1u) * 8192u;
        PIN4(har, haw, udst, xsrc);
        STAMP(28);
        while (__builtin_amdgcn_s_memrealtime() < t_att + XGATE) __builtin_amdgcn_s_sleep(2);
        u32x4 xv;
        for (unsigned spins = 0;;) {
            xv = LD16(xsrc, 0);
            if (__all(mx4(xv, 0u) != SENT)) break;
            __builtin_amdgcn_s_sleep(SLPX);
            if (++spins > kSpinCap) break;
            asm volatile("" ::: "memory");
        }
        STAMP(29);
        {
            f32x2_t v[4];
#pragma unroll
            for (int q = 0; q < 4; ++q) v[q] = f32x2_t{lo16(xv[q]), hi16(xv[q])};
            const f32x2_t p1 = (v[0] + v[1]) + (v[2] + v[3]), p2 = (v[0] * v[0] + v[1] * v[1]) + (v[2] * v[2] + v[3] * v[3]);
            float mb, rden;
            ln_stats(p1[0] + p1[1], p2[0] + p2[1], mb, rden);
            u32x4 hr;
#pragma unroll
            for (int q = 0; q < 4; ++q) {
                const f32x2_t h = ((v[q] - mb) * rden) * f32x2_t{scv[2 * q], scv[2 * q + 1]} + f32x2_t{shv[2 * q], shv[2 * q + 1]};
                hr[q] = pk2(h[0], h[1]);
            }
            *LP(u32x4, haw) = hr;
        }
        wg_barrier();
        if (wv >= 6) {
            bf16x8 af[8];
#pragma unroll
            for (int q = 0; q < 8; ++q) af[q] = *LP(bf16x8, har + q * 128);
            __builtin_amdgcn_sched_barrier(0);
            f32x4 d0 = {0.f, 0.f, 0.f, 0.f}, d1 = {0.f, 0.f, 0.f, 0.f};
#pragma unroll
            for (int q = 0; q < 8; ++q) { d0 = MFMA16(wk0[q], af[q], d0); d1 = MFMA16(wk1[q], af[q], d1); }
            const f32x4 own = nl < 8 ? d0 : d1;
#pragma unroll
            for (int e = 0; e < 4; ++e) { float v = own[e] + dppf<0x128>(own[e]); asm("" : "+v"(v)); d0[e] = fmaxf(v, 0.f); }
            u32x2 uv = {pk2(d0[0], d0[1]), pk2(d0[2], d0[3])};
            if (__builtin_expect(max(uv[0], uv[1]) == SENT, 0)) uv = u32x2{pk2s(d0[0], d0[1]), pk2s(d0[2], d0[3])};
            __builtin_amdgcn_raw_buffer_store_b64(uv, rw, udst, 0, 16);
            STAMP(30);
        }
        STAMP_FLUSH();
        return;
    }
    {
        const int n0 = (x * 4 + (s - 7)) * 16;
        const char* mws = (const char*)(p.w2 + (size_t)wv * 224 * 512 + n0);
#pragma unroll
        for (int i = 0; i < 7; ++i) dma16<NTM & 32>((unsigned)((lane >> 1) * 1024 + (lane & 1) * 16), mws + i * 32768, rgn + i * 1024);
        const int fb = lane >> 3, fc = (lane & 7) * 2;
        u32x2 gb = {0u, 0u};
        if (wv == 0) {
            flag_put(WS_FA, id - 128);
            gb = mod_get(WS_MOD + fb * STR + (1536 + n0 + fc) * 8);
            flags_wait(WS_FA + x * STR + lane * 16);
            if (id == 23 * 8 && lane == 0) __builtin_amdgcn_raw_buffer_store_b64(u32x2{tlo, thi}, rw, WS_EPOCH, 0, 16);
            PIN(gb);
        }
        VMW(0);
        bf16x8 wfr[7];
        const unsigned trl = (unsigned)(g * 8 + (nl >> 2)) * 32u + (unsigned)(lane & 3) * 8u;
#pragma unroll
        for (int ks = 0; ks < 7; ++ks) { wfr[ks] = tr_frag(rgn + ks * 1024 + trl, rgn + ks * 1024 + trl + 128); PIN(wfr[ks]); }
        unsigned usrc = (unsigned)(WS_U + wv * STR + (g >> 1) * 256 + (nl & 7) * 32 + (g & 1) * 16) + ((npm >> x) & 1u) * 8192u;
        unsigned rdw = L_REDM + wv * 512 + (nl & 7) * 64 + g * 16, rdr = L_REDM + lane * 8;
        unsigned short* outp = p.out + fb * 512 + n0 + fc;
        PIN(usrc); PIN(rdw); PIN(rdr); PIN(outp);
        STAMP(28);
        unsigned xg = 0u;
        if (wv == 0) {
            while (__builtin_amdgcn_s_memrealtime() < t_att + XGATE) __builtin_amdgcn_s_sleep(2);
            for (unsigned spins = 0;;) {
                xg = __builtin_amdgcn_raw_buffer_load_b32(rw, (unsigned)(WS_X1 + x * STR + (fb * 512 + n0 + fc) * 2) + ((npm >> x) & 1u) * 8192u, 0, 16);
                if (__all(xg != SENT)) break;
                __builtin_amdgcn_s_sleep(SLPD);
                if (++spins > kSpinCap) break;
                asm volatile("" ::: "memory");
            }
            __builtin_amdgcn_s_sleep(SLPG);
        }
        wg_barrier();
        u32x4 uf[7];
        for (unsigned spins = 0;;) {
#pragma unroll
            for (int ks = 0; ks < 7; ++ks) uf[ks] = LD16(usrc + ks * 512, 0);
            unsigned m = 0u;
#pragma unroll
            for (int ks = 0; ks < 7; ++ks) m = mx4(uf[ks], m);
            if (__all(m != SENT)) break;
            if (++spins > kSpinCap) break;
            asm volatile("" ::: "memory");
        }
        STAMP(29);
        f32x4 d = {0.f, 0.f, 0.f, 0.f}, d2 = {0.f, 0.f, 0.f, 0.f};
#pragma unroll
        for (int ks = 0; ks < 7; ++ks) {
            if (ks & 1) d2 = MFMA16(wfr[ks], BF8(uf[ks]), d2);
            else d = MFMA16(wfr[ks], BF8(uf[ks]), d);
        }
        *LP(f32x4, rdw) = d + d2;
        wg_barrier();
        if (wv == 0) {
            f32x2_t c[8];
#pragma unroll
            for (int w2 = 0; w2 < 8; ++w2) c[w2] = *LP(f32x2_t, rdr + w2 * 512);
            const f32x2_t sm = ((c[0] + c[1]) + (c[2] + c[3])) + ((c[4] + c[5]) + (c[6] + c[7]));
            unsigned t = pk2(sm[0], sm[1]);
            t = pk2(__uint_as_float(gb[0]) * lo16(t), __uint_as_float(gb[1]) * hi16(t));
            *(GAS unsigned*)outp = pk2(lo16(xg) + lo16(t), hi16(xg) + hi16(t));
            { const unsigned long long r1_ = __builtin_amdgcn_s_memrealtime(); const unsigned dl = (unsigned)(t01 - t00) * 16u;
              while (__builtin_amdgcn_s_memrealtime() < r1_ + dl) __builtin_amdgcn_s_sleep(1); }
        }
        STAMP_FLUSH();
    }
}

extern "C" void kernel_launch(void* const* d_in, const int* in_sizes, int n_in,
                              void* d_out, int out_size, void* d_ws, size_t ws_size,
                              hipStream_t stream) {
    Params p;
    p.x = (const unsigned short*)d_in[0];
    p.ckv = (const unsigned short*)d_in[1];
    p.w_q = (const unsigned short*)d_in[2];
    p.w_uk = (const unsigned short*)d_in[3];
    p.w_uv = (const unsigned short*)d_in[4];
    p.w_o = (const unsigned short*)d_in[5];
    p.w1 = (const unsigned short*)d_in[7];
    p.w2 = (const unsigned short*)d_in[8];
    p.c = (const unsigned short*)d_in[9];
    p.w_mod = (const unsigned short*)d_in[10];
    p.ws = (char*)d_ws;
    p.out = (unsigned short*)d_out;
    (void)hipFuncSetAttribute((const void*)k_main, hipFuncAttributeMaxDynamicSharedMemorySize, 163840);
    k_main<<<256, 512, 163840, stream>>>(p);
}
```

```cpp
#include <hip/hip_runtime.h>
#include <hip/hip_bf16.h>
#include <stdint.h>

#define NH 32
#define SK 4096
#define DFF 1792
#define GI 18
#define ROTC 1
#define PFS 2
#define XNO 0
#define CPJ 7
#define NTM 16
#define QHELP 4
#define HP 13
#define NTH 0
#define W0 2
#define SL0 3
#define LMPF 0
#define SLPB 20
#define MD0 0
#define MD1 0
#define NEARP 1
#define NCAL 2
#define TLBW 0
#define TG 500
#define JPERM 1
#define NPOST 6
#define XS3 1
#define WG 2
#define TWD 680
#define LMD 250
#define PFM 57
#define CPFE 35300
#define SLPM 0
#define SLPD 12
#define SLPG 8
#define SLPX 1
#define SLPH 4

using u16x8 = __attribute__((ext_vector_type(8))) unsigned short;
using s16x4 = __attribute__((ext_vector_type(4))) short;
using bf16x8 = __attribute__((ext_vector_type(8))) short;
using f32x16 = __attribute__((ext_vector_type(16))) float;
using f32x4 = __attribute__((ext_vector_type(4))) float;
using u32x4 = __attribute__((ext_vector_type(4))) unsigned;
using u32x2 = __attribute__((ext_vector_type(2))) unsigned;

#define GAS __attribute__((address_space(1)))
#define LAS __attribute__((address_space(3)))
typedef GAS unsigned long long gu64;
#define RLX_AGENT __ATOMIC_RELAXED, __HIP_MEMORY_SCOPE_AGENT
#define SENT 0xffffffffu
#define TAGC 0x9e3779b9u

__device__ __forceinline__ float bf2f(unsigned short u) { return __uint_as_float(((unsigned)u) << 16); }
typedef __attribute__((ext_vector_type(2))) __bf16 bf16x2_t;
typedef __attribute__((ext_vector_type(2))) float f32x2_t;
__device__ __forceinline__ unsigned pk2(float a, float b) {
    f32x2_t v = {a, b};
    bf16x2_t r = __builtin_convertvector(v, bf16x2_t);
    return __builtin_bit_cast(unsigned, r);
}
__device__ __forceinline__ unsigned pk2s(float a, float b) {
    const unsigned r = pk2(a, b);
    return r == SENT ? 0x7fc07fc0u : r;
}
__device__ __forceinline__ unsigned f32s(float a) {
    const unsigned r = __float_as_uint(a);
    return r == SENT ? 0x7fc00000u : r;
}
__device__ __forceinline__ unsigned short f2bf(float f) { return (unsigned short)(pk2(f, 0.f) & 0xffffu); }
__device__ __forceinline__ float rbf(float f) { return __uint_as_float(pk2(f, 0.f) << 16); }
__device__ __forceinline__ float lo16(unsigned u) { return __uint_as_float(u << 16); }
__device__ __forceinline__ float hi16(unsigned u) { return __uint_as_float(u & 0xffff0000u); }
__device__ __forceinline__ unsigned umax3(unsigned a, unsigned b, unsigned c) { return max(max(a, b), c); }
__device__ __forceinline__ unsigned mx4(const u32x4& v, unsigned m) { return umax3(v[2], v[3], umax3(v[0], v[1], m)); }
__device__ __forceinline__ float fmax3(float a, float b, float c) { float r; asm("v_max3_f32 %0, %1, %2, %3" : "=v"(r) : "v"(a), "v"(b), "v"(c)); return r; }
template <int C> __device__ __forceinline__ float dppf(float v) { return __int_as_float(__builtin_amdgcn_update_dpp(0, __float_as_int(v), C, 0xf, 0xf, true)); }
__device__ __forceinline__ float sum16(float v) { v += dppf<0xB1>(v); v += dppf<0x4E>(v); v += dppf<0x141>(v); v += dppf<0x140>(v); return v; }

__device__ __forceinline__ void ln_stats(float p1, float p2, float& mb, float& rden) {
    auto t = __builtin_amdgcn_permlane32_swap(__float_as_uint(p1), __float_as_uint(p2), false, false);
    float a = sum16(__uint_as_float(t[0]) + __uint_as_float(t[1]));
    auto t2 = __builtin_amdgcn_permlane16_swap(__float_as_uint(a), __float_as_uint(a), false, false);
    a = (__uint_as_float(t2[0]) + __uint_as_float(t2[1])) * (1.f / 512.f);
    auto t3 = __builtin_amdgcn_permlane32_swap(__float_as_uint(a), __float_as_uint(a), false, false);
    const float m = __uint_as_float(t3[0]);
    const float var = rbf(fmaxf(fmaf(-m, m, __uint_as_float(t3[1])), 0.f));
    mb = rbf(m);
    rden = __builtin_amdgcn_rcpf(rbf(__builtin_amdgcn_sqrtf(rbf(var + 1e-5f))));
}
#define STR 262144
#define WS_Q 0
#define WS_H 32768
#define WS_X1 65536
#define WS_ML 81920
#define WS_MOD 98304
#define WS_MB 139264
#define WS_U 114688
#define WS_FI 131072
#define WS_FA 132096
#define WS_FB 133120
#define WS_NP 134144
#define WS_NM 134400
#define WS_EPOCH 135168
#define WS_CAL 147456
#define WS_OUTP 2097152
#define WS_PART 4194304
#define WS_END 12582912

#define LP(T, off) ((LAS T*)(size_t)(unsigned)(off))
#define STAMP(k) do {} while (0)
#define STAMP_FLUSH() do {} while (0)
__device__ __forceinline__ s16x4 tr4(unsigned a) { return __builtin_amdgcn_ds_read_tr16_b64_v4i16(LP(s16x4, a)); }
__device__ __forceinline__ bf16x8 cat8(s16x4 a, s16x4 b) { return __builtin_shufflevector(a, b, 0, 1, 2, 3, 4, 5, 6, 7); }
__device__ __forceinline__ bf16x8 tr_frag(unsigned a0, unsigned a1) { return cat8(tr4(a0), tr4(a1)); }
__device__ __forceinline__ unsigned swz(unsigned row) { return ((row & 3) << 2) | ((row >> 2) & 3); }
__device__ __forceinline__ unsigned off_b(unsigned row, unsigned ch) { return 256u * row + 16u * (ch ^ swz(row)); }
__device__ __forceinline__ unsigned tile_off(unsigned key, unsigned chunk) { return ((key >> 5) * 4 + (chunk >> 4)) * 8192u + off_b(key & 31, chunk & 15); }
__device__ __forceinline__ void wg_barrier() { asm volatile("s_waitcnt lgkmcnt(0)\n\ts_barrier" ::: "memory"); }
template <int NT> __device__ __forceinline__ void dma16(unsigned voff, const void* sbase, unsigned lds_dst) {
    if (NT) asm volatile("s_mov_b32 m0, %2\n\ts_nop 0\n\tglobal_load_lds_dwordx4 %0, %1 nt" :: "v"(voff), "s"(sbase), "s"(lds_dst) : "memory", "m0");
    else asm volatile("s_mov_b32 m0, %2\n\ts_nop 0\n\tglobal_load_lds_dwordx4 %0, %1" :: "v"(voff), "s"(sbase), "s"(lds_dst) : "memory", "m0");
}
template <int NT> __device__ __forceinline__ void dma16x(unsigned voff, unsigned vx, const void* sbase, unsigned lds_dst) {
    unsigned t;
#define DMAX(pol, c) asm volatile("s_mov_b32 m0, %3\n\tv_xor_b32 %0, %4, %1\n\tglobal_load_lds_dwordx4 %0, %2" pol : "=&v"(t) : "v"(voff), "s"(sbase), "s"(lds_dst), c(vx) : "memory", "m0")
    if (__builtin_constant_p(vx)) { if (NT) DMAX(" nt", "i"); else DMAX("", "i"); }
    else { if (NT) DMAX(" nt", "s"); else DMAX("", "s"); }
}
template <int NT> __device__ __forceinline__ void pf4(unsigned voff, const void* sbase, unsigned lds_dst) {
    if (NT) asm volatile("s_mov_b32 m0, %2\n\ts_nop 0\n\tglobal_load_lds_dword %0, %1 nt" :: "v"(voff), "s"(sbase), "s"(lds_dst) : "memory", "m0");
    else asm volatile("s_mov_b32 m0, %2\n\ts_nop 0\n\tglobal_load_lds_dword %0, %1" :: "v"(voff), "s"(sbase), "s"(lds_dst) : "memory", "m0");
}
__device__ __forceinline__ void pf4s(unsigned voff, const void* sbase, unsigned lds_dst) {
    asm volatile("s_mov_b32 m0, %2\n\ts_nop 0\n\tglobal_load_lds_dword %0, %1 sc1" :: "v"(voff), "s"(sbase), "s"(lds_dst) : "memory", "m0");
}
#define XGATE 300
#define SPINCAP (1u << 20)
constexpr unsigned kSpinCap = SPINCAP;
struct Params {
    const unsigned short* x;
    const unsigned short* ckv;
    const unsigned short* w_q;
    const unsigned short* w_uk;
    const unsigned short* w_uv;
    const unsigned short* w_o;
    const unsigned short* w1;
    const unsigned short* w2;
    const unsigned short* c;
    const unsigned short* w_mod;
    char* ws;
    unsigned short* out;
    char* dbgp;
};
#define VMW(n) asm volatile("s_waitcnt vmcnt(%0)" :: "n"(n) : "memory")
#define PIN(x) asm volatile("" : "+v"(x))
#define PIN4(a, b, c, d) asm volatile("" : "+v"(a), "+v"(b), "+v"(c), "+v"(d) :: "memory")
#define MFMA16(a, b, c) __builtin_amdgcn_mfma_f32_16x16x32_bf16(a, b, c, 0, 0, 0)
#define MFMA32(a, b, c) __builtin_amdgcn_mfma_f32_32x32x16_bf16(a, b, c, 0, 0, 0)
#define BF8(x) __builtin_bit_cast(bf16x8, x)
#define ST16(v, off, soff) __builtin_amdgcn_raw_buffer_store_b128(v, rw, off, soff, 16)
#define LD16(off, soff) __builtin_amdgcn_raw_buffer_load_b128(rw, off, soff, 16)
#define SCL (0.08838834764831845f * 1.4426950408889634f)
#define BIGV 1.0e18f
#define QD 4
#define QE 4
#define L_SCR 131072
#define HS_LD 520

__global__ __launch_bounds__(512, 2) void k_main(Params p) {
    unsigned long long pc;
    asm volatile("s_getpc_b64 %0" : "=s"(pc));
    const int id = blockIdx.x, tid = threadIdx.x;
    int lane = tid & 63;
    const int wv = __builtin_amdgcn_readfirstlane(tid >> 6);
    int g = lane >> 4, nl = lane & 15;
    auto rederive = [&]() {
        asm volatile("v_mbcnt_lo_u32_b32 %0, -1, 0\n\tv_mbcnt_hi_u32_b32 %0, -1, %0" : "=v"(lane));
        g = lane >> 4; nl = lane & 15;
    };
    const int x = id & 7, j0 = id >> 3, j = JPERM && j0 < 16 ? 15 - j0 : j0;
    if (wv < 5 && j == 15) {
        asm volatile("s_mov_b32 m0, %2\n\ts_nop 0\n\tglobal_load_lds_dword %0, %1" :: "v"(min((unsigned)((wv * 64 + lane) * 128), (unsigned)CPFE)), "s"(pc), "s"(L_SCR + 31744) : "memory", "m0");
    }
    if ((j >> 2) == 3) {
        dma16<0>((unsigned)(((nl & 7) * 512 + (nl >> 3) * 32 + g * 8) * 2 + wv * 128), p.c, wv * 16384 + 8192);
        dma16<0>((unsigned)tid * 16u, p.x, wv * 16384 + 9216);
    }
    char* ws = p.ws;
    STAMP(0);
    const __amdgpu_buffer_rsrc_t rw = __builtin_amdgcn_make_buffer_rsrc(ws, 0, WS_END, 0x00020000);
    const u32x4 s4 = {SENT, SENT, SENT, SENT};
    unsigned long long cnt = 0ull;
    auto cnt_load = [&]() { asm volatile("global_load_dwordx2 %0, %1, %2 sc1" : "+v"(cnt) : "v"((unsigned)WS_EPOCH), "s"(ws) : "memory"); };
    unsigned tlo = 0u, thi = 0u;
    auto tag_take = [&]() {
        asm volatile("" : "+v"(cnt) :: "memory");
        const unsigned long long t = cnt + 1ull;
        tlo = __builtin_amdgcn_readfirstlane((unsigned)t); thi = __builtin_amdgcn_readfirstlane((unsigned)(t >> 32));
    };
    auto flag_put = [&](unsigned base, unsigned idx) {
        if (lane < 8) __builtin_amdgcn_raw_buffer_store_b64(u32x2{tlo, thi}, rw, base + lane * STR + idx * 8, 0, 16);
    };
    auto tagged_wait = [&](unsigned off) {
        u32x4 v;
#pragma unroll 1
        for (unsigned spins = 0; spins < kSpinCap; ++spins) {
            asm volatile("buffer_load_dwordx4 %0, %1, %2, 0 offen sc1\n\ts_waitcnt vmcnt(0)" : "=v"(v) : "v"(off), "s"(rw) : "memory");
            if (__all((v[2] == tlo) & (v[3] == thi))) break;
            __builtin_amdgcn_s_sleep(8);
        }
        return v;
    };
    auto flags_ok = [&](const u32x4& f) { return (f[0] == tlo) & (f[1] == thi) & (f[2] == tlo) & (f[3] == thi); };
    auto flags_wait = [&](unsigned off) {
        for (unsigned spins = 0;;) {
            u32x4 f;
            asm volatile("buffer_load_dwordx4 %0, %1, %2, 0 offen sc1\n\ts_waitcnt vmcnt(0)" : "=v"(f) : "v"(off), "s"(rw) : "memory");
            if (__all(flags_ok(f))) break;
            __builtin_amdgcn_s_sleep(16);
            if (++spins > kSpinCap) break;
            asm volatile("" ::: "memory");
        }
    };
    unsigned pcl, frl, frs;
    auto dma_piece = [&](const unsigned short* src, unsigned ldb, unsigned dst, int n, int nt) {
        const unsigned v0 = (lane >> 3) * ldb + pcl, v1 = v0 ^ 64u;
#pragma unroll
        for (int i = 0; i < n; ++i) {
            if (nt) dma16<1>((i & 1) ? v1 : v0, (const char*)src + (size_t)i * 8 * ldb, dst + i * 1024);
            else dma16<0>((i & 1) ? v1 : v0, (const char*)src + (size_t)i * 8 * ldb, dst + i * 1024);
        }
    };
    auto piece_frag = [&](unsigned base, int ks, int t) {
        const unsigned a = base + frl + ((t ^ frs) << 5) + ks * 4096;
        return tr_frag(a, a + 512);
    };
    const unsigned rgn = (unsigned)(wv * 16384);

    if (j < 16) {
        const unsigned long long t_start = __builtin_amdgcn_s_memrealtime();
        rederive();
        pcl = (unsigned)(((((lane & 7) >> 1) ^ ((lane >> 4) & 1)) * 32) + (lane & 1) * 16);
        frl = (unsigned)((g * 8 + (nl >> 2)) * 128 + (lane & 3) * 8); frs = (unsigned)((nl >> 3) | ((g & 1) << 1));
        const int th = x * 4 + (j & 3);
        if (j < 12) {
            const int qt = j >> 2, rot = qt * 5;
            const bool pub = wv < (qt < 2 ? 3 : 2);
            const unsigned qo = (unsigned)(WS_Q + (th & 7) * STR + ((nl & 7) * 4 + (th >> 3)) * 1024 + (qt * 192 + wv * 64 + g * 16 + (nl >> 3) * 8) * 2);
            const unsigned vb = (unsigned)(g * 8192 + ((nl ^ (g << 2)) << 4));
            const char* wuk = (const char*)(p.w_uk + th * 128) + (size_t)qt * 192 * 8192;
            const char* wq = (const char*)(p.w_q + th * 128) + (size_t)wv * 32768;
            const unsigned L_U = 114688;
            {
                const int k0 = qt < 2 ? wv * 6 : wv * 4;
#pragma unroll
                for (int i = 0; i < 6; ++i) {
                    if (i < 4 || qt < 2) dma16<NTM & 2>(vb ^ (((k0 + i) & 3) << 4), wuk + (size_t)(k0 + i) * 32768, L_U + (k0 + i) * 1024);
                    if (i == 4) VMW(4);
                }
            }
            const unsigned vq = vb ^ ((wv & 3) << 4);
            auto wq_dma = [&](int ps) { dma16<NTM & 2>(vq, wq + (size_t)((ps + rot) & 15) * 262144, ps * 8192 + wv * 1024); };
            bf16x8 uk[4][4], aq[16];
            u32x4 fi, hv[4];
            unsigned fio = (unsigned)(WS_FI + x * STR + (lane & 7) * 16), hsrc, hsrc2, ta0, ta1, tb0, tb1, qo2, hw0, hw1, hr, hr2, qw, thx = 0u;
            const unsigned L_HI = L_SCR, L_QI = L_SCR + 8192;
            auto h_load = [&]() {
                asm volatile("buffer_load_dwordx4 %0, %4, %6, 0 offen sc1\n\tbuffer_load_dwordx4 %1, %4, %6, 0 offen offset:1024 sc1\n\t"
                             "buffer_load_dwordx4 %2, %5, %6, 0 offen sc1\n\tbuffer_load_dwordx4 %3, %5, %6, 0 offen offset:1024 sc1"
                             : "+v"(hv[0]), "+v"(hv[1]), "+v"(hv[2]), "+v"(hv[3]) : "v"(hsrc), "v"(hsrc2), "s"(rw) : "memory");
            };
            auto h_bad = [&]() {
                PIN4(hv[0], hv[1], hv[2], hv[3]);
                unsigned bad = (hv[0][1] ^ tlo) | (hv[0][3] ^ thx) | (hv[1][1] ^ tlo) | (hv[1][3] ^ thx) | (hv[2][1] ^ tlo) | (hv[2][3] ^ thx) | (hv[3][1] ^ tlo) | (hv[3][3] ^ thx);
                PIN(bad);
                return bad;
            };
#pragma unroll
            for (int ps = 0; ps < 16; ++ps) {
                wq_dma(ps);
                if (ps == 4) cnt_load();
                if (ps == HP) { h_load(); asm volatile("buffer_load_dwordx4 %0, %1, %2, 0 offen sc1" : "=v"(fi) : "v"(fio), "s"(rw) : "memory"); }
                if (ps >= 4 && ps < 8) VMW(5); else if (ps >= HP && ps < HP + 4) VMW(9); else VMW(4);
                if (ps == 3) {
                    asm volatile("s_barrier" ::: "memory");
                    if (pub) {
#pragma unroll
                        for (int t = 0; t < 4; ++t)
#pragma unroll
                            for (int s = 0; s < 4; ++s) uk[t][s] = *LP(bf16x8, L_U + off_b(wv * 64 + (nl >> 2) * 16 + t * 4 + (nl & 3), s * 4 + g));
                    }
                    STAMP(1);
                }
                if (ps == 5) {
                    hsrc = (unsigned)(WS_H + x * STR + wv * 2048 + lane * 16); hsrc2 = hsrc + 16384;
                    ta0 = off_b(8 * g + (nl >> 2), 2 * wv + ((lane & 3) >> 1)) + 8 * (lane & 1); ta1 = off_b(8 * g + 4 + (nl >> 2), 2 * wv + ((lane & 3) >> 1)) + 8 * (lane & 1);
                    tb0 = ta0 + 65536; tb1 = ta1 + 65536; qo2 = qo;
                    hw0 = L_HI + ((2 * wv - rot) & 15) * 512 + (lane >> 4) * 128 + (lane & 7) * 16 + ((lane >> 3) & 1) * 8;
                    hw1 = L_HI + ((2 * wv + 1 - rot) & 15) * 512 + (lane >> 4) * 128 + (lane & 7) * 16 + ((lane >> 3) & 1) * 8;
                    hr2 = L_HI + g * 128 + (nl & 7) * 16 + (nl >> 3) * 512;
                    PIN(hr2);
                    hr = L_HI + g * 128 + (nl & 7) * 16; qw = L_QI + (wv >> 1) * 512 + ((wv & 1) * 2 + (g >> 1)) * 128 + (nl & 7) * 16 + (g & 1) * 8;
                    PIN4(hsrc, hsrc2, ta0, ta1); PIN4(tb0, tb1, qo2, hw0); PIN(hw1); PIN(hr); PIN(qw);
                }
                if (ps == 8) { tag_take(); thx = tlo ^ thi ^ TAGC; }
                if (ps == 9) {
#pragma unroll
                    for (int t = 0; t < 4; ++t) PIN4(uk[t][0], uk[t][1], uk[t][2], uk[t][3]);
                    wg_barrier();
#pragma unroll
                    for (int q = 0; q < 6; ++q) aq[q] = tr_frag(ta0 + q * 8192, ta1 + q * 8192);
                }
                if (ps == 13) {
                    asm volatile("s_barrier" ::: "memory");
#pragma unroll
                    for (int q = 6; q < 10; ++q) aq[q] = q < 8 ? tr_frag(ta0 + q * 8192, ta1 + q * 8192) : tr_frag(tb0 + (q - 8) * 8192, tb1 + (q - 8) * 8192);
                }
            }
            STAMP(15);
            if (HP + 4 > 15) VMW(HP + 4 > 15 ? 15 - HP : 0);
            if (!__all(h_bad() == 0u)) {
                for (unsigned spins = 0;;) {
                    h_load();
                    VMW(0);
                    if (__all(h_bad() == 0u)) break;
                    __builtin_amdgcn_s_sleep(SLPH);
                    if (++spins > kSpinCap) break;
                }
            }
            {
                unsigned hh[4];
#pragma unroll
                for (int q = 0; q < 4; ++q) {
                    const unsigned t = hv[2 + (q >> 1)][(q & 1) * 2], sh = hv[q >> 1][(q & 1) * 2];
                    hh[q] = pk2(lo16(t) + lo16(sh), hi16(t) + hi16(sh));
                }
                *LP(u32x2, hw0) = u32x2{hh[0], hh[1]};
                *LP(u32x2, hw1) = u32x2{hh[2], hh[3]};
            }
            STAMP(3);
            asm volatile("s_waitcnt vmcnt(0) lgkmcnt(0)\n\ts_barrier" : "+v"(fi) :: "memory");
            STAMP(2);
            {
                bf16x8 hb[8];
#pragma unroll
                for (int q = 0; q < 5; ++q) hb[q] = *LP(bf16x8, hr2 + q * 1024);
#pragma unroll
                for (int ps = 10; ps < 16; ++ps) {
                    if (!(ps & 1)) hb[ps >> 1] = *LP(bf16x8, hr2 + (ps >> 1) * 1024);
                    aq[ps] = tr_frag(tb0 + (ps - 8) * 8192, tb1 + (ps - 8) * 8192);
                }
                __builtin_amdgcn_sched_barrier(0);
                f32x4 d0 = {0.f, 0.f, 0.f, 0.f}, d1 = {0.f, 0.f, 0.f, 0.f};
#pragma unroll
                for (int ps = 0; ps < 16; ps += 2) {
                    d0 = MFMA16(aq[ps], hb[ps >> 1], d0); d1 = MFMA16(aq[ps + 1], hb[ps >> 1], d1);
                    if (ps == 6) { unsigned bad = (fi[0] ^ tlo) | (fi[1] ^ thi) | (fi[2] ^ tlo) | (fi[3] ^ thi); PIN(bad); if (!__all(bad == 0u)) flags_wait(fio); }
                }
#pragma unroll
                for (int e = 0; e < 4; ++e) { float v = d0[e] + dppf<0x128>(d1[e]); asm("" : "+v"(v)); d0[e] = v; }
                if (nl < 8) *LP(u32x2, qw) = u32x2{pk2(d0[0], d0[1]), pk2(d0[2], d0[3])};
            }
            wg_barrier();
            if (pub) {
                bf16x8 qb[4];
#pragma unroll
                for (int s = 0; s < 4; ++s) qb[s] = *LP(bf16x8, hr + 8192 + s * 512);
                __builtin_amdgcn_sched_barrier(0);
                f32x4 d[4];
#pragma unroll
                for (int t = 0; t < 4; ++t) d[t] = MFMA16(uk[t][0], qb[0], (f32x4{0.f, 0.f, 0.f, 0.f}));
#pragma unroll
                for (int s = 1; s < 4; ++s)
#pragma unroll
                    for (int t = 0; t < 4; ++t) d[t] = MFMA16(uk[t][s], qb[s], d[t]);
                const f32x4 a = nl & 8 ? d[2] : d[0], b = nl & 8 ? d[3] : d[1];
                u32x4 qv = {pk2(a[0], a[1]), pk2(a[2], a[3]), pk2(b[0], b[1]), pk2(b[2], b[3])};
                if (__builtin_expect(mx4(qv, 0u) == SENT, 0)) qv = u32x4{pk2s(a[0], a[1]), pk2s(a[2], a[3]), pk2s(b[0], b[1]), pk2s(b[2], b[3])};
                ST16(qv, qo2, 0);
            }
            STAMP(4);
            wg_barrier();
            if (LMPF && wv >= 3 && wv < 6) {
                const unsigned n = (unsigned)(j * 3 + wv - 3);
                if (n < 32) pf4<0>(((n & 7u) * 64 + lane) * 6144u + 2048u + (x * 4 + (n >> 3)) * 128u, p.w_mod, L_SCR + 31744);
            }
            if (QHELP && wv < 3 && j + 12 * wv < 32) {
                const int hh = j + 12 * wv;
#pragma unroll 1
                for (unsigned spins = 0; spins < 64; ++spins) {
                    __builtin_amdgcn_s_sleep(QHELP);
                    const u32x4 v = LD16((unsigned)(WS_Q + (hh & 7) * STR + (x * 4 + (hh >> 3)) * 1024 + lane * 16), 0);
                    if (__all(mx4(v, 0u) != SENT)) break;
                    asm volatile("" ::: "memory");
                }
                STAMP(6);
            }
        } else {
            const int cg = x * 2 + (j & 1);
            if (j < 14) {
                dma_piece(p.w_mod + (size_t)wv * 64 * 3072 + cg * 64, 6144, rgn, 8, NTM & 2);
                cnt_load();
                VMW(9);
            } else {
                cnt_load();
                ST16(s4, (unsigned)(WS_Q + wv * STR + cg * 2048 + lane * 16), 0); ST16(s4, (unsigned)(WS_Q + wv * STR + cg * 2048 + lane * 16), 1024);
                VMW(0);
                tag_take();
                wg_barrier();
                if (wv == 7) flag_put(WS_FI, cg);
                STAMP(15);
                if (j == 14) {
                    u32x2 best = {~0u, ~0u};
#pragma unroll
                    for (int r = 0; r < 2 * NCAL; ++r) {
                        const unsigned hf = (r ^ wv) & 1;
                        VMW(0);
                        const unsigned long long t0 = __builtin_amdgcn_s_memtime();
                        if (lane < 8) ST16(s4, WS_CAL + x * STR + hf * 8192 + (wv * 8 + r) * 128 + lane * 16, 0);
                        VMW(0);
                        const unsigned dt = (unsigned)(__builtin_amdgcn_s_memtime() - t0);
                        if (hf) best[1] = min(best[1], dt); else best[0] = min(best[0], dt);
                    }
                    *LP(u32x2, 16384 * 7 + 14336 + wv * 8) = best;
                    wg_barrier();
                    if (wv == 0 && lane < 8) {
                        u32x2 m = *LP(u32x2, 16384 * 7 + 14336 + lane * 8);
#pragma unroll
                        for (int o = 1; o < 8; o <<= 1) { m[0] = min(m[0], (unsigned)__shfl_xor(m[0], o)); m[1] = min(m[1], (unsigned)__shfl_xor(m[1], o)); }
                        ST16((u32x4{(unsigned)max(-1000, min(1000, (int)m[1] - (int)m[0])), 0u, tlo, thi}), (unsigned)(WS_NP + lane * STR + x * 16), 0);
                    }
                    if (wv == 0) {
                        const int d = (int)tagged_wait(WS_NP + x * STR + (lane & 7) * 16)[0];
                        int t = (lane & 4) ? -d : d;
                        t += __builtin_amdgcn_update_dpp(0, t, 0xB1, 0xf, 0xf, true);
                        t += __builtin_amdgcn_update_dpp(0, t, 0x4E, 0xf, 0xf, true);
                        t += __builtin_amdgcn_update_dpp(0, t, 0x141, 0xf, 0xf, true);
                        const bool n1 = abs(d) > 160 ? d < 0 : (t < 0) != ((lane & 4) != 0);
                        const unsigned mk = NEARP ? (unsigned)__ballot(n1) & 0xffu : 0u;
                        if (lane == 0) ST16((u32x4{mk, 0u, tlo, thi}), (unsigned)(WS_NM + x * STR), 0);
                    }
                    STAMP(9);
                }
            }
            const u16x8 cf = *LP(u16x8, rgn + 8192 + lane * 16), xa = *LP(u16x8, rgn + 9216 + lane * 16);
            u32x4 af[2];
#pragma unroll
            for (int q = 0; q < 4; ++q) {
                const float a = bf2f(cf[2 * q]), b = bf2f(cf[2 * q + 1]);
                af[0][q] = pk2(a * __builtin_amdgcn_rcpf(1.f + __expf(-a)), b * __builtin_amdgcn_rcpf(1.f + __expf(-b)));
                af[1][q] = (unsigned)__builtin_amdgcn_update_dpp(0, (int)af[0][q], 0x128, 0xf, 0xf, true);
            }
            const unsigned L_MS = L_SCR;
            unsigned wa[4], fr[4];
#pragma unroll
            for (int t = 0; t < 4; ++t) { wa[t] = L_MS + wv * 2048 + (nl & 7) * 256 + (((t * 4 + g) ^ (nl & 7)) << 4) + (nl >> 3) * 16384; fr[t] = rgn + frl + ((t ^ frs) << 5); }
            PIN4(wa[0], wa[1], wa[2], wa[3]); PIN4(fr[0], fr[1], fr[2], fr[3]); PIN(af[0]); PIN(af[1]);
            const unsigned L_XN = 12288;
            auto mod_mm = [&](bool ln) {
                VMW(0);
                bf16x8 f[2][4];
#pragma unroll
                for (int ks = 0; ks < 2; ++ks)
#pragma unroll
                    for (int t = 0; t < 4; ++t) f[ks][t] = tr_frag(fr[t] + ks * 4096, fr[t] + ks * 4096 + 512);
                __builtin_amdgcn_sched_barrier(0);
                f32x4 d[4];
#pragma unroll
                for (int t = 0; t < 4; ++t) d[t] = MFMA16(f[0][t], BF8(af[0]), (f32x4{0.f, 0.f, 0.f, 0.f}));
#pragma unroll
                for (int t = 0; t < 4; ++t) d[t] = MFMA16(f[1][t], BF8(af[1]), d[t]);
#pragma unroll
                for (int t = 0; t < 4; ++t) *LP(f32x4, wa[t]) = d[t];
                if (ln) {
                    float v[8];
#pragma unroll
                    for (int q = 0; q < 8; ++q) v[q] = bf2f(xa[q]);
                    const float p1 = ((v[0] + v[1]) + (v[2] + v[3])) + ((v[4] + v[5]) + (v[6] + v[7]));
                    const float p2 = ((v[0] * v[0] + v[1] * v[1]) + (v[2] * v[2] + v[3] * v[3])) + ((v[4] * v[4] + v[5] * v[5]) + (v[6] * v[6] + v[7] * v[7]));
                    float mb, rden;
                    ln_stats(p1, p2, mb, rden);
                    u32x4 r;
#pragma unroll
                    for (int q = 0; q < 4; ++q) { const unsigned d = pk2(v[2 * q] - mb, v[2 * q + 1] - mb); r[q] = pk2(lo16(d) * rden, hi16(d) * rden); }
                    *LP(u32x4, L_XN + wv * 16384 + lane * 16) = r;
                }
                wg_barrier();
            };
            auto sum8 = [&](unsigned a) {
                f32x4 u[8];
#pragma unroll
                for (int w2 = 0; w2 < 8; ++w2) u[w2] = *LP(f32x4, a + w2 * 2048);
                __builtin_amdgcn_sched_barrier(0);
                return ((u[0] + u[1]) + (u[2] + u[3])) + ((u[4] + u[5]) + (u[6] + u[7]));
            };
            if (j < 14) {
                const int hb = lane & 7, c4 = 2 * wv + (nl >> 3);
                unsigned hoff = (unsigned)(WS_H + g * STR + cg * 2048 + wv * 256 + nl * 16);
                unsigned sa = L_MS + hb * 256 + ((c4 ^ hb) << 4);
                unsigned xa2 = L_XN + hb * 16384 + (cg & 7) * 128 + c4 * 8;
                PIN(hoff); PIN(sa); PIN(xa2);
                STAMP(1);
                mod_mm(cg >= 8);
                STAMP(2);
                const u32x2 xn = *LP(u32x2, xa2);
                const f32x4 v = sum8(sa);
                tag_take();
                const unsigned thx = tlo ^ thi ^ TAGC;
                u32x2 r = {pk2(v[0], v[1]), pk2(v[2], v[3])};
                if (cg >= 8) {
#pragma unroll
                    for (int q = 0; q < 2; ++q) { const unsigned sc = pk2(1.f + lo16(r[q]), 1.f + hi16(r[q])); r[q] = pk2(lo16(xn[q]) * lo16(sc), hi16(xn[q]) * hi16(sc)); }
                }
                ST16((u32x4{r[0], tlo, r[1], thx}), hoff, 0);
                ST16((u32x4{r[0], tlo, r[1], thx}), hoff, 4 * STR);
                STAMP(5);
            }
            wg_barrier();
            while (__builtin_amdgcn_s_memrealtime() < t_start + LMD) __builtin_amdgcn_s_sleep(2);
            const int lg = x * 4 + (j & 3);
            dma_piece(p.w_mod + (size_t)wv * 64 * 3072 + 1024 + lg * 64, 6144, rgn, 8, NTM & 4);
            mod_mm(false);
            if (lg >= 8 && lg < 24) {
                if (wv == 0) {
                    const int b = lane >> 3, c8 = lane & 7;
                    const f32x4 v0 = sum8(L_MS + b * 256 + (((2 * c8) ^ b) << 4)), v1 = sum8(L_MS + b * 256 + (((2 * c8 + 1) ^ b) << 4));
                    ST16((u32x4{pk2(v0[0], v0[1]), pk2(v0[2], v0[3]), pk2(v1[0], v1[1]), pk2(v1[2], v1[3])}), (unsigned)(WS_MB + b * STR + (lg - 8) * 128 + c8 * 16), 0);
                }
            } else if (wv < 4) {
                const int b = wv * 2 + (lane >> 5), cp = lane & 31;
                f32x2_t u[8];
#pragma unroll
                for (int w2 = 0; w2 < 8; ++w2) u[w2] = *LP(f32x2_t, L_MS + w2 * 2048 + b * 256 + (((cp >> 1) ^ b) << 4) + (cp & 1) * 8);
                const f32x2_t v = ((u[0] + u[1]) + (u[2] + u[3])) + ((u[4] + u[5]) + (u[6] + u[7]));
                ST16((u32x4{__float_as_uint(rbf(v[0])), __float_as_uint(rbf(v[1])), tlo, thi}), (unsigned)(WS_MOD + b * STR + (lg * 32 + cp) * 16), 0);
            }
            STAMP(6);
        }
        const int bq = (j >> 2) & 1, dh = j >> 3, src = th * 2 + dh, rb = bq * 4 + (src >> 4), rc = src & 15;
        wg_barrier();
        if (TLBW && wv == 6) {
            if (lane < 4) pf4s((unsigned)(WS_PART + (bq * 4 + lane) * 1048576 + th * 32768), ws, L_SCR + 31744);
            pf4s((unsigned)(lane * 65536 + th * 256), p.w_uv, L_SCR + 31744);
            if (lane < 2) pf4s((unsigned)(th * 131072 + lane * 65536), p.w_o, L_SCR + 31744);
        }
        while (__builtin_amdgcn_s_memrealtime() < t_start + TWD - 110) __builtin_amdgcn_s_sleep(2);
        const unsigned npm = __builtin_amdgcn_readfirstlane(tagged_wait(WS_NM + x * STR)[0]), np = (npm >> x) & 1u;
        unsigned oo = (unsigned)(WS_OUTP + ((bq * 4 + (nl & 3)) * 16 + wv * 2 + (nl >> 3)) * 16384 + (src ^ wv) * 128 + ((nl >> 2) & 1) * 64 + g * 16) + ((~npm >> ((nl & 3) * 2 + (wv >> 2))) & 1u) * 8192u;
        const int rcol = rc * 32 + (lane & 7) * 4 + (lane >> 5) * 2;
        unsigned xdst = (unsigned)(WS_X1 + ((lane >> 3) & 3) * STR + (rb * 512 + rcol) * 2), xdst2 = xdst + 4 * STR + ((npm >> (4 + ((lane >> 3) & 3))) & 1u) * 8192u;
        xdst += ((npm >> ((lane >> 3) & 3)) & 1u) * 8192u;
        ST16(s4, oo, 0);
        if (wv == 7) { __builtin_amdgcn_raw_buffer_store_b32(SENT, rw, xdst, 0, 16); __builtin_amdgcn_raw_buffer_store_b32(SENT, rw, xdst2, 0, 16); }
        VMW(0);
        wg_barrier();
        if (wv == 0) flag_put(WS_FB, id);
        while (__builtin_amdgcn_s_memrealtime() < t_start + TWD) __builtin_amdgcn_s_sleep(2);
        asm volatile("s_barrier" ::: "memory");
        STAMP(7);
        if ((PFM & 8) && wv == 0 && lane < 32) pf4<NTM & 8>((unsigned)((j * 32 + lane) * 8192 + x * 1024 + ((3u - ((unsigned)(size_t)p.w_uv >> 7)) & 7u) * 128), p.w_uv, L_SCR + 31744);
        if (bq) dma_piece(p.w_o + (size_t)(th * 128 + dh * 64) * 512 + wv * 64, 1024, rgn + 8192, 8, NTM & 8);
        dma_piece(p.w_uv + ((size_t)wv * 64 * NH + th) * 128 + dh * 64, 8192, rgn, 8, NTM & 8);
        if (!bq) dma_piece(p.w_o + (size_t)(th * 128 + dh * 64) * 512 + wv * 64, 1024, rgn + 8192, 8, NTM & 8);
        u32x4 fb, gm;
        unsigned xg;
        VMW(4);
        if (wv == 7)
            asm volatile("buffer_load_dwordx4 %0, %3, %5, 0 offen sc1\n\tbuffer_load_dwordx4 %1, %4, %5, 0 offen sc1\n\tglobal_load_dword %2, %6, %7" : "=&v"(fb), "=&v"(gm), "=&v"(xg)
                         : "v"((unsigned)(WS_FB + x * STR + lane * 16)), "v"((unsigned)(WS_MOD + rb * STR + rcol * 8)), "s"(rw), "v"((unsigned)(rb * 512 + rcol) * 2u), "s"(p.x) : "memory");
        VMW(0);
        asm volatile("" : "+v"(fb), "+v"(gm), "+v"(xg));
        STAMP(8);
        bf16x8 fuv[2][4], fwo[2][4];
        unsigned pfa[4];
#pragma unroll
        for (int t = 0; t < 4; ++t) { pfa[t] = rgn + frl + ((t ^ frs) << 5); PIN(pfa[t]); }
#pragma unroll
        for (int ks = 0; ks < 2; ++ks)
#pragma unroll
            for (int t = 0; t < 4; ++t) {
                const unsigned a = rgn + (unsigned)((g * 4 + (nl >> 2)) * 128 + (lane & 3) * 8) + ((t ^ (unsigned)((nl >> 3) | (g & 2))) << 5) + ks * 4096;
                fuv[ks][t] = tr_frag(a, a + 2048);
                fwo[ks][t] = tr_frag(pfa[t] + 8192 + ks * 4096, pfa[t] + 8192 + ks * 4096 + 512);
            }
#pragma unroll
        for (int ks = 0; ks < 2; ++ks) { PIN4(fuv[ks][0], fuv[ks][1], fuv[ks][2], fuv[ks][3]); PIN4(fwo[ks][0], fwo[ks][1], fwo[ks][2], fwo[ks][3]); }
        const unsigned L_WGT = L_SCR, L_MSK = L_SCR + 256, L_RED = L_SCR + 1024;
        unsigned psrc = (unsigned)(WS_PART + (bq * 4 * 32 + th) * 32768 + (wv >> 2) * 16384 + (wv & 3) * 2048 + ((lane >> 3) ^ (th & 7)) * 128) + np * 8192u + pcl, psrc1 = (psrc ^ 64u) + 1024;
        unsigned psrc2 = psrc ^ 8192u, psrc3 = psrc1 ^ 8192u;
        PIN(psrc2); PIN(psrc3); PIN(xdst2);
        unsigned rw2 = L_RED + wv * 1024 + (nl & 3) * 256 + g * 16;
        unsigned wa0 = (nl & 3) == (g >> 1) ? L_WGT + (nl & 4) * 32 + g * 16 : L_WGT + 272, wa1 = (nl & 3) == 2 + (g >> 1) ? L_WGT + (nl & 4) * 32 + 64 + g * 16 : L_WGT + 272;
        unsigned cr = rgn + (nl & 3) * 128 + g * 16, r2r = L_RED + lane * 16, cw = rgn + lane * 8;
        unsigned osrc = (unsigned)(WS_OUTP + (rb * 16 + rc) * 16384 + lane * 16) + (np ^ 1u) * 8192u;
        PIN(psrc); PIN(psrc1); PIN(rw2); PIN4(wa0, wa1, cr, r2r); PIN4(cw, oo, osrc, xdst);
        float mlm = 0.f, mll = 0.f;
        if (wv == 0) *LP(unsigned, L_WGT + 256 + lane * 4) = 0u;
        wg_barrier();
        const bool early = __all(flags_ok(fb) & (gm[2] == tlo) & (gm[3] == thi));
        u32x2 ga = {gm[0], gm[1]};
        PIN(ga);
        bf16x8 pa[2][4], wh[2];
        {
            u32x4 ml = {0u, 0u, ~tlo, ~thi};
            const unsigned mlsrc = (unsigned)(WS_ML + (bq * 4 + g) * STR + nl * 512 + th * 16) + ((npm >> (x & 6)) & 1u) * 8192u;
            unsigned long long done = 0ull;
            unsigned lm = L_MSK;
            PIN(lm);
#pragma unroll 1
            for (unsigned spins = 0; spins < kSpinCap; ++spins) {
                unsigned long long m;
                if (wv == 0) {
                    if ((ml[2] != tlo) | (ml[3] != thi)) ml = LD16(mlsrc, 0);
                    m = __ballot((ml[2] == tlo) & (ml[3] == thi));
                    unsigned long long f = m & (m >> 4);
                    f &= f >> 2; f &= f >> 1;
                    asm volatile("ds_write_b64 %0, %1" :: "v"(lm), "v"(f) : "memory");
                    m = m == ~0ull ? 0x0101010101010101ull : 0ull;
                } else {
                    unsigned long long t;
                    asm volatile("ds_read_b64 %0, %1\n\ts_waitcnt lgkmcnt(0)" : "=v"(t) : "v"(lm) : "memory");
                    m = ((unsigned long long)(unsigned)__builtin_amdgcn_readfirstlane((unsigned)(t >> 32)) << 32) | (unsigned)__builtin_amdgcn_readfirstlane((unsigned)t);
                    m &= 0x0101010101010101ull & ~done;
                }
                if (m) {
                    done |= m;
#define PLD(bit, off, v, base) "s_bitcmp1_b64 %0, " #bit "\n\ts_cbranch_scc0 1f\n\ts_add_u32 m0, %1, " #off "\n\ts_nop 0\n\tglobal_load_lds_dwordx4 " v ", " base " sc1\n1:\n\t"
                    asm volatile(PLD(0, 0, "%2", "%4") PLD(8, 1024, "%3", "%4") PLD(16, 2048, "%2", "%5") PLD(24, 3072, "%3", "%5")
                                 PLD(32, 4096, "%8", "%6") PLD(40, 5120, "%9", "%6") PLD(48, 6144, "%8", "%7") PLD(56, 7168, "%9", "%7")
                                 :: "s"(m), "s"(rgn), "v"(psrc), "v"(psrc1), "s"(ws), "s"(ws + 1048576), "s"(ws + 2097152), "s"(ws + 3145728), "v"(psrc2), "v"(psrc3) : "memory", "m0", "scc");
                    if (done == 0x0101010101010101ull) break;
                } else if (SLPM > 0 && wv == 0) __builtin_amdgcn_s_sleep(SLPM);
            }
            mlm = __uint_as_float(ml[0]); mll = __uint_as_float(ml[1]);
            STAMP(10);
            if (wv == 0) {
                float M = mlm;
                M = fmaxf(M, dppf<0xB1>(M)); M = fmaxf(M, dppf<0x4E>(M)); M = fmaxf(M, dppf<0x141>(M)); M = fmaxf(M, dppf<0x140>(M));
                const float e = __builtin_amdgcn_exp2f(mlm - M);
                const float w = e * __builtin_amdgcn_rcpf(sum16(e * mll));
                const unsigned short hi = f2bf(w);
                *LP(unsigned short, L_WGT + lane * 2) = hi;
                *LP(unsigned short, L_WGT + 128 + lane * 2) = f2bf(w - bf2f(hi));
            }
            wg_barrier();
            wh[0] = *LP(bf16x8, wa0); wh[1] = *LP(bf16x8, wa1);
            __builtin_amdgcn_sched_barrier(0);
            VMW(0);
#pragma unroll
            for (int ks = 0; ks < 2; ++ks)
#pragma unroll
                for (int t = 0; t < 4; ++t) pa[ks][t] = tr_frag(pfa[t] + ks * 4096, pfa[t] + ks * 4096 + 512);
        }
        STAMP(11);
        {
            f32x4 d[4];
#pragma unroll
            for (int t = 0; t < 4; ++t) d[t] = MFMA16(pa[0][t], wh[0], (f32x4{0.f, 0.f, 0.f, 0.f}));
#pragma unroll
            for (int t = 0; t < 4; ++t) d[t] = MFMA16(pa[1][t], wh[1], d[t]);
#pragma unroll
            for (int t = 0; t < 4; ++t)
#pragma unroll
                for (int e = 0; e < 4; ++e) { float v = d[t][e] + dppf<0x124>(d[t][e]); asm("" : "+v"(v)); d[t][e] = v; }
            const u32x4 o0 = {pk2(d[0][0], d[0][1]), pk2(d[0][2], d[0][3]), pk2(d[1][0], d[1][1]), pk2(d[1][2], d[1][3])};
            const u32x4 o1 = {pk2(d[2][0], d[2][1]), pk2(d[2][2], d[2][3]), pk2(d[3][0], d[3][1]), pk2(d[3][2], d[3][3])};
            f32x4 y[4];
#pragma unroll
            for (int t = 0; t < 4; ++t) y[t] = MFMA16(fuv[0][t], BF8(o0), (f32x4{0.f, 0.f, 0.f, 0.f}));
#pragma unroll
            for (int t = 0; t < 4; ++t) y[t] = MFMA16(fuv[1][t], BF8(o1), y[t]);
            if (nl < 4) {
#pragma unroll
                for (int t = 0; t < 4; ++t) *LP(f32x4, rw2 + t * 64) = y[t];
            }
        }
        wg_barrier();
        {
            f32x4 cs[8];
#pragma unroll
            for (int w2 = 0; w2 < 8; ++w2) cs[w2] = *LP(f32x4, r2r + w2 * 1024);
            const f32x4 c4 = ((cs[0] + cs[1]) + (cs[2] + cs[3])) + ((cs[4] + cs[5]) + (cs[6] + cs[7]));
            *LP(u32x2, cw) = u32x2{pk2(c4[0], c4[1]), pk2(c4[2], c4[3])};
            asm volatile("s_waitcnt lgkmcnt(0)" ::: "memory");
            const u32x4 cx0 = *LP(u32x4, cr), cx1 = *LP(u32x4, cr + 64);
            f32x4 o[4];
#pragma unroll
            for (int q = 0; q < 4; ++q) o[q] = MFMA16(fwo[0][q], BF8(cx0), (f32x4{0.f, 0.f, 0.f, 0.f}));
#pragma unroll
            for (int q = 0; q < 4; ++q) o[q] = MFMA16(fwo[1][q], BF8(cx1), o[q]);
            const f32x4 sel = (nl & 8) ? ((nl & 4) ? o[3] : o[2]) : ((nl & 4) ? o[1] : o[0]);
            u32x4 sv = __builtin_bit_cast(u32x4, sel);
            if (__builtin_expect(mx4(sv, 0u) == SENT, 0)) sv = u32x4{f32s(sel[0]), f32s(sel[1]), f32s(sel[2]), f32s(sel[3])};
            ST16(sv, oo, 0);
        }
        STAMP(12);
        wg_barrier();
        if (wv == 7) {
            if (!early) {
                flags_wait(WS_FB + x * STR + lane * 16);
                { const u32x4 v = tagged_wait(WS_MOD + rb * STR + rcol * 8); ga = u32x2{v[0], v[1]}; }
                PIN(ga);
            }
            u32x4 op[8];
            for (unsigned spins = 0;;) {
#pragma unroll
                for (int k = 0; k < 8; ++k) op[k] = LD16(osrc + (k & 3) * 1024, (k >> 2) * 4096);
                unsigned m = 0u;
#pragma unroll
                for (int k = 0; k < 8; ++k) m = mx4(op[k], m);
                if (__all(m != SENT)) break;
                if (++spins > kSpinCap) break;
                asm volatile("" ::: "memory");
            }
            STAMP(13);
            const f32x4 v = ((__builtin_bit_cast(f32x4, op[0]) + __builtin_bit_cast(f32x4, op[1])) + (__builtin_bit_cast(f32x4, op[2]) + __builtin_bit_cast(f32x4, op[3]))) +
                            ((__builtin_bit_cast(f32x4, op[4]) + __builtin_bit_cast(f32x4, op[5])) + (__builtin_bit_cast(f32x4, op[6]) + __builtin_bit_cast(f32x4, op[7])));
            float w[2];
#pragma unroll
            for (int q = 0; q < 2; ++q) {
                auto t = __builtin_amdgcn_permlane32_swap(__float_as_uint(v[q]), __float_as_uint(v[2 + q]), false, false);
                w[q] = __uint_as_float(t[0]) + __uint_as_float(t[1]);
                auto u = __builtin_amdgcn_permlane16_swap(__float_as_uint(w[q]), __float_as_uint(w[q]), false, false);
                w[q] = __uint_as_float(u[0]) + __uint_as_float(u[1]);
                w[q] += dppf<0x128>(w[q]);
            }
            unsigned t = pk2(w[0], w[1]);
            t = pk2(__uint_as_float(ga[0]) * lo16(t), __uint_as_float(ga[1]) * hi16(t));
            t = pk2s(lo16(xg) + lo16(t), hi16(xg) + hi16(t));
            __builtin_amdgcn_raw_buffer_store_b32(t, rw, xdst, 0, 16);
            __builtin_amdgcn_raw_buffer_store_b32(t, rw, xdst2, 0, 16);
            STAMP(14);
        }
        STAMP_FLUSH();
        return;
    }

    const int s = j - 16;
    const unsigned long long t_s = __builtin_amdgcn_s_memrealtime();
    if (wv == 0) cnt_load();
    {
        const unsigned ln = (unsigned)(s * 64 + lane);
        if ((PFM & 1) && wv == 0) pf4<NTM & 1>((ln & 511u) * 6144u + (x * 2 + (ln >> 9)) * 128u, p.w_mod, L_SCR);
        if ((PFM & 2) && wv == 1) {
            const unsigned short* w = s < 8 ? p.w_uk : p.w_q;
            pf4<NTM & 1>((ln & 511u) * 8192u + x * 1024u + ((3u - ((unsigned)(size_t)w >> 7)) & 7u) * 128u, w, L_SCR);
        }
        if ((PFM & 48) == 48) {
            const unsigned r0 = (unsigned)(s * 4 + (wv & 3)), row = PFS == 0 ? r0 * 8 + (lane >> 3) : PFS == 1 ? (lane >> 3) * 64 + r0 : (r0 >> 5) * 256 + (lane >> 3) * 32 + (r0 & 31);
            pf4<NTM & 1>(row * 8192u + x * 1024u + (lane & 7u) * 128u, wv < 4 ? p.w_uk : p.w_q, L_SCR);
        }
        else if ((PFM & 16) && wv >= 4) { const unsigned l2 = (unsigned)((s * 4 + (wv & 3)) * 64 + lane); pf4<NTM & 1>((l2 >> 3) * 8192u + x * 1024u + (l2 & 7u) * 128u, p.w_q, L_SCR); }
        else if ((PFM & 32) && wv >= 4) { const unsigned l2 = (unsigned)((s * 4 + (wv & 3)) * 64 + lane); pf4<NTM & 1>((l2 >> 3) * 8192u + x * 1024u + (l2 & 7u) * 128u, p.w_uk, L_SCR); }
        if ((PFM & 4) && (wv & 6) == 2) { const unsigned l2 = (unsigned)((s * 2 + (wv & 1)) * 64 + lane); pf4<NTM & 1>((l2 & 511u) * 8192u + (x * 4 + (l2 >> 9)) * 256u, p.w_q, L_SCR); }
    }
    const int ug = x * 7 + s;
    const unsigned vb = (unsigned)(g * 1024 + ((nl ^ (g << 2)) << 4)), lb = (unsigned)((wv >> 1) * 32768 + (wv & 1) * 4096);
    const char* ck = (const char*)p.ckv + ((size_t)x * SK + s * 256 + wv * 16) * 1024;
    const unsigned rot = ROTC ? (unsigned)s & 3u : 0u;
    const unsigned vbr = vb ^ (rot << 8);
    const char* ck2 = ck + 131072;
    auto ck_dma = [&](unsigned i) { dma16x<NTM & 16>(vbr, ((i & 3u) << 4) | (((i >> 2) & 3u) << 8) | ((i & 3u) << 12), (i >> 4) ? ck2 : ck, lb + ((i >> 2) & 3u) * 8192u + (i & 3u) * 1024u); };
    bf16x8 ka[16];
    s16x4 va[8][4];
    unsigned kad[4], vad[4], vad2[4];
    auto kq = [](unsigned u) { return (0x78u >> (2 * u)) & 3u; };
    auto ka_reads = [&](int cb) {
#pragma unroll
        for (int u = 0; u < 4; ++u) ka[cb * 4 + u] = *LP(bf16x8, kad[u] + cb * 8192);
    };
    auto va_reads = [&]() {
#pragma unroll
        for (int kk = 0; kk < 8; ++kk)
#pragma unroll
            for (int q = 0; q < 4; ++q) va[kk][q] = tr4((kk < 4 ? vad[q] : vad2[q]) + ((kk >> 1) & 1) * 32768 + (kk & 1) * 4096);
    };
    auto va_read1 = [&](int kk) {
#pragma unroll
        for (int q = 0; q < 4; ++q) va[kk][q] = tr4((kk < 4 ? vad[q] : vad2[q]) + ((kk >> 1) & 1) * 32768 + (kk & 1) * 4096);
    };
    auto va_pin = [&]() {
#pragma unroll
        for (int q = 0; q < 8; ++q) PIN4(va[q][0], va[q][1], va[q][2], va[q][3]);
    };
    u32x4 fa = {0u, 0u, 0u, 0u};
    const unsigned fao = (unsigned)(WS_FI + x * STR + (lane & 7) * 16);
    constexpr int LAST = GI + NPOST > 19 ? GI + NPOST : 19;
    const unsigned qsrc = (unsigned)(WS_Q + (wv & 1) * 4 * STR + (x * 4 + (wv >> 1)) * 1024) + (g ^ rot) * 256 + ((nl ^ ((wv * 4) & 15)) << 4);
    auto q_dma = [&](int k) {
        unsigned t;
        asm volatile("s_mov_b32 m0, %3\n\tv_xor_b32 %0, %4, %1\n\tglobal_load_lds_dwordx4 %0, %2 sc1" : "=&v"(t) : "v"(qsrc), "s"(ws), "s"(L_SCR + (wv * 4 + k) * 1024), "i"(k * (STR + 16)) : "memory", "m0");
    };
#pragma unroll
    for (int i = 0; i <= LAST; ++i) {
        ck_dma(i);
        if (i == 3) {
#pragma unroll
            for (int u = 0; u < 4; ++u) {
                kad[u] = tile_off(wv * 16 + kq(nl >> 2) * 4 + (nl & 3), 4 * u + g);
                vad[u] = tile_off(8 * (lane >> 5) + 4 * (u & 1) + (nl >> 2), wv * 8 + 4 * (u >> 1) + 2 * (g & 1) + ((lane & 3) >> 1)) + 8 * (lane & 1);
                vad2[u] = vad[u] + 65536;
                PIN(kad[u]); PIN(vad[u]); PIN(vad2[u]);
            }
        }
        if (i > GI && i < GI + NPOST) { if (i < GI + 4) VMW(8); } else if (i == GI + NPOST) VMW(NPOST); else if (i < NTH) { if (i >= W0) VMW(W0); } else if (i >= 4) VMW(4);
        if (i == 15 && wv == 0) {
            PIN(fa);
            tag_take();
            if (!__all(flags_ok(fa))) flags_wait(fao);
        }
        if ((i & 3) == 3 && i >= 7 && i <= 19) {
            const int cb = (i - 7) >> 2;
            if (cb > 0) { PIN4(ka[cb * 4 - 4], ka[cb * 4 - 3], ka[cb * 4 - 2], ka[cb * 4 - 1]); va_pin(); }
            wg_barrier();
            ka_reads(cb);
            if ((wv >> 1) == cb) va_reads();
            STAMP(16 + cb);
        }
        if (TLBW && i == 12) {
            if (wv == 1 && lane < 32) pf4s((unsigned)(WS_PART + x * 1048576 + lane * 32768 + s * 128), ws, L_SCR + 1024);
            if (wv == 2 && lane < 28 && s >= 7) pf4s((unsigned)(lane * 65536 + x * 128), p.w2, L_SCR + 1024);
            if (wv == 3 && lane < 28 && (s < 7 || s >= 11)) pf4s((unsigned)(lane * 65536 + x * 128), p.w1, L_SCR + 1024);
            if (wv == 4 && lane < 1 && s >= 7 && s < 11) pf4s(0u, p.out, L_SCR + 1024);
        }
        if (i == 10 && wv == 0) asm volatile("buffer_load_dwordx4 %0, %1, %2, 0 offen sc1" : "+v"(fa) : "v"(fao), "s"(rw) : "memory");
        if (i == GI && TG > 0) { while (__builtin_amdgcn_s_memrealtime() < t_s + TG) __builtin_amdgcn_s_sleep(1); }
        if (i == GI && WG < 4) VMW(WG);
        if (i == GI) { q_dma(0); q_dma(1); q_dma(2); q_dma(3); }
    }
    const unsigned L_EXM = L_SCR - 1024, L_BIG = L_SCR + 512 + 128;
    unsigned nx = LAST + 1, npm = 0u;
    unsigned qad[4];
    {
        unsigned qck = L_SCR + wv * 4096 + lane * 16;
        u32x4 qb[4];
#pragma unroll
        for (int k = 0; k < 4; ++k) qb[k] = *LP(u32x4, qck + k * 1024);
#pragma unroll
        for (int k = 0; k < 4; ++k) { qad[k] = L_SCR + nl * 1024 + (((4 * k + g) ^ nl) << 4); PIN(qad[k]); }
        STAMP(20);
        if (!__all(mx4(qb[0], mx4(qb[1], mx4(qb[2], mx4(qb[3], 0u)))) != SENT)) {
            for (unsigned spins = 0;;) {
#pragma unroll
                for (int k = 0; k < 4; ++k)
                    if (!__all(mx4(qb[k], 0u) != SENT)) q_dma(k);
                if (nx <= 25) {
#pragma unroll
                    for (int i = 0; i < 3; ++i) ck_dma(nx + i);
                    nx += 3;
                    VMW(3);
                } else {
                    for (; nx < 28; ++nx) ck_dma(nx);
                    VMW(0);
                }
#pragma unroll
                for (int k = 0; k < 4; ++k) qb[k] = *LP(u32x4, qck + k * 1024);
                if (__all(mx4(qb[0], mx4(qb[1], mx4(qb[2], mx4(qb[3], 0u)))) != SENT)) break;
                if (++spins > kSpinCap) break;
            }
        }
    }
    constexpr int NE = LAST + 1 + SL0 + 4 < 31 ? LAST + 1 + SL0 + 4 : 31;
    auto slot = [&](int i) { if (i < 28) { if (nx <= (unsigned)i) ck_dma(i); } else ck_dma(i); };
    const unsigned kg = kq(g);
    unsigned exw = L_EXM + ((nl + (lane >> 5) * 16) * 8 + wv) * 4, exr = L_EXM + nl * 32;
    unsigned pwr = L_SCR + nl * 1024 + 768 + (((2 * wv + (kg >> 1)) ^ nl) << 4) + (kg & 1) * 8, esw = L_SCR + nl * 1024 + 512 + ((wv ^ (nl & 7)) << 4) + g * 4;
    unsigned prd = L_SCR + (lane & 31) * 1024 + 768 + (((lane >> 5) ^ nl) << 4);
    PIN4(exw, exr, pwr, esw); PIN(prd);
    PIN4(ka[12], ka[13], ka[14], ka[15]); va_pin();
    wg_barrier();
    STAMP(21);
    bf16x8 qr[8][2];
    auto q_ld = [&](int ks, int hg) { return *LP(bf16x8, qad[ks & 3] + (ks >> 2) * 256 + hg * 16384); };
    auto p_ld = [&](int kk) { return *LP(bf16x8, prd ^ (kk << 5)); };
    float ls0, ls1, m0, m1;
    f32x4 sa, sb;
    f32x16 o[2];
    unsigned sw[4], srd, p0, mlo, esr, fbo;
    u32x2 tg;
    u32x4 nq;
    auto probs = [&](float (&e)[8]) {
#pragma unroll
        for (int r = 0; r < 4; ++r) { e[r] = __builtin_amdgcn_exp2f(fmaf(sa[r], SCL, -m0)); e[4 + r] = __builtin_amdgcn_exp2f(fmaf(sb[r], SCL, -m1)); }
        *LP(u32x2, pwr) = u32x2{pk2(e[0], e[1]), pk2(e[2], e[3])};
        *LP(u32x2, pwr + 16384) = u32x2{pk2(e[4], e[5]), pk2(e[6], e[7])};
    };
    auto wave_max = [&]() {
        const float a = fmaxf(fmaxf(sa[0], sa[1]), fmaxf(sa[2], sa[3])), b = fmaxf(fmaxf(sb[0], sb[1]), fmaxf(sb[2], sb[3]));
        auto u = __builtin_amdgcn_permlane32_swap(__float_as_uint(a), __float_as_uint(b), false, false);
        const float c = fmax3(__uint_as_float(u[0]), __uint_as_float(u[1]), __uint_as_float(u[1]));
        auto v = __builtin_amdgcn_permlane16_swap(__float_as_uint(c), __float_as_uint(c), false, false);
        return fmax3(__uint_as_float(v[0]), __uint_as_float(v[1]), __uint_as_float(v[1]));
    };
    auto max8 = [&](unsigned a) {
        const f32x4 a0 = *LP(f32x4, a), a1 = *LP(f32x4, a + 16);
        return fmax3(fmax3(a0[0], a0[1], a0[2]), fmax3(a0[3], a1[0], a1[1]), fmax3(a1[2], a1[3], a1[3])) * SCL;
    };
    {
        bf16x8 qs[8][2];
#pragma unroll
        for (int ks = 0; ks < 8; ++ks) { qr[ks][0] = q_ld(ks + 8, 0); qr[ks][1] = q_ld(ks + 8, 1); }
        __builtin_amdgcn_sched_barrier(0);
#pragma unroll
        for (int i = LAST + 1; i < LAST + 1 + SL0; ++i) if (i < NE) slot(i);
        sa = f32x4{0.f, 0.f, 0.f, 0.f}; sb = sa;
#pragma unroll
        for (int n = 0; n < 16; ++n) {
            const int ks = (n + 8) & 15;
            sa = MFMA16(ka[ks], ks < 8 ? qs[ks][0] : qr[ks - 8][0], sa);
            sb = MFMA16(ka[ks], ks < 8 ? qs[ks][1] : qr[ks - 8][1], sb);
            if (n >= 8 - QD && n < 16 - QD) { qs[n + QD - 8][0] = q_ld(n + QD - 8, 0); qs[n + QD - 8][1] = q_ld(n + QD - 8, 1); }
            if ((n & 3) == 3 && LAST + 1 + SL0 + (n >> 2) < NE) slot(LAST + 1 + SL0 + (n >> 2));
            __builtin_amdgcn_sched_barrier(0);
        }
        STAMP(1);
        *LP(float, exw) = wave_max();
        wg_barrier();
        m0 = max8(exr); m1 = max8(exr + 512);
        VMW(NE - 24);
        ka_reads(0); ka_reads(1);
#pragma unroll
        for (int ks = 0; ks < QE; ++ks) { qs[ks][0] = q_ld(ks, 0); qs[ks][1] = q_ld(ks, 1); }
        __builtin_amdgcn_sched_barrier(0);
        float e[8];
        probs(e);
        ls0 = (e[0] + e[1]) + (e[2] + e[3]); ls1 = (e[4] + e[5]) + (e[6] + e[7]);
        PIN(ls0); PIN(ls1);
        if (wv == 0) *LP(u32x4, L_BIG) = u32x4{0u, 0u, tlo, thi};
        VMW(NE - 28);
        wg_barrier();
        STAMP(22);
        static_assert(NE >= 28, "");
        bf16x8 pf[8];
        tg = *LP(u32x2, L_BIG + 8);
#pragma unroll
        for (int i = NE; i < 32; ++i) slot(i);
        sa = f32x4{0.f, 0.f, 0.f, 0.f}; sb = sa;
        __builtin_amdgcn_sched_barrier(0);
#pragma unroll
        for (int ks = 0; ks < 8; ++ks) {
            sa = MFMA16(ka[ks], qs[ks][0], sa); sb = MFMA16(ka[ks], qs[ks][1], sb);
            if (ks + QE < 8) { qs[ks + QE][0] = q_ld(ks + QE, 0); qs[ks + QE][1] = q_ld(ks + QE, 1); }
            else pf[ks + QE - 8] = p_ld(ks + QE - 8);
            __builtin_amdgcn_sched_barrier(0);
        }
#pragma unroll
        for (int i = 0; i < 16; ++i) { o[0][i] = 0.f; o[1][i] = 0.f; }
        STAMP(2);
#pragma unroll
        for (int kk = 0; kk < 4; ++kk) {
            if (kk == 0) {
#pragma unroll
                for (int q = QE; q < 8; ++q) pf[q] = p_ld(q);
            }
            if (kk == 1) ka_reads(2);
            o[0] = MFMA32(cat8(va[kk][0], va[kk][1]), pf[kk], o[0]);
            o[1] = MFMA32(cat8(va[kk][2], va[kk][3]), pf[kk], o[1]);
            __builtin_amdgcn_sched_barrier(0);
        }
        PIN4(pf[4], pf[5], pf[6], pf[7]); PIN4(ka[8], ka[9], ka[10], ka[11]); PIN(tg);
        STAMP(3);
#pragma unroll
        for (int kk = 4; kk < 8; ++kk) {
            if (wv < 6) { va_read1(kk - 4); if (kk > 4) va_read1(kk - 1); }
            sa = MFMA16(ka[kk + 4], qr[kk - 4][0], sa); sb = MFMA16(ka[kk + 4], qr[kk - 4][1], sb);
            o[0] = MFMA32(cat8(va[kk][0], va[kk][1]), pf[kk], o[0]);
            o[1] = MFMA32(cat8(va[kk][2], va[kk][3]), pf[kk], o[1]);
            __builtin_amdgcn_sched_barrier(0);
        }
        if (wv < 6) va_read1(7);
        STAMP(5);
        tlo = __builtin_amdgcn_readfirstlane(tg[0]); thi = __builtin_amdgcn_readfirstlane(tg[1]);
        rederive();
        fbo = (unsigned)(WS_FB + x * STR + lane * 16);
        asm volatile("buffer_load_dwordx4 %0, off, %1, %2 sc1" : "=v"(nq) : "s"(rw), "s"(WS_NM + x * STR) : "memory");
        const unsigned head = lane & 31, r8 = lane >> 3, cw = wv ^ (rot * 2);
        srd = lb + r8 * 128 + (((lane & 7) ^ r8) << 4);
        sw[0] = lb + head * 128 + (((lane >> 5) ^ (head & 7)) << 4);
        sw[1] = sw[0] ^ 32u; sw[2] = sw[0] ^ 64u; sw[3] = sw[0] ^ 96u;
        p0 = (unsigned)(WS_PART + x * 1048576) + (cw >> 2) * 16384 + (cw & 3) * 2048 + r8 * 32768 + ((s ^ r8) * 8 + (lane & 7)) * 16;
        PIN4(sw[0], sw[1], sw[2], sw[3]); PIN(srd); PIN(p0); PIN(fbo);
    }
    STAMP(23);
    unsigned po[4];
    float mrun;
    {
        VMW(1);
        STAMP(24);
        ka_reads(3);
        asm volatile("" : "=v"(fa));
        if (wv == 0 && s < 11) asm volatile("buffer_load_dwordx4 %0, %1, %2, 0 offen sc1" : "+v"(fa) : "v"(fbo), "s"(rw) : "memory");
        __builtin_amdgcn_sched_barrier(0);
#pragma unroll
        for (int ks = 12; ks < 16; ++ks) { sa = MFMA16(ka[ks], qr[ks - 8][0], sa); sb = MFMA16(ka[ks], qr[ks - 8][1], sb); }
        bf16x8 pf[8];
#pragma unroll 1
        for (bool first = true;; first = false) {
            float e[8];
            probs(e);
            const float n0 = (e[0] + e[1]) + (e[2] + e[3]), n1 = (e[4] + e[5]) + (e[6] + e[7]);
            *LP(float, esw) = ls0 + n0; *LP(float, esw + 16384) = ls1 + n1;
            if (__builtin_expect(!(n0 + n1 <= BIGV), 0)) *LP(unsigned, L_BIG) = 1u;
            va_pin();
            wg_barrier();
            STAMP(25);
            PIN(prd);
            const unsigned big = *LP(unsigned, L_BIG);
            if ((wv >> 1) == 3) va_reads();
#pragma unroll
            for (int kk = 0; kk < 8; ++kk) { pf[kk] = p_ld(kk); __builtin_amdgcn_sched_barrier(0); }
            if (__builtin_expect(__builtin_amdgcn_readfirstlane(big) == 0 || !first, 1)) break;
            const unsigned ex2 = L_SCR + 512 + 160;
            PIN(sa); PIN(sb);
            rederive();
            *LP(float, ex2 + (nl + (lane >> 5) * 16) * 1024 + wv * 4) = wave_max();
            wg_barrier();
            const float x0 = fmaxf(max8(ex2 + nl * 1024), m0), x1 = fmaxf(max8(ex2 + nl * 1024 + 16384), m1);
            const float al0 = __builtin_amdgcn_exp2f(m0 - x0), al1 = __builtin_amdgcn_exp2f(m1 - x1), al = (lane & 16) ? al1 : al0;
            m0 = x0; m1 = x1; ls0 *= al0; ls1 *= al1;
            o[0] *= al; o[1] *= al;
        }
        mrun = (lane & 16) ? m1 : m0;
        auto stage = [&](int t2, int gp) {
            const unsigned lo0 = pk2(o[t2][8 * gp + 0], o[t2][8 * gp + 1]), lo1 = pk2(o[t2][8 * gp + 2], o[t2][8 * gp + 3]);
            const unsigned hi0 = pk2(o[t2][8 * gp + 4], o[t2][8 * gp + 5]), hi1 = pk2(o[t2][8 * gp + 6], o[t2][8 * gp + 7]);
            auto r0 = __builtin_amdgcn_permlane32_swap(lo0, hi0, false, false);
            auto r1 = __builtin_amdgcn_permlane32_swap(lo1, hi1, false, false);
            *LP(u32x4, sw[t2 * 2 + gp]) = u32x4{r0[0], r1[0], r0[1], r1[1]};
        };
#pragma unroll
        for (int kk = 0; kk < 8; ++kk) {
            o[0] = MFMA32(cat8(va[kk][0], va[kk][1]), pf[kk], o[0]);
            if (kk == 1) {
                asm volatile("s_waitcnt vmcnt(0)" : "+v"(nq) :: "memory");
                npm = __builtin_amdgcn_readfirstlane(nq[0]);
                if ((unsigned)__builtin_amdgcn_readfirstlane(nq[2]) != tlo || (unsigned)__builtin_amdgcn_readfirstlane(nq[3]) != thi) npm = __builtin_amdgcn_readfirstlane(tagged_wait(WS_NM + x * STR)[0]);
            }
            if (kk == 3 || kk == 5) {
                const unsigned v = (npm ^ ((x & 2) ? 0xffu : 0u)) >> (lane >> 5);
#pragma unroll
                for (int i = kk - 3; i < kk - 1; ++i) { po[i] = p0 + ((v >> (2 * i)) & 1u) * 8192u; PIN(po[i]); }
            }
            __builtin_amdgcn_sched_barrier(0);
        }
#pragma unroll
        for (int kk = 0; kk < 8; ++kk) {
            o[1] = MFMA32(cat8(va[kk][2], va[kk][3]), pf[kk], o[1]);
            if (kk == 2) { PIN(o[0]); stage(0, 0); }
            if (kk == 4) stage(0, 1);
            __builtin_amdgcn_sched_barrier(0);
        }
        stage(1, 0); stage(1, 1);
        asm volatile("s_waitcnt lgkmcnt(0)" ::: "memory");
        u32x4 w[4];
#pragma unroll
        for (int i = 0; i < 4; ++i) w[i] = *LP(u32x4, srd + i * 1024);
#pragma unroll
        for (int i = 0; i < 4; ++i) ST16(w[i], po[i], i * 262144);
        __builtin_amdgcn_sched_barrier(0);
        STAMP(26);
        u32x4 mlrec;
        if (wv == 0) {
            const unsigned head = lane & 31;
            esr = L_SCR + head * 1024 + 512 + ((head & 7) << 4);
            mlo = (unsigned)(WS_ML + x * STR + s * 512 + head * 16) + ((npm >> ((head >> 3) * 2)) & 1u) * 8192u;
            PIN(mlo);
            f32x4 b8[8];
#pragma unroll
            for (int q = 0; q < 8; ++q) b8[q] = *LP(f32x4, esr ^ (q << 4));
            const f32x4 bs = ((b8[0] + b8[1]) + (b8[2] + b8[3])) + ((b8[4] + b8[5]) + (b8[6] + b8[7]));
            mlrec = u32x4{__float_as_uint(mrun), __float_as_uint((bs[0] + bs[1]) + (bs[2] + bs[3])), tlo, thi};
            PIN(mlrec);
        }
        VMW(0);
        wg_barrier();
        if (wv == 0) {
            if (lane < 32) ST16(mlrec, mlo, 0);
            PIN(fa);
            if (s < 11 && !__all(flags_ok(fa))) flags_wait(fbo);
        }
        STAMP(27);
        wg_barrier();
    }
    const unsigned long long t_att = __builtin_amdgcn_s_memrealtime();
    rederive();
    while (__builtin_amdgcn_s_memrealtime() < t_att + (s >= 11 ? MD0 : MD1)) __builtin_amdgcn_s_sleep(2);
    if (s >= 11) {
        const unsigned n = (unsigned)((s - 11) * 8 + wv);
        if (n < 28) pf4<NTM & 32>((n * 64 + lane) * 1024u + x * 128u, p.w2, 0);
        { const unsigned l2 = n * 64 + lane; pf4<NTM & 32>((l2 / 5u) * (DFF * 2) + min(((unsigned)x * 448u & ~127u) + (l2 % 5u) * 128u, DFF * 2 - 4u), p.w1, 0); }
        if (wv == 0) flag_put(WS_FA, id - 128);
        STAMP_FLUSH();
        return;
    }
    const unsigned L_H2S = L_SCR, L_REDM = L_SCR + 16384;
    if (s < 7) {
        {
            const char* w1s = (const char*)(p.w1 + (size_t)wv * 64 * DFF + ug * 32);
#pragma unroll
            for (int i = 0; i < 4; ++i) dma16<NTM & 32>((unsigned)((lane >> 2) * DFF * 2 + (lane & 3) * 16), w1s + (size_t)i * 16 * DFF * 2, wv * 4096 + i * 1024);
        }
        const int b = wv;
        if (wv == 0) ST16(s4, (unsigned)(WS_U + x * STR + (lane >> 5) * 8192 + s * 512 + (lane & 31) * 16), 0);
        const u32x4 shf = LD16((unsigned)(WS_MB + b * STR + lane * 16), 0), scl = LD16((unsigned)(WS_MB + b * STR + 1024 + lane * 16), 0);
        VMW(0);
        if (wv == 0) flag_put(WS_FA, id - 128);
        wg_barrier();
        bf16x8 wk0[8], wk1[8];
        if (wv >= 6) {
#pragma unroll
            for (int q = 0; q < 8; ++q) {
                const unsigned a = (unsigned)(q * 4096 + (g * 8 + (nl >> 2)) * 64 + (wv & 1) * 32 + (lane & 3) * 8);
                wk0[q] = tr_frag(a, a + 256); wk1[q] = tr_frag(a + 2048, a + 2304);
            }
#pragma unroll
            for (int q = 0; q < 8; q += 2) PIN4(wk0[q], wk0[q + 1], wk1[q], wk1[q + 1]);
        }
        float scv[8], shv[8];
#pragma unroll
        for (int q = 0; q < 4; ++q) {
            scv[2 * q] = rbf(1.f + lo16(scl[q])); scv[2 * q + 1] = rbf(1.f + hi16(scl[q])); shv[2 * q] = lo16(shf[q]); shv[2 * q + 1] = hi16(shf[q]);
            PIN4(scv[2 * q], scv[2 * q + 1], shv[2 * q], shv[2 * q + 1]);
        }
        unsigned har = L_H2S + ((lane & 7) * HS_LD + g * 8 + (nl >> 3) * 32) * 2, haw = L_H2S + (b * HS_LD + lane * 8) * 2;
        unsigned udst = (unsigned)(WS_U + x * STR + (nl >> 3) * 8192 + (s * 2 + (wv & 1)) * 256 + ((nl & 7) * 16 + g * 4) * 2), xsrc = (unsigned)(WS_X1 + x * STR + (b * 512 + lane * 8) * 2) + ((npm >> x) & 1u) * 8192u;
        PIN4(har, haw, udst, xsrc);
        STAMP(28);
        while (__builtin_amdgcn_s_memrealtime() < t_att + XGATE) __builtin_amdgcn_s_sleep(2);
        u32x4 xv;
        for (unsigned spins = 0;;) {
            xv = LD16(xsrc, 0);
            if (__all(mx4(xv, 0u) != SENT)) break;
            __builtin_amdgcn_s_sleep(SLPX);
            if (++spins > kSpinCap) break;
            asm volatile("" ::: "memory");
        }
        STAMP(29);
        {
            f32x2_t v[4];
#pragma unroll
            for (int q = 0; q < 4; ++q) v[q] = f32x2_t{lo16(xv[q]), hi16(xv[q])};
            const f32x2_t p1 = (v[0] + v[1]) + (v[2] + v[3]), p2 = (v[0] * v[0] + v[1] * v[1]) + (v[2] * v[2] + v[3] * v[3]);
            float mb, rden;
            ln_stats(p1[0] + p1[1], p2[0] + p2[1], mb, rden);
            u32x4 hr;
#pragma unroll
            for (int q = 0; q < 4; ++q) {
                const f32x2_t h = ((v[q] - mb) * rden) * f32x2_t{scv[2 * q], scv[2 * q + 1]} + f32x2_t{shv[2 * q], shv[2 * q + 1]};
                hr[q] = pk2(h[0], h[1]);
            }
            *LP(u32x4, haw) = hr;
        }
        wg_barrier();
        if (wv >= 6) {
            bf16x8 af[8];
#pragma unroll
            for (int q = 0; q < 8; ++q) af[q] = *LP(bf16x8, har + q * 128);
            __builtin_amdgcn_sched_barrier(0);
            f32x4 d0 = {0.f, 0.f, 0.f, 0.f}, d1 = {0.f, 0.f, 0.f, 0.f};
#pragma unroll
            for (int q = 0; q < 8; ++q) { d0 = MFMA16(wk0[q], af[q], d0); d1 = MFMA16(wk1[q], af[q], d1); }
            const f32x4 own = nl < 8 ? d0 : d1;
#pragma unroll
            for (int e = 0; e < 4; ++e) { float v = own[e] + dppf<0x128>(own[e]); asm("" : "+v"(v)); d0[e] = fmaxf(v, 0.f); }
            u32x2 uv = {pk2(d0[0], d0[1]), pk2(d0[2], d0[3])};
            if (__builtin_expect(max(uv[0], uv[1]) == SENT, 0)) uv = u32x2{pk2s(d0[0], d0[1]), pk2s(d0[2], d0[3])};
            __builtin_amdgcn_raw_buffer_store_b64(uv, rw, udst, 0, 16);
            STAMP(30);
        }
        STAMP_FLUSH();
        return;
    }
    {
        const int n0 = (x * 4 + (s - 7)) * 16;
        const char* mws = (const char*)(p.w2 + (size_t)wv * 224 * 512 + n0);
#pragma unroll
        for (int i = 0; i < 7; ++i) dma16<NTM & 32>((unsigned)((lane >> 1) * 1024 + (lane & 1) * 16), mws + i * 32768, rgn + i * 1024);
        const int fb = lane >> 3, fc = (lane & 7) * 2;
        u32x2 gb = {0u, 0u};
        if (wv == 0) {
            flag_put(WS_FA, id - 128);
            { const u32x4 v = tagged_wait(WS_MOD + fb * STR + (1536 + n0 + fc) * 8); gb = u32x2{v[0], v[1]}; }
            flags_wait(WS_FA + x * STR + lane * 16);
            if (id == 23 * 8 && lane == 0) __builtin_amdgcn_raw_buffer_store_b64(u32x2{tlo, thi}, rw, WS_EPOCH, 0, 16);
            PIN(gb);
        }
        VMW(0);
        bf16x8 wfr[7];
        const unsigned trl = (unsigned)(g * 8 + (nl >> 2)) * 32u + (unsigned)(lane & 3) * 8u;
#pragma unroll
        for (int ks = 0; ks < 7; ++ks) { wfr[ks] = tr_frag(rgn + ks * 1024 + trl, rgn + ks * 1024 + trl + 128); PIN(wfr[ks]); }
        unsigned usrc = (unsigned)(WS_U + wv * STR + (g >> 1) * 256 + (nl & 7) * 32 + (g & 1) * 16) + ((npm >> x) & 1u) * 8192u;
        unsigned rdw = L_REDM + wv * 512 + (nl & 7) * 64 + g * 16, rdr = L_REDM + lane * 8;
        unsigned short* outp = p.out + fb * 512 + n0 + fc;
        PIN(usrc); PIN(rdw); PIN(rdr); PIN(outp);
        STAMP(28);
        unsigned xg = 0u;
        if (wv == 0) {
            while (__builtin_amdgcn_s_memrealtime() < t_att + XGATE) __builtin_amdgcn_s_sleep(2);
            for (unsigned spins = 0;;) {
                xg = __builtin_amdgcn_raw_buffer_load_b32(rw, (unsigned)(WS_X1 + x * STR + (fb * 512 + n0 + fc) * 2) + ((npm >> x) & 1u) * 8192u, 0, 16);
                if (__all(xg != SENT)) break;
                __builtin_amdgcn_s_sleep(SLPD);
                if (++spins > kSpinCap) break;
                asm volatile("" ::: "memory");
            }
            __builtin_amdgcn_s_sleep(SLPG);
        }
        wg_barrier();
        u32x4 uf[7];
        for (unsigned spins = 0;;) {
#pragma unroll
            for (int ks = 0; ks < 7; ++ks) uf[ks] = LD16(usrc + ks * 512, 0);
            unsigned m = 0u;
#pragma unroll
            for (int ks = 0; ks < 7; ++ks) m = mx4(uf[ks], m);
            if (__all(m != SENT)) break;
            if (++spins > kSpinCap) break;
            asm volatile("" ::: "memory");
        }
        STAMP(29);
        f32x4 d = {0.f, 0.f, 0.f, 0.f}, d2 = {0.f, 0.f, 0.f, 0.f};
#pragma unroll
        for (int ks = 0; ks < 7; ++ks) {
            if (ks & 1) d2 = MFMA16(wfr[ks], BF8(uf[ks]), d2);
            else d = MFMA16(wfr[ks], BF8(uf[ks]), d);
        }
        *LP(f32x4, rdw) = d + d2;
        wg_barrier();
        if (wv == 0) {
            f32x2_t c[8];
#pragma unroll
            for (int w2 = 0; w2 < 8; ++w2) c[w2] = *LP(f32x2_t, rdr + w2 * 512);
            const f32x2_t sm = ((c[0] + c[1]) + (c[2] + c[3])) + ((c[4] + c[5]) + (c[6] + c[7]));
            unsigned t = pk2(sm[0], sm[1]);
            t = pk2(__uint_as_float(gb[0]) * lo16(t), __uint_as_float(gb[1]) * hi16(t));
            *(GAS unsigned*)outp = pk2(lo16(xg) + lo16(t), hi16(xg) + hi16(t));
            { const unsigned long long r1_ = __builtin_amdgcn_s_memrealtime(); const unsigned dl = ((size_t)p.x == 0 ? 0u : (unsigned)((size_t)p.dbgp >> 12) & 511u) * 10u; while (__builtin_amdgcn_s_memrealtime() < r1_ + dl) __builtin_amdgcn_s_sleep(1); }
        }
        STAMP_FLUSH();
    }
}

extern "C" void kernel_launch(void* const* d_in, const int* in_sizes, int n_in,
                              void* d_out, int out_size, void* d_ws, size_t ws_size,
                              hipStream_t stream) {
    Params p;
    p.x = (const unsigned short*)d_in[0];
    p.ckv = (const unsigned short*)d_in[1];
    p.w_q = (const unsigned short*)d_in[2];
    p.w_uk = (const unsigned short*)d_in[3];
    p.w_uv = (const unsigned short*)d_in[4];
    p.w_o = (const unsigned short*)d_in[5];
    p.w1 = (const unsigned short*)d_in[7];
    p.w2 = (const unsigned short*)d_in[8];
    p.c = (const unsigned short*)d_in[9];
    p.w_mod = (const unsigned short*)d_in[10];
    p.ws = (char*)(((size_t)d_ws + 2097151) & ~(size_t)2097151);
    p.out = (unsigned short*)d_out;
    p.dbgp = (char*)d_ws;
    (void)hipFuncSetAttribute((const void*)k_main, hipFuncAttributeMaxDynamicSharedMemorySize, 163840);
    k_main<<<256, 512, 163840, stream>>>(p);
}
```

```cpp
#include <hip/hip_runtime.h>
#include <hip/hip_bf16.h>
#include <stdint.h>

#define NH 32
#define SK 4096
#define DFF 1792
#define GI 18
#define ROTC 1
#define PFS 2
#define XNO 0
#define CPJ 7
#define NTM 16
#define QHELP 4
#define HP 13
#define NTH 0
#define W0 2
#define SL0 3
#define LMPF 0
#define SLPB 20
#define MD0 0
#define MD1 0
#define NEARP 1
#define NCAL 2
#define TLBW 0
#define TG 500
#define JPERM 1
#define NPOST 6
#define XS3 1
#define WG 2
#define TWD 680
#define LMD 250
#define PFM 57
#define CPFE 35300
#define SLPM 0
#define SLPD 12
#define SLPG 8
#define SLPX 1
#define SLPH 4

using u16x8 = __attribute__((ext_vector_type(8))) unsigned short;
using s16x4 = __attribute__((ext_vector_type(4))) short;
using bf16x8 = __attribute__((ext_vector_type(8))) short;
using f32x16 = __attribute__((ext_vector_type(16))) float;
using f32x4 = __attribute__((ext_vector_type(4))) float;
using u32x4 = __attribute__((ext_vector_type(4))) unsigned;
using u32x2 = __attribute__((ext_vector_type(2))) unsigned;

#define GAS __attribute__((address_space(1)))
#define LAS __attribute__((address_space(3)))
typedef GAS unsigned long long gu64;
#define RLX_AGENT __ATOMIC_RELAXED, __HIP_MEMORY_SCOPE_AGENT
#define SENT 0xffffffffu
#define TAGC 0x9e3779b9u

__device__ __forceinline__ float bf2f(unsigned short u) { return __uint_as_float(((unsigned)u) << 16); }
typedef __attribute__((ext_vector_type(2))) __bf16 bf16x2_t;
typedef __attribute__((ext_vector_type(2))) float f32x2_t;
__device__ __forceinline__ unsigned pk2(float a, float b) {
    f32x2_t v = {a, b};
    bf16x2_t r = __builtin_convertvector(v, bf16x2_t);
    return __builtin_bit_cast(unsigned, r);
}
__device__ __forceinline__ unsigned pk2s(float a, float b) {
    const unsigned r = pk2(a, b);
    return r == SENT ? 0x7fc07fc0u : r;
}
__device__ __forceinline__ unsigned f32s(float a) {
    const unsigned r = __float_as_uint(a);
    return r == SENT ? 0x7fc00000u : r;
}
__device__ __forceinline__ unsigned short f2bf(float f) { return (unsigned short)(pk2(f, 0.f) & 0xffffu); }
__device__ __forceinline__ float rbf(float f) { return __uint_as_float(pk2(f, 0.f) << 16); }
__device__ __forceinline__ float lo16(unsigned u) { return __uint_as_float(u << 16); }
__device__ __forceinline__ float hi16(unsigned u) { return __uint_as_float(u & 0xffff0000u); }
__device__ __forceinline__ unsigned umax3(unsigned a, unsigned b, unsigned c) { return max(max(a, b), c); }
__device__ __forceinline__ unsigned mx4(const u32x4& v, unsigned m) { return umax3(v[2], v[3], umax3(v[0], v[1], m)); }
__device__ __forceinline__ float fmax3(float a, float b, float c) { float r; asm("v_max3_f32 %0, %1, %2, %3" : "=v"(r) : "v"(a), "v"(b), "v"(c)); return r; }
template <int C> __device__ __forceinline__ float dppf(float v) { return __int_as_float(__builtin_amdgcn_update_dpp(0, __float_as_int(v), C, 0xf, 0xf, true)); }
__device__ __forceinline__ float sum16(float v) { v += dppf<0xB1>(v); v += dppf<0x4E>(v); v += dppf<0x141>(v); v += dppf<0x140>(v); return v; }

__device__ __forceinline__ void ln_stats(float p1, float p2, float& mb, float& rden) {
    auto t = __builtin_amdgcn_permlane32_swap(__float_as_uint(p1), __float_as_uint(p2), false, false);
    float a = sum16(__uint_as_float(t[0]) + __uint_as_float(t[1]));
    auto t2 = __builtin_amdgcn_permlane16_swap(__float_as_uint(a), __float_as_uint(a), false, false);
    a = (__uint_as_float(t2[0]) + __uint_as_float(t2[1])) * (1.f / 512.f);
    auto t3 = __builtin_amdgcn_permlane32_swap(__float_as_uint(a), __float_as_uint(a), false, false);
    const float m = __uint_as_float(t3[0]);
    const float var = rbf(fmaxf(fmaf(-m, m, __uint_as_float(t3[1])), 0.f));
    mb = rbf(m);
    rden = __builtin_amdgcn_rcpf(rbf(__builtin_amdgcn_sqrtf(rbf(var + 1e-5f))));
}
#define STR 262144
#define WS_Q 0
#define WS_H 32768
#define WS_X1 65536
#define WS_ML 81920
#define WS_MOD 98304
#define WS_MB 139264
#define WS_U 114688
#define WS_FI 131072
#define WS_FA 132096
#define WS_FB 133120
#define WS_NP 134144
#define WS_NM 134400
#define WS_EPOCH 135168
#define WS_CAL 147456
#define WS_OUTP 2097152
#define WS_PART 4194304
#define WS_END 12582912

#define LP(T, off) ((LAS T*)(size_t)(unsigned)(off))
#define STAMP(k) do {} while (0)
#define STAMP_FLUSH() do {} while (0)
__device__ __forceinline__ s16x4 tr4(unsigned a) { return __builtin_amdgcn_ds_read_tr16_b64_v4i16(LP(s16x4, a)); }
__device__ __forceinline__ bf16x8 cat8(s16x4 a, s16x4 b) { return __builtin_shufflevector(a, b, 0, 1, 2, 3, 4, 5, 6, 7); }
__device__ __forceinline__ bf16x8 tr_frag(unsigned a0, unsigned a1) { return cat8(tr4(a0), tr4(a1)); }
__device__ __forceinline__ unsigned swz(unsigned row) { return ((row & 3) << 2) | ((row >> 2) & 3); }
__device__ __forceinline__ unsigned off_b(unsigned row, unsigned ch) { return 256u * row + 16u * (ch ^ swz(row)); }
__device__ __forceinline__ unsigned tile_off(unsigned key, unsigned chunk) { return ((key >> 5) * 4 + (chunk >> 4)) * 8192u + off_b(key & 31, chunk & 15); }
__device__ __forceinline__ void wg_barrier() { asm volatile("s_waitcnt lgkmcnt(0)\n\ts_barrier" ::: "memory"); }
template <int NT> __device__ __forceinline__ void dma16(unsigned voff, const void* sbase, unsigned lds_dst) {
    if (NT) asm volatile("s_mov_b32 m0, %2\n\ts_nop 0\n\tglobal_load_lds_dwordx4 %0, %1 nt" :: "v"(voff), "s"(sbase), "s"(lds_dst) : "memory", "m0");
    else asm volatile("s_mov_b32 m0, %2\n\ts_nop 0\n\tglobal_load_lds_dwordx4 %0, %1" :: "v"(voff), "s"(sbase), "s"(lds_dst) : "memory", "m0");
}
template <int NT> __device__ __forceinline__ void dma16x(unsigned voff, unsigned vx, const void* sbase, unsigned lds_dst) {
    unsigned t;
#define DMAX(pol, c) asm volatile("s_mov_b32 m0, %3\n\tv_xor_b32 %0, %4, %1\n\tglobal_load_lds_dwordx4 %0, %2" pol : "=&v"(t) : "v"(voff), "s"(sbase), "s"(lds_dst), c(vx) : "memory", "m0")
    if (__builtin_constant_p(vx)) { if (NT) DMAX(" nt", "i"); else DMAX("", "i"); }
    else { if (NT) DMAX(" nt", "s"); else DMAX("", "s"); }
}
template <int NT> __device__ __forceinline__ void pf4(unsigned voff, const void* sbase, unsigned lds_dst) {
    if (NT) asm volatile("s_mov_b32 m0, %2\n\ts_nop 0\n\tglobal_load_lds_dword %0, %1 nt" :: "v"(voff), "s"(sbase), "s"(lds_dst) : "memory", "m0");
    else asm volatile("s_mov_b32 m0, %2\n\ts_nop 0\n\tglobal_load_lds_dword %0, %1" :: "v"(voff), "s"(sbase), "s"(lds_dst) : "memory", "m0");
}
__device__ __forceinline__ void pf4s(unsigned voff, const void* sbase, unsigned lds_dst) {
    asm volatile("s_mov_b32 m0, %2\n\ts_nop 0\n\tglobal_load_lds_dword %0, %1 sc1" :: "v"(voff), "s"(sbase), "s"(lds_dst) : "memory", "m0");
}
#define XGATE 300
#define SPINCAP (1u << 20)
constexpr unsigned kSpinCap = SPINCAP;
struct Params {
    const unsigned short* x;
    const unsigned short* ckv;
    const unsigned short* w_q;
    const unsigned short* w_uk;
    const unsigned short* w_uv;
    const unsigned short* w_o;
    const unsigned short* w1;
    const unsigned short* w2;
    const unsigned short* c;
    const unsigned short* w_mod;
    char* ws;
    unsigned short* out;
};
#define VMW(n) asm volatile("s_waitcnt vmcnt(%0)" :: "n"(n) : "memory")
#define PIN(x) asm volatile("" : "+v"(x))
#define PIN4(a, b, c, d) asm volatile("" : "+v"(a), "+v"(b), "+v"(c), "+v"(d) :: "memory")
#define MFMA16(a, b, c) __builtin_amdgcn_mfma_f32_16x16x32_bf16(a, b, c, 0, 0, 0)
#define MFMA32(a, b, c) __builtin_amdgcn_mfma_f32_32x32x16_bf16(a, b, c, 0, 0, 0)
#define BF8(x) __builtin_bit_cast(bf16x8, x)
#define ST16(v, off, soff) __builtin_amdgcn_raw_buffer_store_b128(v, rw, off, soff, 16)
#define LD16(off, soff) __builtin_amdgcn_raw_buffer_load_b128(rw, off, soff, 16)
#define SCL (0.08838834764831845f * 1.4426950408889634f)
#define BIGV 1.0e18f
#define QD 4
#define QE 4
#define L_SCR 131072
#define HS_LD 520

__global__ __launch_bounds__(512, 2) void k_main(Params p) {
    unsigned long long t00; asm volatile("s_memrealtime %0" : "=s"(t00));
    unsigned long long pc;
    asm volatile("s_getpc_b64 %0" : "=s"(pc));
    const int id = blockIdx.x, tid = threadIdx.x;
    int lane = tid & 63;
    const int wv = __builtin_amdgcn_readfirstlane(tid >> 6);
    int g = lane >> 4, nl = lane & 15;
    auto rederive = [&]() {
        asm volatile("v_mbcnt_lo_u32_b32 %0, -1, 0\n\tv_mbcnt_hi_u32_b32 %0, -1, %0" : "=v"(lane));
        g = lane >> 4; nl = lane & 15;
    };
    const int x = id & 7, j0 = id >> 3, j = JPERM && j0 < 16 ? 15 - j0 : j0;
    if (wv < 5 && j == 15) {
        asm volatile("s_mov_b32 m0, %2\n\ts_nop 0\n\tglobal_load_lds_dword %0, %1" :: "v"(min((unsigned)((wv * 64 + lane) * 128), (unsigned)CPFE)), "s"(pc), "s"(L_SCR + 31744) : "memory", "m0");
    }
    if ((j >> 2) == 3) {
        dma16<0>((unsigned)(((nl & 7) * 512 + (nl >> 3) * 32 + g * 8) * 2 + wv * 128), p.c, wv * 16384 + 8192);
        dma16<0>((unsigned)tid * 16u, p.x, wv * 16384 + 9216);
    }
    char* ws = p.ws;
    STAMP(0);
    const __amdgpu_buffer_rsrc_t rw = __builtin_amdgcn_make_buffer_rsrc(ws, 0, WS_END, 0x00020000);
    const u32x4 s4 = {SENT, SENT, SENT, SENT};
    unsigned long long cnt = 0ull;
    auto cnt_load = [&]() { asm volatile("global_load_dwordx2 %0, %1, %2 sc1" : "+v"(cnt) : "v"((unsigned)WS_EPOCH), "s"(ws) : "memory"); };
    unsigned tlo = 0u, thi = 0u;
    auto tag_take = [&]() {
        asm volatile("" : "+v"(cnt) :: "memory");
        const unsigned long long t = cnt + 1ull;
        tlo = __builtin_amdgcn_readfirstlane((unsigned)t); thi = __builtin_amdgcn_readfirstlane((unsigned)(t >> 32));
    };
    auto flag_put = [&](unsigned base, unsigned idx) {
        if (lane < 8) __builtin_amdgcn_raw_buffer_store_b64(u32x2{tlo, thi}, rw, base + lane * STR + idx * 8, 0, 16);
    };
    auto tagged_wait = [&](unsigned off) {
        u32x4 v;
#pragma unroll 1
        for (unsigned spins = 0; spins < kSpinCap; ++spins) {
            asm volatile("buffer_load_dwordx4 %0, %1, %2, 0 offen sc1\n\ts_waitcnt vmcnt(0)" : "=v"(v) : "v"(off), "s"(rw) : "memory");
            if (__all((v[2] == tlo) & (v[3] == thi))) break;
            __builtin_amdgcn_s_sleep(8);
        }
        return v;
    };
    auto flags_ok = [&](const u32x4& f) { return (f[0] == tlo) & (f[1] == thi) & (f[2] == tlo) & (f[3] == thi); };
    auto flags_wait = [&](unsigned off) {
        for (unsigned spins = 0;;) {
            u32x4 f;
            asm volatile("buffer_load_dwordx4 %0, %1, %2, 0 offen sc1\n\ts_waitcnt vmcnt(0)" : "=v"(f) : "v"(off), "s"(rw) : "memory");
            if (__all(flags_ok(f))) break;
            __builtin_amdgcn_s_sleep(16);
            if (++spins > kSpinCap) break;
            asm volatile("" ::: "memory");
        }
    };
    unsigned pcl, frl, frs;
    auto dma_piece = [&](const unsigned short* src, unsigned ldb, unsigned dst, int n, int nt) {
        const unsigned v0 = (lane >> 3) * ldb + pcl, v1 = v0 ^ 64u;
#pragma unroll
        for (int i = 0; i < n; ++i) {
            if (nt) dma16<1>((i & 1) ? v1 : v0, (const char*)src + (size_t)i * 8 * ldb, dst + i * 1024);
            else dma16<0>((i & 1) ? v1 : v0, (const char*)src + (size_t)i * 8 * ldb, dst + i * 1024);
        }
    };
    auto piece_frag = [&](unsigned base, int ks, int t) {
        const unsigned a = base + frl + ((t ^ frs) << 5) + ks * 4096;
        return tr_frag(a, a + 512);
    };
    const unsigned rgn = (unsigned)(wv * 16384);

    if (j < 16) {
        const unsigned long long t_start = __builtin_amdgcn_s_memrealtime();
        rederive();
        pcl = (unsigned)(((((lane & 7) >> 1) ^ ((lane >> 4) & 1)) * 32) + (lane & 1) * 16);
        frl = (unsigned)((g * 8 + (nl >> 2)) * 128 + (lane & 3) * 8); frs = (unsigned)((nl >> 3) | ((g & 1) << 1));
        const int th = x * 4 + (j & 3);
        if (j < 12) {
            const int qt = j >> 2, rot = qt * 5;
            const bool pub = wv < (qt < 2 ? 3 : 2);
            const unsigned qo = (unsigned)(WS_Q + (th & 7) * STR + ((nl & 7) * 4 + (th >> 3)) * 1024 + (qt * 192 + wv * 64 + g * 16 + (nl >> 3) * 8) * 2);
            const unsigned vb = (unsigned)(g * 8192 + ((nl ^ (g << 2)) << 4));
            const char* wuk = (const char*)(p.w_uk + th * 128) + (size_t)qt * 192 * 8192;
            const char* wq = (const char*)(p.w_q + th * 128) + (size_t)wv * 32768;
            const unsigned L_U = 114688;
            {
                const int k0 = qt < 2 ? wv * 6 : wv * 4;
#pragma unroll
                for (int i = 0; i < 6; ++i) {
                    if (i < 4 || qt < 2) dma16<NTM & 2>(vb ^ (((k0 + i) & 3) << 4), wuk + (size_t)(k0 + i) * 32768, L_U + (k0 + i) * 1024);
                    if (i == 4) VMW(4);
                }
            }
            const unsigned vq = vb ^ ((wv & 3) << 4);
            auto wq_dma = [&](int ps) { dma16<NTM & 2>(vq, wq + (size_t)((ps + rot) & 15) * 262144, ps * 8192 + wv * 1024); };
            bf16x8 uk[4][4], aq[16];
            u32x4 fi, hv[4];
            unsigned fio = (unsigned)(WS_FI + x * STR + (lane & 7) * 16), hsrc, hsrc2, ta0, ta1, tb0, tb1, qo2, hw0, hw1, hr, hr2, qw, thx = 0u;
            const unsigned L_HI = L_SCR, L_QI = L_SCR + 8192;
            auto h_load = [&]() {
                asm volatile("buffer_load_dwordx4 %0, %4, %6, 0 offen sc1\n\tbuffer_load_dwordx4 %1, %4, %6, 0 offen offset:1024 sc1\n\t"
                             "buffer_load_dwordx4 %2, %5, %6, 0 offen sc1\n\tbuffer_load_dwordx4 %3, %5, %6, 0 offen offset:1024 sc1"
                             : "+v"(hv[0]), "+v"(hv[1]), "+v"(hv[2]), "+v"(hv[3]) : "v"(hsrc), "v"(hsrc2), "s"(rw) : "memory");
            };
            auto h_bad = [&]() {
                PIN4(hv[0], hv[1], hv[2], hv[3]);
                unsigned bad = (hv[0][1] ^ tlo) | (hv[0][3] ^ thx) | (hv[1][1] ^ tlo) | (hv[1][3] ^ thx) | (hv[2][1] ^ tlo) | (hv[2][3] ^ thx) | (hv[3][1] ^ tlo) | (hv[3][3] ^ thx);
                PIN(bad);
                return bad;
            };
#pragma unroll
            for (int ps = 0; ps < 16; ++ps) {
                wq_dma(ps);
                if (ps == 4) cnt_load();
                if (ps == HP) { h_load(); asm volatile("buffer_load_dwordx4 %0, %1, %2, 0 offen sc1" : "=v"(fi) : "v"(fio), "s"(rw) : "memory"); }
                if (ps >= 4 && ps < 8) VMW(5); else if (ps >= HP && ps < HP + 4) VMW(9); else VMW(4);
                if (ps == 3) {
                    asm volatile("s_barrier" ::: "memory");
                    if (pub) {
#pragma unroll
                        for (int t = 0; t < 4; ++t)
#pragma unroll
                            for (int s = 0; s < 4; ++s) uk[t][s] = *LP(bf16x8, L_U + off_b(wv * 64 + (nl >> 2) * 16 + t * 4 + (nl & 3), s * 4 + g));
                    }
                    STAMP(1);
                }
                if (ps == 5) {
                    hsrc = (unsigned)(WS_H + x * STR + wv * 2048 + lane * 16); hsrc2 = hsrc + 16384;
                    ta0 = off_b(8 * g + (nl >> 2), 2 * wv + ((lane & 3) >> 1)) + 8 * (lane & 1); ta1 = off_b(8 * g + 4 + (nl >> 2), 2 * wv + ((lane & 3) >> 1)) + 8 * (lane & 1);
                    tb0 = ta0 + 65536; tb1 = ta1 + 65536; qo2 = qo;
                    hw0 = L_HI + ((2 * wv - rot) & 15) * 512 + (lane >> 4) * 128 + (lane & 7) * 16 + ((lane >> 3) & 1) * 8;
                    hw1 = L_HI + ((2 * wv + 1 - rot) & 15) * 512 + (lane >> 4) * 128 + (lane & 7) * 16 + ((lane >> 3) & 1) * 8;
                    hr2 = L_HI + g * 128 + (nl & 7) * 16 + (nl >> 3) * 512;
                    PIN(hr2);
                    hr = L_HI + g * 128 + (nl & 7) * 16; qw = L_QI + (wv >> 1) * 512 + ((wv & 1) * 2 + (g >> 1)) * 128 + (nl & 7) * 16 + (g & 1) * 8;
                    PIN4(hsrc, hsrc2, ta0, ta1); PIN4(tb0, tb1, qo2, hw0); PIN(hw1); PIN(hr); PIN(qw);
                }
                if (ps == 8) { tag_take(); thx = tlo ^ thi ^ TAGC; }
                if (ps == 9) {
#pragma unroll
                    for (int t = 0; t < 4; ++t) PIN4(uk[t][0], uk[t][1], uk[t][2], uk[t][3]);
                    wg_barrier();
#pragma unroll
                    for (int q = 0; q < 6; ++q) aq[q] = tr_frag(ta0 + q * 8192, ta1 + q * 8192);
                }
                if (ps == 13) {
                    asm volatile("s_barrier" ::: "memory");
#pragma unroll
                    for (int q = 6; q < 10; ++q) aq[q] = q < 8 ? tr_frag(ta0 + q * 8192, ta1 + q * 8192) : tr_frag(tb0 + (q - 8) * 8192, tb1 + (q - 8) * 8192);
                }
            }
            STAMP(15);
            if (HP + 4 > 15) VMW(HP + 4 > 15 ? 15 - HP : 0);
            if (!__all(h_bad() == 0u)) {
                for (unsigned spins = 0;;) {
                    h_load();
                    VMW(0);
                    if (__all(h_bad() == 0u)) break;
                    __builtin_amdgcn_s_sleep(SLPH);
                    if (++spins > kSpinCap) break;
                }
            }
            {
                unsigned hh[4];
#pragma unroll
                for (int q = 0; q < 4; ++q) {
                    const unsigned t = hv[2 + (q >> 1)][(q & 1) * 2], sh = hv[q >> 1][(q & 1) * 2];
                    hh[q] = pk2(lo16(t) + lo16(sh), hi16(t) + hi16(sh));
                }
                *LP(u32x2, hw0) = u32x2{hh[0], hh[1]};
                *LP(u32x2, hw1) = u32x2{hh[2], hh[3]};
            }
            STAMP(3);
            asm volatile("s_waitcnt vmcnt(0) lgkmcnt(0)\n\ts_barrier" : "+v"(fi) :: "memory");
            STAMP(2);
            {
                bf16x8 hb[8];
#pragma unroll
                for (int q = 0; q < 5; ++q) hb[q] = *LP(bf16x8, hr2 + q * 1024);
#pragma unroll
                for (int ps = 10; ps < 16; ++ps) {
                    if (!(ps & 1)) hb[ps >> 1] = *LP(bf16x8, hr2 + (ps >> 1) * 1024);
                    aq[ps] = tr_frag(tb0 + (ps - 8) * 8192, tb1 + (ps - 8) * 8192);
                }
                __builtin_amdgcn_sched_barrier(0);
                f32x4 d0 = {0.f, 0.f, 0.f, 0.f}, d1 = {0.f, 0.f, 0.f, 0.f};
#pragma unroll
                for (int ps = 0; ps < 16; ps += 2) {
                    d0 = MFMA16(aq[ps], hb[ps >> 1], d0); d1 = MFMA16(aq[ps + 1], hb[ps >> 1], d1);
                    if (ps == 6) { unsigned bad = (fi[0] ^ tlo) | (fi[1] ^ thi) | (fi[2] ^ tlo) | (fi[3] ^ thi); PIN(bad); if (!__all(bad == 0u)) flags_wait(fio); }
                }
#pragma unroll
                for (int e = 0; e < 4; ++e) { float v = d0[e] + dppf<0x128>(d1[e]); asm("" : "+v"(v)); d0[e] = v; }
                if (nl < 8) *LP(u32x2, qw) = u32x2{pk2(d0[0], d0[1]), pk2(d0[2], d0[3])};
            }
            wg_barrier();
            if (pub) {
                bf16x8 qb[4];
#pragma unroll
                for (int s = 0; s < 4; ++s) qb[s] = *LP(bf16x8, hr + 8192 + s * 512);
                __builtin_amdgcn_sched_barrier(0);
                f32x4 d[4];
#pragma unroll
                for (int t = 0; t < 4; ++t) d[t] = MFMA16(uk[t][0], qb[0], (f32x4{0.f, 0.f, 0.f, 0.f}));
#pragma unroll
                for (int s = 1; s < 4; ++s)
#pragma unroll
                    for (int t = 0; t < 4; ++t) d[t] = MFMA16(uk[t][s], qb[s], d[t]);
                const f32x4 a = nl & 8 ? d[2] : d[0], b = nl & 8 ? d[3] : d[1];
                u32x4 qv = {pk2(a[0], a[1]), pk2(a[2], a[3]), pk2(b[0], b[1]), pk2(b[2], b[3])};
                if (__builtin_expect(mx4(qv, 0u) == SENT, 0)) qv = u32x4{pk2s(a[0], a[1]), pk2s(a[2], a[3]), pk2s(b[0], b[1]), pk2s(b[2], b[3])};
                ST16(qv, qo2, 0);
            }
            STAMP(4);
            wg_barrier();
            if (LMPF && wv >= 3 && wv < 6) {
                const unsigned n = (unsigned)(j * 3 + wv - 3);
                if (n < 32) pf4<0>(((n & 7u) * 64 + lane) * 6144u + 2048u + (x * 4 + (n >> 3)) * 128u, p.w_mod, L_SCR + 31744);
            }
            if (QHELP && wv < 3 && j + 12 * wv < 32) {
                const int hh = j + 12 * wv;
#pragma unroll 1
                for (unsigned spins = 0; spins < 64; ++spins) {
                    __builtin_amdgcn_s_sleep(QHELP);
                    const u32x4 v = LD16((unsigned)(WS_Q + (hh & 7) * STR + (x * 4 + (hh >> 3)) * 1024 + lane * 16), 0);
                    if (__all(mx4(v, 0u) != SENT)) break;
                    asm volatile("" ::: "memory");
                }
                STAMP(6);
            }
        } else {
            const int cg = x * 2 + (j & 1);
            if (j < 14) {
                dma_piece(p.w_mod + (size_t)wv * 64 * 3072 + cg * 64, 6144, rgn, 8, NTM & 2);
                cnt_load();
                VMW(9);
            } else {
                cnt_load();
                ST16(s4, (unsigned)(WS_Q + wv * STR + cg * 2048 + lane * 16), 0); ST16(s4, (unsigned)(WS_Q + wv * STR + cg * 2048 + lane * 16), 1024);
                VMW(0);
                tag_take();
                wg_barrier();
                if (wv == 7) flag_put(WS_FI, cg);
                STAMP(15);
                if (j == 14) {
                    u32x2 best = {~0u, ~0u};
#pragma unroll
                    for (int r = 0; r < 2 * NCAL; ++r) {
                        const unsigned hf = (r ^ wv) & 1;
                        VMW(0);
                        const unsigned long long t0 = __builtin_amdgcn_s_memtime();
                        if (lane < 8) ST16(s4, WS_CAL + x * STR + hf * 8192 + (wv * 8 + r) * 128 + lane * 16, 0);
                        VMW(0);
                        const unsigned dt = (unsigned)(__builtin_amdgcn_s_memtime() - t0);
                        if (hf) best[1] = min(best[1], dt); else best[0] = min(best[0], dt);
                    }
                    *LP(u32x2, 16384 * 7 + 14336 + wv * 8) = best;
                    wg_barrier();
                    if (wv == 0 && lane < 8) {
                        u32x2 m = *LP(u32x2, 16384 * 7 + 14336 + lane * 8);
#pragma unroll
                        for (int o = 1; o < 8; o <<= 1) { m[0] = min(m[0], (unsigned)__shfl_xor(m[0], o)); m[1] = min(m[1], (unsigned)__shfl_xor(m[1], o)); }
                        ST16((u32x4{(unsigned)max(-1000, min(1000, (int)m[1] - (int)m[0])), 0u, tlo, thi}), (unsigned)(WS_NP + lane * STR + x * 16), 0);
                    }
                    if (wv == 0) {
                        const int d = (int)tagged_wait(WS_NP + x * STR + (lane & 7) * 16)[0];
                        int t = (lane & 4) ? -d : d;
                        t += __builtin_amdgcn_update_dpp(0, t, 0xB1, 0xf, 0xf, true);
                        t += __builtin_amdgcn_update_dpp(0, t, 0x4E, 0xf, 0xf, true);
                        t += __builtin_amdgcn_update_dpp(0, t, 0x141, 0xf, 0xf, true);
                        const bool n1 = abs(d) > 160 ? d < 0 : (t < 0) != ((lane & 4) != 0);
                        const unsigned mk = NEARP ? (unsigned)__ballot(n1) & 0xffu : 0u;
                        if (lane == 0) ST16((u32x4{mk, 0u, tlo, thi}), (unsigned)(WS_NM + x * STR), 0);
                    }
                    STAMP(9);
                }
            }
            const u16x8 cf = *LP(u16x8, rgn + 8192 + lane * 16), xa = *LP(u16x8, rgn + 9216 + lane * 16);
            u32x4 af[2];
#pragma unroll
            for (int q = 0; q < 4; ++q) {
                const float a = bf2f(cf[2 * q]), b = bf2f(cf[2 * q + 1]);
                af[0][q] = pk2(a * __builtin_amdgcn_rcpf(1.f + __expf(-a)), b * __builtin_amdgcn_rcpf(1.f + __expf(-b)));
                af[1][q] = (unsigned)__builtin_amdgcn_update_dpp(0, (int)af[0][q], 0x128, 0xf, 0xf, true);
            }
            const unsigned L_MS = L_SCR;
            unsigned wa[4], fr[4];
#pragma unroll
            for (int t = 0; t < 4; ++t) { wa[t] = L_MS + wv * 2048 + (nl & 7) * 256 + (((t * 4 + g) ^ (nl & 7)) << 4) + (nl >> 3) * 16384; fr[t] = rgn + frl + ((t ^ frs) << 5); }
            PIN4(wa[0], wa[1], wa[2], wa[3]); PIN4(fr[0], fr[1], fr[2], fr[3]); PIN(af[0]); PIN(af[1]);
            const unsigned L_XN = 12288;
            auto mod_mm = [&](bool ln) {
                VMW(0);
                bf16x8 f[2][4];
#pragma unroll
                for (int ks = 0; ks < 2; ++ks)
#pragma unroll
                    for (int t = 0; t < 4; ++t) f[ks][t] = tr_frag(fr[t] + ks * 4096, fr[t] + ks * 4096 + 512);
                __builtin_amdgcn_sched_barrier(0);
                f32x4 d[4];
#pragma unroll
                for (int t = 0; t < 4; ++t) d[t] = MFMA16(f[0][t], BF8(af[0]), (f32x4{0.f, 0.f, 0.f, 0.f}));
#pragma unroll
                for (int t = 0; t < 4; ++t) d[t] = MFMA16(f[1][t], BF8(af[1]), d[t]);
#pragma unroll
                for (int t = 0; t < 4; ++t) *LP(f32x4, wa[t]) = d[t];
                if (ln) {
                    float v[8];
#pragma unroll
                    for (int q = 0; q < 8; ++q) v[q] = bf2f(xa[q]);
                    const float p1 = ((v[0] + v[1]) + (v[2] + v[3])) + ((v[4] + v[5]) + (v[6] + v[7]));
                    const float p2 = ((v[0] * v[0] + v[1] * v[1]) + (v[2] * v[2] + v[3] * v[3])) + ((v[4] * v[4] + v[5] * v[5]) + (v[6] * v[6] + v[7] * v[7]));
                    float mb, rden;
                    ln_stats(p1, p2, mb, rden);
                    u32x4 r;
#pragma unroll
                    for (int q = 0; q < 4; ++q) { const unsigned d = pk2(v[2 * q] - mb, v[2 * q + 1] - mb); r[q] = pk2(lo16(d) * rden, hi16(d) * rden); }
                    *LP(u32x4, L_XN + wv * 16384 + lane * 16) = r;
                }
                wg_barrier();
            };
            auto sum8 = [&](unsigned a) {
                f32x4 u[8];
#pragma unroll
                for (int w2 = 0; w2 < 8; ++w2) u[w2] = *LP(f32x4, a + w2 * 2048);
                __builtin_amdgcn_sched_barrier(0);
                return ((u[0] + u[1]) + (u[2] + u[3])) + ((u[4] + u[5]) + (u[6] + u[7]));
            };
            if (j < 14) {
                const int hb = lane & 7, c4 = 2 * wv + (nl >> 3);
                unsigned hoff = (unsigned)(WS_H + g * STR + cg * 2048 + wv * 256 + nl * 16);
                unsigned sa = L_MS + hb * 256 + ((c4 ^ hb) << 4);
                unsigned xa2 = L_XN + hb * 16384 + (cg & 7) * 128 + c4 * 8;
                PIN(hoff); PIN(sa); PIN(xa2);
                STAMP(1);
                mod_mm(cg >= 8);
                STAMP(2);
                const u32x2 xn = *LP(u32x2, xa2);
                const f32x4 v = sum8(sa);
                tag_take();
                const unsigned thx = tlo ^ thi ^ TAGC;
                u32x2 r = {pk2(v[0], v[1]), pk2(v[2], v[3])};
                if (cg >= 8) {
#pragma unroll
                    for (int q = 0; q < 2; ++q) { const unsigned sc = pk2(1.f + lo16(r[q]), 1.f + hi16(r[q])); r[q] = pk2(lo16(xn[q]) * lo16(sc), hi16(xn[q]) * hi16(sc)); }
                }
                ST16((u32x4{r[0], tlo, r[1], thx}), hoff, 0);
                ST16((u32x4{r[0], tlo, r[1], thx}), hoff, 4 * STR);
                STAMP(5);
            }
            wg_barrier();
            while (__builtin_amdgcn_s_memrealtime() < t_start + LMD) __builtin_amdgcn_s_sleep(2);
            const int lg = x * 4 + (j & 3);
            dma_piece(p.w_mod + (size_t)wv * 64 * 3072 + 1024 + lg * 64, 6144, rgn, 8, NTM & 4);
            mod_mm(false);
            if (lg >= 8 && lg < 24) {
                if (wv == 0) {
                    const int b = lane >> 3, c8 = lane & 7;
                    const f32x4 v0 = sum8(L_MS + b * 256 + (((2 * c8) ^ b) << 4)), v1 = sum8(L_MS + b * 256 + (((2 * c8 + 1) ^ b) << 4));
                    ST16((u32x4{pk2(v0[0], v0[1]), pk2(v0[2], v0[3]), pk2(v1[0], v1[1]), pk2(v1[2], v1[3])}), (unsigned)(WS_MB + b * STR + (lg - 8) * 128 + c8 * 16), 0);
                }
            } else if (wv < 4) {
                const int b = wv * 2 + (lane >> 5), cp = lane & 31;
                f32x2_t u[8];
#pragma unroll
                for (int w2 = 0; w2 < 8; ++w2) u[w2] = *LP(f32x2_t, L_MS + w2 * 2048 + b * 256 + (((cp >> 1) ^ b) << 4) + (cp & 1) * 8);
                const f32x2_t v = ((u[0] + u[1]) + (u[2] + u[3])) + ((u[4] + u[5]) + (u[6] + u[7]));
                ST16((u32x4{__float_as_uint(rbf(v[0])), __float_as_uint(rbf(v[1])), tlo, thi}), (unsigned)(WS_MOD + b * STR + (lg * 32 + cp) * 16), 0);
            }
            STAMP(6);
        }
        const int bq = (j >> 2) & 1, dh = j >> 3, src = th * 2 + dh, rb = bq * 4 + (src >> 4), rc = src & 15;
        wg_barrier();
        if (TLBW && wv == 6) {
            if (lane < 4) pf4s((unsigned)(WS_PART + (bq * 4 + lane) * 1048576 + th * 32768), ws, L_SCR + 31744);
            pf4s((unsigned)(lane * 65536 + th * 256), p.w_uv, L_SCR + 31744);
            if (lane < 2) pf4s((unsigned)(th * 131072 + lane * 65536), p.w_o, L_SCR + 31744);
        }
        while (__builtin_amdgcn_s_memrealtime() < t_start + TWD - 110) __builtin_amdgcn_s_sleep(2);
        const unsigned npm = __builtin_amdgcn_readfirstlane(tagged_wait(WS_NM + x * STR)[0]), np = (npm >> x) & 1u;
        unsigned oo = (unsigned)(WS_OUTP + ((bq * 4 + (nl & 3)) * 16 + wv * 2 + (nl >> 3)) * 16384 + (src ^ wv) * 128 + ((nl >> 2) & 1) * 64 + g * 16) + ((~npm >> ((nl & 3) * 2 + (wv >> 2))) & 1u) * 8192u;
        const int rcol = rc * 32 + (lane & 7) * 4 + (lane >> 5) * 2;
        unsigned xdst = (unsigned)(WS_X1 + ((lane >> 3) & 3) * STR + (rb * 512 + rcol) * 2), xdst2 = xdst + 4 * STR + ((npm >> (4 + ((lane >> 3) & 3))) & 1u) * 8192u;
        xdst += ((npm >> ((lane >> 3) & 3)) & 1u) * 8192u;
        ST16(s4, oo, 0);
        if (wv == 7) { __builtin_amdgcn_raw_buffer_store_b32(SENT, rw, xdst, 0, 16); __builtin_amdgcn_raw_buffer_store_b32(SENT, rw, xdst2, 0, 16); }
        VMW(0);
        wg_barrier();
        if (wv == 0) flag_put(WS_FB, id);
        while (__builtin_amdgcn_s_memrealtime() < t_start + TWD) __builtin_amdgcn_s_sleep(2);
        asm volatile("s_barrier" ::: "memory");
        STAMP(7);
        if ((PFM & 8) && wv == 0 && lane < 32) pf4<NTM & 8>((unsigned)((j * 32 + lane) * 8192 + x * 1024 + ((3u - ((unsigned)(size_t)p.w_uv >> 7)) & 7u) * 128), p.w_uv, L_SCR + 31744);
        if (bq) dma_piece(p.w_o + (size_t)(th * 128 + dh * 64) * 512 + wv * 64, 1024, rgn + 8192, 8, NTM & 8);
        dma_piece(p.w_uv + ((size_t)wv * 64 * NH + th) * 128 + dh * 64, 8192, rgn, 8, NTM & 8);
        if (!bq) dma_piece(p.w_o + (size_t)(th * 128 + dh * 64) * 512 + wv * 64, 1024, rgn + 8192, 8, NTM & 8);
        u32x4 fb, gm;
        unsigned xg;
        VMW(4);
        if (wv == 7)
            asm volatile("buffer_load_dwordx4 %0, %3, %5, 0 offen sc1\n\tbuffer_load_dwordx4 %1, %4, %5, 0 offen sc1\n\tglobal_load_dword %2, %6, %7" : "=&v"(fb), "=&v"(gm), "=&v"(xg)
                         : "v"((unsigned)(WS_FB + x * STR + lane * 16)), "v"((unsigned)(WS_MOD + rb * STR + rcol * 8)), "s"(rw), "v"((unsigned)(rb * 512 + rcol) * 2u), "s"(p.x) : "memory");
        VMW(0);
        asm volatile("" : "+v"(fb), "+v"(gm), "+v"(xg));
        STAMP(8);
        bf16x8 fuv[2][4], fwo[2][4];
        unsigned pfa[4];
#pragma unroll
        for (int t = 0; t < 4; ++t) { pfa[t] = rgn + frl + ((t ^ frs) << 5); PIN(pfa[t]); }
#pragma unroll
        for (int ks = 0; ks < 2; ++ks)
#pragma unroll
            for (int t = 0; t < 4; ++t) {
                const unsigned a = rgn + (unsigned)((g * 4 + (nl >> 2)) * 128 + (lane & 3) * 8) + ((t ^ (unsigned)((nl >> 3) | (g & 2))) << 5) + ks * 4096;
                fuv[ks][t] = tr_frag(a, a + 2048);
                fwo[ks][t] = tr_frag(pfa[t] + 8192 + ks * 4096, pfa[t] + 8192 + ks * 4096 + 512);
            }
#pragma unroll
        for (int ks = 0; ks < 2; ++ks) { PIN4(fuv[ks][0], fuv[ks][1], fuv[ks][2], fuv[ks][3]); PIN4(fwo[ks][0], fwo[ks][1], fwo[ks][2], fwo[ks][3]); }
        const unsigned L_WGT = L_SCR, L_MSK = L_SCR + 256, L_RED = L_SCR + 1024;
        unsigned psrc = (unsigned)(WS_PART + (bq * 4 * 32 + th) * 32768 + (wv >> 2) * 16384 + (wv & 3) * 2048 + ((lane >> 3) ^ (th & 7)) * 128) + np * 8192u + pcl, psrc1 = (psrc ^ 64u) + 1024;
        unsigned psrc2 = psrc ^ 8192u, psrc3 = psrc1 ^ 8192u;
        PIN(psrc2); PIN(psrc3); PIN(xdst2);
        unsigned rw2 = L_RED + wv * 1024 + (nl & 3) * 256 + g * 16;
        unsigned wa0 = (nl & 3) == (g >> 1) ? L_WGT + (nl & 4) * 32 + g * 16 : L_WGT + 272, wa1 = (nl & 3) == 2 + (g >> 1) ? L_WGT + (nl & 4) * 32 + 64 + g * 16 : L_WGT + 272;
        unsigned cr = rgn + (nl & 3) * 128 + g * 16, r2r = L_RED + lane * 16, cw = rgn + lane * 8;
        unsigned osrc = (unsigned)(WS_OUTP + (rb * 16 + rc) * 16384 + lane * 16) + (np ^ 1u) * 8192u;
        PIN(psrc); PIN(psrc1); PIN(rw2); PIN4(wa0, wa1, cr, r2r); PIN4(cw, oo, osrc, xdst);
        float mlm = 0.f, mll = 0.f;
        if (wv == 0) *LP(unsigned, L_WGT + 256 + lane * 4) = 0u;
        wg_barrier();
        const bool early = __all(flags_ok(fb) & (gm[2] == tlo) & (gm[3] == thi));
        u32x2 ga = {gm[0], gm[1]};
        PIN(ga);
        bf16x8 pa[2][4], wh[2];
        {
            u32x4 ml = {0u, 0u, ~tlo, ~thi};
            const unsigned mlsrc = (unsigned)(WS_ML + (bq * 4 + g) * STR + nl * 512 + th * 16) + ((npm >> (x & 6)) & 1u) * 8192u;
            unsigned long long done = 0ull;
            unsigned lm = L_MSK;
            PIN(lm);
#pragma unroll 1
            for (unsigned spins = 0; spins < kSpinCap; ++spins) {
                unsigned long long m;
                if (wv == 0) {
                    if ((ml[2] != tlo) | (ml[3] != thi)) ml = LD16(mlsrc, 0);
                    m = __ballot((ml[2] == tlo) & (ml[3] == thi));
                    unsigned long long f = m & (m >> 4);
                    f &= f >> 2; f &= f >> 1;
                    asm volatile("ds_write_b64 %0, %1" :: "v"(lm), "v"(f) : "memory");
                    m = m == ~0ull ? 0x0101010101010101ull : 0ull;
                } else {
                    unsigned long long t;
                    asm volatile("ds_read_b64 %0, %1\n\ts_waitcnt lgkmcnt(0)" : "=v"(t) : "v"(lm) : "memory");
                    m = ((unsigned long long)(unsigned)__builtin_amdgcn_readfirstlane((unsigned)(t >> 32)) << 32) | (unsigned)__builtin_amdgcn_readfirstlane((unsigned)t);
                    m &= 0x0101010101010101ull & ~done;
                }
                if (m) {
                    done |= m;
#define PLD(bit, off, v, base) "s_bitcmp1_b64 %0, " #bit "\n\ts_cbranch_scc0 1f\n\ts_add_u32 m0, %1, " #off "\n\ts_nop 0\n\tglobal_load_lds_dwordx4 " v ", " base " sc1\n1:\n\t"
                    asm volatile(PLD(0, 0, "%2", "%4") PLD(8, 1024, "%3", "%4") PLD(16, 2048, "%2", "%5") PLD(24, 3072, "%3", "%5")
                                 PLD(32, 4096, "%8", "%6") PLD(40, 5120, "%9", "%6") PLD(48, 6144, "%8", "%7") PLD(56, 7168, "%9", "%7")
                                 :: "s"(m), "s"(rgn), "v"(psrc), "v"(psrc1), "s"(ws), "s"(ws + 1048576), "s"(ws + 2097152), "s"(ws + 3145728), "v"(psrc2), "v"(psrc3) : "memory", "m0", "scc");
                    if (done == 0x0101010101010101ull) break;
                } else if (SLPM > 0 && wv == 0) __builtin_amdgcn_s_sleep(SLPM);
            }
            mlm = __uint_as_float(ml[0]); mll = __uint_as_float(ml[1]);
            STAMP(10);
            if (wv == 0) {
                float M = mlm;
                M = fmaxf(M, dppf<0xB1>(M)); M = fmaxf(M, dppf<0x4E>(M)); M = fmaxf(M, dppf<0x141>(M)); M = fmaxf(M, dppf<0x140>(M));
                const float e = __builtin_amdgcn_exp2f(mlm - M);
                const float w = e * __builtin_amdgcn_rcpf(sum16(e * mll));
                const unsigned short hi = f2bf(w);
                *LP(unsigned short, L_WGT + lane * 2) = hi;
                *LP(unsigned short, L_WGT + 128 + lane * 2) = f2bf(w - bf2f(hi));
            }
            wg_barrier();
            wh[0] = *LP(bf16x8, wa0); wh[1] = *LP(bf16x8, wa1);
            __builtin_amdgcn_sched_barrier(0);
            VMW(0);
#pragma unroll
            for (int ks = 0; ks < 2; ++ks)
#pragma unroll
                for (int t = 0; t < 4; ++t) pa[ks][t] = tr_frag(pfa[t] + ks * 4096, pfa[t] + ks * 4096 + 512);
        }
        STAMP(11);
        {
            f32x4 d[4];
#pragma unroll
            for (int t = 0; t < 4; ++t) d[t] = MFMA16(pa[0][t], wh[0], (f32x4{0.f, 0.f, 0.f, 0.f}));
#pragma unroll
            for (int t = 0; t < 4; ++t) d[t] = MFMA16(pa[1][t], wh[1], d[t]);
#pragma unroll
            for (int t = 0; t < 4; ++t)
#pragma unroll
                for (int e = 0; e < 4; ++e) { float v = d[t][e] + dppf<0x124>(d[t][e]); asm("" : "+v"(v)); d[t][e] = v; }
            const u32x4 o0 = {pk2(d[0][0], d[0][1]), pk2(d[0][2], d[0][3]), pk2(d[1][0], d[1][1]), pk2(d[1][2], d[1][3])};
            const u32x4 o1 = {pk2(d[2][0], d[2][1]), pk2(d[2][2], d[2][3]), pk2(d[3][0], d[3][1]), pk2(d[3][2], d[3][3])};
            f32x4 y[4];
#pragma unroll
            for (int t = 0; t < 4; ++t) y[t] = MFMA16(fuv[0][t], BF8(o0), (f32x4{0.f, 0.f, 0.f, 0.f}));
#pragma unroll
            for (int t = 0; t < 4; ++t) y[t] = MFMA16(fuv[1][t], BF8(o1), y[t]);
            if (nl < 4) {
#pragma unroll
                for (int t = 0; t < 4; ++t) *LP(f32x4, rw2 + t * 64) = y[t];
            }
        }
        wg_barrier();
        {
            f32x4 cs[8];
#pragma unroll
            for (int w2 = 0; w2 < 8; ++w2) cs[w2] = *LP(f32x4, r2r + w2 * 1024);
            const f32x4 c4 = ((cs[0] + cs[1]) + (cs[2] + cs[3])) + ((cs[4] + cs[5]) + (cs[6] + cs[7]));
            *LP(u32x2, cw) = u32x2{pk2(c4[0], c4[1]), pk2(c4[2], c4[3])};
            asm volatile("s_waitcnt lgkmcnt(0)" ::: "memory");
            const u32x4 cx0 = *LP(u32x4, cr), cx1 = *LP(u32x4, cr + 64);
            f32x4 o[4];
#pragma unroll
            for (int q = 0; q < 4; ++q) o[q] = MFMA16(fwo[0][q], BF8(cx0), (f32x4{0.f, 0.f, 0.f, 0.f}));
#pragma unroll
            for (int q = 0; q < 4; ++q) o[q] = MFMA16(fwo[1][q], BF8(cx1), o[q]);
            const f32x4 sel = (nl & 8) ? ((nl & 4) ? o[3] : o[2]) : ((nl & 4) ? o[1] : o[0]);
            u32x4 sv = __builtin_bit_cast(u32x4, sel);
            if (__builtin_expect(mx4(sv, 0u) == SENT, 0)) sv = u32x4{f32s(sel[0]), f32s(sel[1]), f32s(sel[2]), f32s(sel[3])};
            ST16(sv, oo, 0);
        }
        STAMP(12);
        wg_barrier();
        if (wv == 7) {
            if (!early) {
                flags_wait(WS_FB + x * STR + lane * 16);
                { const u32x4 v = tagged_wait(WS_MOD + rb * STR + rcol * 8); ga = u32x2{v[0], v[1]}; }
                PIN(ga);
            }
            u32x4 op[8];
            for (unsigned spins = 0;;) {
#pragma unroll
                for (int k = 0; k < 8; ++k) op[k] = LD16(osrc + (k & 3) * 1024, (k >> 2) * 4096);
                unsigned m = 0u;
#pragma unroll
                for (int k = 0; k < 8; ++k) m = mx4(op[k], m);
                if (__all(m != SENT)) break;
                if (++spins > kSpinCap) break;
                asm volatile("" ::: "memory");
            }
            STAMP(13);
            const f32x4 v = ((__builtin_bit_cast(f32x4, op[0]) + __builtin_bit_cast(f32x4, op[1])) + (__builtin_bit_cast(f32x4, op[2]) + __builtin_bit_cast(f32x4, op[3]))) +
                            ((__builtin_bit_cast(f32x4, op[4]) + __builtin_bit_cast(f32x4, op[5])) + (__builtin_bit_cast(f32x4, op[6]) + __builtin_bit_cast(f32x4, op[7])));
            float w[2];
#pragma unroll
            for (int q = 0; q < 2; ++q) {
                auto t = __builtin_amdgcn_permlane32_swap(__float_as_uint(v[q]), __float_as_uint(v[2 + q]), false, false);
                w[q] = __uint_as_float(t[0]) + __uint_as_float(t[1]);
                auto u = __builtin_amdgcn_permlane16_swap(__float_as_uint(w[q]), __float_as_uint(w[q]), false, false);
                w[q] = __uint_as_float(u[0]) + __uint_as_float(u[1]);
                w[q] += dppf<0x128>(w[q]);
            }
            unsigned t = pk2(w[0], w[1]);
            t = pk2(__uint_as_float(ga[0]) * lo16(t), __uint_as_float(ga[1]) * hi16(t));
            t = pk2s(lo16(xg) + lo16(t), hi16(xg) + hi16(t));
            __builtin_amdgcn_raw_buffer_store_b32(t, rw, xdst, 0, 16);
            __builtin_amdgcn_raw_buffer_store_b32(t, rw, xdst2, 0, 16);
            STAMP(14);
        }
        STAMP_FLUSH();
        return;
    }

    const int s = j - 16;
    unsigned long long t01;
    const unsigned long long t_s = __builtin_amdgcn_s_memrealtime();
    if (wv == 0) cnt_load();
    {
        const unsigned ln = (unsigned)(s * 64 + lane);
        if ((PFM & 1) && wv == 0) pf4<NTM & 1>((ln & 511u) * 6144u + (x * 2 + (ln >> 9)) * 128u, p.w_mod, L_SCR);
        if ((PFM & 2) && wv == 1) {
            const unsigned short* w = s < 8 ? p.w_uk : p.w_q;
            pf4<NTM & 1>((ln & 511u) * 8192u + x * 1024u + ((3u - ((unsigned)(size_t)w >> 7)) & 7u) * 128u, w, L_SCR);
        }
        if ((PFM & 48) == 48) {
            const unsigned r0 = (unsigned)(s * 4 + (wv & 3)), row = PFS == 0 ? r0 * 8 + (lane >> 3) : PFS == 1 ? (lane >> 3) * 64 + r0 : (r0 >> 5) * 256 + (lane >> 3) * 32 + (r0 & 31);
            pf4<NTM & 1>(row * 8192u + x * 1024u + (lane & 7u) * 128u, wv < 4 ? p.w_uk : p.w_q, L_SCR);
        }
        else if ((PFM & 16) && wv >= 4) { const unsigned l2 = (unsigned)((s * 4 + (wv & 3)) * 64 + lane); pf4<NTM & 1>((l2 >> 3) * 8192u + x * 1024u + (l2 & 7u) * 128u, p.w_q, L_SCR); }
        else if ((PFM & 32) && wv >= 4) { const unsigned l2 = (unsigned)((s * 4 + (wv & 3)) * 64 + lane); pf4<NTM & 1>((l2 >> 3) * 8192u + x * 1024u + (l2 & 7u) * 128u, p.w_uk, L_SCR); }
        if ((PFM & 4) && (wv & 6) == 2) { const unsigned l2 = (unsigned)((s * 2 + (wv & 1)) * 64 + lane); pf4<NTM & 1>((l2 & 511u) * 8192u + (x * 4 + (l2 >> 9)) * 256u, p.w_q, L_SCR); }
    }
    const int ug = x * 7 + s;
    const unsigned vb = (unsigned)(g * 1024 + ((nl ^ (g << 2)) << 4)), lb = (unsigned)((wv >> 1) * 32768 + (wv & 1) * 4096);
    const char* ck = (const char*)p.ckv + ((size_t)x * SK + s * 256 + wv * 16) * 1024;
    const unsigned rot = ROTC ? (unsigned)s & 3u : 0u;
    const unsigned vbr = vb ^ (rot << 8);
    const char* ck2 = ck + 131072;
    auto ck_dma = [&](unsigned i) { dma16x<NTM & 16>(vbr, ((i & 3u) << 4) | (((i >> 2) & 3u) << 8) | ((i & 3u) << 12), (i >> 4) ? ck2 : ck, lb + ((i >> 2) & 3u) * 8192u + (i & 3u) * 1024u); };
    bf16x8 ka[16];
    s16x4 va[8][4];
    unsigned kad[4], vad[4], vad2[4];
    auto kq = [](unsigned u) { return (0x78u >> (2 * u)) & 3u; };
    auto ka_reads = [&](int cb) {
#pragma unroll
        for (int u = 0; u < 4; ++u) ka[cb * 4 + u] = *LP(bf16x8, kad[u] + cb * 8192);
    };
    auto va_reads = [&]() {
#pragma unroll
        for (int kk = 0; kk < 8; ++kk)
#pragma unroll
            for (int q = 0; q < 4; ++q) va[kk][q] = tr4((kk < 4 ? vad[q] : vad2[q]) + ((kk >> 1) & 1) * 32768 + (kk & 1) * 4096);
    };
    auto va_read1 = [&](int kk) {
#pragma unroll
        for (int q = 0; q < 4; ++q) va[kk][q] = tr4((kk < 4 ? vad[q] : vad2[q]) + ((kk >> 1) & 1) * 32768 + (kk & 1) * 4096);
    };
    auto va_pin = [&]() {
#pragma unroll
        for (int q = 0; q < 8; ++q) PIN4(va[q][0], va[q][1], va[q][2], va[q][3]);
    };
    u32x4 fa = {0u, 0u, 0u, 0u};
    const unsigned fao = (unsigned)(WS_FI + x * STR + (lane & 7) * 16);
    constexpr int LAST = GI + NPOST > 19 ? GI + NPOST : 19;
    const unsigned qsrc = (unsigned)(WS_Q + (wv & 1) * 4 * STR + (x * 4 + (wv >> 1)) * 1024) + (g ^ rot) * 256 + ((nl ^ ((wv * 4) & 15)) << 4);
    auto q_dma = [&](int k) {
        unsigned t;
        asm volatile("s_mov_b32 m0, %3\n\tv_xor_b32 %0, %4, %1\n\tglobal_load_lds_dwordx4 %0, %2 sc1" : "=&v"(t) : "v"(qsrc), "s"(ws), "s"(L_SCR + (wv * 4 + k) * 1024), "i"(k * (STR + 16)) : "memory", "m0");
    };
#pragma unroll
    for (int i = 0; i <= LAST; ++i) {
        ck_dma(i);
        if (i == 0) asm volatile("s_memrealtime %0\n\ts_waitcnt lgkmcnt(0)" : "=s"(t01) :: "memory");
        if (i == 3) {
#pragma unroll
            for (int u = 0; u < 4; ++u) {
                kad[u] = tile_off(wv * 16 + kq(nl >> 2) * 4 + (nl & 3), 4 * u + g);
                vad[u] = tile_off(8 * (lane >> 5) + 4 * (u & 1) + (nl >> 2), wv * 8 + 4 * (u >> 1) + 2 * (g & 1) + ((lane & 3) >> 1)) + 8 * (lane & 1);
                vad2[u] = vad[u] + 65536;
                PIN(kad[u]); PIN(vad[u]); PIN(vad2[u]);
            }
        }
        if (i > GI && i < GI + NPOST) { if (i < GI + 4) VMW(8); } else if (i == GI + NPOST) VMW(NPOST); else if (i < NTH) { if (i >= W0) VMW(W0); } else if (i >= 4) VMW(4);
        if (i == 15 && wv == 0) {
            PIN(fa);
            tag_take();
            if (!__all(flags_ok(fa))) flags_wait(fao);
        }
        if ((i & 3) == 3 && i >= 7 && i <= 19) {
            const int cb = (i - 7) >> 2;
            if (cb > 0) { PIN4(ka[cb * 4 - 4], ka[cb * 4 - 3], ka[cb * 4 - 2], ka[cb * 4 - 1]); va_pin(); }
            wg_barrier();
            ka_reads(cb);
            if ((wv >> 1) == cb) va_reads();
            STAMP(16 + cb);
        }
        if (TLBW && i == 12) {
            if (wv == 1 && lane < 32) pf4s((unsigned)(WS_PART + x * 1048576 + lane * 32768 + s * 128), ws, L_SCR + 1024);
            if (wv == 2 && lane < 28 && s >= 7) pf4s((unsigned)(lane * 65536 + x * 128), p.w2, L_SCR + 1024);
            if (wv == 3 && lane < 28 && (s < 7 || s >= 11)) pf4s((unsigned)(lane * 65536 + x * 128), p.w1, L_SCR + 1024);
            if (wv == 4 && lane < 1 && s >= 7 && s < 11) pf4s(0u, p.out, L_SCR + 1024);
        }
        if (i == 10 && wv == 0) asm volatile("buffer_load_dwordx4 %0, %1, %2, 0 offen sc1" : "+v"(fa) : "v"(fao), "s"(rw) : "memory");
        if (i == GI && TG > 0) { while (__builtin_amdgcn_s_memrealtime() < t_s + TG) __builtin_amdgcn_s_sleep(1); }
        if (i == GI && WG < 4) VMW(WG);
        if (i == GI) { q_dma(0); q_dma(1); q_dma(2); q_dma(3); }
    }
    const unsigned L_EXM = L_SCR - 1024, L_BIG = L_SCR + 512 + 128;
    unsigned nx = LAST + 1, npm = 0u;
    unsigned qad[4];
    {
        unsigned qck = L_SCR + wv * 4096 + lane * 16;
        u32x4 qb[4];
#pragma unroll
        for (int k = 0; k < 4; ++k) qb[k] = *LP(u32x4, qck + k * 1024);
#pragma unroll
        for (int k = 0; k < 4; ++k) { qad[k] = L_SCR + nl * 1024 + (((4 * k + g) ^ nl) << 4); PIN(qad[k]); }
        STAMP(20);
        if (!__all(mx4(qb[0], mx4(qb[1], mx4(qb[2], mx4(qb[3], 0u)))) != SENT)) {
            for (unsigned spins = 0;;) {
#pragma unroll
                for (int k = 0; k < 4; ++k)
                    if (!__all(mx4(qb[k], 0u) != SENT)) q_dma(k);
                if (nx <= 25) {
#pragma unroll
                    for (int i = 0; i < 3; ++i) ck_dma(nx + i);
                    nx += 3;
                    VMW(3);
                } else {
                    for (; nx < 28; ++nx) ck_dma(nx);
                    VMW(0);
                }
#pragma unroll
                for (int k = 0; k < 4; ++k) qb[k] = *LP(u32x4, qck + k * 1024);
                if (__all(mx4(qb[0], mx4(qb[1], mx4(qb[2], mx4(qb[3], 0u)))) != SENT)) break;
                if (++spins > kSpinCap) break;
            }
        }
    }
    constexpr int NE = LAST + 1 + SL0 + 4 < 31 ? LAST + 1 + SL0 + 4 : 31;
    auto slot = [&](int i) { if (i < 28) { if (nx <= (unsigned)i) ck_dma(i); } else ck_dma(i); };
    const unsigned kg = kq(g);
    unsigned exw = L_EXM + ((nl + (lane >> 5) * 16) * 8 + wv) * 4, exr = L_EXM + nl * 32;
    unsigned pwr = L_SCR + nl * 1024 + 768 + (((2 * wv + (kg >> 1)) ^ nl) << 4) + (kg & 1) * 8, esw = L_SCR + nl * 1024 + 512 + ((wv ^ (nl & 7)) << 4) + g * 4;
    unsigned prd = L_SCR + (lane & 31) * 1024 + 768 + (((lane >> 5) ^ nl) << 4);
    PIN4(exw, exr, pwr, esw); PIN(prd);
    PIN4(ka[12], ka[13], ka[14], ka[15]); va_pin();
    wg_barrier();
    STAMP(21);
    bf16x8 qr[8][2];
    auto q_ld = [&](int ks, int hg) { return *LP(bf16x8, qad[ks & 3] + (ks >> 2) * 256 + hg * 16384); };
    auto p_ld = [&](int kk) { return *LP(bf16x8, prd ^ (kk << 5)); };
    float ls0, ls1, m0, m1;
    f32x4 sa, sb;
    f32x16 o[2];
    unsigned sw[4], srd, p0, mlo, esr, fbo;
    u32x2 tg;
    u32x4 nq;
    auto probs = [&](float (&e)[8]) {
#pragma unroll
        for (int r = 0; r < 4; ++r) { e[r] = __builtin_amdgcn_exp2f(fmaf(sa[r], SCL, -m0)); e[4 + r] = __builtin_amdgcn_exp2f(fmaf(sb[r], SCL, -m1)); }
        *LP(u32x2, pwr) = u32x2{pk2(e[0], e[1]), pk2(e[2], e[3])};
        *LP(u32x2, pwr + 16384) = u32x2{pk2(e[4], e[5]), pk2(e[6], e[7])};
    };
    auto wave_max = [&]() {
        const float a = fmaxf(fmaxf(sa[0], sa[1]), fmaxf(sa[2], sa[3])), b = fmaxf(fmaxf(sb[0], sb[1]), fmaxf(sb[2], sb[3]));
        auto u = __builtin_amdgcn_permlane32_swap(__float_as_uint(a), __float_as_uint(b), false, false);
        const float c = fmax3(__uint_as_float(u[0]), __uint_as_float(u[1]), __uint_as_float(u[1]));
        auto v = __builtin_amdgcn_permlane16_swap(__float_as_uint(c), __float_as_uint(c), false, false);
        return fmax3(__uint_as_float(v[0]), __uint_as_float(v[1]), __uint_as_float(v[1]));
    };
    auto max8 = [&](unsigned a) {
        const f32x4 a0 = *LP(f32x4, a), a1 = *LP(f32x4, a + 16);
        return fmax3(fmax3(a0[0], a0[1], a0[2]), fmax3(a0[3], a1[0], a1[1]), fmax3(a1[2], a1[3], a1[3])) * SCL;
    };
    {
        bf16x8 qs[8][2];
#pragma unroll
        for (int ks = 0; ks < 8; ++ks) { qr[ks][0] = q_ld(ks + 8, 0); qr[ks][1] = q_ld(ks + 8, 1); }
        __builtin_amdgcn_sched_barrier(0);
#pragma unroll
        for (int i = LAST + 1; i < LAST + 1 + SL0; ++i) if (i < NE) slot(i);
        sa = f32x4{0.f, 0.f, 0.f, 0.f}; sb = sa;
#pragma unroll
        for (int n = 0; n < 16; ++n) {
            const int ks = (n + 8) & 15;
            sa = MFMA16(ka[ks], ks < 8 ? qs[ks][0] : qr[ks - 8][0], sa);
            sb = MFMA16(ka[ks], ks < 8 ? qs[ks][1] : qr[ks - 8][1], sb);
            if (n >= 8 - QD && n < 16 - QD) { qs[n + QD - 8][0] = q_ld(n + QD - 8, 0); qs[n + QD - 8][1] = q_ld(n + QD - 8, 1); }
            if ((n & 3) == 3 && LAST + 1 + SL0 + (n >> 2) < NE) slot(LAST + 1 + SL0 + (n >> 2));
            __builtin_amdgcn_sched_barrier(0);
        }
        STAMP(1);
        *LP(float, exw) = wave_max();
        wg_barrier();
        m0 = max8(exr); m1 = max8(exr + 512);
        VMW(NE - 24);
        ka_reads(0); ka_reads(1);
#pragma unroll
        for (int ks = 0; ks < QE; ++ks) { qs[ks][0] = q_ld(ks, 0); qs[ks][1] = q_ld(ks, 1); }
        __builtin_amdgcn_sched_barrier(0);
        float e[8];
        probs(e);
        ls0 = (e[0] + e[1]) + (e[2] + e[3]); ls1 = (e[4] + e[5]) + (e[6] + e[7]);
        PIN(ls0); PIN(ls1);
        if (wv == 0) *LP(u32x4, L_BIG) = u32x4{0u, 0u, tlo, thi};
        VMW(NE - 28);
        wg_barrier();
        STAMP(22);
        static_assert(NE >= 28, "");
        bf16x8 pf[8];
        tg = *LP(u32x2, L_BIG + 8);
#pragma unroll
        for (int i = NE; i < 32; ++i) slot(i);
        sa = f32x4{0.f, 0.f, 0.f, 0.f}; sb = sa;
        __builtin_amdgcn_sched_barrier(0);
#pragma unroll
        for (int ks = 0; ks < 8; ++ks) {
            sa = MFMA16(ka[ks], qs[ks][0], sa); sb = MFMA16(ka[ks], qs[ks][1], sb);
            if (ks + QE < 8) { qs[ks + QE][0] = q_ld(ks + QE, 0); qs[ks + QE][1] = q_ld(ks + QE, 1); }
            else pf[ks + QE - 8] = p_ld(ks + QE - 8);
            __builtin_amdgcn_sched_barrier(0);
        }
#pragma unroll
        for (int i = 0; i < 16; ++i) { o[0][i] = 0.f; o[1][i] = 0.f; }
        STAMP(2);
#pragma unroll
        for (int kk = 0; kk < 4; ++kk) {
            if (kk == 0) {
#pragma unroll
                for (int q = QE; q < 8; ++q) pf[q] = p_ld(q);
            }
            if (kk == 1) ka_reads(2);
            o[0] = MFMA32(cat8(va[kk][0], va[kk][1]), pf[kk], o[0]);
            o[1] = MFMA32(cat8(va[kk][2], va[kk][3]), pf[kk], o[1]);
            __builtin_amdgcn_sched_barrier(0);
        }
        PIN4(pf[4], pf[5], pf[6], pf[7]); PIN4(ka[8], ka[9], ka[10], ka[11]); PIN(tg);
        STAMP(3);
#pragma unroll
        for (int kk = 4; kk < 8; ++kk) {
            if (wv < 6) { va_read1(kk - 4); if (kk > 4) va_read1(kk - 1); }
            sa = MFMA16(ka[kk + 4], qr[kk - 4][0], sa); sb = MFMA16(ka[kk + 4], qr[kk - 4][1], sb);
            o[0] = MFMA32(cat8(va[kk][0], va[kk][1]), pf[kk], o[0]);
            o[1] = MFMA32(cat8(va[kk][2], va[kk][3]), pf[kk], o[1]);
            __builtin_amdgcn_sched_barrier(0);
        }
        if (wv < 6) va_read1(7);
        STAMP(5);
        tlo = __builtin_amdgcn_readfirstlane(tg[0]); thi = __builtin_amdgcn_readfirstlane(tg[1]);
        rederive();
        fbo = (unsigned)(WS_FB + x * STR + lane * 16);
        asm volatile("buffer_load_dwordx4 %0, off, %1, %2 sc1" : "=v"(nq) : "s"(rw), "s"(WS_NM + x * STR) : "memory");
        const unsigned head = lane & 31, r8 = lane >> 3, cw = wv ^ (rot * 2);
        srd = lb + r8 * 128 + (((lane & 7) ^ r8) << 4);
        sw[0] = lb + head * 128 + (((lane >> 5) ^ (head & 7)) << 4);
        sw[1] = sw[0] ^ 32u; sw[2] = sw[0] ^ 64u; sw[3] = sw[0] ^ 96u;
        p0 = (unsigned)(WS_PART + x * 1048576) + (cw >> 2) * 16384 + (cw & 3) * 2048 + r8 * 32768 + ((s ^ r8) * 8 + (lane & 7)) * 16;
        PIN4(sw[0], sw[1], sw[2], sw[3]); PIN(srd); PIN(p0); PIN(fbo);
    }
    STAMP(23);
    unsigned po[4];
    float mrun;
    {
        VMW(1);
        STAMP(24);
        ka_reads(3);
        asm volatile("" : "=v"(fa));
        if (wv == 0 && s < 11) asm volatile("buffer_load_dwordx4 %0, %1, %2, 0 offen sc1" : "+v"(fa) : "v"(fbo), "s"(rw) : "memory");
        __builtin_amdgcn_sched_barrier(0);
#pragma unroll
        for (int ks = 12; ks < 16; ++ks) { sa = MFMA16(ka[ks], qr[ks - 8][0], sa); sb = MFMA16(ka[ks], qr[ks - 8][1], sb); }
        bf16x8 pf[8];
#pragma unroll 1
        for (bool first = true;; first = false) {
            float e[8];
            probs(e);
            const float n0 = (e[0] + e[1]) + (e[2] + e[3]), n1 = (e[4] + e[5]) + (e[6] + e[7]);
            *LP(float, esw) = ls0 + n0; *LP(float, esw + 16384) = ls1 + n1;
            if (__builtin_expect(!(n0 + n1 <= BIGV), 0)) *LP(unsigned, L_BIG) = 1u;
            va_pin();
            wg_barrier();
            STAMP(25);
            PIN(prd);
            const unsigned big = *LP(unsigned, L_BIG);
            if ((wv >> 1) == 3) va_reads();
#pragma unroll
            for (int kk = 0; kk < 8; ++kk) { pf[kk] = p_ld(kk); __builtin_amdgcn_sched_barrier(0); }
            if (__builtin_expect(__builtin_amdgcn_readfirstlane(big) == 0 || !first, 1)) break;
            const unsigned ex2 = L_SCR + 512 + 160;
            PIN(sa); PIN(sb);
            rederive();
            *LP(float, ex2 + (nl + (lane >> 5) * 16) * 1024 + wv * 4) = wave_max();
            wg_barrier();
            const float x0 = fmaxf(max8(ex2 + nl * 1024), m0), x1 = fmaxf(max8(ex2 + nl * 1024 + 16384), m1);
            const float al0 = __builtin_amdgcn_exp2f(m0 - x0), al1 = __builtin_amdgcn_exp2f(m1 - x1), al = (lane & 16) ? al1 : al0;
            m0 = x0; m1 = x1; ls0 *= al0; ls1 *= al1;
            o[0] *= al; o[1] *= al;
        }
        mrun = (lane & 16) ? m1 : m0;
        auto stage = [&](int t2, int gp) {
            const unsigned lo0 = pk2(o[t2][8 * gp + 0], o[t2][8 * gp + 1]), lo1 = pk2(o[t2][8 * gp + 2], o[t2][8 * gp + 3]);
            const unsigned hi0 = pk2(o[t2][8 * gp + 4], o[t2][8 * gp + 5]), hi1 = pk2(o[t2][8 * gp + 6], o[t2][8 * gp + 7]);
            auto r0 = __builtin_amdgcn_permlane32_swap(lo0, hi0, false, false);
            auto r1 = __builtin_amdgcn_permlane32_swap(lo1, hi1, false, false);
            *LP(u32x4, sw[t2 * 2 + gp]) = u32x4{r0[0], r1[0], r0[1], r1[1]};
        };
#pragma unroll
        for (int kk = 0; kk < 8; ++kk) {
            o[0] = MFMA32(cat8(va[kk][0], va[kk][1]), pf[kk], o[0]);
            if (kk == 1) {
                asm volatile("s_waitcnt vmcnt(0)" : "+v"(nq) :: "memory");
                npm = __builtin_amdgcn_readfirstlane(nq[0]);
                if ((unsigned)__builtin_amdgcn_readfirstlane(nq[2]) != tlo || (unsigned)__builtin_amdgcn_readfirstlane(nq[3]) != thi) npm = __builtin_amdgcn_readfirstlane(tagged_wait(WS_NM + x * STR)[0]);
            }
            if (kk == 3 || kk == 5) {
                const unsigned v = (npm ^ ((x & 2) ? 0xffu : 0u)) >> (lane >> 5);
#pragma unroll
                for (int i = kk - 3; i < kk - 1; ++i) { po[i] = p0 + ((v >> (2 * i)) & 1u) * 8192u; PIN(po[i]); }
            }
            __builtin_amdgcn_sched_barrier(0);
        }
#pragma unroll
        for (int kk = 0; kk < 8; ++kk) {
            o[1] = MFMA32(cat8(va[kk][2], va[kk][3]), pf[kk], o[1]);
            if (kk == 2) { PIN(o[0]); stage(0, 0); }
            if (kk == 4) stage(0, 1);
            __builtin_amdgcn_sched_barrier(0);
        }
        stage(1, 0); stage(1, 1);
        asm volatile("s_waitcnt lgkmcnt(0)" ::: "memory");
        u32x4 w[4];
#pragma unroll
        for (int i = 0; i < 4; ++i) w[i] = *LP(u32x4, srd + i * 1024);
#pragma unroll
        for (int i = 0; i < 4; ++i) ST16(w[i], po[i], i * 262144);
        __builtin_amdgcn_sched_barrier(0);
        STAMP(26);
        u32x4 mlrec;
        if (wv == 0) {
            const unsigned head = lane & 31;
            esr = L_SCR + head * 1024 + 512 + ((head & 7) << 4);
            mlo = (unsigned)(WS_ML + x * STR + s * 512 + head * 16) + ((npm >> ((head >> 3) * 2)) & 1u) * 8192u;
            PIN(mlo);
            f32x4 b8[8];
#pragma unroll
            for (int q = 0; q < 8; ++q) b8[q] = *LP(f32x4, esr ^ (q << 4));
            const f32x4 bs = ((b8[0] + b8[1]) + (b8[2] + b8[3])) + ((b8[4] + b8[5]) + (b8[6] + b8[7]));
            mlrec = u32x4{__float_as_uint(mrun), __float_as_uint((bs[0] + bs[1]) + (bs[2] + bs[3])), tlo, thi};
            PIN(mlrec);
        }
        VMW(0);
        wg_barrier();
        if (wv == 0) {
            if (lane < 32) ST16(mlrec, mlo, 0);
            PIN(fa);
            if (s < 11 && !__all(flags_ok(fa))) flags_wait(fbo);
        }
        STAMP(27);
        wg_barrier();
    }
    const unsigned long long t_att = __builtin_amdgcn_s_memrealtime();
    rederive();
    while (__builtin_amdgcn_s_memrealtime() < t_att + (s >= 11 ? MD0 : MD1)) __builtin_amdgcn_s_sleep(2);
    if (s >= 11) {
        const unsigned n = (unsigned)((s - 11) * 8 + wv);
        if (n < 28) pf4<NTM & 32>((n * 64 + lane) * 1024u + x * 128u, p.w2, 0);
        { const unsigned l2 = n * 64 + lane; pf4<NTM & 32>((l2 / 5u) * (DFF * 2) + min(((unsigned)x * 448u & ~127u) + (l2 % 5u) * 128u, DFF * 2 - 4u), p.w1, 0); }
        if (wv == 0) flag_put(WS_FA, id - 128);
        STAMP_FLUSH();
        return;
    }
    const unsigned L_H2S = L_SCR, L_REDM = L_SCR + 16384;
    if (s < 7) {
        {
            const char* w1s = (const char*)(p.w1 + (size_t)wv * 64 * DFF + ug * 32);
#pragma unroll
            for (int i = 0; i < 4; ++i) dma16<NTM & 32>((unsigned)((lane >> 2) * DFF * 2 + (lane & 3) * 16), w1s + (size_t)i * 16 * DFF * 2, wv * 4096 + i * 1024);
        }
        const int b = wv;
        if (wv == 0) ST16(s4, (unsigned)(WS_U + x * STR + (lane >> 5) * 8192 + s * 512 + (lane & 31) * 16), 0);
        const u32x4 shf = LD16((unsigned)(WS_MB + b * STR + lane * 16), 0), scl = LD16((unsigned)(WS_MB + b * STR + 1024 + lane * 16), 0);
        VMW(0);
        if (wv == 0) flag_put(WS_FA, id - 128);
        wg_barrier();
        bf16x8 wk0[8], wk1[8];
        if (wv >= 6) {
#pragma unroll
            for (int q = 0; q < 8; ++q) {
                const unsigned a = (unsigned)(q * 4096 + (g * 8 + (nl >> 2)) * 64 + (wv & 1) * 32 + (lane & 3) * 8);
                wk0[q] = tr_frag(a, a + 256); wk1[q] = tr_frag(a + 2048, a + 2304);
            }
#pragma unroll
            for (int q = 0; q < 8; q += 2) PIN4(wk0[q], wk0[q + 1], wk1[q], wk1[q + 1]);
        }
        float scv[8], shv[8];
#pragma unroll
        for (int q = 0; q < 4; ++q) {
            scv[2 * q] = rbf(1.f + lo16(scl[q])); scv[2 * q + 1] = rbf(1.f + hi16(scl[q])); shv[2 * q] = lo16(shf[q]); shv[2 * q + 1] = hi16(shf[q]);
            PIN4(scv[2 * q], scv[2 * q + 1], shv[2 * q], shv[2 * q + 1]);
        }
        unsigned har = L_H2S + ((lane & 7) * HS_LD + g * 8 + (nl >> 3) * 32) * 2, haw = L_H2S + (b * HS_LD + lane * 8) * 2;
        unsigned udst = (unsigned)(WS_U + x * STR + (nl >> 3) * 8192 + (s * 2 + (wv & 1)) * 256 + ((nl & 7) * 16 + g * 4) * 2), xsrc = (unsigned)(WS_X1 + x * STR + (b * 512 + lane * 8) * 2) + ((npm >> x) & 1u) * 8192u;
        PIN4(har, haw, udst, xsrc);
        STAMP(28);
        while (__builtin_amdgcn_s_memrealtime() < t_att + XGATE) __builtin_amdgcn_s_sleep(2);
        u32x4 xv;
        for (unsigned spins = 0;;) {
            xv = LD16(xsrc, 0);
            if (__all(mx4(xv, 0u) != SENT)) break;
            __builtin_amdgcn_s_sleep(SLPX);
            if (++spins > kSpinCap) break;
            asm volatile("" ::: "memory");
        }
        STAMP(29);
        {
            f32x2_t v[4];
#pragma unroll
            for (int q = 0; q < 4; ++q) v[q] = f32x2_t{lo16(xv[q]), hi16(xv[q])};
            const f32x2_t p1 = (v[0] + v[1]) + (v[2] + v[3]), p2 = (v[0] * v[0] + v[1] * v[1]) + (v[2] * v[2] + v[3] * v[3]);
            float mb, rden;
            ln_stats(p1[0] + p1[1], p2[0] + p2[1], mb, rden);
            u32x4 hr;
#pragma unroll
            for (int q = 0; q < 4; ++q) {
                const f32x2_t h = ((v[q] - mb) * rden) * f32x2_t{scv[2 * q], scv[2 * q + 1]} + f32x2_t{shv[2 * q], shv[2 * q + 1]};
                hr[q] = pk2(h[0], h[1]);
            }
            *LP(u32x4, haw) = hr;
        }
        wg_barrier();
        if (wv >= 6) {
            bf16x8 af[8];
#pragma unroll
            for (int q = 0; q < 8; ++q) af[q] = *LP(bf16x8, har + q * 128);
            __builtin_amdgcn_sched_barrier(0);
            f32x4 d0 = {0.f, 0.f, 0.f, 0.f}, d1 = {0.f, 0.f, 0.f, 0.f};
#pragma unroll
            for (int q = 0; q < 8; ++q) { d0 = MFMA16(wk0[q], af[q], d0); d1 = MFMA16(wk1[q], af[q], d1); }
            const f32x4 own = nl < 8 ? d0 : d1;
#pragma unroll
            for (int e = 0; e < 4; ++e) { float v = own[e] + dppf<0x128>(own[e]); asm("" : "+v"(v)); d0[e] = fmaxf(v, 0.f); }
            u32x2 uv = {pk2(d0[0], d0[1]), pk2(d0[2], d0[3])};
            if (__builtin_expect(max(uv[0], uv[1]) == SENT, 0)) uv = u32x2{pk2s(d0[0], d0[1]), pk2s(d0[2], d0[3])};
            __builtin_amdgcn_raw_buffer_store_b64(uv, rw, udst, 0, 16);
            STAMP(30);
        }
        STAMP_FLUSH();
        return;
    }
    {
        const int n0 = (x * 4 + (s - 7)) * 16;
        const char* mws = (const char*)(p.w2 + (size_t)wv * 224 * 512 + n0);
#pragma unroll
        for (int i = 0; i < 7; ++i) dma16<NTM & 32>((unsigned)((lane >> 1) * 1024 + (lane & 1) * 16), mws + i * 32768, rgn + i * 1024);
        const int fb = lane >> 3, fc = (lane & 7) * 2;
        u32x2 gb = {0u, 0u};
        if (wv == 0) {
            flag_put(WS_FA, id - 128);
            { const u32x4 v = tagged_wait(WS_MOD + fb * STR + (1536 + n0 + fc) * 8); gb = u32x2{v[0], v[1]}; }
            flags_wait(WS_FA + x * STR + lane * 16);
            if (id == 23 * 8 && lane == 0) __builtin_amdgcn_raw_buffer_store_b64(u32x2{tlo, thi}, rw, WS_EPOCH, 0, 16);
            PIN(gb);
        }
        VMW(0);
        bf16x8 wfr[7];
        const unsigned trl = (unsigned)(g * 8 + (nl >> 2)) * 32u + (unsigned)(lane & 3) * 8u;
#pragma unroll
        for (int ks = 0; ks < 7; ++ks) { wfr[ks] = tr_frag(rgn + ks * 1024 + trl, rgn + ks * 1024 + trl + 128); PIN(wfr[ks]); }
        unsigned usrc = (unsigned)(WS_U + wv * STR + (g >> 1) * 256 + (nl & 7) * 32 + (g & 1) * 16) + ((npm >> x) & 1u) * 8192u;
        unsigned rdw = L_REDM + wv * 512 + (nl & 7) * 64 + g * 16, rdr = L_REDM + lane * 8;
        unsigned short* outp = p.out + fb * 512 + n0 + fc;
        PIN(usrc); PIN(rdw); PIN(rdr); PIN(outp);
        STAMP(28);
        unsigned xg = 0u;
        if (wv == 0) {
            while (__builtin_amdgcn_s_memrealtime() < t_att + XGATE) __builtin_amdgcn_s_sleep(2);
            for (unsigned spins = 0;;) {
                xg = __builtin_amdgcn_raw_buffer_load_b32(rw, (unsigned)(WS_X1 + x * STR + (fb * 512 + n0 + fc) * 2) + ((npm >> x) & 1u) * 8192u, 0, 16);
                if (__all(xg != SENT)) break;
                __builtin_amdgcn_s_sleep(SLPD);
                if (++spins > kSpinCap) break;
                asm volatile("" ::: "memory");
            }
            __builtin_amdgcn_s_sleep(SLPG);
        }
        wg_barrier();
        u32x4 uf[7];
        for (unsigned spins = 0;;) {
#pragma unroll
            for (int ks = 0; ks < 7; ++ks) uf[ks] = LD16(usrc + ks * 512, 0);
            unsigned m = 0u;
#pragma unroll
            for (int ks = 0; ks < 7; ++ks) m = mx4(uf[ks], m);
            if (__all(m != SENT)) break;
            if (++spins > kSpinCap) break;
            asm volatile("" ::: "memory");
        }
        STAMP(29);
        f32x4 d = {0.f, 0.f, 0.f, 0.f}, d2 = {0.f, 0.f, 0.f, 0.f};
#pragma unroll
        for (int ks = 0; ks < 7; ++ks) {
            if (ks & 1) d2 = MFMA16(wfr[ks], BF8(uf[ks]), d2);
            else d = MFMA16(wfr[ks], BF8(uf[ks]), d);
        }
        *LP(f32x4, rdw) = d + d2;
        wg_barrier();
        if (wv == 0) {
            f32x2_t c[8];
#pragma unroll
            for (int w2 = 0; w2 < 8; ++w2) c[w2] = *LP(f32x2_t, rdr + w2 * 512);
            const f32x2_t sm = ((c[0] + c[1]) + (c[2] + c[3])) + ((c[4] + c[5]) + (c[6] + c[7]));
            unsigned t = pk2(sm[0], sm[1]);
            t = pk2(__uint_as_float(gb[0]) * lo16(t), __uint_as_float(gb[1]) * hi16(t));
            *(GAS unsigned*)outp = pk2(lo16(xg) + lo16(t), hi16(xg) + hi16(t));
            { const unsigned long long r1_ = __builtin_amdgcn_s_memrealtime(); const unsigned dl = (unsigned)(t01 - t00) * 16u;
              while (__builtin_amdgcn_s_memrealtime() < r1_ + dl) __builtin_amdgcn_s_sleep(1); }
        }
        STAMP_FLUSH();
    }
}

extern "C" void kernel_launch(void* const* d_in, const int* in_sizes, int n_in,
                              void* d_out, int out_size, void* d_ws, size_t ws_size,
                              hipStream_t stream) {
    Params p;
    p.x = (const unsigned short*)d_in[0];
    p.ckv = (const unsigned short*)d_in[1];
    p.w_q = (const unsigned short*)d_in[2];
    p.w_uk = (const unsigned short*)d_in[3];
    p.w_uv = (const unsigned short*)d_in[4];
    p.w_o = (const unsigned short*)d_in[5];
    p.w1 = (const unsigned short*)d_in[7];
    p.w2 = (const unsigned short*)d_in[8];
    p.c = (const unsigned short*)d_in[9];
    p.w_mod = (const unsigned short*)d_in[10];
    p.ws = (char*)(((size_t)d_ws + 2097151) & ~(size_t)2097151);
    p.out = (unsigned short*)d_out;
    (void)hipFuncSetAttribute((const void*)k_main, hipFuncAttributeMaxDynamicSharedMemorySize, 163840);
    k_main<<<256, 512, 163840, stream>>>(p);
}
```

```cpp
#define TG 500
#include <hip/hip_runtime.h>
#include <hip/hip_bf16.h>
#include <stdint.h>

#define NH 32
#define SK 4096
#define DFF 1792
#define GI 20
#define ROTC 1
#define PFS 2
#define XNO 0
#define CPJ 7
#define NTM 16
#define QHELP 4
#define HP 13
#define NTH 0
#define W0 2
#define SL0 1
#define LMPF 0
#define SLPB 20
#define MD0 0
#define MD1 0
#define NEARP 1
#define NCAL 2
#define TLBW 2
#define JPERM 1
#define NPOST 4
#define XS3 1
#define WG 2
#define NP2 2
#define QKM 1
#define HILO 0
#define YSEL 1
#define TWD 680
#define LMD 250
#define PFM 57
#define CPFE 35500
#define SLPM 0
#define SLPD 12
#define SLPG 8
#define SLPX 1
#define SLPH 4

using u16x8 = __attribute__((ext_vector_type(8))) unsigned short;
using s16x4 = __attribute__((ext_vector_type(4))) short;
using bf16x8 = __attribute__((ext_vector_type(8))) short;
using f32x16 = __attribute__((ext_vector_type(16))) float;
using f32x4 = __attribute__((ext_vector_type(4))) float;
using u32x4 = __attribute__((ext_vector_type(4))) unsigned;
using u32x2 = __attribute__((ext_vector_type(2))) unsigned;

#define GAS __attribute__((address_space(1)))
#define LAS __attribute__((address_space(3)))
typedef GAS unsigned long long gu64;
#define RLX_AGENT __ATOMIC_RELAXED, __HIP_MEMORY_SCOPE_AGENT
#define SENT 0xffffffffu
#define TAGC 0x9e3779b9u

__device__ __forceinline__ float bf2f(unsigned short u) { return __uint_as_float(((unsigned)u) << 16); }
typedef __attribute__((ext_vector_type(2))) __bf16 bf16x2_t;
typedef __attribute__((ext_vector_type(2))) float f32x2_t;
__device__ __forceinline__ unsigned pk2(float a, float b) {
    f32x2_t v = {a, b};
    bf16x2_t r = __builtin_convertvector(v, bf16x2_t);
    return __builtin_bit_cast(unsigned, r);
}
__device__ __forceinline__ unsigned nosent(unsigned r) { return min(r, SENT - 1u); }
__device__ __forceinline__ unsigned pk2s(float a, float b) { return nosent(pk2(a, b)); }
__device__ __forceinline__ unsigned short f2bf(float f) { return (unsigned short)(pk2(f, 0.f) & 0xffffu); }
__device__ __forceinline__ float rbf(float f) { return __uint_as_float(pk2(f, 0.f) << 16); }
__device__ __forceinline__ float lo16(unsigned u) { return __uint_as_float(u << 16); }
__device__ __forceinline__ float hi16(unsigned u) { return __uint_as_float(u & 0xffff0000u); }
__device__ __forceinline__ unsigned umax3(unsigned a, unsigned b, unsigned c) { return max(max(a, b), c); }
__device__ __forceinline__ unsigned mx4(const u32x4& v, unsigned m) { return umax3(v[2], v[3], umax3(v[0], v[1], m)); }
__device__ __forceinline__ float fmax3(float a, float b, float c) { float r; asm("v_max3_f32 %0, %1, %2, %3" : "=v"(r) : "v"(a), "v"(b), "v"(c)); return r; }
template <int C> __device__ __forceinline__ float dppf(float v) { return __int_as_float(__builtin_amdgcn_update_dpp(0, __float_as_int(v), C, 0xf, 0xf, true)); }
__device__ __forceinline__ float sum16(float v) { v += dppf<0xB1>(v); v += dppf<0x4E>(v); v += dppf<0x141>(v); v += dppf<0x140>(v); return v; }

__device__ __forceinline__ void ln_stats(float p1, float p2, float& mb, float& rden) {
    auto t = __builtin_amdgcn_permlane32_swap(__float_as_uint(p1), __float_as_uint(p2), false, false);
    float a = sum16(__uint_as_float(t[0]) + __uint_as_float(t[1]));
    auto t2 = __builtin_amdgcn_permlane16_swap(__float_as_uint(a), __float_as_uint(a), false, false);
    a = (__uint_as_float(t2[0]) + __uint_as_float(t2[1])) * (1.f / 512.f);
    auto t3 = __builtin_amdgcn_permlane32_swap(__float_as_uint(a), __float_as_uint(a), false, false);
    const float m = __uint_as_float(t3[0]);
    const float var = rbf(fmaxf(fmaf(-m, m, __uint_as_float(t3[1])), 0.f));
    mb = rbf(m);
    rden = __builtin_amdgcn_rcpf(rbf(__builtin_amdgcn_sqrtf(rbf(var + 1e-5f))));
}
#define STR 262144
#define WS_Q 0
#define WS_H 32768
#define WS_X1 65536
#define WS_ML 81920
#define WS_MOD 98304
#define WS_MB 139264
#define WS_U 114688
#define WS_FI 131072
#define WS_FA 132096
#define WS_FB 133120
#define WS_NP 134144
#define WS_NM 134400
#define WS_EPOCH 135168
#define WS_CAL 147456
#define WS_OUTP 2097152
#define WS_PART 4194304
#define WS_END 12582912

#define LP(T, off) ((LAS T*)(size_t)(unsigned)(off))
#define STAMP(k) do {} while (0)
#define STAMP_FLUSH() do {} while (0)
__device__ __forceinline__ s16x4 tr4(unsigned a) { return __builtin_amdgcn_ds_read_tr16_b64_v4i16(LP(s16x4, a)); }
__device__ __forceinline__ bf16x8 cat8(s16x4 a, s16x4 b) { return __builtin_shufflevector(a, b, 0, 1, 2, 3, 4, 5, 6, 7); }
__device__ __forceinline__ bf16x8 tr_frag(unsigned a0, unsigned a1) { return cat8(tr4(a0), tr4(a1)); }
__device__ __forceinline__ unsigned swz(unsigned row) { return ((row & 3) << 2) | ((row >> 2) & 3); }
__device__ __forceinline__ unsigned off_b(unsigned row, unsigned ch) { return 256u * row + 16u * (ch ^ swz(row)); }
__device__ __forceinline__ unsigned tile_off(unsigned key, unsigned chunk) { return ((key >> 5) * 4 + (chunk >> 4)) * 8192u + off_b(key & 31, chunk & 15); }
__device__ __forceinline__ void wg_barrier() { asm volatile("s_waitcnt lgkmcnt(0)\n\ts_barrier" ::: "memory"); }
template <int NT> __device__ __forceinline__ void dma16(unsigned voff, const void* sbase, unsigned lds_dst) {
    if (NT) asm volatile("s_mov_b32 m0, %2\n\ts_nop 0\n\tglobal_load_lds_dwordx4 %0, %1 nt" :: "v"(voff), "s"(sbase), "s"(lds_dst) : "memory", "m0");
    else asm volatile("s_mov_b32 m0, %2\n\ts_nop 0\n\tglobal_load_lds_dwordx4 %0, %1" :: "v"(voff), "s"(sbase), "s"(lds_dst) : "memory", "m0");
}
template <int NT> __device__ __forceinline__ void dma16x(unsigned voff, unsigned vx, const void* sbase, unsigned lds_dst) {
    unsigned t;
#define DMAX(pol, c) asm volatile("s_mov_b32 m0, %3\n\tv_xor_b32 %0, %4, %1\n\tglobal_load_lds_dwordx4 %0, %2" pol : "=&v"(t) : "v"(voff), "s"(sbase), "s"(lds_dst), c(vx) : "memory", "m0")
    if (__builtin_constant_p(vx)) { if (NT) DMAX(" nt", "i"); else DMAX("", "i"); }
    else { if (NT) DMAX(" nt", "s"); else DMAX("", "s"); }
}
template <int NT> __device__ __forceinline__ void pf4(unsigned voff, const void* sbase, unsigned lds_dst) {
    if (NT) asm volatile("s_mov_b32 m0, %2\n\ts_nop 0\n\tglobal_load_lds_dword %0, %1 nt" :: "v"(voff), "s"(sbase), "s"(lds_dst) : "memory", "m0");
    else asm volatile("s_mov_b32 m0, %2\n\ts_nop 0\n\tglobal_load_lds_dword %0, %1" :: "v"(voff), "s"(sbase), "s"(lds_dst) : "memory", "m0");
}
__device__ __forceinline__ void pf4s(unsigned voff, const void* sbase, unsigned lds_dst) {
    asm volatile("s_mov_b32 m0, %2\n\ts_nop 0\n\tglobal_load_lds_dword %0, %1 sc1" :: "v"(voff), "s"(sbase), "s"(lds_dst) : "memory", "m0");
}
#define XGATE 300
#define SPINCAP (1u << 20)
constexpr unsigned kSpinCap = SPINCAP;
struct Params {
    const unsigned short* x;
    const unsigned short* ckv;
    const unsigned short* w_q;
    const unsigned short* w_uk;
    const unsigned short* w_uv;
    const unsigned short* w_o;
    const unsigned short* w1;
    const unsigned short* w2;
    const unsigned short* c;
    const unsigned short* w_mod;
    char* ws;
    unsigned short* out;
};
#define VMW(n) asm volatile("s_waitcnt vmcnt(%0)" :: "n"(n) : "memory")
#define PIN(x) asm volatile("" : "+v"(x))
#define PIN4(a, b, c, d) asm volatile("" : "+v"(a), "+v"(b), "+v"(c), "+v"(d) :: "memory")
#define MFMA16(a, b, c) __builtin_amdgcn_mfma_f32_16x16x32_bf16(a, b, c, 0, 0, 0)
#define MFMA32(a, b, c) __builtin_amdgcn_mfma_f32_32x32x16_bf16(a, b, c, 0, 0, 0)
#define BF8(x) __builtin_bit_cast(bf16x8, x)
#define ST16(v, off, soff) __builtin_amdgcn_raw_buffer_store_b128(v, rw, off, soff, 16)
#define LD16(off, soff) __builtin_amdgcn_raw_buffer_load_b128(rw, off, soff, 16)
#define SCL (0.08838834764831845f * 1.4426950408889634f)
#define BIGV 1.0e18f
#define QD 4
#define QE 4
#define L_SCR 131072
#define HS_LD 520

__global__ __launch_bounds__(512, 2) void k_main(Params p) {
    unsigned long long pc;
    asm volatile("s_getpc_b64 %0" : "=s"(pc));
    const int id = blockIdx.x, tid = threadIdx.x;
    int lane = tid & 63;
    const int wv = __builtin_amdgcn_readfirstlane(tid >> 6);
    int g = lane >> 4, nl = lane & 15;
    auto rederive = [&]() {
        asm volatile("v_mbcnt_lo_u32_b32 %0, -1, 0\n\tv_mbcnt_hi_u32_b32 %0, -1, %0" : "=v"(lane));
        g = lane >> 4; nl = lane & 15;
    };
    const int x = id & 7, j0 = id >> 3, j = JPERM && j0 < 16 ? 15 - j0 : j0;
    if (wv < 5 && j == 15) {
        asm volatile("s_mov_b32 m0, %2\n\ts_nop 0\n\tglobal_load_lds_dword %0, %1" :: "v"(min((unsigned)((wv * 64 + lane) * 128), (unsigned)CPFE)), "s"(pc), "s"(L_SCR + 31744) : "memory", "m0");
    }
    if ((j >> 2) == 3) {
        dma16<0>((unsigned)(((nl & 7) * 512 + (nl >> 3) * 32 + g * 8) * 2 + wv * 128), p.c, wv * 16384 + 8192);
        dma16<0>((unsigned)tid * 16u, p.x, wv * 16384 + 9216);
    }
    char* ws = p.ws;
    if (TLBW && j == 15 && wv == 5 && lane < 2) pf4s((unsigned)((x * 2 + lane) * 2097152), p.ckv, L_SCR + 31744);
    STAMP(0);
    const __amdgpu_buffer_rsrc_t rw = __builtin_amdgcn_make_buffer_rsrc(ws, 0, WS_END, 0x00020000);
    const u32x4 s4 = {SENT, SENT, SENT, SENT};
    unsigned long long cnt = 0ull;
    auto cnt_load = [&]() { asm volatile("global_load_dwordx2 %0, %1, %2 sc1" : "+v"(cnt) : "v"((unsigned)WS_EPOCH), "s"(ws) : "memory"); };
    unsigned tlo = 0u, thi = 0u;
    auto tag_take = [&]() {
        asm volatile("" : "+v"(cnt) :: "memory");
        const unsigned long long t = cnt + 1ull;
        tlo = __builtin_amdgcn_readfirstlane((unsigned)t); thi = __builtin_amdgcn_readfirstlane((unsigned)(t >> 32));
    };
    auto flag_put = [&](unsigned base, unsigned idx) {
        if (lane < 8) __builtin_amdgcn_raw_buffer_store_b64(u32x2{tlo, thi}, rw, base + lane * STR + idx * 8, 0, 16);
    };
    auto tagged_wait = [&](unsigned off) {
        u32x4 v;
#pragma unroll 1
        for (unsigned spins = 0; spins < kSpinCap; ++spins) {
            asm volatile("buffer_load_dwordx4 %0, %1, %2, 0 offen sc1\n\ts_waitcnt vmcnt(0)" : "=v"(v) : "v"(off), "s"(rw) : "memory");
            if (__all((v[2] == tlo) & (v[3] == thi))) break;
            __builtin_amdgcn_s_sleep(8);
        }
        return v;
    };
    auto flags_ok = [&](const u32x4& f) { return (f[0] == tlo) & (f[1] == thi) & (f[2] == tlo) & (f[3] == thi); };
    auto flags_wait = [&](unsigned off) {
        for (unsigned spins = 0;;) {
            u32x4 f;
            asm volatile("buffer_load_dwordx4 %0, %1, %2, 0 offen sc1\n\ts_waitcnt vmcnt(0)" : "=v"(f) : "v"(off), "s"(rw) : "memory");
            if (__all(flags_ok(f))) break;
            __builtin_amdgcn_s_sleep(16);
            if (++spins > kSpinCap) break;
            asm volatile("" ::: "memory");
        }
    };
    unsigned pcl, frl, frs;
    auto dma_piece = [&](const unsigned short* src, unsigned ldb, unsigned dst, int n, int nt) {
        const unsigned v0 = (lane >> 3) * ldb + pcl, v1 = v0 ^ 64u;
#pragma unroll
        for (int i = 0; i < n; ++i) {
            if (nt) dma16<1>((i & 1) ? v1 : v0, (const char*)src + (size_t)i * 8 * ldb, dst + i * 1024);
            else dma16<0>((i & 1) ? v1 : v0, (const char*)src + (size_t)i * 8 * ldb, dst + i * 1024);
        }
    };
    auto piece_frag = [&](unsigned base, int ks, int t) {
        const unsigned a = base + frl + ((t ^ frs) << 5) + ks * 4096;
        return tr_frag(a, a + 512);
    };
    const unsigned rgn = (unsigned)(wv * 16384);

    if (j < 16) {
        const unsigned long long t_start = __builtin_amdgcn_s_memrealtime();
        rederive();
        pcl = (unsigned)(((((lane & 7) >> 1) ^ ((lane >> 4) & 1)) * 32) + (lane & 1) * 16);
        frl = (unsigned)((g * 8 + (nl >> 2)) * 128 + (lane & 3) * 8); frs = (unsigned)((nl >> 3) | ((g & 1) << 1));
        const int th = x * 4 + (j & 3);
        if (j < 12) {
            const int qt = j >> 2, rot = qt * 5;
            const bool pub = wv < (qt < 2 ? 3 : 2);
            const unsigned qo = (unsigned)(WS_Q + (th & 7) * STR + ((nl & 7) * 4 + (th >> 3)) * 1024 + (qt * 192 + wv * 64 + g * 16 + (nl >> 3) * 8) * 2);
            const unsigned vb = (unsigned)(g * 8192 + ((nl ^ (g << 2)) << 4));
            const char* wuk = (const char*)(p.w_uk + th * 128) + (size_t)qt * 192 * 8192;
            const char* wq = (const char*)(p.w_q + th * 128) + (size_t)wv * 32768;
            const unsigned L_U = 114688;
            {
                const int k0 = qt < 2 ? wv * 6 : wv * 4;
#pragma unroll
                for (int i = 0; i < 6; ++i) {
                    if (i < 4 || qt < 2) dma16<NTM & 2>(vb ^ (((k0 + i) & 3) << 4), wuk + (size_t)(k0 + i) * 32768, L_U + (k0 + i) * 1024);
                    if (i == 4) VMW(4);
                }
            }
            const unsigned vq = vb ^ ((wv & 3) << 4);
            auto wq_dma = [&](int ps) { dma16<NTM & 2>(vq, wq + (size_t)((ps + rot) & 15) * 262144, ps * 8192 + wv * 1024); };
            bf16x8 uk[4][4], aq[16];
            u32x4 fi, hv[4];
            unsigned fio = (unsigned)(WS_FI + x * STR + (lane & 7) * 16), hsrc, hsrc2, ta0, ta1, tb0, tb1, qo2, hw0, hw1, hr, hr2, qr0, qr1, qw, thx = 0u;
            const unsigned L_HI = L_SCR, L_QI = L_SCR + 8192;
            auto h_load = [&]() {
                asm volatile("buffer_load_dwordx4 %0, %4, %6, 0 offen sc1\n\tbuffer_load_dwordx4 %1, %4, %6, 0 offen offset:1024 sc1\n\t"
                             "buffer_load_dwordx4 %2, %5, %6, 0 offen sc1\n\tbuffer_load_dwordx4 %3, %5, %6, 0 offen offset:1024 sc1"
                             : "+v"(hv[0]), "+v"(hv[1]), "+v"(hv[2]), "+v"(hv[3]) : "v"(hsrc), "v"(hsrc2), "s"(rw) : "memory");
            };
            auto h_bad = [&]() {
                PIN4(hv[0], hv[1], hv[2], hv[3]);
                unsigned bad = (hv[0][1] ^ tlo) | (hv[0][3] ^ thx) | (hv[1][1] ^ tlo) | (hv[1][3] ^ thx) | (hv[2][1] ^ tlo) | (hv[2][3] ^ thx) | (hv[3][1] ^ tlo) | (hv[3][3] ^ thx);
                PIN(bad);
                return bad;
            };
#pragma unroll
            for (int ps = 0; ps < 16; ++ps) {
                wq_dma(ps);
                if (ps == 4) cnt_load();
                if (ps == HP) { h_load(); asm volatile("buffer_load_dwordx4 %0, %1, %2, 0 offen sc1" : "=v"(fi) : "v"(fio), "s"(rw) : "memory"); }
                if (ps >= 4 && ps < 8) VMW(5); else if (ps >= HP && ps < HP + 4) VMW(9); else VMW(4);
                if (ps == 3) {
                    asm volatile("s_barrier" ::: "memory");
                    if (pub) {
#pragma unroll
                        for (int t = 0; t < 4; ++t)
#pragma unroll
                            for (int s = 0; s < 4; ++s) uk[t][s] = *LP(bf16x8, L_U + off_b(wv * 64 + (nl >> 2) * 16 + t * 4 + (nl & 3), s * 4 + g));
                    }
                    STAMP(1);
                }
                if (ps == 5) {
                    hsrc = (unsigned)(WS_H + x * STR + wv * 2048 + lane * 16); hsrc2 = hsrc + 16384;
                    ta0 = off_b(8 * g + (nl >> 2), 2 * wv + ((lane & 3) >> 1)) + 8 * (lane & 1); ta1 = off_b(8 * g + 4 + (nl >> 2), 2 * wv + ((lane & 3) >> 1)) + 8 * (lane & 1);
                    tb0 = ta0 + 65536; tb1 = ta1 + 65536; qo2 = qo;
                    hw0 = L_HI + ((2 * wv - rot) & 15) * 512 + (lane >> 4) * 128 + (lane & 7) * 16 + ((lane >> 3) & 1) * 8;
                    hw1 = L_HI + ((2 * wv + 1 - rot) & 15) * 512 + (lane >> 4) * 128 + (lane & 7) * 16 + ((lane >> 3) & 1) * 8;
                    hr2 = L_HI + g * 128 + (nl & 7) * 16 + (nl >> 3) * 512;
                    PIN(hr2);
                    qr0 = L_QI + g * 128 + (nl & 7) * 16 + (nl >> 3) * 2048; qr1 = qr0 ^ 2048u;
                    PIN(qr0); PIN(qr1);
                    hr = L_HI + g * 128 + (nl & 7) * 16; qw = L_QI + (wv >> 1) * 512 + ((wv & 1) * 2 + (g >> 1)) * 128 + (nl & 7) * 16 + (g & 1) * 8;
                    PIN4(hsrc, hsrc2, ta0, ta1); PIN4(tb0, tb1, qo2, hw0); PIN(hw1); PIN(qw);
                }
                if (ps == 8) { tag_take(); thx = tlo ^ thi ^ TAGC; }
                if (ps == 9) {
#pragma unroll
                    for (int t = 0; t < 4; ++t) PIN4(uk[t][0], uk[t][1], uk[t][2], uk[t][3]);
                    wg_barrier();
                    *LP(unsigned, L_QI + 2048 + wv * 256 + lane * 4) = 0u;
#pragma unroll
                    for (int q = 0; q < 6; ++q) aq[q] = tr_frag(ta0 + q * 8192, ta1 + q * 8192);
                }
                if (ps == 13) {
                    asm volatile("s_barrier" ::: "memory");
#pragma unroll
                    for (int q = 6; q < 10; ++q) aq[q] = q < 8 ? tr_frag(ta0 + q * 8192, ta1 + q * 8192) : tr_frag(tb0 + (q - 8) * 8192, tb1 + (q - 8) * 8192);
                }
            }
            STAMP(15);
            if (HP + 4 > 15) VMW(HP + 4 > 15 ? 15 - HP : 0);
            if (!__all(h_bad() == 0u)) {
                for (unsigned spins = 0;;) {
                    h_load();
                    VMW(0);
                    if (__all(h_bad() == 0u)) break;
                    __builtin_amdgcn_s_sleep(SLPH);
                    if (++spins > kSpinCap) break;
                }
            }
            {
                unsigned hh[4];
#pragma unroll
                for (int q = 0; q < 4; ++q) {
                    const unsigned t = hv[2 + (q >> 1)][(q & 1) * 2], sh = hv[q >> 1][(q & 1) * 2];
                    hh[q] = pk2(lo16(t) + lo16(sh), hi16(t) + hi16(sh));
                }
                *LP(u32x2, hw0) = u32x2{hh[0], hh[1]};
                *LP(u32x2, hw1) = u32x2{hh[2], hh[3]};
            }
            STAMP(3);
            asm volatile("s_waitcnt vmcnt(0) lgkmcnt(0)\n\ts_barrier" : "+v"(fi) :: "memory");
            STAMP(2);
            {
                bf16x8 hb[8];
#pragma unroll
                for (int q = 0; q < 5; ++q) hb[q] = *LP(bf16x8, hr2 + q * 1024);
#pragma unroll
                for (int ps = 10; ps < 16; ++ps) {
                    if (!(ps & 1)) hb[ps >> 1] = *LP(bf16x8, hr2 + (ps >> 1) * 1024);
                    aq[ps] = tr_frag(tb0 + (ps - 8) * 8192, tb1 + (ps - 8) * 8192);
                }
                __builtin_amdgcn_sched_barrier(0);
                f32x4 d0 = {0.f, 0.f, 0.f, 0.f}, d1 = {0.f, 0.f, 0.f, 0.f};
#pragma unroll
                for (int ps = 0; ps < 16; ps += 2) {
                    d0 = MFMA16(aq[ps], hb[ps >> 1], d0); d1 = MFMA16(aq[ps + 1], hb[ps >> 1], d1);
                    if (ps == 6) { unsigned bad = (fi[0] ^ tlo) | (fi[1] ^ thi) | (fi[2] ^ tlo) | (fi[3] ^ thi); PIN(bad); if (!__all(bad == 0u)) flags_wait(fio); }
                }
#pragma unroll
                for (int e = 0; e < 4; ++e) { float v = d0[e] + dppf<0x128>(d1[e]); asm("" : "+v"(v)); d0[e] = v; }
                if (nl < 8) *LP(u32x2, qw) = u32x2{pk2(d0[0], d0[1]), pk2(d0[2], d0[3])};
            }
            wg_barrier();
            if (pub) {
                bf16x8 qb[2][4];
#pragma unroll
                for (int s = 0; s < 4; ++s) { qb[0][s] = *LP(bf16x8, qr0 + s * 512); qb[1][s] = *LP(bf16x8, qr1 + s * 512); }
                __builtin_amdgcn_sched_barrier(0);
                f32x4 a = {0.f, 0.f, 0.f, 0.f}, b = a;
#pragma unroll
                for (int s = 0; s < 4; ++s) {
                    a = MFMA16(uk[0][s], qb[0][s], a); b = MFMA16(uk[1][s], qb[0][s], b);
                    a = MFMA16(uk[2][s], qb[1][s], a); b = MFMA16(uk[3][s], qb[1][s], b);
                }
                const u32x4 qv = {pk2s(a[0], a[1]), pk2s(a[2], a[3]), pk2s(b[0], b[1]), pk2s(b[2], b[3])};
                ST16(qv, qo2, 0);
            }
            STAMP(4);
            wg_barrier();
            if (LMPF && wv >= 3 && wv < 6) {
                const unsigned n = (unsigned)(j * 3 + wv - 3);
                if (n < 32) pf4<0>(((n & 7u) * 64 + lane) * 6144u + 2048u + (x * 4 + (n >> 3)) * 128u, p.w_mod, L_SCR + 31744);
            }
            if (QHELP && wv < 3 && j + 12 * wv < 32) {
                const int hh = j + 12 * wv;
#pragma unroll 1
                for (unsigned spins = 0; spins < 64; ++spins) {
                    __builtin_amdgcn_s_sleep(QHELP);
                    const u32x4 v = LD16((unsigned)(WS_Q + (hh & 7) * STR + (x * 4 + (hh >> 3)) * 1024 + lane * 16), 0);
                    if (__all(mx4(v, 0u) != SENT)) break;
                    asm volatile("" ::: "memory");
                }
                STAMP(6);
            }
        } else {
            const int cg = x * 2 + (j & 1);
            if (j < 14) {
                dma_piece(p.w_mod + (size_t)wv * 64 * 3072 + cg * 64, 6144, rgn, 8, NTM & 2);
                cnt_load();
                VMW(9);
            } else {
                cnt_load();
                ST16(s4, (unsigned)(WS_Q + wv * STR + cg * 2048 + lane * 16), 0); ST16(s4, (unsigned)(WS_Q + wv * STR + cg * 2048 + lane * 16), 1024);
                VMW(0);
                tag_take();
                wg_barrier();
                if (wv == 7) flag_put(WS_FI, cg);
                STAMP(15);
                if (j == 14) {
                    u32x2 best = {~0u, ~0u};
#pragma unroll
                    for (int r = 0; r < 2 * NCAL; ++r) {
                        const unsigned hf = (r ^ wv) & 1;
                        VMW(0);
                        const unsigned long long t0 = __builtin_amdgcn_s_memtime();
                        if (lane < 8) ST16(s4, WS_CAL + x * STR + hf * 8192 + (wv * 8 + r) * 128 + lane * 16, 0);
                        VMW(0);
                        const unsigned dt = (unsigned)(__builtin_amdgcn_s_memtime() - t0);
                        if (hf) best[1] = min(best[1], dt); else best[0] = min(best[0], dt);
                    }
                    *LP(u32x2, 16384 * 7 + 14336 + wv * 8) = best;
                    wg_barrier();
                    if (wv == 0 && lane < 8) {
                        u32x2 m = *LP(u32x2, 16384 * 7 + 14336 + lane * 8);
#pragma unroll
                        for (int o = 1; o < 8; o <<= 1) { m[0] = min(m[0], (unsigned)__shfl_xor(m[0], o)); m[1] = min(m[1], (unsigned)__shfl_xor(m[1], o)); }
                        ST16((u32x4{(unsigned)max(-1000, min(1000, (int)m[1] - (int)m[0])), 0u, tlo, thi}), (unsigned)(WS_NP + lane * STR + x * 16), 0);
                    }
                    if (wv == 0) {
                        const int d = (int)tagged_wait(WS_NP + x * STR + (lane & 7) * 16)[0];
                        int t = (lane & 4) ? -d : d;
                        t += __builtin_amdgcn_update_dpp(0, t, 0xB1, 0xf, 0xf, true);
                        t += __builtin_amdgcn_update_dpp(0, t, 0x4E, 0xf, 0xf, true);
                        t += __builtin_amdgcn_update_dpp(0, t, 0x141, 0xf, 0xf, true);
                        const bool n1 = abs(d) > 160 ? d < 0 : (t < 0) != ((lane & 4) != 0);
                        const unsigned mk = NEARP ? (unsigned)__ballot(n1) & 0xffu : 0u;
                        if (lane == 0) ST16((u32x4{mk, 0u, tlo, thi}), (unsigned)(WS_NM + x * STR), 0);
                    }
                    STAMP(9);
                }
            }
            const u16x8 cf = *LP(u16x8, rgn + 8192 + lane * 16), xa = *LP(u16x8, rgn + 9216 + lane * 16);
            u32x4 af[2];
#pragma unroll
            for (int q = 0; q < 4; ++q) {
                const float a = bf2f(cf[2 * q]), b = bf2f(cf[2 * q + 1]);
                af[0][q] = pk2(a * __builtin_amdgcn_rcpf(1.f + __expf(-a)), b * __builtin_amdgcn_rcpf(1.f + __expf(-b)));
                af[1][q] = (unsigned)__builtin_amdgcn_update_dpp(0, (int)af[0][q], 0x128, 0xf, 0xf, true);
            }
            const unsigned L_MS = L_SCR;
            unsigned wa[4], fr[4];
#pragma unroll
            for (int t = 0; t < 4; ++t) { wa[t] = L_MS + wv * 2048 + (nl & 7) * 256 + (((t * 4 + g) ^ (nl & 7)) << 4) + (nl >> 3) * 16384; fr[t] = rgn + frl + ((t ^ frs) << 5); }
            PIN4(wa[0], wa[1], wa[2], wa[3]); PIN4(fr[0], fr[1], fr[2], fr[3]); PIN(af[0]); PIN(af[1]);
            const unsigned L_XN = 12288;
            auto mod_mm = [&](bool ln) {
                VMW(0);
                bf16x8 f[2][4];
#pragma unroll
                for (int ks = 0; ks < 2; ++ks)
#pragma unroll
                    for (int t = 0; t < 4; ++t) f[ks][t] = tr_frag(fr[t] + ks * 4096, fr[t] + ks * 4096 + 512);
                __builtin_amdgcn_sched_barrier(0);
                f32x4 d[4];
#pragma unroll
                for (int t = 0; t < 4; ++t) d[t] = MFMA16(f[0][t], BF8(af[0]), (f32x4{0.f, 0.f, 0.f, 0.f}));
#pragma unroll
                for (int t = 0; t < 4; ++t) d[t] = MFMA16(f[1][t], BF8(af[1]), d[t]);
#pragma unroll
                for (int t = 0; t < 4; ++t) *LP(f32x4, wa[t]) = d[t];
                if (ln) {
                    float v[8];
#pragma unroll
                    for (int q = 0; q < 8; ++q) v[q] = bf2f(xa[q]);
                    const float p1 = ((v[0] + v[1]) + (v[2] + v[3])) + ((v[4] + v[5]) + (v[6] + v[7]));
                    const float p2 = ((v[0] * v[0] + v[1] * v[1]) + (v[2] * v[2] + v[3] * v[3])) + ((v[4] * v[4] + v[5] * v[5]) + (v[6] * v[6] + v[7] * v[7]));
                    float mb, rden;
                    ln_stats(p1, p2, mb, rden);
                    u32x4 r;
#pragma unroll
                    for (int q = 0; q < 4; ++q) { const unsigned d = pk2(v[2 * q] - mb, v[2 * q + 1] - mb); r[q] = pk2(lo16(d) * rden, hi16(d) * rden); }
                    *LP(u32x4, L_XN + wv * 16384 + lane * 16) = r;
                }
                wg_barrier();
            };
            auto sum8 = [&](unsigned a) {
                f32x4 u[8];
#pragma unroll
                for (int w2 = 0; w2 < 8; ++w2) u[w2] = *LP(f32x4, a + w2 * 2048);
                __builtin_amdgcn_sched_barrier(0);
                return ((u[0] + u[1]) + (u[2] + u[3])) + ((u[4] + u[5]) + (u[6] + u[7]));
            };
            if (j < 14) {
                const int hb = lane & 7, c4 = 2 * wv + (nl >> 3);
                unsigned hoff = (unsigned)(WS_H + g * STR + cg * 2048 + wv * 256 + nl * 16);
                unsigned sa = L_MS + hb * 256 + ((c4 ^ hb) << 4);
                unsigned xa2 = L_XN + hb * 16384 + (cg & 7) * 128 + c4 * 8;
                PIN(hoff); PIN(sa); PIN(xa2);
                STAMP(1);
                mod_mm(cg >= 8);
                STAMP(2);
                const u32x2 xn = *LP(u32x2, xa2);
                const f32x4 v = sum8(sa);
                tag_take();
                const unsigned thx = tlo ^ thi ^ TAGC;
                u32x2 r = {pk2(v[0], v[1]), pk2(v[2], v[3])};
                if (cg >= 8) {
#pragma unroll
                    for (int q = 0; q < 2; ++q) { const unsigned sc = pk2(1.f + lo16(r[q]), 1.f + hi16(r[q])); r[q] = pk2(lo16(xn[q]) * lo16(sc), hi16(xn[q]) * hi16(sc)); }
                }
                ST16((u32x4{r[0], tlo, r[1], thx}), hoff, 0);
                ST16((u32x4{r[0], tlo, r[1], thx}), hoff, 4 * STR);
                STAMP(5);
            }
            wg_barrier();
            while (__builtin_amdgcn_s_memrealtime() < t_start + LMD) __builtin_amdgcn_s_sleep(2);
            const int lg = x * 4 + (j & 3);
            dma_piece(p.w_mod + (size_t)wv * 64 * 3072 + 1024 + lg * 64, 6144, rgn, 8, NTM & 4);
            mod_mm(false);
            if (lg >= 8 && lg < 24) {
                if (wv == 0) {
                    const int b = lane >> 3, c8 = lane & 7;
                    const f32x4 v0 = sum8(L_MS + b * 256 + (((2 * c8) ^ b) << 4)), v1 = sum8(L_MS + b * 256 + (((2 * c8 + 1) ^ b) << 4));
                    ST16((u32x4{pk2(v0[0], v0[1]), pk2(v0[2], v0[3]), pk2(v1[0], v1[1]), pk2(v1[2], v1[3])}), (unsigned)(WS_MB + b * STR + (lg - 8) * 128 + c8 * 16), 0);
                }
            } else if (wv < 4) {
                const int b = wv * 2 + (lane >> 5), cp = lane & 31;
                f32x2_t u[8];
#pragma unroll
                for (int w2 = 0; w2 < 8; ++w2) u[w2] = *LP(f32x2_t, L_MS + w2 * 2048 + b * 256 + (((cp >> 1) ^ b) << 4) + (cp & 1) * 8);
                const f32x2_t v = ((u[0] + u[1]) + (u[2] + u[3])) + ((u[4] + u[5]) + (u[6] + u[7]));
                ST16((u32x4{__float_as_uint(rbf(v[0])), __float_as_uint(rbf(v[1])), tlo, thi}), (unsigned)(WS_MOD + b * STR + (lg * 32 + cp) * 16), 0);
            }
            STAMP(6);
        }
        const int bq = (j >> 2) & 1, dh = j >> 3, src = th * 2 + dh, rb = bq * 4 + (src >> 4), rc = src & 15;
        wg_barrier();
        if (TLBW > 1 && j == 0) {
            if (wv == 4 && lane < 5) pf4s((unsigned)((lane + 1) * 2097152 + 8064), ws, L_SCR + 31744);
            if (wv == 5 && lane < 2) { pf4s((unsigned)(lane * 2097152), p.w_uv, L_SCR + 31744); pf4s((unsigned)(lane * 2097152), p.w_o, L_SCR + 31744); }
            if (wv == 6 && lane < 1) { pf4s(0u, p.w1, L_SCR + 31744); pf4s(0u, p.w2, L_SCR + 31744); pf4s(0u, p.out, L_SCR + 31744); }
        }
        while (__builtin_amdgcn_s_memrealtime() < t_start + TWD - 110) __builtin_amdgcn_s_sleep(2);
        const unsigned npm = __builtin_amdgcn_readfirstlane(tagged_wait(WS_NM + x * STR)[0]), np = (npm >> x) & 1u;
        unsigned oo = (unsigned)(WS_OUTP + ((bq * 4 + (nl & 3)) * 16 + wv * 2 + (nl >> 3)) * 16384 + (src ^ wv) * 128 + ((nl >> 2) & 1) * 64 + g * 16) + ((~npm >> ((nl & 3) * 2 + (wv >> 2))) & 1u) * 8192u;
        const int rcol = rc * 32 + (lane & 7) * 4 + (lane >> 5) * 2;
        unsigned xdst = (unsigned)(WS_X1 + ((lane >> 3) & 3) * STR + (rb * 512 + rcol) * 2), xdst2 = xdst + 4 * STR + ((npm >> (4 + ((lane >> 3) & 3))) & 1u) * 8192u;
        xdst += ((npm >> ((lane >> 3) & 3)) & 1u) * 8192u;
        ST16(s4, oo, 0);
        if (wv == 7) { __builtin_amdgcn_raw_buffer_store_b32(SENT, rw, xdst, 0, 16); __builtin_amdgcn_raw_buffer_store_b32(SENT, rw, xdst2, 0, 16); }
        VMW(0);
        wg_barrier();
        if (wv == 0) flag_put(WS_FB, id);
        while (__builtin_amdgcn_s_memrealtime() < t_start + TWD) __builtin_amdgcn_s_sleep(2);
        asm volatile("s_barrier" ::: "memory");
        STAMP(7);
        if ((PFM & 8) && wv == 0 && lane < 32) pf4<NTM & 8>((unsigned)((j * 32 + lane) * 8192 + x * 1024 + ((3u - ((unsigned)(size_t)p.w_uv >> 7)) & 7u) * 128), p.w_uv, L_SCR + 31744);
        if (bq) dma_piece(p.w_o + (size_t)(th * 128 + dh * 64) * 512 + wv * 64, 1024, rgn + 8192, 8, NTM & 8);
        dma_piece(p.w_uv + ((size_t)wv * 64 * NH + th) * 128 + dh * 64, 8192, rgn, 8, NTM & 8);
        if (!bq) dma_piece(p.w_o + (size_t)(th * 128 + dh * 64) * 512 + wv * 64, 1024, rgn + 8192, 8, NTM & 8);
        u32x4 fb, gm;
        unsigned xg;
        VMW(4);
        if (wv == 7)
            asm volatile("buffer_load_dwordx4 %0, %3, %5, 0 offen sc1\n\tbuffer_load_dwordx4 %1, %4, %5, 0 offen sc1\n\tglobal_load_dword %2, %6, %7" : "=&v"(fb), "=&v"(gm), "=&v"(xg)
                         : "v"((unsigned)(WS_FB + x * STR + lane * 16)), "v"((unsigned)(WS_MOD + rb * STR + rcol * 8)), "s"(rw), "v"((unsigned)(rb * 512 + rcol) * 2u), "s"(p.x) : "memory");
        VMW(0);
        asm volatile("" : "+v"(fb), "+v"(gm), "+v"(xg));
        STAMP(8);
        bf16x8 fuv[2][4], fwo[2][4];
        unsigned pfa[4];
#pragma unroll
        for (int t = 0; t < 4; ++t) { pfa[t] = rgn + frl + ((t ^ frs) << 5); PIN(pfa[t]); }
#pragma unroll
        for (int ks = 0; ks < 2; ++ks)
#pragma unroll
            for (int t = 0; t < 4; ++t) {
                const unsigned a = rgn + (unsigned)((g * 4 + (nl >> 2)) * 128 + (lane & 3) * 8) + ((t ^ (unsigned)((nl >> 3) | (g & 2))) << 5) + ks * 4096;
                fuv[ks][t] = tr_frag(a, a + 2048);
                fwo[ks][t] = tr_frag(pfa[t] + 8192 + ks * 4096, pfa[t] + 8192 + ks * 4096 + 512);
            }
#pragma unroll
        for (int ks = 0; ks < 2; ++ks) { PIN4(fuv[ks][0], fuv[ks][1], fuv[ks][2], fuv[ks][3]); PIN4(fwo[ks][0], fwo[ks][1], fwo[ks][2], fwo[ks][3]); }
        const unsigned L_WGT = L_SCR, L_MSK = L_SCR + 256, L_RED = L_SCR + 1024;
        unsigned psrc = (unsigned)(WS_PART + (bq * 4 * 32 + th) * 32768 + (wv >> 2) * 16384 + (wv & 3) * 2048 + ((lane >> 3) ^ (th & 7)) * 128) + np * 8192u + pcl, psrc1 = (psrc ^ 64u) + 1024;
        unsigned psrc2 = psrc ^ 8192u, psrc3 = psrc1 ^ 8192u;
        PIN(psrc2); PIN(psrc3); PIN(xdst2);
        unsigned rw2 = L_RED + wv * 1024 + (nl & 3) * 256 + g * 16 + (YSEL ? (nl >> 2) * 64 : 0);
        unsigned wa0 = (nl & 3) == (g >> 1) ? L_WGT + (HILO ? nl & 4 : 0) * 32 + g * 16 : L_WGT + 272, wa1 = (nl & 3) == 2 + (g >> 1) ? L_WGT + (HILO ? nl & 4 : 0) * 32 + 64 + g * 16 : L_WGT + 272;
        unsigned cr = rgn + (nl & 3) * 128 + g * 16;
        PIN(cr);
        unsigned r2r = L_RED + lane * 16, cw = rgn + lane * 8;
        unsigned osrc = (unsigned)(WS_OUTP + (rb * 16 + rc) * 16384 + lane * 16) + (np ^ 1u) * 8192u;
        PIN(psrc); PIN(psrc1); PIN(rw2); PIN(wa0); PIN(wa1); PIN(r2r); PIN4(cw, oo, osrc, xdst);
        float mlm = 0.f, mll = 0.f;
        if (wv == 0) *LP(unsigned, L_WGT + 256 + lane * 4) = 0u;
        wg_barrier();
        const bool early = __all(flags_ok(fb) & (gm[2] == tlo) & (gm[3] == thi));
        u32x2 ga = {gm[0], gm[1]};
        PIN(ga);
        bf16x8 pa[2][4], wh[2];
        {
            u32x4 ml = {0u, 0u, ~tlo, ~thi};
            const unsigned mlsrc = (unsigned)(WS_ML + (bq * 4 + g) * STR + nl * 512 + th * 16) + ((npm >> (x & 6)) & 1u) * 8192u;
            unsigned long long done = 0ull;
            unsigned lm = L_MSK;
            PIN(lm);
#pragma unroll 1
            for (unsigned spins = 0; spins < kSpinCap; ++spins) {
                unsigned long long m;
                if (wv == 0) {
                    if ((ml[2] != tlo) | (ml[3] != thi)) ml = LD16(mlsrc, 0);
                    m = __ballot((ml[2] == tlo) & (ml[3] == thi));
                    unsigned long long f = m & (m >> 4);
                    f &= f >> 2; f &= f >> 1;
                    asm volatile("ds_write_b64 %0, %1" :: "v"(lm), "v"(f) : "memory");
                    m = m == ~0ull ? 0x0101010101010101ull : 0ull;
                } else {
                    unsigned long long t;
                    asm volatile("ds_read_b64 %0, %1\n\ts_waitcnt lgkmcnt(0)" : "=v"(t) : "v"(lm) : "memory");
                    m = ((unsigned long long)(unsigned)__builtin_amdgcn_readfirstlane((unsigned)(t >> 32)) << 32) | (unsigned)__builtin_amdgcn_readfirstlane((unsigned)t);
                    m &= 0x0101010101010101ull & ~done;
                }
                if (m) {
                    done |= m;
#define PLD(bit, off, v, base) "s_bitcmp1_b64 %0, " #bit "\n\ts_cbranch_scc0 1f\n\ts_add_u32 m0, %1, " #off "\n\ts_nop 0\n\tglobal_load_lds_dwordx4 " v ", " base " sc1\n1:\n\t"
                    asm volatile(PLD(0, 0, "%2", "%4") PLD(8, 1024, "%3", "%4") PLD(16, 2048, "%2", "%5") PLD(24, 3072, "%3", "%5")
                                 PLD(32, 4096, "%8", "%6") PLD(40, 5120, "%9", "%6") PLD(48, 6144, "%8", "%7") PLD(56, 7168, "%9", "%7")
                                 :: "s"(m), "s"(rgn), "v"(psrc), "v"(psrc1), "s"(ws), "s"(ws + 1048576), "s"(ws + 2097152), "s"(ws + 3145728), "v"(psrc2), "v"(psrc3) : "memory", "m0", "scc");
                    if (done == 0x0101010101010101ull) break;
                } else if (SLPM > 0 && wv == 0) __builtin_amdgcn_s_sleep(SLPM);
            }
            mlm = __uint_as_float(ml[0]); mll = __uint_as_float(ml[1]);
            STAMP(10);
            if (wv == 0) {
                float M = mlm;
                M = fmaxf(M, dppf<0xB1>(M)); M = fmaxf(M, dppf<0x4E>(M)); M = fmaxf(M, dppf<0x141>(M)); M = fmaxf(M, dppf<0x140>(M));
                const float e = __builtin_amdgcn_exp2f(mlm - M);
                const float w = e * __builtin_amdgcn_rcpf(sum16(e * mll));
                const unsigned short hi = f2bf(w);
                *LP(unsigned short, L_WGT + lane * 2) = hi;
                if (HILO) *LP(unsigned short, L_WGT + 128 + lane * 2) = f2bf(w - bf2f(hi));
            }
            wg_barrier();
            wh[0] = *LP(bf16x8, wa0); wh[1] = *LP(bf16x8, wa1);
            __builtin_amdgcn_sched_barrier(0);
            VMW(0);
#pragma unroll
            for (int ks = 0; ks < 2; ++ks)
#pragma unroll
                for (int t = 0; t < 4; ++t) pa[ks][t] = tr_frag(pfa[t] + ks * 4096, pfa[t] + ks * 4096 + 512);
        }
        STAMP(11);
        {
            f32x4 d[4];
#pragma unroll
            for (int t = 0; t < 4; ++t) d[t] = MFMA16(pa[0][t], wh[0], (f32x4{0.f, 0.f, 0.f, 0.f}));
#pragma unroll
            for (int t = 0; t < 4; ++t) d[t] = MFMA16(pa[1][t], wh[1], d[t]);
#pragma unroll
            for (int t = 0; t < 4; ++t)
#pragma unroll
                for (int e = 0; e < 4; ++e) if (HILO) { float v = d[t][e] + dppf<0x124>(d[t][e]); asm("" : "+v"(v)); d[t][e] = v; }
            const u32x4 o0 = {pk2(d[0][0], d[0][1]), pk2(d[0][2], d[0][3]), pk2(d[1][0], d[1][1]), pk2(d[1][2], d[1][3])};
            const u32x4 o1 = {pk2(d[2][0], d[2][1]), pk2(d[2][2], d[2][3]), pk2(d[3][0], d[3][1]), pk2(d[3][2], d[3][3])};
            f32x4 y[4];
#pragma unroll
            for (int t = 0; t < 4; ++t) y[t] = MFMA16(fuv[0][t], BF8(o0), (f32x4{0.f, 0.f, 0.f, 0.f}));
#pragma unroll
            for (int t = 0; t < 4; ++t) y[t] = MFMA16(fuv[1][t], BF8(o1), y[t]);
            if (YSEL) *LP(f32x4, rw2) = (nl & 8) ? ((nl & 4) ? y[3] : y[2]) : ((nl & 4) ? y[1] : y[0]);
            else if (nl < 4) {
#pragma unroll
                for (int t = 0; t < 4; ++t) *LP(f32x4, rw2 + t * 64) = y[t];
            }
        }
        wg_barrier();
        {
            f32x4 cs[8];
#pragma unroll
            for (int w2 = 0; w2 < 8; ++w2) cs[w2] = *LP(f32x4, r2r + w2 * 1024);
            const f32x4 c4 = ((cs[0] + cs[1]) + (cs[2] + cs[3])) + ((cs[4] + cs[5]) + (cs[6] + cs[7]));
            *LP(u32x2, cw) = u32x2{pk2(c4[0], c4[1]), pk2(c4[2], c4[3])};
            asm volatile("s_waitcnt lgkmcnt(0)" ::: "memory");
            const u32x4 cx0 = *LP(u32x4, cr), cx1 = *LP(u32x4, cr + 64);
            f32x4 o[4];
#pragma unroll
            for (int q = 0; q < 4; ++q) o[q] = MFMA16(fwo[0][q], BF8(cx0), (f32x4{0.f, 0.f, 0.f, 0.f}));
#pragma unroll
            for (int q = 0; q < 4; ++q) o[q] = MFMA16(fwo[1][q], BF8(cx1), o[q]);
            const f32x4 oa = (nl & 8) ? ((nl & 4) ? o[3] : o[2]) : ((nl & 4) ? o[1] : o[0]);
            const u32x4 sv = {nosent(__float_as_uint(oa[0])), nosent(__float_as_uint(oa[1])), nosent(__float_as_uint(oa[2])), nosent(__float_as_uint(oa[3]))};
            ST16(sv, oo, 0);
        }
        STAMP(12);
        wg_barrier();
        if (wv == 7) {
            if (!early) {
                flags_wait(WS_FB + x * STR + lane * 16);
                { const u32x4 v = tagged_wait(WS_MOD + rb * STR + rcol * 8); ga = u32x2{v[0], v[1]}; }
                PIN(ga);
            }
            u32x4 op[8];
            for (unsigned spins = 0;;) {
#pragma unroll
                for (int k = 0; k < 8; ++k) op[k] = LD16(osrc + (k & 3) * 1024, (k >> 2) * 4096);
                unsigned m = 0u;
#pragma unroll
                for (int k = 0; k < 8; ++k) m = mx4(op[k], m);
                if (__all(m != SENT)) break;
                if (++spins > kSpinCap) break;
                asm volatile("" ::: "memory");
            }
            STAMP(13);
            const f32x4 v = ((__builtin_bit_cast(f32x4, op[0]) + __builtin_bit_cast(f32x4, op[1])) + (__builtin_bit_cast(f32x4, op[2]) + __builtin_bit_cast(f32x4, op[3]))) +
                            ((__builtin_bit_cast(f32x4, op[4]) + __builtin_bit_cast(f32x4, op[5])) + (__builtin_bit_cast(f32x4, op[6]) + __builtin_bit_cast(f32x4, op[7])));
            float w[2];
#pragma unroll
            for (int q = 0; q < 2; ++q) {
                auto t = __builtin_amdgcn_permlane32_swap(__float_as_uint(v[q]), __float_as_uint(v[2 + q]), false, false);
                w[q] = __uint_as_float(t[0]) + __uint_as_float(t[1]);
                auto u = __builtin_amdgcn_permlane16_swap(__float_as_uint(w[q]), __float_as_uint(w[q]), false, false);
                w[q] = __uint_as_float(u[0]) + __uint_as_float(u[1]);
                w[q] += dppf<0x128>(w[q]);
            }
            unsigned t = pk2(w[0], w[1]);
            t = pk2(__uint_as_float(ga[0]) * lo16(t), __uint_as_float(ga[1]) * hi16(t));
            t = pk2s(lo16(xg) + lo16(t), hi16(xg) + hi16(t));
            __builtin_amdgcn_raw_buffer_store_b32(t, rw, xdst, 0, 16);
            __builtin_amdgcn_raw_buffer_store_b32(t, rw, xdst2, 0, 16);
            STAMP(14);
        }
        STAMP_FLUSH();
        return;
    }

    const int s = j - 16;
    const unsigned long long t_s = __builtin_amdgcn_s_memrealtime();
    if (wv == 0) cnt_load();
    {
        const unsigned ln = (unsigned)(s * 64 + lane);
        if ((PFM & 1) && wv == 0) pf4<NTM & 1>((ln & 511u) * 6144u + (x * 2 + (ln >> 9)) * 128u, p.w_mod, L_SCR);
        if ((PFM & 2) && wv == 1) {
            const unsigned short* w = s < 8 ? p.w_uk : p.w_q;
            pf4<NTM & 1>((ln & 511u) * 8192u + x * 1024u + ((3u - ((unsigned)(size_t)w >> 7)) & 7u) * 128u, w, L_SCR);
        }
        if ((PFM & 48) == 48) {
            const unsigned r0 = (unsigned)(s * 4 + (wv & 3)), row = PFS == 0 ? r0 * 8 + (lane >> 3) : PFS == 1 ? (lane >> 3) * 64 + r0 : (r0 >> 5) * 256 + (lane >> 3) * 32 + (r0 & 31);
            pf4<NTM & 1>(row * 8192u + x * 1024u + (lane & 7u) * 128u, wv < 4 ? p.w_uk : p.w_q, L_SCR);
        }
        else if ((PFM & 16) && wv >= 4) { const unsigned l2 = (unsigned)((s * 4 + (wv & 3)) * 64 + lane); pf4<NTM & 1>((l2 >> 3) * 8192u + x * 1024u + (l2 & 7u) * 128u, p.w_q, L_SCR); }
        else if ((PFM & 32) && wv >= 4) { const unsigned l2 = (unsigned)((s * 4 + (wv & 3)) * 64 + lane); pf4<NTM & 1>((l2 >> 3) * 8192u + x * 1024u + (l2 & 7u) * 128u, p.w_uk, L_SCR); }
        if ((PFM & 4) && (wv & 6) == 2) { const unsigned l2 = (unsigned)((s * 2 + (wv & 1)) * 64 + lane); pf4<NTM & 1>((l2 & 511u) * 8192u + (x * 4 + (l2 >> 9)) * 256u, p.w_q, L_SCR); }
    }
    const int ug = x * 7 + s;
    const unsigned vb = (unsigned)(g * 1024 + ((nl ^ (g << 2)) << 4)), lb = (unsigned)((wv >> 1) * 32768 + (wv & 1) * 4096);
    const char* ck = (const char*)p.ckv + ((size_t)x * SK + s * 256 + wv * 16) * 1024;
    const unsigned rot = ROTC ? (unsigned)s & 3u : 0u;
    const unsigned vbr = vb ^ (rot << 8);
    const char* ck2 = ck + 131072;
    auto ck_dma = [&](unsigned i) { dma16x<NTM & 16>(vbr, ((i & 3u) << 4) | (((i >> 2) & 3u) << 8) | ((i & 3u) << 12), (i >> 4) ? ck2 : ck, lb + ((i >> 2) & 3u) * 8192u + (i & 3u) * 1024u); };
    bf16x8 ka[16];
    s16x4 va[8][4];
    unsigned kad[4], vad[4], vad2[4];
    auto kq = [](unsigned u) { return (0x78u >> (2 * u)) & 3u; };
    auto ka_reads = [&](int cb) {
#pragma unroll
        for (int u = 0; u < 4; ++u) ka[cb * 4 + u] = *LP(bf16x8, kad[u] + cb * 8192);
    };
    auto va_reads = [&]() {
#pragma unroll
        for (int kk = 0; kk < 8; ++kk)
#pragma unroll
            for (int q = 0; q < 4; ++q) va[kk][q] = tr4((kk < 4 ? vad[q] : vad2[q]) + ((kk >> 1) & 1) * 32768 + (kk & 1) * 4096);
    };
    auto va_read1 = [&](int kk) {
#pragma unroll
        for (int q = 0; q < 4; ++q) va[kk][q] = tr4((kk < 4 ? vad[q] : vad2[q]) + ((kk >> 1) & 1) * 32768 + (kk & 1) * 4096);
    };
    auto va_pin = [&]() {
#pragma unroll
        for (int q = 0; q < 8; ++q) PIN4(va[q][0], va[q][1], va[q][2], va[q][3]);
    };
    u32x4 fa = {0u, 0u, 0u, 0u};
    const unsigned fao = (unsigned)(WS_FI + x * STR + (lane & 7) * 16);
    constexpr int LAST0 = GI + NPOST > 19 ? GI + NPOST : 19, LAST = LAST0 + NP2;
    const unsigned qsrc = (unsigned)(WS_Q + (wv & 1) * 4 * STR + (x * 4 + (wv >> 1)) * 1024) + (g ^ rot) * 256 + ((nl ^ ((wv * 4) & 15)) << 4);
    auto q_dma = [&](int k) {
        unsigned t;
        asm volatile("s_mov_b32 m0, %3\n\tv_xor_b32 %0, %4, %1\n\tglobal_load_lds_dwordx4 %0, %2 sc1" : "=&v"(t) : "v"(qsrc), "s"(ws), "s"(L_SCR + (wv * 4 + k) * 1024), "i"(k * (STR + 16)) : "memory", "m0");
    };
#pragma unroll
    for (int i = 0; i <= LAST0; ++i) {
        ck_dma(i);
        if (i == 3) {
#pragma unroll
            for (int u = 0; u < 4; ++u) {
                kad[u] = tile_off(wv * 16 + kq(nl >> 2) * 4 + (nl & 3), 4 * u + g);
                vad[u] = tile_off(8 * (lane >> 5) + 4 * (u & 1) + (nl >> 2), wv * 8 + 4 * (u >> 1) + 2 * (g & 1) + ((lane & 3) >> 1)) + 8 * (lane & 1);
                vad2[u] = vad[u] + 65536;
                PIN(kad[u]); PIN(vad[u]); PIN(vad2[u]);
            }
        }
        if (i > GI && i < GI + NPOST) { if (i < GI + 4) VMW(8); } else if (i == GI + NPOST) VMW(NPOST); else if (i < NTH) { if (i >= W0) VMW(W0); } else if (i >= 4) VMW(4);
        if (i == 15 && wv == 0) {
            PIN(fa);
            tag_take();
            if (!__all(flags_ok(fa))) flags_wait(fao);
        }
        if ((i & 3) == 3 && i >= 7 && i <= 19) {
            const int cb = (i - 7) >> 2;
            if (cb > 0) { PIN4(ka[cb * 4 - 4], ka[cb * 4 - 3], ka[cb * 4 - 2], ka[cb * 4 - 1]); va_pin(); }
            wg_barrier();
            ka_reads(cb);
            if ((wv >> 1) == cb) va_reads();
            STAMP(16 + cb);
        }
        if (i == 10 && wv == 0) asm volatile("buffer_load_dwordx4 %0, %1, %2, 0 offen sc1" : "+v"(fa) : "v"(fao), "s"(rw) : "memory");
        if (i == GI && TG > 0) { while (__builtin_amdgcn_s_memrealtime() < t_s + TG) __builtin_amdgcn_s_sleep(1); }
        if (i == GI && WG < 4) VMW(WG);
        if (i == GI) { q_dma(0); q_dma(1); q_dma(2); q_dma(3); asm volatile("s_barrier" ::: "memory"); }
    }
    const unsigned L_EXM = L_SCR - 1024, L_BIG = L_SCR + 512 + 128;
    unsigned nx = LAST + 1, npm = 0u;
    unsigned qad[4];
    {
        unsigned qck = L_SCR + wv * 4096 + lane * 16;
        u32x4 qb[4];
#pragma unroll
        for (int k = 0; k < 4; ++k) qb[k] = *LP(u32x4, qck + k * 1024);
#pragma unroll
        for (int i = LAST0 + 1; i <= LAST; ++i) ck_dma(i);
#pragma unroll
        for (int k = 0; k < 4; ++k) { qad[k] = L_SCR + nl * 1024 + (((4 * k + g) ^ nl) << 4); PIN(qad[k]); }
        STAMP(20);
        if (!__all(mx4(qb[0], mx4(qb[1], mx4(qb[2], mx4(qb[3], 0u)))) != SENT)) {
            for (unsigned spins = 0;;) {
#pragma unroll
                for (int k = 0; k < 4; ++k)
                    if (!__all(mx4(qb[k], 0u) != SENT)) q_dma(k);
                if (nx <= 25) {
#pragma unroll
                    for (int i = 0; i < 3; ++i) ck_dma(nx + i);
                    nx += 3;
                    VMW(3);
                } else {
                    for (; nx < 28; ++nx) ck_dma(nx);
                    VMW(0);
                }
#pragma unroll
                for (int k = 0; k < 4; ++k) qb[k] = *LP(u32x4, qck + k * 1024);
                if (__all(mx4(qb[0], mx4(qb[1], mx4(qb[2], mx4(qb[3], 0u)))) != SENT)) break;
                if (++spins > kSpinCap) break;
            }
        }
    }
    constexpr int NE = LAST + 1 + SL0 + 4 < 31 ? LAST + 1 + SL0 + 4 : 31;
    auto slot = [&](int i) { if (i < 28) { if (nx <= (unsigned)i) ck_dma(i); } else ck_dma(i); };
    const unsigned kg = kq(g);
    unsigned exw = L_EXM + ((nl + (lane >> 5) * 16) * 8 + wv) * 4, exr = L_EXM + nl * 32;
    unsigned pwr = L_SCR + nl * 1024 + 768 + (((2 * wv + (kg >> 1)) ^ nl) << 4) + (kg & 1) * 8, esw = L_SCR + nl * 1024 + 512 + ((wv ^ (nl & 7)) << 4) + g * 4;
    unsigned prd = L_SCR + (lane & 31) * 1024 + 768 + (((lane >> 5) ^ nl) << 4);
    PIN4(exw, exr, pwr, esw); PIN(prd);
    PIN4(ka[12], ka[13], ka[14], ka[15]); va_pin();
    wg_barrier();
    STAMP(21);
    bf16x8 qr[8][2];
    auto q_ld = [&](int ks, int hg) { return *LP(bf16x8, qad[ks & 3] + (ks >> 2) * 256 + hg * 16384); };
    auto p_ld = [&](int kk) { return *LP(bf16x8, prd ^ (kk << 5)); };
    float ls0, ls1, m0, m1;
    f32x4 sa, sb;
    f32x16 o[2];
    unsigned sw[4], srd, p0, mlo, esr, fbo;
    u32x2 tg;
    u32x4 nq;
    auto probs = [&](float (&e)[8]) {
#pragma unroll
        for (int r = 0; r < 4; ++r) { e[r] = __builtin_amdgcn_exp2f(fmaf(sa[r], SCL, -m0)); e[4 + r] = __builtin_amdgcn_exp2f(fmaf(sb[r], SCL, -m1)); }
        *LP(u32x2, pwr) = u32x2{pk2(e[0], e[1]), pk2(e[2], e[3])};
        *LP(u32x2, pwr + 16384) = u32x2{pk2(e[4], e[5]), pk2(e[6], e[7])};
    };
    auto wave_max = [&]() {
        const float a = fmaxf(fmaxf(sa[0], sa[1]), fmaxf(sa[2], sa[3])), b = fmaxf(fmaxf(sb[0], sb[1]), fmaxf(sb[2], sb[3]));
        auto u = __builtin_amdgcn_permlane32_swap(__float_as_uint(a), __float_as_uint(b), false, false);
        const float c = fmax3(__uint_as_float(u[0]), __uint_as_float(u[1]), __uint_as_float(u[1]));
        auto v = __builtin_amdgcn_permlane16_swap(__float_as_uint(c), __float_as_uint(c), false, false);
        return fmax3(__uint_as_float(v[0]), __uint_as_float(v[1]), __uint_as_float(v[1]));
    };
    auto max8 = [&](unsigned a) {
        const f32x4 a0 = *LP(f32x4, a), a1 = *LP(f32x4, a + 16);
        return fmax3(fmax3(a0[0], a0[1], a0[2]), fmax3(a0[3], a1[0], a1[1]), fmax3(a1[2], a1[3], a1[3])) * SCL;
    };
    {
        bf16x8 qs[8][2];
#pragma unroll
        for (int ks = 0; ks < 8; ++ks) { qr[ks][0] = q_ld(ks + 8, 0); qr[ks][1] = q_ld(ks + 8, 1); }
        __builtin_amdgcn_sched_barrier(0);
#pragma unroll
        for (int i = LAST + 1; i < LAST + 1 + SL0; ++i) if (i < NE) slot(i);
        sa = f32x4{0.f, 0.f, 0.f, 0.f}; sb = sa;
        f32x4 s2a = sa, s2b = sa;
        bf16x8 kb[4];
#pragma unroll
        for (int n = 0; n < 16; ++n) {
            const int ks = (n + 8) & 15;
            sa = MFMA16(ka[ks], ks < 8 ? qs[ks][0] : qr[ks - 8][0], sa);
            sb = MFMA16(ka[ks], ks < 8 ? qs[ks][1] : qr[ks - 8][1], sb);
            if (QKM && n == 4) { VMW(LAST + SL0 + 1 - 19); for (int u = 0; u < 4; ++u) kb[u] = *LP(bf16x8, kad[u]); }
            if (QKM && ks < 4) { s2a = MFMA16(kb[ks], qs[ks][0], s2a); s2b = MFMA16(kb[ks], qs[ks][1], s2b); }
            if (n >= 8 - QD && n < 16 - QD) { qs[n + QD - 8][0] = q_ld(n + QD - 8, 0); qs[n + QD - 8][1] = q_ld(n + QD - 8, 1); }
            if ((n & 3) == 3 && LAST + 1 + SL0 + (n >> 2) < NE) slot(LAST + 1 + SL0 + (n >> 2));
            __builtin_amdgcn_sched_barrier(0);
        }
        STAMP(1);
        *LP(float, exw) = wave_max();
        wg_barrier();
        m0 = max8(exr); m1 = max8(exr + 512);
        VMW(NE - 24);
        if (!QKM) ka_reads(0);
        ka_reads(1);
#pragma unroll
        for (int ks = 4 * QKM; ks < 4 * QKM + QE; ++ks) { qs[ks][0] = q_ld(ks, 0); qs[ks][1] = q_ld(ks, 1); }
        __builtin_amdgcn_sched_barrier(0);
        float e[8];
        probs(e);
        ls0 = (e[0] + e[1]) + (e[2] + e[3]); ls1 = (e[4] + e[5]) + (e[6] + e[7]);
        PIN(ls0); PIN(ls1);
        if (wv == 0) *LP(u32x4, L_BIG) = u32x4{0u, 0u, tlo, thi};
        VMW(NE - 28);
        wg_barrier();
        STAMP(22);
        static_assert(NE >= 28, "");
        bf16x8 pf[8];
        tg = *LP(u32x2, L_BIG + 8);
#pragma unroll
        for (int i = NE; i < 32; ++i) slot(i);
        sa = s2a; sb = s2b;
        if (QKM) { pf[0] = p_ld(0); pf[1] = p_ld(1); }
        __builtin_amdgcn_sched_barrier(0);
#pragma unroll
        for (int ks = 4 * QKM; ks < 8; ++ks) {
            sa = MFMA16(ka[ks], qs[ks][0], sa); sb = MFMA16(ka[ks], qs[ks][1], sb);
            if (ks + QE < 8) { qs[ks + QE][0] = q_ld(ks + QE, 0); qs[ks + QE][1] = q_ld(ks + QE, 1); }
            else if (!QKM) pf[ks + QE - 8] = p_ld(ks + QE - 8);
            __builtin_amdgcn_sched_barrier(0);
        }
#pragma unroll
        for (int i = 0; i < 16; ++i) { o[0][i] = 0.f; o[1][i] = 0.f; }
        STAMP(2);
#pragma unroll
        for (int kk = 0; kk < 4; ++kk) {
            if (kk == 0) {
#pragma unroll
                for (int q = QKM ? 2 : QE; q < 8; ++q) pf[q] = p_ld(q);
            }
            if (kk == 1) ka_reads(2);
            o[0] = MFMA32(cat8(va[kk][0], va[kk][1]), pf[kk], o[0]);
            o[1] = MFMA32(cat8(va[kk][2], va[kk][3]), pf[kk], o[1]);
            __builtin_amdgcn_sched_barrier(0);
        }
        PIN4(pf[4], pf[5], pf[6], pf[7]); PIN4(ka[8], ka[9], ka[10], ka[11]); PIN(tg);
        STAMP(3);
#pragma unroll
        for (int kk = 4; kk < 8; ++kk) {
            if (wv < 6) { va_read1(kk - 4); if (kk > 4) va_read1(kk - 1); }
            sa = MFMA16(ka[kk + 4], qr[kk - 4][0], sa); sb = MFMA16(ka[kk + 4], qr[kk - 4][1], sb);
            o[0] = MFMA32(cat8(va[kk][0], va[kk][1]), pf[kk], o[0]);
            o[1] = MFMA32(cat8(va[kk][2], va[kk][3]), pf[kk], o[1]);
            __builtin_amdgcn_sched_barrier(0);
        }
        if (wv < 6) va_read1(7);
        STAMP(5);
        tlo = __builtin_amdgcn_readfirstlane(tg[0]); thi = __builtin_amdgcn_readfirstlane(tg[1]);
        rederive();
        fbo = (unsigned)(WS_FB + x * STR + lane * 16);
        asm volatile("buffer_load_dwordx4 %0, off, %1, %2 sc1" : "=v"(nq) : "s"(rw), "s"(WS_NM + x * STR) : "memory");
        const unsigned head = lane & 31, r8 = lane >> 3, cw = wv ^ (rot * 2);
        srd = lb + r8 * 128 + (((lane & 7) ^ r8) << 4);
        sw[0] = lb + head * 128 + (((lane >> 5) ^ (head & 7)) << 4);
        sw[1] = sw[0] ^ 32u; sw[2] = sw[0] ^ 64u; sw[3] = sw[0] ^ 96u;
        p0 = (unsigned)(WS_PART + x * 1048576) + (cw >> 2) * 16384 + (cw & 3) * 2048 + r8 * 32768 + ((s ^ r8) * 8 + (lane & 7)) * 16;
        PIN4(sw[0], sw[1], sw[2], sw[3]); PIN(srd); PIN(p0); PIN(fbo);
    }
    STAMP(23);
    unsigned po[4];
    float mrun;
    {
        VMW(1);
        STAMP(24);
        ka_reads(3);
        asm volatile("" : "=v"(fa));
        if (wv == 0 && s < 11) asm volatile("buffer_load_dwordx4 %0, %1, %2, 0 offen sc1" : "+v"(fa) : "v"(fbo), "s"(rw) : "memory");
        __builtin_amdgcn_sched_barrier(0);
#pragma unroll
        for (int ks = 12; ks < 16; ++ks) { sa = MFMA16(ka[ks], qr[ks - 8][0], sa); sb = MFMA16(ka[ks], qr[ks - 8][1], sb); }
        bf16x8 pf[8];
#pragma unroll 1
        for (bool first = true;; first = false) {
            float e[8];
            probs(e);
            const float n0 = (e[0] + e[1]) + (e[2] + e[3]), n1 = (e[4] + e[5]) + (e[6] + e[7]);
            *LP(float, esw) = ls0 + n0; *LP(float, esw + 16384) = ls1 + n1;
            if (__builtin_expect(!(n0 + n1 <= BIGV), 0)) *LP(unsigned, L_BIG) = 1u;
            va_pin();
            wg_barrier();
            STAMP(25);
            PIN(prd);
            const unsigned big = *LP(unsigned, L_BIG);
            if ((wv >> 1) == 3) va_reads();
#pragma unroll
            for (int kk = 0; kk < 8; ++kk) { pf[kk] = p_ld(kk); __builtin_amdgcn_sched_barrier(0); }
            if (__builtin_expect(__builtin_amdgcn_readfirstlane(big) == 0 || !first, 1)) break;
            const unsigned ex2 = L_SCR + 512 + 160;
            PIN(sa); PIN(sb);
            rederive();
            *LP(float, ex2 + (nl + (lane >> 5) * 16) * 1024 + wv * 4) = wave_max();
            wg_barrier();
            const float x0 = fmaxf(max8(ex2 + nl * 1024), m0), x1 = fmaxf(max8(ex2 + nl * 1024 + 16384), m1);
            const float al0 = __builtin_amdgcn_exp2f(m0 - x0), al1 = __builtin_amdgcn_exp2f(m1 - x1), al = (lane & 16) ? al1 : al0;
            m0 = x0; m1 = x1; ls0 *= al0; ls1 *= al1;
            o[0] *= al; o[1] *= al;
        }
        mrun = (lane & 16) ? m1 : m0;
        auto stage = [&](int t2, int gp) {
            const unsigned lo0 = pk2(o[t2][8 * gp + 0], o[t2][8 * gp + 1]), lo1 = pk2(o[t2][8 * gp + 2], o[t2][8 * gp + 3]);
            const unsigned hi0 = pk2(o[t2][8 * gp + 4], o[t2][8 * gp + 5]), hi1 = pk2(o[t2][8 * gp + 6], o[t2][8 * gp + 7]);
            auto r0 = __builtin_amdgcn_permlane32_swap(lo0, hi0, false, false);
            auto r1 = __builtin_amdgcn_permlane32_swap(lo1, hi1, false, false);
            *LP(u32x4, sw[t2 * 2 + gp]) = u32x4{r0[0], r1[0], r0[1], r1[1]};
        };
#pragma unroll
        for (int kk = 0; kk < 8; ++kk) {
            o[0] = MFMA32(cat8(va[kk][0], va[kk][1]), pf[kk], o[0]);
            if (kk == 1) {
                asm volatile("s_waitcnt vmcnt(0)" : "+v"(nq) :: "memory");
                npm = __builtin_amdgcn_readfirstlane(nq[0]);
                if ((unsigned)__builtin_amdgcn_readfirstlane(nq[2]) != tlo || (unsigned)__builtin_amdgcn_readfirstlane(nq[3]) != thi) npm = __builtin_amdgcn_readfirstlane(tagged_wait(WS_NM + x * STR)[0]);
            }
            if (kk == 3 || kk == 5) {
                const unsigned v = (npm ^ ((x & 2) ? 0xffu : 0u)) >> (lane >> 5);
#pragma unroll
                for (int i = kk - 3; i < kk - 1; ++i) { po[i] = p0 + ((v >> (2 * i)) & 1u) * 8192u; PIN(po[i]); }
            }
            __builtin_amdgcn_sched_barrier(0);
        }
#pragma unroll
        for (int kk = 0; kk < 8; ++kk) {
            o[1] = MFMA32(cat8(va[kk][2], va[kk][3]), pf[kk], o[1]);
            if (kk == 2) { PIN(o[0]); stage(0, 0); }
            if (kk == 4) stage(0, 1);
            __builtin_amdgcn_sched_barrier(0);
        }
        stage(1, 0); stage(1, 1);
        asm volatile("s_waitcnt lgkmcnt(0)" ::: "memory");
        u32x4 w[4];
#pragma unroll
        for (int i = 0; i < 4; ++i) w[i] = *LP(u32x4, srd + i * 1024);
#pragma unroll
        for (int i = 0; i < 4; ++i) ST16(w[i], po[i], i * 262144);
        __builtin_amdgcn_sched_barrier(0);
        STAMP(26);
        u32x4 mlrec;
        if (wv == 0) {
            const unsigned head = lane & 31;
            esr = L_SCR + head * 1024 + 512 + ((head & 7) << 4);
            mlo = (unsigned)(WS_ML + x * STR + s * 512 + head * 16) + ((npm >> ((head >> 3) * 2)) & 1u) * 8192u;
            PIN(mlo);
            f32x4 b8[8];
#pragma unroll
            for (int q = 0; q < 8; ++q) b8[q] = *LP(f32x4, esr ^ (q << 4));
            const f32x4 bs = ((b8[0] + b8[1]) + (b8[2] + b8[3])) + ((b8[4] + b8[5]) + (b8[6] + b8[7]));
            mlrec = u32x4{__float_as_uint(mrun), __float_as_uint((bs[0] + bs[1]) + (bs[2] + bs[3])), tlo, thi};
            PIN(mlrec);
        }
        VMW(0);
        wg_barrier();
        if (wv == 0) {
            if (lane < 32) ST16(mlrec, mlo, 0);
            PIN(fa);
            if (s < 11 && !__all(flags_ok(fa))) flags_wait(fbo);
        }
        STAMP(27);
        wg_barrier();
    }
    const unsigned long long t_att = __builtin_amdgcn_s_memrealtime();
    rederive();
    while (__builtin_amdgcn_s_memrealtime() < t_att + (s >= 11 ? MD0 : MD1)) __builtin_amdgcn_s_sleep(2);
    if (s >= 11) {
        const unsigned n = (unsigned)((s - 11) * 8 + wv);
        if (n < 28) pf4<NTM & 32>((n * 64 + lane) * 1024u + x * 128u, p.w2, 0);
        { const unsigned l2 = n * 64 + lane; pf4<NTM & 32>((l2 / 5u) * (DFF * 2) + min(((unsigned)x * 448u & ~127u) + (l2 % 5u) * 128u, DFF * 2 - 4u), p.w1, 0); }
        if (wv == 0) flag_put(WS_FA, id - 128);
        STAMP_FLUSH();
        return;
    }
    const unsigned L_H2S = L_SCR, L_REDM = L_SCR + 16384;
    if (s < 7) {
        {
            const char* w1s = (const char*)(p.w1 + (size_t)wv * 64 * DFF + ug * 32);
#pragma unroll
            for (int i = 0; i < 4; ++i) dma16<NTM & 32>((unsigned)((lane >> 2) * DFF * 2 + (lane & 3) * 16), w1s + (size_t)i * 16 * DFF * 2, wv * 4096 + i * 1024);
        }
        const int b = wv;
        if (wv == 0) ST16(s4, (unsigned)(WS_U + x * STR + (lane >> 5) * 8192 + s * 512 + (lane & 31) * 16), 0);
        const u32x4 shf = LD16((unsigned)(WS_MB + b * STR + lane * 16), 0), scl = LD16((unsigned)(WS_MB + b * STR + 1024 + lane * 16), 0);
        VMW(0);
        if (wv == 0) flag_put(WS_FA, id - 128);
        wg_barrier();
        bf16x8 wk0[8], wk1[8];
        if (wv >= 6) {
#pragma unroll
            for (int q = 0; q < 8; ++q) {
                const unsigned a = (unsigned)(q * 4096 + (g * 8 + (nl >> 2)) * 64 + (wv & 1) * 32 + (lane & 3) * 8);
                wk0[q] = tr_frag(a, a + 256); wk1[q] = tr_frag(a + 2048, a + 2304);
            }
#pragma unroll
            for (int q = 0; q < 8; q += 2) PIN4(wk0[q], wk0[q + 1], wk1[q], wk1[q + 1]);
        }
        float scv[8], shv[8];
#pragma unroll
        for (int q = 0; q < 4; ++q) {
            scv[2 * q] = rbf(1.f + lo16(scl[q])); scv[2 * q + 1] = rbf(1.f + hi16(scl[q])); shv[2 * q] = lo16(shf[q]); shv[2 * q + 1] = hi16(shf[q]);
            PIN4(scv[2 * q], scv[2 * q + 1], shv[2 * q], shv[2 * q + 1]);
        }
        unsigned har = L_H2S + ((lane & 7) * HS_LD + g * 8 + (nl >> 3) * 32) * 2, haw = L_H2S + (b * HS_LD + lane * 8) * 2;
        unsigned udst = (unsigned)(WS_U + x * STR + (nl >> 3) * 8192 + (s * 2 + (wv & 1)) * 256 + ((nl & 7) * 16 + g * 4) * 2), xsrc = (unsigned)(WS_X1 + x * STR + (b * 512 + lane * 8) * 2) + ((npm >> x) & 1u) * 8192u;
        PIN4(har, haw, udst, xsrc);
        STAMP(28);
        while (__builtin_amdgcn_s_memrealtime() < t_att + XGATE) __builtin_amdgcn_s_sleep(2);
        u32x4 xv;
        for (unsigned spins = 0;;) {
            xv = LD16(xsrc, 0);
            if (__all(mx4(xv, 0u) != SENT)) break;
            __builtin_amdgcn_s_sleep(SLPX);
            if (++spins > kSpinCap) break;
            asm volatile("" ::: "memory");
        }
        STAMP(29);
        {
            f32x2_t v[4];
#pragma unroll
            for (int q = 0; q < 4; ++q) v[q] = f32x2_t{lo16(xv[q]), hi16(xv[q])};
            const f32x2_t p1 = (v[0] + v[1]) + (v[2] + v[3]), p2 = (v[0] * v[0] + v[1] * v[1]) + (v[2] * v[2] + v[3] * v[3]);
            float mb, rden;
            ln_stats(p1[0] + p1[1], p2[0] + p2[1], mb, rden);
            u32x4 hr;
#pragma unroll
            for (int q = 0; q < 4; ++q) {
                const f32x2_t h = ((v[q] - mb) * rden) * f32x2_t{scv[2 * q], scv[2 * q + 1]} + f32x2_t{shv[2 * q], shv[2 * q + 1]};
                hr[q] = pk2(h[0], h[1]);
            }
            *LP(u32x4, haw) = hr;
        }
        wg_barrier();
        if (wv >= 6) {
            bf16x8 af[8];
#pragma unroll
            for (int q = 0; q < 8; ++q) af[q] = *LP(bf16x8, har + q * 128);
            __builtin_amdgcn_sched_barrier(0);
            f32x4 d0 = {0.f, 0.f, 0.f, 0.f}, d1 = {0.f, 0.f, 0.f, 0.f};
#pragma unroll
            for (int q = 0; q < 8; ++q) { d0 = MFMA16(wk0[q], af[q], d0); d1 = MFMA16(wk1[q], af[q], d1); }
            const f32x4 own = nl < 8 ? d0 : d1;
#pragma unroll
            for (int e = 0; e < 4; ++e) { float v = own[e] + dppf<0x128>(own[e]); asm("" : "+v"(v)); d0[e] = fmaxf(v, 0.f); }
            const u32x2 uv = {pk2s(d0[0], d0[1]), pk2s(d0[2], d0[3])};
            __builtin_amdgcn_raw_buffer_store_b64(uv, rw, udst, 0, 16);
            STAMP(30);
        }
        STAMP_FLUSH();
        return;
    }
    {
        const int n0 = (x * 4 + (s - 7)) * 16;
        const char* mws = (const char*)(p.w2 + (size_t)wv * 224 * 512 + n0);
#pragma unroll
        for (int i = 0; i < 7; ++i) dma16<NTM & 32>((unsigned)((lane >> 1) * 1024 + (lane & 1) * 16), mws + i * 32768, rgn + i * 1024);
        const int fb = lane >> 3, fc = (lane & 7) * 2;
        u32x2 gb = {0u, 0u};
        if (wv == 0) {
            flag_put(WS_FA, id - 128);
            { const u32x4 v = tagged_wait(WS_MOD + fb * STR + (1536 + n0 + fc) * 8); gb = u32x2{v[0], v[1]}; }
            flags_wait(WS_FA + x * STR + lane * 16);
            if (id == 23 * 8 && lane == 0) __builtin_amdgcn_raw_buffer_store_b64(u32x2{tlo, thi}, rw, WS_EPOCH, 0, 16);
            PIN(gb);
        }
        VMW(0);
        bf16x8 wfr[7];
        const unsigned trl = (unsigned)(g * 8 + (nl >> 2)) * 32u + (unsigned)(lane & 3) * 8u;
#pragma unroll
        for (int ks = 0; ks < 7; ++ks) { wfr[ks] = tr_frag(rgn + ks * 1024 + trl, rgn + ks * 1024 + trl + 128); PIN(wfr[ks]); }
        unsigned usrc = (unsigned)(WS_U + wv * STR + (g >> 1) * 256 + (nl & 7) * 32 + (g & 1) * 16) + ((npm >> x) & 1u) * 8192u;
        unsigned rdw = L_REDM + wv * 512 + (nl & 7) * 64 + g * 16, rdr = L_REDM + lane * 8;
        unsigned short* outp = p.out + fb * 512 + n0 + fc;
        PIN(usrc); PIN(rdw); PIN(rdr); PIN(outp);
        STAMP(28);
        unsigned xg = 0u;
        if (wv == 0) {
            while (__builtin_amdgcn_s_memrealtime() < t_att + XGATE) __builtin_amdgcn_s_sleep(2);
            for (unsigned spins = 0;;) {
                xg = __builtin_amdgcn_raw_buffer_load_b32(rw, (unsigned)(WS_X1 + x * STR + (fb * 512 + n0 + fc) * 2) + ((npm >> x) & 1u) * 8192u, 0, 16);
                if (__all(xg != SENT)) break;
                __builtin_amdgcn_s_sleep(SLPD);
                if (++spins > kSpinCap) break;
                asm volatile("" ::: "memory");
            }
            __builtin_amdgcn_s_sleep(SLPG);
        }
        wg_barrier();
        u32x4 uf[7];
        for (unsigned spins = 0;;) {
#pragma unroll
            for (int ks = 0; ks < 7; ++ks) uf[ks] = LD16(usrc + ks * 512, 0);
            unsigned m = 0u;
#pragma unroll
            for (int ks = 0; ks < 7; ++ks) m = mx4(uf[ks], m);
            if (__all(m != SENT)) break;
            if (++spins > kSpinCap) break;
            asm volatile("" ::: "memory");
        }
        STAMP(29);
        f32x4 d = {0.f, 0.f, 0.f, 0.f}, d2 = {0.f, 0.f, 0.f, 0.f};
#pragma unroll
        for (int ks = 0; ks < 7; ++ks) {
            if (ks & 1) d2 = MFMA16(wfr[ks], BF8(uf[ks]), d2);
            else d = MFMA16(wfr[ks], BF8(uf[ks]), d);
        }
        *LP(f32x4, rdw) = d + d2;
        wg_barrier();
        if (wv == 0) {
            f32x2_t c[8];
#pragma unroll
            for (int w2 = 0; w2 < 8; ++w2) c[w2] = *LP(f32x2_t, rdr + w2 * 512);
            const f32x2_t sm = ((c[0] + c[1]) + (c[2] + c[3])) + ((c[4] + c[5]) + (c[6] + c[7]));
            unsigned t = pk2(sm[0], sm[1]);
            t = pk2(__uint_as_float(gb[0]) * lo16(t), __uint_as_float(gb[1]) * hi16(t));
            *(GAS unsigned*)outp = pk2(lo16(xg) + lo16(t), hi16(xg) + hi16(t));
            STAMP(31);
        }
        STAMP_FLUSH();
    }
}

extern "C" void kernel_launch(void* const* d_in, const int* in_sizes, int n_in,
                              void* d_out, int out_size, void* d_ws, size_t ws_size,
                              hipStream_t stream) {
    Params p;
    p.x = (const unsigned short*)d_in[0];
    p.ckv = (const unsigned short*)d_in[1];
    p.w_q = (const unsigned short*)d_in[2];
    p.w_uk = (const unsigned short*)d_in[3];
    p.w_uv = (const unsigned short*)d_in[4];
    p.w_o = (const unsigned short*)d_in[5];
    p.w1 = (const unsigned short*)d_in[7];
    p.w2 = (const unsigned short*)d_in[8];
    p.c = (const unsigned short*)d_in[9];
    p.w_mod = (const unsigned short*)d_in[10];
    p.ws = (char*)(((size_t)d_ws + 2097151) & ~(size_t)2097151);
    p.out = (unsigned short*)d_out;
    (void)hipFuncSetAttribute((const void*)k_main, hipFuncAttributeMaxDynamicSharedMemorySize, 163840);
    k_main<<<256, 512, 163840, stream>>>(p);
}
```

```cpp
#include <hip/hip_runtime.h>
#include <hip/hip_bf16.h>
#include <stdint.h>

#define NH 32
#define SK 4096
#define DFF 1792
#define GI 20
#define ROTC 1
#define PFS 2
#define XNO 0
#define CPJ 7
#define NTM 16
#define QHELP 4
#define HP 13
#define NTH 0
#define W0 2
#define SL0 1
#define LMPF 0
#define SLPB 20
#define MD0 0
#define MD1 0
#define NEARP 1
#define NCAL 2
#define TLBW 2
#define TG 500
#define JPERM 1
#define NPOST 4
#define XS3 1
#define WG 2
#define NP2 2
#define QKM 1
#define HILO 0
#define YSEL 1
#define TWD 680
#define LMD 250
#define PFM 57
#define CPFE 35500
#define SLPM 0
#define SLPD 12
#define SLPG 8
#define SLPX 1
#define SLPH 4

using u16x8 = __attribute__((ext_vector_type(8))) unsigned short;
using s16x4 = __attribute__((ext_vector_type(4))) short;
using bf16x8 = __attribute__((ext_vector_type(8))) short;
using f32x16 = __attribute__((ext_vector_type(16))) float;
using f32x4 = __attribute__((ext_vector_type(4))) float;
using u32x4 = __attribute__((ext_vector_type(4))) unsigned;
using u32x2 = __attribute__((ext_vector_type(2))) unsigned;

#define GAS __attribute__((address_space(1)))
#define LAS __attribute__((address_space(3)))
typedef GAS unsigned long long gu64;
#define RLX_AGENT __ATOMIC_RELAXED, __HIP_MEMORY_SCOPE_AGENT
#define SENT 0xffffffffu
#define TAGC 0x9e3779b9u

__device__ __forceinline__ float bf2f(unsigned short u) { return __uint_as_float(((unsigned)u) << 16); }
typedef __attribute__((ext_vector_type(2))) __bf16 bf16x2_t;
typedef __attribute__((ext_vector_type(2))) float f32x2_t;
__device__ __forceinline__ unsigned pk2(float a, float b) {
    f32x2_t v = {a, b};
    bf16x2_t r = __builtin_convertvector(v, bf16x2_t);
    return __builtin_bit_cast(unsigned, r);
}
__device__ __forceinline__ unsigned nosent(unsigned r) { return min(r, SENT - 1u); }
__device__ __forceinline__ unsigned pk2s(float a, float b) { return nosent(pk2(a, b)); }
__device__ __forceinline__ unsigned short f2bf(float f) { return (unsigned short)(pk2(f, 0.f) & 0xffffu); }
__device__ __forceinline__ float rbf(float f) { return __uint_as_float(pk2(f, 0.f) << 16); }
__device__ __forceinline__ float lo16(unsigned u) { return __uint_as_float(u << 16); }
__device__ __forceinline__ float hi16(unsigned u) { return __uint_as_float(u & 0xffff0000u); }
__device__ __forceinline__ unsigned umax3(unsigned a, unsigned b, unsigned c) { return max(max(a, b), c); }
__device__ __forceinline__ unsigned mx4(const u32x4& v, unsigned m) { return umax3(v[2], v[3], umax3(v[0], v[1], m)); }
__device__ __forceinline__ float fmax3(float a, float b, float c) { float r; asm("v_max3_f32 %0, %1, %2, %3" : "=v"(r) : "v"(a), "v"(b), "v"(c)); return r; }
template <int C> __device__ __forceinline__ float dppf(float v) { return __int_as_float(__builtin_amdgcn_update_dpp(0, __float_as_int(v), C, 0xf, 0xf, true)); }
__device__ __forceinline__ float sum16(float v) { v += dppf<0xB1>(v); v += dppf<0x4E>(v); v += dppf<0x141>(v); v += dppf<0x140>(v); return v; }

__device__ __forceinline__ void ln_stats(float p1, float p2, float& mb, float& rden) {
    auto t = __builtin_amdgcn_permlane32_swap(__float_as_uint(p1), __float_as_uint(p2), false, false);
    float a = sum16(__uint_as_float(t[0]) + __uint_as_float(t[1]));
    auto t2 = __builtin_amdgcn_permlane16_swap(__float_as_uint(a), __float_as_uint(a), false, false);
    a = (__uint_as_float(t2[0]) + __uint_as_float(t2[1])) * (1.f / 512.f);
    auto t3 = __builtin_amdgcn_permlane32_swap(__float_as_uint(a), __float_as_uint(a), false, false);
    const float m = __uint_as_float(t3[0]);
    const float var = rbf(fmaxf(fmaf(-m, m, __uint_as_float(t3[1])), 0.f));
    mb = rbf(m);
    rden = __builtin_amdgcn_rcpf(rbf(__builtin_amdgcn_sqrtf(rbf(var + 1e-5f))));
}
#define STR 262144
#define WS_Q 0
#define WS_H 32768
#define WS_X1 65536
#define WS_ML 81920
#define WS_MOD 98304
#define WS_MB 139264
#define WS_U 114688
#define WS_FI 131072
#define WS_FA 132096
#define WS_FB 133120
#define WS_NP 134144
#define WS_NM 134400
#define WS_EPOCH 135168
#define WS_CAL 147456
#define WS_OUTP 2097152
#define WS_PART 4194304
#define WS_END 12582912

#define LP(T, off) ((LAS T*)(size_t)(unsigned)(off))
#define STAMP(k) do {} while (0)
#define STAMP_FLUSH() do {} while (0)
__device__ __forceinline__ s16x4 tr4(unsigned a) { return __builtin_amdgcn_ds_read_tr16_b64_v4i16(LP(s16x4, a)); }
__device__ __forceinline__ bf16x8 cat8(s16x4 a, s16x4 b) { return __builtin_shufflevector(a, b, 0, 1, 2, 3, 4, 5, 6, 7); }
__device__ __forceinline__ bf16x8 tr_frag(unsigned a0, unsigned a1) { return cat8(tr4(a0), tr4(a1)); }
__device__ __forceinline__ unsigned swz(unsigned row) { return ((row & 3) << 2) | ((row >> 2) & 3); }
__device__ __forceinline__ unsigned off_b(unsigned row, unsigned ch) { return 256u * row + 16u * (ch ^ swz(row)); }
__device__ __forceinline__ unsigned tile_off(unsigned key, unsigned chunk) { return ((key >> 5) * 4 + (chunk >> 4)) * 8192u + off_b(key & 31, chunk & 15); }
__device__ __forceinline__ void wg_barrier() { asm volatile("s_waitcnt lgkmcnt(0)\n\ts_barrier" ::: "memory"); }
template <int NT> __device__ __forceinline__ void dma16(unsigned voff, const void* sbase, unsigned lds_dst) {
    if (NT) asm volatile("s_mov_b32 m0, %2\n\ts_nop 0\n\tglobal_load_lds_dwordx4 %0, %1 nt" :: "v"(voff), "s"(sbase), "s"(lds_dst) : "memory", "m0");
    else asm volatile("s_mov_b32 m0, %2\n\ts_nop 0\n\tglobal_load_lds_dwordx4 %0, %1" :: "v"(voff), "s"(sbase), "s"(lds_dst) : "memory", "m0");
}
template <int NT> __device__ __forceinline__ void dma16x(unsigned voff, unsigned vx, const void* sbase, unsigned lds_dst) {
    unsigned t;
#define DMAX(pol, c) asm volatile("s_mov_b32 m0, %3\n\tv_xor_b32 %0, %4, %1\n\tglobal_load_lds_dwordx4 %0, %2" pol : "=&v"(t) : "v"(voff), "s"(sbase), "s"(lds_dst), c(vx) : "memory", "m0")
    if (__builtin_constant_p(vx)) { if (NT) DMAX(" nt", "i"); else DMAX("", "i"); }
    else { if (NT) DMAX(" nt", "s"); else DMAX("", "s"); }
}
template <int NT> __device__ __forceinline__ void pf4(unsigned voff, const void* sbase, unsigned lds_dst) {
    if (NT) asm volatile("s_mov_b32 m0, %2\n\ts_nop 0\n\tglobal_load_lds_dword %0, %1 nt" :: "v"(voff), "s"(sbase), "s"(lds_dst) : "memory", "m0");
    else asm volatile("s_mov_b32 m0, %2\n\ts_nop 0\n\tglobal_load_lds_dword %0, %1" :: "v"(voff), "s"(sbase), "s"(lds_dst) : "memory", "m0");
}
__device__ __forceinline__ void pf4s(unsigned voff, const void* sbase, unsigned lds_dst) {
    asm volatile("s_mov_b32 m0, %2\n\ts_nop 0\n\tglobal_load_lds_dword %0, %1 sc1" :: "v"(voff), "s"(sbase), "s"(lds_dst) : "memory", "m0");
}
#define XGATE 300
#define SPINCAP (1u << 20)
constexpr unsigned kSpinCap = SPINCAP;
struct Params {
    const unsigned short* x;
    const unsigned short* ckv;
    const unsigned short* w_q;
    const unsigned short* w_uk;
    const unsigned short* w_uv;
    const unsigned short* w_o;
    const unsigned short* w1;
    const unsigned short* w2;
    const unsigned short* c;
    const unsigned short* w_mod;
    char* ws;
    unsigned short* out;
};
#define VMW(n) asm volatile("s_waitcnt vmcnt(%0)" :: "n"(n) : "memory")
#define PIN(x) asm volatile("" : "+v"(x))
#define PIN4(a, b, c, d) asm volatile("" : "+v"(a), "+v"(b), "+v"(c), "+v"(d) :: "memory")
#define MFMA16(a, b, c) __builtin_amdgcn_mfma_f32_16x16x32_bf16(a, b, c, 0, 0, 0)
#define MFMA32(a, b, c) __builtin_amdgcn_mfma_f32_32x32x16_bf16(a, b, c, 0, 0, 0)
#define BF8(x) __builtin_bit_cast(bf16x8, x)
#define ST16(v, off, soff) __builtin_amdgcn_raw_buffer_store_b128(v, rw, off, soff, 16)
#define LD16(off, soff) __builtin_amdgcn_raw_buffer_load_b128(rw, off, soff, 16)
#define SCL (0.08838834764831845f * 1.4426950408889634f)
#define BIGV 1.0e18f
#define QD 4
#define QE 4
#define L_SCR 131072
#define HS_LD 520

__global__ __launch_bounds__(512, 2) void k_main(Params p) {
    unsigned long long t00; asm volatile("s_memrealtime %0" : "=s"(t00));
    unsigned long long pc;
    asm volatile("s_getpc_b64 %0" : "=s"(pc));
    const int id = blockIdx.x, tid = threadIdx.x;
    int lane = tid & 63;
    const int wv = __builtin_amdgcn_readfirstlane(tid >> 6);
    int g = lane >> 4, nl = lane & 15;
    auto rederive = [&]() {
        asm volatile("v_mbcnt_lo_u32_b32 %0, -1, 0\n\tv_mbcnt_hi_u32_b32 %0, -1, %0" : "=v"(lane));
        g = lane >> 4; nl = lane & 15;
    };
    const int x = id & 7, j0 = id >> 3, j = JPERM && j0 < 16 ? 15 - j0 : j0;
    if (wv < 5 && j == 15) {
        asm volatile("s_mov_b32 m0, %2\n\ts_nop 0\n\tglobal_load_lds_dword %0, %1" :: "v"(min((unsigned)((wv * 64 + lane) * 128), (unsigned)CPFE)), "s"(pc), "s"(L_SCR + 31744) : "memory", "m0");
    }
    if ((j >> 2) == 3) {
        dma16<0>((unsigned)(((nl & 7) * 512 + (nl >> 3) * 32 + g * 8) * 2 + wv * 128), p.c, wv * 16384 + 8192);
        dma16<0>((unsigned)tid * 16u, p.x, wv * 16384 + 9216);
    }
    char* ws = p.ws;
    if (TLBW && j == 15 && wv == 5 && lane < 2) pf4s((unsigned)((x * 2 + lane) * 2097152), p.ckv, L_SCR + 31744);
    STAMP(0);
    const __amdgpu_buffer_rsrc_t rw = __builtin_amdgcn_make_buffer_rsrc(ws, 0, WS_END, 0x00020000);
    const u32x4 s4 = {SENT, SENT, SENT, SENT};
    unsigned long long cnt = 0ull;
    auto cnt_load = [&]() { asm volatile("global_load_dwordx2 %0, %1, %2 sc1" : "+v"(cnt) : "v"((unsigned)WS_EPOCH), "s"(ws) : "memory"); };
    unsigned tlo = 0u, thi = 0u;
    auto tag_take = [&]() {
        asm volatile("" : "+v"(cnt) :: "memory");
        const unsigned long long t = cnt + 1ull;
        tlo = __builtin_amdgcn_readfirstlane((unsigned)t); thi = __builtin_amdgcn_readfirstlane((unsigned)(t >> 32));
    };
    auto flag_put = [&](unsigned base, unsigned idx) {
        if (lane < 8) __builtin_amdgcn_raw_buffer_store_b64(u32x2{tlo, thi}, rw, base + lane * STR + idx * 8, 0, 16);
    };
    auto tagged_wait = [&](unsigned off) {
        u32x4 v;
#pragma unroll 1
        for (unsigned spins = 0; spins < kSpinCap; ++spins) {
            asm volatile("buffer_load_dwordx4 %0, %1, %2, 0 offen sc1\n\ts_waitcnt vmcnt(0)" : "=v"(v) : "v"(off), "s"(rw) : "memory");
            if (__all((v[2] == tlo) & (v[3] == thi))) break;
            __builtin_amdgcn_s_sleep(8);
        }
        return v;
    };
    auto flags_ok = [&](const u32x4& f) { return (f[0] == tlo) & (f[1] == thi) & (f[2] == tlo) & (f[3] == thi); };
    auto flags_wait = [&](unsigned off) {
        for (unsigned spins = 0;;) {
            u32x4 f;
            asm volatile("buffer_load_dwordx4 %0, %1, %2, 0 offen sc1\n\ts_waitcnt vmcnt(0)" : "=v"(f) : "v"(off), "s"(rw) : "memory");
            if (__all(flags_ok(f))) break;
            __builtin_amdgcn_s_sleep(16);
            if (++spins > kSpinCap) break;
            asm volatile("" ::: "memory");
        }
    };
    unsigned pcl, frl, frs;
    auto dma_piece = [&](const unsigned short* src, unsigned ldb, unsigned dst, int n, int nt) {
        const unsigned v0 = (lane >> 3) * ldb + pcl, v1 = v0 ^ 64u;
#pragma unroll
        for (int i = 0; i < n; ++i) {
            if (nt) dma16<1>((i & 1) ? v1 : v0, (const char*)src + (size_t)i * 8 * ldb, dst + i * 1024);
            else dma16<0>((i & 1) ? v1 : v0, (const char*)src + (size_t)i * 8 * ldb, dst + i * 1024);
        }
    };
    auto piece_frag = [&](unsigned base, int ks, int t) {
        const unsigned a = base + frl + ((t ^ frs) << 5) + ks * 4096;
        return tr_frag(a, a + 512);
    };
    const unsigned rgn = (unsigned)(wv * 16384);

    if (j < 16) {
        const unsigned long long t_start = __builtin_amdgcn_s_memrealtime();
        rederive();
        pcl = (unsigned)(((((lane & 7) >> 1) ^ ((lane >> 4) & 1)) * 32) + (lane & 1) * 16);
        frl = (unsigned)((g * 8 + (nl >> 2)) * 128 + (lane & 3) * 8); frs = (unsigned)((nl >> 3) | ((g & 1) << 1));
        const int th = x * 4 + (j & 3);
        if (j < 12) {
            const int qt = j >> 2, rot = qt * 5;
            const bool pub = wv < (qt < 2 ? 3 : 2);
            const unsigned qo = (unsigned)(WS_Q + (th & 7) * STR + ((nl & 7) * 4 + (th >> 3)) * 1024 + (qt * 192 + wv * 64 + g * 16 + (nl >> 3) * 8) * 2);
            const unsigned vb = (unsigned)(g * 8192 + ((nl ^ (g << 2)) << 4));
            const char* wuk = (const char*)(p.w_uk + th * 128) + (size_t)qt * 192 * 8192;
            const char* wq = (const char*)(p.w_q + th * 128) + (size_t)wv * 32768;
            const unsigned L_U = 114688;
            {
                const int k0 = qt < 2 ? wv * 6 : wv * 4;
#pragma unroll
                for (int i = 0; i < 6; ++i) {
                    if (i < 4 || qt < 2) dma16<NTM & 2>(vb ^ (((k0 + i) & 3) << 4), wuk + (size_t)(k0 + i) * 32768, L_U + (k0 + i) * 1024);
                    if (i == 4) VMW(4);
                }
            }
            const unsigned vq = vb ^ ((wv & 3) << 4);
            auto wq_dma = [&](int ps) { dma16<NTM & 2>(vq, wq + (size_t)((ps + rot) & 15) * 262144, ps * 8192 + wv * 1024); };
            bf16x8 uk[4][4], aq[16];
            u32x4 fi, hv[4];
            unsigned fio = (unsigned)(WS_FI + x * STR + (lane & 7) * 16), hsrc, hsrc2, ta0, ta1, tb0, tb1, qo2, hw0, hw1, hr, hr2, qr0, qr1, qw, thx = 0u;
            const unsigned L_HI = L_SCR, L_QI = L_SCR + 8192;
            auto h_load = [&]() {
                asm volatile("buffer_load_dwordx4 %0, %4, %6, 0 offen sc1\n\tbuffer_load_dwordx4 %1, %4, %6, 0 offen offset:1024 sc1\n\t"
                             "buffer_load_dwordx4 %2, %5, %6, 0 offen sc1\n\tbuffer_load_dwordx4 %3, %5, %6, 0 offen offset:1024 sc1"
                             : "+v"(hv[0]), "+v"(hv[1]), "+v"(hv[2]), "+v"(hv[3]) : "v"(hsrc), "v"(hsrc2), "s"(rw) : "memory");
            };
            auto h_bad = [&]() {
                PIN4(hv[0], hv[1], hv[2], hv[3]);
                unsigned bad = (hv[0][1] ^ tlo) | (hv[0][3] ^ thx) | (hv[1][1] ^ tlo) | (hv[1][3] ^ thx) | (hv[2][1] ^ tlo) | (hv[2][3] ^ thx) | (hv[3][1] ^ tlo) | (hv[3][3] ^ thx);
                PIN(bad);
                return bad;
            };
#pragma unroll
            for (int ps = 0; ps < 16; ++ps) {
                wq_dma(ps);
                if (ps == 4) cnt_load();
                if (ps == HP) { h_load(); asm volatile("buffer_load_dwordx4 %0, %1, %2, 0 offen sc1" : "=v"(fi) : "v"(fio), "s"(rw) : "memory"); }
                if (ps >= 4 && ps < 8) VMW(5); else if (ps >= HP && ps < HP + 4) VMW(9); else VMW(4);
                if (ps == 3) {
                    asm volatile("s_barrier" ::: "memory");
                    if (pub) {
#pragma unroll
                        for (int t = 0; t < 4; ++t)
#pragma unroll
                            for (int s = 0; s < 4; ++s) uk[t][s] = *LP(bf16x8, L_U + off_b(wv * 64 + (nl >> 2) * 16 + t * 4 + (nl & 3), s * 4 + g));
                    }
                    STAMP(1);
                }
                if (ps == 5) {
                    hsrc = (unsigned)(WS_H + x * STR + wv * 2048 + lane * 16); hsrc2 = hsrc + 16384;
                    ta0 = off_b(8 * g + (nl >> 2), 2 * wv + ((lane & 3) >> 1)) + 8 * (lane & 1); ta1 = off_b(8 * g + 4 + (nl >> 2), 2 * wv + ((lane & 3) >> 1)) + 8 * (lane & 1);
                    tb0 = ta0 + 65536; tb1 = ta1 + 65536; qo2 = qo;
                    hw0 = L_HI + ((2 * wv - rot) & 15) * 512 + (lane >> 4) * 128 + (lane & 7) * 16 + ((lane >> 3) & 1) * 8;
                    hw1 = L_HI + ((2 * wv + 1 - rot) & 15) * 512 + (lane >> 4) * 128 + (lane & 7) * 16 + ((lane >> 3) & 1) * 8;
                    hr2 = L_HI + g * 128 + (nl & 7) * 16 + (nl >> 3) * 512;
                    PIN(hr2);
                    qr0 = L_QI + g * 128 + (nl & 7) * 16 + (nl >> 3) * 2048; qr1 = qr0 ^ 2048u;
                    PIN(qr0); PIN(qr1);
                    hr = L_HI + g * 128 + (nl & 7) * 16; qw = L_QI + (wv >> 1) * 512 + ((wv & 1) * 2 + (g >> 1)) * 128 + (nl & 7) * 16 + (g & 1) * 8;
                    PIN4(hsrc, hsrc2, ta0, ta1); PIN4(tb0, tb1, qo2, hw0); PIN(hw1); PIN(qw);
                }
                if (ps == 8) { tag_take(); thx = tlo ^ thi ^ TAGC; }
                if (ps == 9) {
#pragma unroll
                    for (int t = 0; t < 4; ++t) PIN4(uk[t][0], uk[t][1], uk[t][2], uk[t][3]);
                    wg_barrier();
                    *LP(unsigned, L_QI + 2048 + wv * 256 + lane * 4) = 0u;
#pragma unroll
                    for (int q = 0; q < 6; ++q) aq[q] = tr_frag(ta0 + q * 8192, ta1 + q * 8192);
                }
                if (ps == 13) {
                    asm volatile("s_barrier" ::: "memory");
#pragma unroll
                    for (int q = 6; q < 10; ++q) aq[q] = q < 8 ? tr_frag(ta0 + q * 8192, ta1 + q * 8192) : tr_frag(tb0 + (q - 8) * 8192, tb1 + (q - 8) * 8192);
                }
            }
            STAMP(15);
            if (HP + 4 > 15) VMW(HP + 4 > 15 ? 15 - HP : 0);
            if (!__all(h_bad() == 0u)) {
                for (unsigned spins = 0;;) {
                    h_load();
                    VMW(0);
                    if (__all(h_bad() == 0u)) break;
                    __builtin_amdgcn_s_sleep(SLPH);
                    if (++spins > kSpinCap) break;
                }
            }
            {
                unsigned hh[4];
#pragma unroll
                for (int q = 0; q < 4; ++q) {
                    const unsigned t = hv[2 + (q >> 1)][(q & 1) * 2], sh = hv[q >> 1][(q & 1) * 2];
                    hh[q] = pk2(lo16(t) + lo16(sh), hi16(t) + hi16(sh));
                }
                *LP(u32x2, hw0) = u32x2{hh[0], hh[1]};
                *LP(u32x2, hw1) = u32x2{hh[2], hh[3]};
            }
            STAMP(3);
            asm volatile("s_waitcnt vmcnt(0) lgkmcnt(0)\n\ts_barrier" : "+v"(fi) :: "memory");
            STAMP(2);
            {
                bf16x8 hb[8];
#pragma unroll
                for (int q = 0; q < 5; ++q) hb[q] = *LP(bf16x8, hr2 + q * 1024);
#pragma unroll
                for (int ps = 10; ps < 16; ++ps) {
                    if (!(ps & 1)) hb[ps >> 1] = *LP(bf16x8, hr2 + (ps >> 1) * 1024);
                    aq[ps] = tr_frag(tb0 + (ps - 8) * 8192, tb1 + (ps - 8) * 8192);
                }
                __builtin_amdgcn_sched_barrier(0);
                f32x4 d0 = {0.f, 0.f, 0.f, 0.f}, d1 = {0.f, 0.f, 0.f, 0.f};
#pragma unroll
                for (int ps = 0; ps < 16; ps += 2) {
                    d0 = MFMA16(aq[ps], hb[ps >> 1], d0); d1 = MFMA16(aq[ps + 1], hb[ps >> 1], d1);
                    if (ps == 6) { unsigned bad = (fi[0] ^ tlo) | (fi[1] ^ thi) | (fi[2] ^ tlo) | (fi[3] ^ thi); PIN(bad); if (!__all(bad == 0u)) flags_wait(fio); }
                }
#pragma unroll
                for (int e = 0; e < 4; ++e) { float v = d0[e] + dppf<0x128>(d1[e]); asm("" : "+v"(v)); d0[e] = v; }
                if (nl < 8) *LP(u32x2, qw) = u32x2{pk2(d0[0], d0[1]), pk2(d0[2], d0[3])};
            }
            wg_barrier();
            if (pub) {
                bf16x8 qb[2][4];
#pragma unroll
                for (int s = 0; s < 4; ++s) { qb[0][s] = *LP(bf16x8, qr0 + s * 512); qb[1][s] = *LP(bf16x8, qr1 + s * 512); }
                __builtin_amdgcn_sched_barrier(0);
                f32x4 a = {0.f, 0.f, 0.f, 0.f}, b = a;
#pragma unroll
                for (int s = 0; s < 4; ++s) {
                    a = MFMA16(uk[0][s], qb[0][s], a); b = MFMA16(uk[1][s], qb[0][s], b);
                    a = MFMA16(uk[2][s], qb[1][s], a); b = MFMA16(uk[3][s], qb[1][s], b);
                }
                const u32x4 qv = {pk2s(a[0], a[1]), pk2s(a[2], a[3]), pk2s(b[0], b[1]), pk2s(b[2], b[3])};
                ST16(qv, qo2, 0);
            }
            STAMP(4);
            wg_barrier();
            if (LMPF && wv >= 3 && wv < 6) {
                const unsigned n = (unsigned)(j * 3 + wv - 3);
                if (n < 32) pf4<0>(((n & 7u) * 64 + lane) * 6144u + 2048u + (x * 4 + (n >> 3)) * 128u, p.w_mod, L_SCR + 31744);
            }
            if (QHELP && wv < 3 && j + 12 * wv < 32) {
                const int hh = j + 12 * wv;
#pragma unroll 1
                for (unsigned spins = 0; spins < 64; ++spins) {
                    __builtin_amdgcn_s_sleep(QHELP);
                    const u32x4 v = LD16((unsigned)(WS_Q + (hh & 7) * STR + (x * 4 + (hh >> 3)) * 1024 + lane * 16), 0);
                    if (__all(mx4(v, 0u) != SENT)) break;
                    asm volatile("" ::: "memory");
                }
                STAMP(6);
            }
        } else {
            const int cg = x * 2 + (j & 1);
            if (j < 14) {
                dma_piece(p.w_mod + (size_t)wv * 64 * 3072 + cg * 64, 6144, rgn, 8, NTM & 2);
                cnt_load();
                VMW(9);
            } else {
                cnt_load();
                ST16(s4, (unsigned)(WS_Q + wv * STR + cg * 2048 + lane * 16), 0); ST16(s4, (unsigned)(WS_Q + wv * STR + cg * 2048 + lane * 16), 1024);
                VMW(0);
                tag_take();
                wg_barrier();
                if (wv == 7) flag_put(WS_FI, cg);
                STAMP(15);
                if (j == 14) {
                    u32x2 best = {~0u, ~0u};
#pragma unroll
                    for (int r = 0; r < 2 * NCAL; ++r) {
                        const unsigned hf = (r ^ wv) & 1;
                        VMW(0);
                        const unsigned long long t0 = __builtin_amdgcn_s_memtime();
                        if (lane < 8) ST16(s4, WS_CAL + x * STR + hf * 8192 + (wv * 8 + r) * 128 + lane * 16, 0);
                        VMW(0);
                        const unsigned dt = (unsigned)(__builtin_amdgcn_s_memtime() - t0);
                        if (hf) best[1] = min(best[1], dt); else best[0] = min(best[0], dt);
                    }
                    *LP(u32x2, 16384 * 7 + 14336 + wv * 8) = best;
                    wg_barrier();
                    if (wv == 0 && lane < 8) {
                        u32x2 m = *LP(u32x2, 16384 * 7 + 14336 + lane * 8);
#pragma unroll
                        for (int o = 1; o < 8; o <<= 1) { m[0] = min(m[0], (unsigned)__shfl_xor(m[0], o)); m[1] = min(m[1], (unsigned)__shfl_xor(m[1], o)); }
                        ST16((u32x4{(unsigned)max(-1000, min(1000, (int)m[1] - (int)m[0])), 0u, tlo, thi}), (unsigned)(WS_NP + lane * STR + x * 16), 0);
                    }
                    if (wv == 0) {
                        const int d = (int)tagged_wait(WS_NP + x * STR + (lane & 7) * 16)[0];
                        int t = (lane & 4) ? -d : d;
                        t += __builtin_amdgcn_update_dpp(0, t, 0xB1, 0xf, 0xf, true);
                        t += __builtin_amdgcn_update_dpp(0, t, 0x4E, 0xf, 0xf, true);
                        t += __builtin_amdgcn_update_dpp(0, t, 0x141, 0xf, 0xf, true);
                        const bool n1 = abs(d) > 160 ? d < 0 : (t < 0) != ((lane & 4) != 0);
                        const unsigned mk = NEARP ? (unsigned)__ballot(n1) & 0xffu : 0u;
                        if (lane == 0) ST16((u32x4{mk, 0u, tlo, thi}), (unsigned)(WS_NM + x * STR), 0);
                    }
                    STAMP(9);
                }
            }
            const u16x8 cf = *LP(u16x8, rgn + 8192 + lane * 16), xa = *LP(u16x8, rgn + 9216 + lane * 16);
            u32x4 af[2];
#pragma unroll
            for (int q = 0; q < 4; ++q) {
                const float a = bf2f(cf[2 * q]), b = bf2f(cf[2 * q + 1]);
                af[0][q] = pk2(a * __builtin_amdgcn_rcpf(1.f + __expf(-a)), b * __builtin_amdgcn_rcpf(1.f + __expf(-b)));
                af[1][q] = (unsigned)__builtin_amdgcn_update_dpp(0, (int)af[0][q], 0x128, 0xf, 0xf, true);
            }
            const unsigned L_MS = L_SCR;
            unsigned wa[4], fr[4];
#pragma unroll
            for (int t = 0; t < 4; ++t) { wa[t] = L_MS + wv * 2048 + (nl & 7) * 256 + (((t * 4 + g) ^ (nl & 7)) << 4) + (nl >> 3) * 16384; fr[t] = rgn + frl + ((t ^ frs) << 5); }
            PIN4(wa[0], wa[1], wa[2], wa[3]); PIN4(fr[0], fr[1], fr[2], fr[3]); PIN(af[0]); PIN(af[1]);
            const unsigned L_XN = 12288;
            auto mod_mm = [&](bool ln) {
                VMW(0);
                bf16x8 f[2][4];
#pragma unroll
                for (int ks = 0; ks < 2; ++ks)
#pragma unroll
                    for (int t = 0; t < 4; ++t) f[ks][t] = tr_frag(fr[t] + ks * 4096, fr[t] + ks * 4096 + 512);
                __builtin_amdgcn_sched_barrier(0);
                f32x4 d[4];
#pragma unroll
                for (int t = 0; t < 4; ++t) d[t] = MFMA16(f[0][t], BF8(af[0]), (f32x4{0.f, 0.f, 0.f, 0.f}));
#pragma unroll
                for (int t = 0; t < 4; ++t) d[t] = MFMA16(f[1][t], BF8(af[1]), d[t]);
#pragma unroll
                for (int t = 0; t < 4; ++t) *LP(f32x4, wa[t]) = d[t];
                if (ln) {
                    float v[8];
#pragma unroll
                    for (int q = 0; q < 8; ++q) v[q] = bf2f(xa[q]);
                    const float p1 = ((v[0] + v[1]) + (v[2] + v[3])) + ((v[4] + v[5]) + (v[6] + v[7]));
                    const float p2 = ((v[0] * v[0] + v[1] * v[1]) + (v[2] * v[2] + v[3] * v[3])) + ((v[4] * v[4] + v[5] * v[5]) + (v[6] * v[6] + v[7] * v[7]));
                    float mb, rden;
                    ln_stats(p1, p2, mb, rden);
                    u32x4 r;
#pragma unroll
                    for (int q = 0; q < 4; ++q) { const unsigned d = pk2(v[2 * q] - mb, v[2 * q + 1] - mb); r[q] = pk2(lo16(d) * rden, hi16(d) * rden); }
                    *LP(u32x4, L_XN + wv * 16384 + lane * 16) = r;
                }
                wg_barrier();
            };
            auto sum8 = [&](unsigned a) {
                f32x4 u[8];
#pragma unroll
                for (int w2 = 0; w2 < 8; ++w2) u[w2] = *LP(f32x4, a + w2 * 2048);
                __builtin_amdgcn_sched_barrier(0);
                return ((u[0] + u[1]) + (u[2] + u[3])) + ((u[4] + u[5]) + (u[6] + u[7]));
            };
            if (j < 14) {
                const int hb = lane & 7, c4 = 2 * wv + (nl >> 3);
                unsigned hoff = (unsigned)(WS_H + g * STR + cg * 2048 + wv * 256 + nl * 16);
                unsigned sa = L_MS + hb * 256 + ((c4 ^ hb) << 4);
                unsigned xa2 = L_XN + hb * 16384 + (cg & 7) * 128 + c4 * 8;
                PIN(hoff); PIN(sa); PIN(xa2);
                STAMP(1);
                mod_mm(cg >= 8);
                STAMP(2);
                const u32x2 xn = *LP(u32x2, xa2);
                const f32x4 v = sum8(sa);
                tag_take();
                const unsigned thx = tlo ^ thi ^ TAGC;
                u32x2 r = {pk2(v[0], v[1]), pk2(v[2], v[3])};
                if (cg >= 8) {
#pragma unroll
                    for (int q = 0; q < 2; ++q) { const unsigned sc = pk2(1.f + lo16(r[q]), 1.f + hi16(r[q])); r[q] = pk2(lo16(xn[q]) * lo16(sc), hi16(xn[q]) * hi16(sc)); }
                }
                ST16((u32x4{r[0], tlo, r[1], thx}), hoff, 0);
                ST16((u32x4{r[0], tlo, r[1], thx}), hoff, 4 * STR);
                STAMP(5);
            }
            wg_barrier();
            while (__builtin_amdgcn_s_memrealtime() < t_start + LMD) __builtin_amdgcn_s_sleep(2);
            const int lg = x * 4 + (j & 3);
            dma_piece(p.w_mod + (size_t)wv * 64 * 3072 + 1024 + lg * 64, 6144, rgn, 8, NTM & 4);
            mod_mm(false);
            if (lg >= 8 && lg < 24) {
                if (wv == 0) {
                    const int b = lane >> 3, c8 = lane & 7;
                    const f32x4 v0 = sum8(L_MS + b * 256 + (((2 * c8) ^ b) << 4)), v1 = sum8(L_MS + b * 256 + (((2 * c8 + 1) ^ b) << 4));
                    ST16((u32x4{pk2(v0[0], v0[1]), pk2(v0[2], v0[3]), pk2(v1[0], v1[1]), pk2(v1[2], v1[3])}), (unsigned)(WS_MB + b * STR + (lg - 8) * 128 + c8 * 16), 0);
                }
            } else if (wv < 4) {
                const int b = wv * 2 + (lane >> 5), cp = lane & 31;
                f32x2_t u[8];
#pragma unroll
                for (int w2 = 0; w2 < 8; ++w2) u[w2] = *LP(f32x2_t, L_MS + w2 * 2048 + b * 256 + (((cp >> 1) ^ b) << 4) + (cp & 1) * 8);
                const f32x2_t v = ((u[0] + u[1]) + (u[2] + u[3])) + ((u[4] + u[5]) + (u[6] + u[7]));
                ST16((u32x4{__float_as_uint(rbf(v[0])), __float_as_uint(rbf(v[1])), tlo, thi}), (unsigned)(WS_MOD + b * STR + (lg * 32 + cp) * 16), 0);
            }
            STAMP(6);
        }
        const int bq = (j >> 2) & 1, dh = j >> 3, src = th * 2 + dh, rb = bq * 4 + (src >> 4), rc = src & 15;
        wg_barrier();
        if (tid == 0) __hip_atomic_store((gu64*)(p.ws + (128u << 20)) + id, t00, RLX_AGENT);
        if (TLBW > 1 && j == 0) {
            if (wv == 4 && lane < 5) pf4s((unsigned)((lane + 1) * 2097152 + 8064), ws, L_SCR + 31744);
            if (wv == 5 && lane < 2) { pf4s((unsigned)(lane * 2097152), p.w_uv, L_SCR + 31744); pf4s((unsigned)(lane * 2097152), p.w_o, L_SCR + 31744); }
            if (wv == 6 && lane < 1) { pf4s(0u, p.w1, L_SCR + 31744); pf4s(0u, p.w2, L_SCR + 31744); pf4s(0u, p.out, L_SCR + 31744); }
        }
        while (__builtin_amdgcn_s_memrealtime() < t_start + TWD - 110) __builtin_amdgcn_s_sleep(2);
        const unsigned npm = __builtin_amdgcn_readfirstlane(tagged_wait(WS_NM + x * STR)[0]), np = (npm >> x) & 1u;
        unsigned oo = (unsigned)(WS_OUTP + ((bq * 4 + (nl & 3)) * 16 + wv * 2 + (nl >> 3)) * 16384 + (src ^ wv) * 128 + ((nl >> 2) & 1) * 64 + g * 16) + ((~npm >> ((nl & 3) * 2 + (wv >> 2))) & 1u) * 8192u;
        const int rcol = rc * 32 + (lane & 7) * 4 + (lane >> 5) * 2;
        unsigned xdst = (unsigned)(WS_X1 + ((lane >> 3) & 3) * STR + (rb * 512 + rcol) * 2), xdst2 = xdst + 4 * STR + ((npm >> (4 + ((lane >> 3) & 3))) & 1u) * 8192u;
        xdst += ((npm >> ((lane >> 3) & 3)) & 1u) * 8192u;
        ST16(s4, oo, 0);
        if (wv == 7) { __builtin_amdgcn_raw_buffer_store_b32(SENT, rw, xdst, 0, 16); __builtin_amdgcn_raw_buffer_store_b32(SENT, rw, xdst2, 0, 16); }
        VMW(0);
        wg_barrier();
        if (wv == 0) flag_put(WS_FB, id);
        while (__builtin_amdgcn_s_memrealtime() < t_start + TWD) __builtin_amdgcn_s_sleep(2);
        asm volatile("s_barrier" ::: "memory");
        STAMP(7);
        if ((PFM & 8) && wv == 0 && lane < 32) pf4<NTM & 8>((unsigned)((j * 32 + lane) * 8192 + x * 1024 + ((3u - ((unsigned)(size_t)p.w_uv >> 7)) & 7u) * 128), p.w_uv, L_SCR + 31744);
        if (bq) dma_piece(p.w_o + (size_t)(th * 128 + dh * 64) * 512 + wv * 64, 1024, rgn + 8192, 8, NTM & 8);
        dma_piece(p.w_uv + ((size_t)wv * 64 * NH + th) * 128 + dh * 64, 8192, rgn, 8, NTM & 8);
        if (!bq) dma_piece(p.w_o + (size_t)(th * 128 + dh * 64) * 512 + wv * 64, 1024, rgn + 8192, 8, NTM & 8);
        u32x4 fb, gm;
        unsigned xg;
        VMW(4);
        if (wv == 7)
            asm volatile("buffer_load_dwordx4 %0, %3, %5, 0 offen sc1\n\tbuffer_load_dwordx4 %1, %4, %5, 0 offen sc1\n\tglobal_load_dword %2, %6, %7" : "=&v"(fb), "=&v"(gm), "=&v"(xg)
                         : "v"((unsigned)(WS_FB + x * STR + lane * 16)), "v"((unsigned)(WS_MOD + rb * STR + rcol * 8)), "s"(rw), "v"((unsigned)(rb * 512 + rcol) * 2u), "s"(p.x) : "memory");
        VMW(0);
        asm volatile("" : "+v"(fb), "+v"(gm), "+v"(xg));
        STAMP(8);
        bf16x8 fuv[2][4], fwo[2][4];
        unsigned pfa[4];
#pragma unroll
        for (int t = 0; t < 4; ++t) { pfa[t] = rgn + frl + ((t ^ frs) << 5); PIN(pfa[t]); }
#pragma unroll
        for (int ks = 0; ks < 2; ++ks)
#pragma unroll
            for (int t = 0; t < 4; ++t) {
                const unsigned a = rgn + (unsigned)((g * 4 + (nl >> 2)) * 128 + (lane & 3) * 8) + ((t ^ (unsigned)((nl >> 3) | (g & 2))) << 5) + ks * 4096;
                fuv[ks][t] = tr_frag(a, a + 2048);
                fwo[ks][t] = tr_frag(pfa[t] + 8192 + ks * 4096, pfa[t] + 8192 + ks * 4096 + 512);
            }
#pragma unroll
        for (int ks = 0; ks < 2; ++ks) { PIN4(fuv[ks][0], fuv[ks][1], fuv[ks][2], fuv[ks][3]); PIN4(fwo[ks][0], fwo[ks][1], fwo[ks][2], fwo[ks][3]); }
        const unsigned L_WGT = L_SCR, L_MSK = L_SCR + 256, L_RED = L_SCR + 1024;
        unsigned psrc = (unsigned)(WS_PART + (bq * 4 * 32 + th) * 32768 + (wv >> 2) * 16384 + (wv & 3) * 2048 + ((lane >> 3) ^ (th & 7)) * 128) + np * 8192u + pcl, psrc1 = (psrc ^ 64u) + 1024;
        unsigned psrc2 = psrc ^ 8192u, psrc3 = psrc1 ^ 8192u;
        PIN(psrc2); PIN(psrc3); PIN(xdst2);
        unsigned rw2 = L_RED + wv * 1024 + (nl & 3) * 256 + g * 16 + (YSEL ? (nl >> 2) * 64 : 0);
        unsigned wa0 = (nl & 3) == (g >> 1) ? L_WGT + (HILO ? nl & 4 : 0) * 32 + g * 16 : L_WGT + 272, wa1 = (nl & 3) == 2 + (g >> 1) ? L_WGT + (HILO ? nl & 4 : 0) * 32 + 64 + g * 16 : L_WGT + 272;
        unsigned cr = rgn + (nl & 3) * 128 + g * 16;
        PIN(cr);
        unsigned r2r = L_RED + lane * 16, cw = rgn + lane * 8;
        unsigned osrc = (unsigned)(WS_OUTP + (rb * 16 + rc) * 16384 + lane * 16) + (np ^ 1u) * 8192u;
        PIN(psrc); PIN(psrc1); PIN(rw2); PIN(wa0); PIN(wa1); PIN(r2r); PIN4(cw, oo, osrc, xdst);
        float mlm = 0.f, mll = 0.f;
        if (wv == 0) *LP(unsigned, L_WGT + 256 + lane * 4) = 0u;
        wg_barrier();
        const bool early = __all(flags_ok(fb) & (gm[2] == tlo) & (gm[3] == thi));
        u32x2 ga = {gm[0], gm[1]};
        PIN(ga);
        bf16x8 pa[2][4], wh[2];
        {
            u32x4 ml = {0u, 0u, ~tlo, ~thi};
            const unsigned mlsrc = (unsigned)(WS_ML + (bq * 4 + g) * STR + nl * 512 + th * 16) + ((npm >> (x & 6)) & 1u) * 8192u;
            unsigned long long done = 0ull;
            unsigned lm = L_MSK;
            PIN(lm);
#pragma unroll 1
            for (unsigned spins = 0; spins < kSpinCap; ++spins) {
                unsigned long long m;
                if (wv == 0) {
                    if ((ml[2] != tlo) | (ml[3] != thi)) ml = LD16(mlsrc, 0);
                    m = __ballot((ml[2] == tlo) & (ml[3] == thi));
                    unsigned long long f = m & (m >> 4);
                    f &= f >> 2; f &= f >> 1;
                    asm volatile("ds_write_b64 %0, %1" :: "v"(lm), "v"(f) : "memory");
                    m = m == ~0ull ? 0x0101010101010101ull : 0ull;
                } else {
                    unsigned long long t;
                    asm volatile("ds_read_b64 %0, %1\n\ts_waitcnt lgkmcnt(0)" : "=v"(t) : "v"(lm) : "memory");
                    m = ((unsigned long long)(unsigned)__builtin_amdgcn_readfirstlane((unsigned)(t >> 32)) << 32) | (unsigned)__builtin_amdgcn_readfirstlane((unsigned)t);
                    m &= 0x0101010101010101ull & ~done;
                }
                if (m) {
                    done |= m;
#define PLD(bit, off, v, base) "s_bitcmp1_b64 %0, " #bit "\n\ts_cbranch_scc0 1f\n\ts_add_u32 m0, %1, " #off "\n\ts_nop 0\n\tglobal_load_lds_dwordx4 " v ", " base " sc1\n1:\n\t"
                    asm volatile(PLD(0, 0, "%2", "%4") PLD(8, 1024, "%3", "%4") PLD(16, 2048, "%2", "%5") PLD(24, 3072, "%3", "%5")
                                 PLD(32, 4096, "%8", "%6") PLD(40, 5120, "%9", "%6") PLD(48, 6144, "%8", "%7") PLD(56, 7168, "%9", "%7")
                                 :: "s"(m), "s"(rgn), "v"(psrc), "v"(psrc1), "s"(ws), "s"(ws + 1048576), "s"(ws + 2097152), "s"(ws + 3145728), "v"(psrc2), "v"(psrc3) : "memory", "m0", "scc");
                    if (done == 0x0101010101010101ull) break;
                } else if (SLPM > 0 && wv == 0) __builtin_amdgcn_s_sleep(SLPM);
            }
            mlm = __uint_as_float(ml[0]); mll = __uint_as_float(ml[1]);
            STAMP(10);
            if (wv == 0) {
                float M = mlm;
                M = fmaxf(M, dppf<0xB1>(M)); M = fmaxf(M, dppf<0x4E>(M)); M = fmaxf(M, dppf<0x141>(M)); M = fmaxf(M, dppf<0x140>(M));
                const float e = __builtin_amdgcn_exp2f(mlm - M);
                const float w = e * __builtin_amdgcn_rcpf(sum16(e * mll));
                const unsigned short hi = f2bf(w);
                *LP(unsigned short, L_WGT + lane * 2) = hi;
                if (HILO) *LP(unsigned short, L_WGT + 128 + lane * 2) = f2bf(w - bf2f(hi));
            }
            wg_barrier();
            wh[0] = *LP(bf16x8, wa0); wh[1] = *LP(bf16x8, wa1);
            __builtin_amdgcn_sched_barrier(0);
            VMW(0);
#pragma unroll
            for (int ks = 0; ks < 2; ++ks)
#pragma unroll
                for (int t = 0; t < 4; ++t) pa[ks][t] = tr_frag(pfa[t] + ks * 4096, pfa[t] + ks * 4096 + 512);
        }
        STAMP(11);
        {
            f32x4 d[4];
#pragma unroll
            for (int t = 0; t < 4; ++t) d[t] = MFMA16(pa[0][t], wh[0], (f32x4{0.f, 0.f, 0.f, 0.f}));
#pragma unroll
            for (int t = 0; t < 4; ++t) d[t] = MFMA16(pa[1][t], wh[1], d[t]);
#pragma unroll
            for (int t = 0; t < 4; ++t)
#pragma unroll
                for (int e = 0; e < 4; ++e) if (HILO) { float v = d[t][e] + dppf<0x124>(d[t][e]); asm("" : "+v"(v)); d[t][e] = v; }
            const u32x4 o0 = {pk2(d[0][0], d[0][1]), pk2(d[0][2], d[0][3]), pk2(d[1][0], d[1][1]), pk2(d[1][2], d[1][3])};
            const u32x4 o1 = {pk2(d[2][0], d[2][1]), pk2(d[2][2], d[2][3]), pk2(d[3][0], d[3][1]), pk2(d[3][2], d[3][3])};
            f32x4 y[4];
#pragma unroll
            for (int t = 0; t < 4; ++t) y[t] = MFMA16(fuv[0][t], BF8(o0), (f32x4{0.f, 0.f, 0.f, 0.f}));
#pragma unroll
            for (int t = 0; t < 4; ++t) y[t] = MFMA16(fuv[1][t], BF8(o1), y[t]);
            if (YSEL) *LP(f32x4, rw2) = (nl & 8) ? ((nl & 4) ? y[3] : y[2]) : ((nl & 4) ? y[1] : y[0]);
            else if (nl < 4) {
#pragma unroll
                for (int t = 0; t < 4; ++t) *LP(f32x4, rw2 + t * 64) = y[t];
            }
        }
        wg_barrier();
        {
            f32x4 cs[8];
#pragma unroll
            for (int w2 = 0; w2 < 8; ++w2) cs[w2] = *LP(f32x4, r2r + w2 * 1024);
            const f32x4 c4 = ((cs[0] + cs[1]) + (cs[2] + cs[3])) + ((cs[4] + cs[5]) + (cs[6] + cs[7]));
            *LP(u32x2, cw) = u32x2{pk2(c4[0], c4[1]), pk2(c4[2], c4[3])};
            asm volatile("s_waitcnt lgkmcnt(0)" ::: "memory");
            const u32x4 cx0 = *LP(u32x4, cr), cx1 = *LP(u32x4, cr + 64);
            f32x4 o[4];
#pragma unroll
            for (int q = 0; q < 4; ++q) o[q] = MFMA16(fwo[0][q], BF8(cx0), (f32x4{0.f, 0.f, 0.f, 0.f}));
#pragma unroll
            for (int q = 0; q < 4; ++q) o[q] = MFMA16(fwo[1][q], BF8(cx1), o[q]);
            const f32x4 oa = (nl & 8) ? ((nl & 4) ? o[3] : o[2]) : ((nl & 4) ? o[1] : o[0]);
            const u32x4 sv = {nosent(__float_as_uint(oa[0])), nosent(__float_as_uint(oa[1])), nosent(__float_as_uint(oa[2])), nosent(__float_as_uint(oa[3]))};
            ST16(sv, oo, 0);
        }
        STAMP(12);
        wg_barrier();
        if (wv == 7) {
            if (!early) {
                flags_wait(WS_FB + x * STR + lane * 16);
                { const u32x4 v = tagged_wait(WS_MOD + rb * STR + rcol * 8); ga = u32x2{v[0], v[1]}; }
                PIN(ga);
            }
            u32x4 op[8];
            for (unsigned spins = 0;;) {
#pragma unroll
                for (int k = 0; k < 8; ++k) op[k] = LD16(osrc + (k & 3) * 1024, (k >> 2) * 4096);
                unsigned m = 0u;
#pragma unroll
                for (int k = 0; k < 8; ++k) m = mx4(op[k], m);
                if (__all(m != SENT)) break;
                if (++spins > kSpinCap) break;
                asm volatile("" ::: "memory");
            }
            STAMP(13);
            const f32x4 v = ((__builtin_bit_cast(f32x4, op[0]) + __builtin_bit_cast(f32x4, op[1])) + (__builtin_bit_cast(f32x4, op[2]) + __builtin_bit_cast(f32x4, op[3]))) +
                            ((__builtin_bit_cast(f32x4, op[4]) + __builtin_bit_cast(f32x4, op[5])) + (__builtin_bit_cast(f32x4, op[6]) + __builtin_bit_cast(f32x4, op[7])));
            float w[2];
#pragma unroll
            for (int q = 0; q < 2; ++q) {
                auto t = __builtin_amdgcn_permlane32_swap(__float_as_uint(v[q]), __float_as_uint(v[2 + q]), false, false);
                w[q] = __uint_as_float(t[0]) + __uint_as_float(t[1]);
                auto u = __builtin_amdgcn_permlane16_swap(__float_as_uint(w[q]), __float_as_uint(w[q]), false, false);
                w[q] = __uint_as_float(u[0]) + __uint_as_float(u[1]);
                w[q] += dppf<0x128>(w[q]);
            }
            unsigned t = pk2(w[0], w[1]);
            t = pk2(__uint_as_float(ga[0]) * lo16(t), __uint_as_float(ga[1]) * hi16(t));
            t = pk2s(lo16(xg) + lo16(t), hi16(xg) + hi16(t));
            __builtin_amdgcn_raw_buffer_store_b32(t, rw, xdst, 0, 16);
            __builtin_amdgcn_raw_buffer_store_b32(t, rw, xdst2, 0, 16);
            STAMP(14);
        }
        STAMP_FLUSH();
        return;
    }

    const int s = j - 16;
    const unsigned long long t_s = __builtin_amdgcn_s_memrealtime();
    if (wv == 0) cnt_load();
    {
        const unsigned ln = (unsigned)(s * 64 + lane);
        if ((PFM & 1) && wv == 0) pf4<NTM & 1>((ln & 511u) * 6144u + (x * 2 + (ln >> 9)) * 128u, p.w_mod, L_SCR);
        if ((PFM & 2) && wv == 1) {
            const unsigned short* w = s < 8 ? p.w_uk : p.w_q;
            pf4<NTM & 1>((ln & 511u) * 8192u + x * 1024u + ((3u - ((unsigned)(size_t)w >> 7)) & 7u) * 128u, w, L_SCR);
        }
        if ((PFM & 48) == 48) {
            const unsigned r0 = (unsigned)(s * 4 + (wv & 3)), row = PFS == 0 ? r0 * 8 + (lane >> 3) : PFS == 1 ? (lane >> 3) * 64 + r0 : (r0 >> 5) * 256 + (lane >> 3) * 32 + (r0 & 31);
            pf4<NTM & 1>(row * 8192u + x * 1024u + (lane & 7u) * 128u, wv < 4 ? p.w_uk : p.w_q, L_SCR);
        }
        else if ((PFM & 16) && wv >= 4) { const unsigned l2 = (unsigned)((s * 4 + (wv & 3)) * 64 + lane); pf4<NTM & 1>((l2 >> 3) * 8192u + x * 1024u + (l2 & 7u) * 128u, p.w_q, L_SCR); }
        else if ((PFM & 32) && wv >= 4) { const unsigned l2 = (unsigned)((s * 4 + (wv & 3)) * 64 + lane); pf4<NTM & 1>((l2 >> 3) * 8192u + x * 1024u + (l2 & 7u) * 128u, p.w_uk, L_SCR); }
        if ((PFM & 4) && (wv & 6) == 2) { const unsigned l2 = (unsigned)((s * 2 + (wv & 1)) * 64 + lane); pf4<NTM & 1>((l2 & 511u) * 8192u + (x * 4 + (l2 >> 9)) * 256u, p.w_q, L_SCR); }
    }
    const int ug = x * 7 + s;
    const unsigned vb = (unsigned)(g * 1024 + ((nl ^ (g << 2)) << 4)), lb = (unsigned)((wv >> 1) * 32768 + (wv & 1) * 4096);
    const char* ck = (const char*)p.ckv + ((size_t)x * SK + s * 256 + wv * 16) * 1024;
    const unsigned rot = ROTC ? (unsigned)s & 3u : 0u;
    const unsigned vbr = vb ^ (rot << 8);
    const char* ck2 = ck + 131072;
    auto ck_dma = [&](unsigned i) { dma16x<NTM & 16>(vbr, ((i & 3u) << 4) | (((i >> 2) & 3u) << 8) | ((i & 3u) << 12), (i >> 4) ? ck2 : ck, lb + ((i >> 2) & 3u) * 8192u + (i & 3u) * 1024u); };
    bf16x8 ka[16];
    s16x4 va[8][4];
    unsigned kad[4], vad[4], vad2[4];
    auto kq = [](unsigned u) { return (0x78u >> (2 * u)) & 3u; };
    auto ka_reads = [&](int cb) {
#pragma unroll
        for (int u = 0; u < 4; ++u) ka[cb * 4 + u] = *LP(bf16x8, kad[u] + cb * 8192);
    };
    auto va_reads = [&]() {
#pragma unroll
        for (int kk = 0; kk < 8; ++kk)
#pragma unroll
            for (int q = 0; q < 4; ++q) va[kk][q] = tr4((kk < 4 ? vad[q] : vad2[q]) + ((kk >> 1) & 1) * 32768 + (kk & 1) * 4096);
    };
    auto va_read1 = [&](int kk) {
#pragma unroll
        for (int q = 0; q < 4; ++q) va[kk][q] = tr4((kk < 4 ? vad[q] : vad2[q]) + ((kk >> 1) & 1) * 32768 + (kk & 1) * 4096);
    };
    auto va_pin = [&]() {
#pragma unroll
        for (int q = 0; q < 8; ++q) PIN4(va[q][0], va[q][1], va[q][2], va[q][3]);
    };
    u32x4 fa = {0u, 0u, 0u, 0u};
    const unsigned fao = (unsigned)(WS_FI + x * STR + (lane & 7) * 16);
    constexpr int LAST0 = GI + NPOST > 19 ? GI + NPOST : 19, LAST = LAST0 + NP2;
    const unsigned qsrc = (unsigned)(WS_Q + (wv & 1) * 4 * STR + (x * 4 + (wv >> 1)) * 1024) + (g ^ rot) * 256 + ((nl ^ ((wv * 4) & 15)) << 4);
    auto q_dma = [&](int k) {
        unsigned t;
        asm volatile("s_mov_b32 m0, %3\n\tv_xor_b32 %0, %4, %1\n\tglobal_load_lds_dwordx4 %0, %2 sc1" : "=&v"(t) : "v"(qsrc), "s"(ws), "s"(L_SCR + (wv * 4 + k) * 1024), "i"(k * (STR + 16)) : "memory", "m0");
    };
#pragma unroll
    for (int i = 0; i <= LAST0; ++i) {
        ck_dma(i);
        if (i == 3) {
#pragma unroll
            for (int u = 0; u < 4; ++u) {
                kad[u] = tile_off(wv * 16 + kq(nl >> 2) * 4 + (nl & 3), 4 * u + g);
                vad[u] = tile_off(8 * (lane >> 5) + 4 * (u & 1) + (nl >> 2), wv * 8 + 4 * (u >> 1) + 2 * (g & 1) + ((lane & 3) >> 1)) + 8 * (lane & 1);
                vad2[u] = vad[u] + 65536;
                PIN(kad[u]); PIN(vad[u]); PIN(vad2[u]);
            }
        }
        if (i > GI && i < GI + NPOST) { if (i < GI + 4) VMW(8); } else if (i == GI + NPOST) VMW(NPOST); else if (i < NTH) { if (i >= W0) VMW(W0); } else if (i >= 4) VMW(4);
        if (i == 15 && wv == 0) {
            PIN(fa);
            tag_take();
            if (!__all(flags_ok(fa))) flags_wait(fao);
        }
        if ((i & 3) == 3 && i >= 7 && i <= 19) {
            const int cb = (i - 7) >> 2;
            if (cb > 0) { PIN4(ka[cb * 4 - 4], ka[cb * 4 - 3], ka[cb * 4 - 2], ka[cb * 4 - 1]); va_pin(); }
            wg_barrier();
            ka_reads(cb);
            if ((wv >> 1) == cb) va_reads();
            STAMP(16 + cb);
        }
        if (i == 10 && wv == 0) asm volatile("buffer_load_dwordx4 %0, %1, %2, 0 offen sc1" : "+v"(fa) : "v"(fao), "s"(rw) : "memory");
        if (i == GI && TG > 0) { while (__builtin_amdgcn_s_memrealtime() < t_s + TG) __builtin_amdgcn_s_sleep(1); }
        if (i == GI && WG < 4) VMW(WG);
        if (i == GI) { q_dma(0); q_dma(1); q_dma(2); q_dma(3); asm volatile("s_barrier" ::: "memory"); }
    }
    const unsigned L_EXM = L_SCR - 1024, L_BIG = L_SCR + 512 + 128;
    unsigned nx = LAST + 1, npm = 0u;
    unsigned qad[4];
    {
        unsigned qck = L_SCR + wv * 4096 + lane * 16;
        u32x4 qb[4];
#pragma unroll
        for (int k = 0; k < 4; ++k) qb[k] = *LP(u32x4, qck + k * 1024);
#pragma unroll
        for (int i = LAST0 + 1; i <= LAST; ++i) ck_dma(i);
#pragma unroll
        for (int k = 0; k < 4; ++k) { qad[k] = L_SCR + nl * 1024 + (((4 * k + g) ^ nl) << 4); PIN(qad[k]); }
        STAMP(20);
        if (!__all(mx4(qb[0], mx4(qb[1], mx4(qb[2], mx4(qb[3], 0u)))) != SENT)) {
            for (unsigned spins = 0;;) {
#pragma unroll
                for (int k = 0; k < 4; ++k)
                    if (!__all(mx4(qb[k], 0u) != SENT)) q_dma(k);
                if (nx <= 25) {
#pragma unroll
                    for (int i = 0; i < 3; ++i) ck_dma(nx + i);
                    nx += 3;
                    VMW(3);
                } else {
                    for (; nx < 28; ++nx) ck_dma(nx);
                    VMW(0);
                }
#pragma unroll
                for (int k = 0; k < 4; ++k) qb[k] = *LP(u32x4, qck + k * 1024);
                if (__all(mx4(qb[0], mx4(qb[1], mx4(qb[2], mx4(qb[3], 0u)))) != SENT)) break;
                if (++spins > kSpinCap) break;
            }
        }
    }
    constexpr int NE = LAST + 1 + SL0 + 4 < 31 ? LAST + 1 + SL0 + 4 : 31;
    auto slot = [&](int i) { if (i < 28) { if (nx <= (unsigned)i) ck_dma(i); } else ck_dma(i); };
    const unsigned kg = kq(g);
    unsigned exw = L_EXM + ((nl + (lane >> 5) * 16) * 8 + wv) * 4, exr = L_EXM + nl * 32;
    unsigned pwr = L_SCR + nl * 1024 + 768 + (((2 * wv + (kg >> 1)) ^ nl) << 4) + (kg & 1) * 8, esw = L_SCR + nl * 1024 + 512 + ((wv ^ (nl & 7)) << 4) + g * 4;
    unsigned prd = L_SCR + (lane & 31) * 1024 + 768 + (((lane >> 5) ^ nl) << 4);
    PIN4(exw, exr, pwr, esw); PIN(prd);
    PIN4(ka[12], ka[13], ka[14], ka[15]); va_pin();
    wg_barrier();
    STAMP(21);
    bf16x8 qr[8][2];
    auto q_ld = [&](int ks, int hg) { return *LP(bf16x8, qad[ks & 3] + (ks >> 2) * 256 + hg * 16384); };
    auto p_ld = [&](int kk) { return *LP(bf16x8, prd ^ (kk << 5)); };
    float ls0, ls1, m0, m1;
    f32x4 sa, sb;
    f32x16 o[2];
    unsigned sw[4], srd, p0, mlo, esr, fbo;
    u32x2 tg;
    u32x4 nq;
    auto probs = [&](float (&e)[8]) {
#pragma unroll
        for (int r = 0; r < 4; ++r) { e[r] = __builtin_amdgcn_exp2f(fmaf(sa[r], SCL, -m0)); e[4 + r] = __builtin_amdgcn_exp2f(fmaf(sb[r], SCL, -m1)); }
        *LP(u32x2, pwr) = u32x2{pk2(e[0], e[1]), pk2(e[2], e[3])};
        *LP(u32x2, pwr + 16384) = u32x2{pk2(e[4], e[5]), pk2(e[6], e[7])};
    };
    auto wave_max = [&]() {
        const float a = fmaxf(fmaxf(sa[0], sa[1]), fmaxf(sa[2], sa[3])), b = fmaxf(fmaxf(sb[0], sb[1]), fmaxf(sb[2], sb[3]));
        auto u = __builtin_amdgcn_permlane32_swap(__float_as_uint(a), __float_as_uint(b), false, false);
        const float c = fmax3(__uint_as_float(u[0]), __uint_as_float(u[1]), __uint_as_float(u[1]));
        auto v = __builtin_amdgcn_permlane16_swap(__float_as_uint(c), __float_as_uint(c), false, false);
        return fmax3(__uint_as_float(v[0]), __uint_as_float(v[1]), __uint_as_float(v[1]));
    };
    auto max8 = [&](unsigned a) {
        const f32x4 a0 = *LP(f32x4, a), a1 = *LP(f32x4, a + 16);
        return fmax3(fmax3(a0[0], a0[1], a0[2]), fmax3(a0[3], a1[0], a1[1]), fmax3(a1[2], a1[3], a1[3])) * SCL;
    };
    {
        bf16x8 qs[8][2];
#pragma unroll
        for (int ks = 0; ks < 8; ++ks) { qr[ks][0] = q_ld(ks + 8, 0); qr[ks][1] = q_ld(ks + 8, 1); }
        __builtin_amdgcn_sched_barrier(0);
#pragma unroll
        for (int i = LAST + 1; i < LAST + 1 + SL0; ++i) if (i < NE) slot(i);
        sa = f32x4{0.f, 0.f, 0.f, 0.f}; sb = sa;
        f32x4 s2a = sa, s2b = sa;
        bf16x8 kb[4];
#pragma unroll
        for (int n = 0; n < 16; ++n) {
            const int ks = (n + 8) & 15;
            sa = MFMA16(ka[ks], ks < 8 ? qs[ks][0] : qr[ks - 8][0], sa);
            sb = MFMA16(ka[ks], ks < 8 ? qs[ks][1] : qr[ks - 8][1], sb);
            if (QKM && n == 4) { VMW(LAST + SL0 + 1 - 19); for (int u = 0; u < 4; ++u) kb[u] = *LP(bf16x8, kad[u]); }
            if (QKM && ks < 4) { s2a = MFMA16(kb[ks], qs[ks][0], s2a); s2b = MFMA16(kb[ks], qs[ks][1], s2b); }
            if (n >= 8 - QD && n < 16 - QD) { qs[n + QD - 8][0] = q_ld(n + QD - 8, 0); qs[n + QD - 8][1] = q_ld(n + QD - 8, 1); }
            if ((n & 3) == 3 && LAST + 1 + SL0 + (n >> 2) < NE) slot(LAST + 1 + SL0 + (n >> 2));
            __builtin_amdgcn_sched_barrier(0);
        }
        STAMP(1);
        *LP(float, exw) = wave_max();
        wg_barrier();
        m0 = max8(exr); m1 = max8(exr + 512);
        VMW(NE - 24);
        if (!QKM) ka_reads(0);
        ka_reads(1);
#pragma unroll
        for (int ks = 4 * QKM; ks < 4 * QKM + QE; ++ks) { qs[ks][0] = q_ld(ks, 0); qs[ks][1] = q_ld(ks, 1); }
        __builtin_amdgcn_sched_barrier(0);
        float e[8];
        probs(e);
        ls0 = (e[0] + e[1]) + (e[2] + e[3]); ls1 = (e[4] + e[5]) + (e[6] + e[7]);
        PIN(ls0); PIN(ls1);
        if (wv == 0) *LP(u32x4, L_BIG) = u32x4{0u, 0u, tlo, thi};
        VMW(NE - 28);
        wg_barrier();
        STAMP(22);
        static_assert(NE >= 28, "");
        bf16x8 pf[8];
        tg = *LP(u32x2, L_BIG + 8);
#pragma unroll
        for (int i = NE; i < 32; ++i) slot(i);
        sa = s2a; sb = s2b;
        if (QKM) { pf[0] = p_ld(0); pf[1] = p_ld(1); }
        __builtin_amdgcn_sched_barrier(0);
#pragma unroll
        for (int ks = 4 * QKM; ks < 8; ++ks) {
            sa = MFMA16(ka[ks], qs[ks][0], sa); sb = MFMA16(ka[ks], qs[ks][1], sb);
            if (ks + QE < 8) { qs[ks + QE][0] = q_ld(ks + QE, 0); qs[ks + QE][1] = q_ld(ks + QE, 1); }
            else if (!QKM) pf[ks + QE - 8] = p_ld(ks + QE - 8);
            __builtin_amdgcn_sched_barrier(0);
        }
#pragma unroll
        for (int i = 0; i < 16; ++i) { o[0][i] = 0.f; o[1][i] = 0.f; }
        STAMP(2);
#pragma unroll
        for (int kk = 0; kk < 4; ++kk) {
            if (kk == 0) {
#pragma unroll
                for (int q = QKM ? 2 : QE; q < 8; ++q) pf[q] = p_ld(q);
            }
            if (kk == 1) ka_reads(2);
            o[0] = MFMA32(cat8(va[kk][0], va[kk][1]), pf[kk], o[0]);
            o[1] = MFMA32(cat8(va[kk][2], va[kk][3]), pf[kk], o[1]);
            __builtin_amdgcn_sched_barrier(0);
        }
        PIN4(pf[4], pf[5], pf[6], pf[7]); PIN4(ka[8], ka[9], ka[10], ka[11]); PIN(tg);
        STAMP(3);
#pragma unroll
        for (int kk = 4; kk < 8; ++kk) {
            if (wv < 6) { va_read1(kk - 4); if (kk > 4) va_read1(kk - 1); }
            sa = MFMA16(ka[kk + 4], qr[kk - 4][0], sa); sb = MFMA16(ka[kk + 4], qr[kk - 4][1], sb);
            o[0] = MFMA32(cat8(va[kk][0], va[kk][1]), pf[kk], o[0]);
            o[1] = MFMA32(cat8(va[kk][2], va[kk][3]), pf[kk], o[1]);
            __builtin_amdgcn_sched_barrier(0);
        }
        if (wv < 6) va_read1(7);
        STAMP(5);
        tlo = __builtin_amdgcn_readfirstlane(tg[0]); thi = __builtin_amdgcn_readfirstlane(tg[1]);
        rederive();
        fbo = (unsigned)(WS_FB + x * STR + lane * 16);
        asm volatile("buffer_load_dwordx4 %0, off, %1, %2 sc1" : "=v"(nq) : "s"(rw), "s"(WS_NM + x * STR) : "memory");
        const unsigned head = lane & 31, r8 = lane >> 3, cw = wv ^ (rot * 2);
        srd = lb + r8 * 128 + (((lane & 7) ^ r8) << 4);
        sw[0] = lb + head * 128 + (((lane >> 5) ^ (head & 7)) << 4);
        sw[1] = sw[0] ^ 32u; sw[2] = sw[0] ^ 64u; sw[3] = sw[0] ^ 96u;
        p0 = (unsigned)(WS_PART + x * 1048576) + (cw >> 2) * 16384 + (cw & 3) * 2048 + r8 * 32768 + ((s ^ r8) * 8 + (lane & 7)) * 16;
        PIN4(sw[0], sw[1], sw[2], sw[3]); PIN(srd); PIN(p0); PIN(fbo);
    }
    STAMP(23);
    unsigned po[4];
    float mrun;
    {
        VMW(1);
        STAMP(24);
        ka_reads(3);
        asm volatile("" : "=v"(fa));
        if (wv == 0 && s < 11) asm volatile("buffer_load_dwordx4 %0, %1, %2, 0 offen sc1" : "+v"(fa) : "v"(fbo), "s"(rw) : "memory");
        __builtin_amdgcn_sched_barrier(0);
#pragma unroll
        for (int ks = 12; ks < 16; ++ks) { sa = MFMA16(ka[ks], qr[ks - 8][0], sa); sb = MFMA16(ka[ks], qr[ks - 8][1], sb); }
        bf16x8 pf[8];
#pragma unroll 1
        for (bool first = true;; first = false) {
            float e[8];
            probs(e);
            const float n0 = (e[0] + e[1]) + (e[2] + e[3]), n1 = (e[4] + e[5]) + (e[6] + e[7]);
            *LP(float, esw) = ls0 + n0; *LP(float, esw + 16384) = ls1 + n1;
            if (__builtin_expect(!(n0 + n1 <= BIGV), 0)) *LP(unsigned, L_BIG) = 1u;
            va_pin();
            wg_barrier();
            STAMP(25);
            PIN(prd);
            const unsigned big = *LP(unsigned, L_BIG);
            if ((wv >> 1) == 3) va_reads();
#pragma unroll
            for (int kk = 0; kk < 8; ++kk) { pf[kk] = p_ld(kk); __builtin_amdgcn_sched_barrier(0); }
            if (__builtin_expect(__builtin_amdgcn_readfirstlane(big) == 0 || !first, 1)) break;
            const unsigned ex2 = L_SCR + 512 + 160;
            PIN(sa); PIN(sb);
            rederive();
            *LP(float, ex2 + (nl + (lane >> 5) * 16) * 1024 + wv * 4) = wave_max();
            wg_barrier();
            const float x0 = fmaxf(max8(ex2 + nl * 1024), m0), x1 = fmaxf(max8(ex2 + nl * 1024 + 16384), m1);
            const float al0 = __builtin_amdgcn_exp2f(m0 - x0), al1 = __builtin_amdgcn_exp2f(m1 - x1), al = (lane & 16) ? al1 : al0;
            m0 = x0; m1 = x1; ls0 *= al0; ls1 *= al1;
            o[0] *= al; o[1] *= al;
        }
        mrun = (lane & 16) ? m1 : m0;
        auto stage = [&](int t2, int gp) {
            const unsigned lo0 = pk2(o[t2][8 * gp + 0], o[t2][8 * gp + 1]), lo1 = pk2(o[t2][8 * gp + 2], o[t2][8 * gp + 3]);
            const unsigned hi0 = pk2(o[t2][8 * gp + 4], o[t2][8 * gp + 5]), hi1 = pk2(o[t2][8 * gp + 6], o[t2][8 * gp + 7]);
            auto r0 = __builtin_amdgcn_permlane32_swap(lo0, hi0, false, false);
            auto r1 = __builtin_amdgcn_permlane32_swap(lo1, hi1, false, false);
            *LP(u32x4, sw[t2 * 2 + gp]) = u32x4{r0[0], r1[0], r0[1], r1[1]};
        };
#pragma unroll
        for (int kk = 0; kk < 8; ++kk) {
            o[0] = MFMA32(cat8(va[kk][0], va[kk][1]), pf[kk], o[0]);
            if (kk == 1) {
                asm volatile("s_waitcnt vmcnt(0)" : "+v"(nq) :: "memory");
                npm = __builtin_amdgcn_readfirstlane(nq[0]);
                if ((unsigned)__builtin_amdgcn_readfirstlane(nq[2]) != tlo || (unsigned)__builtin_amdgcn_readfirstlane(nq[3]) != thi) npm = __builtin_amdgcn_readfirstlane(tagged_wait(WS_NM + x * STR)[0]);
            }
            if (kk == 3 || kk == 5) {
                const unsigned v = (npm ^ ((x & 2) ? 0xffu : 0u)) >> (lane >> 5);
#pragma unroll
                for (int i = kk - 3; i < kk - 1; ++i) { po[i] = p0 + ((v >> (2 * i)) & 1u) * 8192u; PIN(po[i]); }
            }
            __builtin_amdgcn_sched_barrier(0);
        }
#pragma unroll
        for (int kk = 0; kk < 8; ++kk) {
            o[1] = MFMA32(cat8(va[kk][2], va[kk][3]), pf[kk], o[1]);
            if (kk == 2) { PIN(o[0]); stage(0, 0); }
            if (kk == 4) stage(0, 1);
            __builtin_amdgcn_sched_barrier(0);
        }
        stage(1, 0); stage(1, 1);
        asm volatile("s_waitcnt lgkmcnt(0)" ::: "memory");
        u32x4 w[4];
#pragma unroll
        for (int i = 0; i < 4; ++i) w[i] = *LP(u32x4, srd + i * 1024);
#pragma unroll
        for (int i = 0; i < 4; ++i) ST16(w[i], po[i], i * 262144);
        __builtin_amdgcn_sched_barrier(0);
        STAMP(26);
        u32x4 mlrec;
        if (wv == 0) {
            const unsigned head = lane & 31;
            esr = L_SCR + head * 1024 + 512 + ((head & 7) << 4);
            mlo = (unsigned)(WS_ML + x * STR + s * 512 + head * 16) + ((npm >> ((head >> 3) * 2)) & 1u) * 8192u;
            PIN(mlo);
            f32x4 b8[8];
#pragma unroll
            for (int q = 0; q < 8; ++q) b8[q] = *LP(f32x4, esr ^ (q << 4));
            const f32x4 bs = ((b8[0] + b8[1]) + (b8[2] + b8[3])) + ((b8[4] + b8[5]) + (b8[6] + b8[7]));
            mlrec = u32x4{__float_as_uint(mrun), __float_as_uint((bs[0] + bs[1]) + (bs[2] + bs[3])), tlo, thi};
            PIN(mlrec);
        }
        VMW(0);
        wg_barrier();
        if (wv == 0) {
            if (lane < 32) ST16(mlrec, mlo, 0);
            PIN(fa);
            if (s < 11 && !__all(flags_ok(fa))) flags_wait(fbo);
        }
        STAMP(27);
        wg_barrier();
    }
    const unsigned long long t_att = __builtin_amdgcn_s_memrealtime();
    if (wv == 0) __hip_atomic_store((gu64*)(p.ws + (128u << 20)) + id, t00, RLX_AGENT);
    rederive();
    while (__builtin_amdgcn_s_memrealtime() < t_att + (s >= 11 ? MD0 : MD1)) __builtin_amdgcn_s_sleep(2);
    if (s >= 11) {
        const unsigned n = (unsigned)((s - 11) * 8 + wv);
        if (n < 28) pf4<NTM & 32>((n * 64 + lane) * 1024u + x * 128u, p.w2, 0);
        { const unsigned l2 = n * 64 + lane; pf4<NTM & 32>((l2 / 5u) * (DFF * 2) + min(((unsigned)x * 448u & ~127u) + (l2 % 5u) * 128u, DFF * 2 - 4u), p.w1, 0); }
        if (wv == 0) flag_put(WS_FA, id - 128);
        STAMP_FLUSH();
        return;
    }
    const unsigned L_H2S = L_SCR, L_REDM = L_SCR + 16384;
    if (s < 7) {
        {
            const char* w1s = (const char*)(p.w1 + (size_t)wv * 64 * DFF + ug * 32);
#pragma unroll
            for (int i = 0; i < 4; ++i) dma16<NTM & 32>((unsigned)((lane >> 2) * DFF * 2 + (lane & 3) * 16), w1s + (size_t)i * 16 * DFF * 2, wv * 4096 + i * 1024);
        }
        const int b = wv;
        if (wv == 0) ST16(s4, (unsigned)(WS_U + x * STR + (lane >> 5) * 8192 + s * 512 + (lane & 31) * 16), 0);
        const u32x4 shf = LD16((unsigned)(WS_MB + b * STR + lane * 16), 0), scl = LD16((unsigned)(WS_MB + b * STR + 1024 + lane * 16), 0);
        VMW(0);
        if (wv == 0) flag_put(WS_FA, id - 128);
        wg_barrier();
        bf16x8 wk0[8], wk1[8];
        if (wv >= 6) {
#pragma unroll
            for (int q = 0; q < 8; ++q) {
                const unsigned a = (unsigned)(q * 4096 + (g * 8 + (nl >> 2)) * 64 + (wv & 1) * 32 + (lane & 3) * 8);
                wk0[q] = tr_frag(a, a + 256); wk1[q] = tr_frag(a + 2048, a + 2304);
            }
#pragma unroll
            for (int q = 0; q < 8; q += 2) PIN4(wk0[q], wk0[q + 1], wk1[q], wk1[q + 1]);
        }
        float scv[8], shv[8];
#pragma unroll
        for (int q = 0; q < 4; ++q) {
            scv[2 * q] = rbf(1.f + lo16(scl[q])); scv[2 * q + 1] = rbf(1.f + hi16(scl[q])); shv[2 * q] = lo16(shf[q]); shv[2 * q + 1] = hi16(shf[q]);
            PIN4(scv[2 * q], scv[2 * q + 1], shv[2 * q], shv[2 * q + 1]);
        }
        unsigned har = L_H2S + ((lane & 7) * HS_LD + g * 8 + (nl >> 3) * 32) * 2, haw = L_H2S + (b * HS_LD + lane * 8) * 2;
        unsigned udst = (unsigned)(WS_U + x * STR + (nl >> 3) * 8192 + (s * 2 + (wv & 1)) * 256 + ((nl & 7) * 16 + g * 4) * 2), xsrc = (unsigned)(WS_X1 + x * STR + (b * 512 + lane * 8) * 2) + ((npm >> x) & 1u) * 8192u;
        PIN4(har, haw, udst, xsrc);
        STAMP(28);
        while (__builtin_amdgcn_s_memrealtime() < t_att + XGATE) __builtin_amdgcn_s_sleep(2);
        u32x4 xv;
        for (unsigned spins = 0;;) {
            xv = LD16(xsrc, 0);
            if (__all(mx4(xv, 0u) != SENT)) break;
            __builtin_amdgcn_s_sleep(SLPX);
            if (++spins > kSpinCap) break;
            asm volatile("" ::: "memory");
        }
        STAMP(29);
        {
            f32x2_t v[4];
#pragma unroll
            for (int q = 0; q < 4; ++q) v[q] = f32x2_t{lo16(xv[q]), hi16(xv[q])};
            const f32x2_t p1 = (v[0] + v[1]) + (v[2] + v[3]), p2 = (v[0] * v[0] + v[1] * v[1]) + (v[2] * v[2] + v[3] * v[3]);
            float mb, rden;
            ln_stats(p1[0] + p1[1], p2[0] + p2[1], mb, rden);
            u32x4 hr;
#pragma unroll
            for (int q = 0; q < 4; ++q) {
                const f32x2_t h = ((v[q] - mb) * rden) * f32x2_t{scv[2 * q], scv[2 * q + 1]} + f32x2_t{shv[2 * q], shv[2 * q + 1]};
                hr[q] = pk2(h[0], h[1]);
            }
            *LP(u32x4, haw) = hr;
        }
        wg_barrier();
        if (wv >= 6) {
            bf16x8 af[8];
#pragma unroll
            for (int q = 0; q < 8; ++q) af[q] = *LP(bf16x8, har + q * 128);
            __builtin_amdgcn_sched_barrier(0);
            f32x4 d0 = {0.f, 0.f, 0.f, 0.f}, d1 = {0.f, 0.f, 0.f, 0.f};
#pragma unroll
            for (int q = 0; q < 8; ++q) { d0 = MFMA16(wk0[q], af[q], d0); d1 = MFMA16(wk1[q], af[q], d1); }
            const f32x4 own = nl < 8 ? d0 : d1;
#pragma unroll
            for (int e = 0; e < 4; ++e) { float v = own[e] + dppf<0x128>(own[e]); asm("" : "+v"(v)); d0[e] = fmaxf(v, 0.f); }
            const u32x2 uv = {pk2s(d0[0], d0[1]), pk2s(d0[2], d0[3])};
            __builtin_amdgcn_raw_buffer_store_b64(uv, rw, udst, 0, 16);
            STAMP(30);
        }
        STAMP_FLUSH();
        return;
    }
    {
        const int n0 = (x * 4 + (s - 7)) * 16;
        const char* mws = (const char*)(p.w2 + (size_t)wv * 224 * 512 + n0);
#pragma unroll
        for (int i = 0; i < 7; ++i) dma16<NTM & 32>((unsigned)((lane >> 1) * 1024 + (lane & 1) * 16), mws + i * 32768, rgn + i * 1024);
        const int fb = lane >> 3, fc = (lane & 7) * 2;
        u32x2 gb = {0u, 0u};
        if (wv == 0) {
            flag_put(WS_FA, id - 128);
            { const u32x4 v = tagged_wait(WS_MOD + fb * STR + (1536 + n0 + fc) * 8); gb = u32x2{v[0], v[1]}; }
            flags_wait(WS_FA + x * STR + lane * 16);
            if (id == 23 * 8 && lane == 0) __builtin_amdgcn_raw_buffer_store_b64(u32x2{tlo, thi}, rw, WS_EPOCH, 0, 16);
            PIN(gb);
        }
        VMW(0);
        bf16x8 wfr[7];
        const unsigned trl = (unsigned)(g * 8 + (nl >> 2)) * 32u + (unsigned)(lane & 3) * 8u;
#pragma unroll
        for (int ks = 0; ks < 7; ++ks) { wfr[ks] = tr_frag(rgn + ks * 1024 + trl, rgn + ks * 1024 + trl + 128); PIN(wfr[ks]); }
        unsigned usrc = (unsigned)(WS_U + wv * STR + (g >> 1) * 256 + (nl & 7) * 32 + (g & 1) * 16) + ((npm >> x) & 1u) * 8192u;
        unsigned rdw = L_REDM + wv * 512 + (nl & 7) * 64 + g * 16, rdr = L_REDM + lane * 8;
        unsigned short* outp = p.out + fb * 512 + n0 + fc;
        PIN(usrc); PIN(rdw); PIN(rdr); PIN(outp);
        STAMP(28);
        unsigned xg = 0u;
        if (wv == 0) {
            while (__builtin_amdgcn_s_memrealtime() < t_att + XGATE) __builtin_amdgcn_s_sleep(2);
            for (unsigned spins = 0;;) {
                xg = __builtin_amdgcn_raw_buffer_load_b32(rw, (unsigned)(WS_X1 + x * STR + (fb * 512 + n0 + fc) * 2) + ((npm >> x) & 1u) * 8192u, 0, 16);
                if (__all(xg != SENT)) break;
                __builtin_amdgcn_s_sleep(SLPD);
                if (++spins > kSpinCap) break;
                asm volatile("" ::: "memory");
            }
            __builtin_amdgcn_s_sleep(SLPG);
        }
        wg_barrier();
        u32x4 uf[7];
        for (unsigned spins = 0;;) {
#pragma unroll
            for (int ks = 0; ks < 7; ++ks) uf[ks] = LD16(usrc + ks * 512, 0);
            unsigned m = 0u;
#pragma unroll
            for (int ks = 0; ks < 7; ++ks) m = mx4(uf[ks], m);
            if (__all(m != SENT)) break;
            if (++spins > kSpinCap) break;
            asm volatile("" ::: "memory");
        }
        STAMP(29);
        f32x4 d = {0.f, 0.f, 0.f, 0.f}, d2 = {0.f, 0.f, 0.f, 0.f};
#pragma unroll
        for (int ks = 0; ks < 7; ++ks) {
            if (ks & 1) d2 = MFMA16(wfr[ks], BF8(uf[ks]), d2);
            else d = MFMA16(wfr[ks], BF8(uf[ks]), d);
        }
        *LP(f32x4, rdw) = d + d2;
        wg_barrier();
        if (wv == 0) {
            f32x2_t c[8];
#pragma unroll
            for (int w2 = 0; w2 < 8; ++w2) c[w2] = *LP(f32x2_t, rdr + w2 * 512);
            const f32x2_t sm = ((c[0] + c[1]) + (c[2] + c[3])) + ((c[4] + c[5]) + (c[6] + c[7]));
            unsigned t = pk2(sm[0], sm[1]);
            t = pk2(__uint_as_float(gb[0]) * lo16(t), __uint_as_float(gb[1]) * hi16(t));
            *(GAS unsigned*)outp = pk2(lo16(xg) + lo16(t), hi16(xg) + hi16(t));
            { const unsigned long long t1 = __builtin_amdgcn_s_memrealtime();
              unsigned long long m = ~0ull, mx = 0ull;
              for (int q = 0; q < 4; ++q) { const unsigned long long v = __hip_atomic_load((gu64*)(p.ws + (128u << 20)) + q * 64 + lane, RLX_AGENT); m = v < m ? v : m; mx = v > mx ? v : mx; }
              for (int o = 1; o < 64; o <<= 1) { const unsigned long long v = __shfl_xor(m, o), w = __shfl_xor(mx, o); m = v < m ? v : m; mx = w > mx ? w : mx; }
              const unsigned dl = min(__builtin_amdgcn_readfirstlane((unsigned)(mx - m)), 5000u) * 16u;
              while (__builtin_amdgcn_s_memrealtime() < t1 + dl) __builtin_amdgcn_s_sleep(1); }
        }
        STAMP_FLUSH();
    }
}

extern "C" void kernel_launch(void* const* d_in, const int* in_sizes, int n_in,
                              void* d_out, int out_size, void* d_ws, size_t ws_size,
                              hipStream_t stream) {
    Params p;
    p.x = (const unsigned short*)d_in[0];
    p.ckv = (const unsigned short*)d_in[1];
    p.w_q = (const unsigned short*)d_in[2];
    p.w_uk = (const unsigned short*)d_in[3];
    p.w_uv = (const unsigned short*)d_in[4];
    p.w_o = (const unsigned short*)d_in[5];
    p.w1 = (const unsigned short*)d_in[7];
    p.w2 = (const unsigned short*)d_in[8];
    p.c = (const unsigned short*)d_in[9];
    p.w_mod = (const unsigned short*)d_in[10];
    p.ws = (char*)(((size_t)d_ws + 2097151) & ~(size_t)2097151);
    p.out = (unsigned short*)d_out;
    (void)hipFuncSetAttribute((const void*)k_main, hipFuncAttributeMaxDynamicSharedMemorySize, 163840);
    k_main<<<256, 512, 163840, stream>>>(p);
}
```

```cpp
#define MEAS 16
#define AVG 1
#include <hip/hip_runtime.h>
#include <hip/hip_bf16.h>
#include <stdint.h>

#define NH 32
#define SK 4096
#define DFF 1792
#define GI 20
#define ROTC 1
#define PFS 2
#define XNO 0
#define CPJ 7
#define NTM 16
#define QHELP 4
#define HP 13
#define NTH 0
#define W0 2
#define SL0 1
#define LMPF 0
#define SLPB 20
#define MD0 0
#define MD1 0
#define NEARP 1
#define NCAL 2
#define TLBW 2
#define TG 510
#define JPERM 1
#define NPOST 4
#define XS3 1
#define WG 2
#define NP2 2
#define QKM 1
#define HILO 0
#define YSEL 1
#define TWD 680
#define LMD 250
#define PFM 57
#define CPFE 35500
#define SLPM 0
#define SLPD 12
#define SLPG 8
#define SLPX 1
#define SLPH 4

using u16x8 = __attribute__((ext_vector_type(8))) unsigned short;
using s16x4 = __attribute__((ext_vector_type(4))) short;
using bf16x8 = __attribute__((ext_vector_type(8))) short;
using f32x16 = __attribute__((ext_vector_type(16))) float;
using f32x4 = __attribute__((ext_vector_type(4))) float;
using u32x4 = __attribute__((ext_vector_type(4))) unsigned;
using u32x2 = __attribute__((ext_vector_type(2))) unsigned;

#define GAS __attribute__((address_space(1)))
#define LAS __attribute__((address_space(3)))
typedef GAS unsigned long long gu64;
#define RLX_AGENT __ATOMIC_RELAXED, __HIP_MEMORY_SCOPE_AGENT
#define SENT 0xffffffffu
#define TAGC 0x9e3779b9u

__device__ __forceinline__ float bf2f(unsigned short u) { return __uint_as_float(((unsigned)u) << 16); }
typedef __attribute__((ext_vector_type(2))) __bf16 bf16x2_t;
typedef __attribute__((ext_vector_type(2))) float f32x2_t;
__device__ __forceinline__ unsigned pk2(float a, float b) {
    f32x2_t v = {a, b};
    bf16x2_t r = __builtin_convertvector(v, bf16x2_t);
    return __builtin_bit_cast(unsigned, r);
}
__device__ __forceinline__ unsigned nosent(unsigned r) { return min(r, SENT - 1u); }
__device__ __forceinline__ unsigned pk2s(float a, float b) { return nosent(pk2(a, b)); }
__device__ __forceinline__ unsigned short f2bf(float f) { return (unsigned short)(pk2(f, 0.f) & 0xffffu); }
__device__ __forceinline__ float rbf(float f) { return __uint_as_float(pk2(f, 0.f) << 16); }
__device__ __forceinline__ float lo16(unsigned u) { return __uint_as_float(u << 16); }
__device__ __forceinline__ float hi16(unsigned u) { return __uint_as_float(u & 0xffff0000u); }
__device__ __forceinline__ unsigned umax3(unsigned a, unsigned b, unsigned c) { return max(max(a, b), c); }
__device__ __forceinline__ unsigned mx4(const u32x4& v, unsigned m) { return umax3(v[2], v[3], umax3(v[0], v[1], m)); }
__device__ __forceinline__ float fmax3(float a, float b, float c) { float r; asm("v_max3_f32 %0, %1, %2, %3" : "=v"(r) : "v"(a), "v"(b), "v"(c)); return r; }
template <int C> __device__ __forceinline__ float dppf(float v) { return __int_as_float(__builtin_amdgcn_update_dpp(0, __float_as_int(v), C, 0xf, 0xf, true)); }
__device__ __forceinline__ float sum16(float v) { v += dppf<0xB1>(v); v += dppf<0x4E>(v); v += dppf<0x141>(v); v += dppf<0x140>(v); return v; }

__device__ __forceinline__ void ln_stats(float p1, float p2, float& mb, float& rden) {
    auto t = __builtin_amdgcn_permlane32_swap(__float_as_uint(p1), __float_as_uint(p2), false, false);
    float a = sum16(__uint_as_float(t[0]) + __uint_as_float(t[1]));
    auto t2 = __builtin_amdgcn_permlane16_swap(__float_as_uint(a), __float_as_uint(a), false, false);
    a = (__uint_as_float(t2[0]) + __uint_as_float(t2[1])) * (1.f / 512.f);
    auto t3 = __builtin_amdgcn_permlane32_swap(__float_as_uint(a), __float_as_uint(a), false, false);
    const float m = __uint_as_float(t3[0]);
    const float var = rbf(fmaxf(fmaf(-m, m, __uint_as_float(t3[1])), 0.f));
    mb = rbf(m);
    rden = __builtin_amdgcn_rcpf(rbf(__builtin_amdgcn_sqrtf(rbf(var + 1e-5f))));
}
#define STR 262144
#define WS_Q 0
#define WS_H 32768
#define WS_X1 65536
#define WS_ML 81920
#define WS_MOD 98304
#define WS_MB 139264
#define WS_U 114688
#define WS_FI 131072
#define WS_FA 132096
#define WS_FB 133120
#define WS_NP 134144
#define WS_NM 134400
#define WS_EPOCH 135168
#define WS_CAL 147456
#define WS_OUTP 2097152
#define WS_PART 4194304
#define WS_END 12582912

#define LP(T, off) ((LAS T*)(size_t)(unsigned)(off))
#define STAMP(k) do { if (((k) == MEAS || ((k) == 0 && (blockIdx.x >> 4) == 7 && wv == 7)) && (threadIdx.x & 63) == 0) __hip_atomic_store((gu64*)(p.ws + (128u << 20)) + ((k) == 0 ? 0 : 2048) + blockIdx.x * 8 + wv, (unsigned long long)__builtin_amdgcn_s_memrealtime(), RLX_AGENT); \
    if ((k) == 31) { \
        const unsigned long long t1 = __builtin_amdgcn_s_memrealtime(); \
        unsigned long long m = ~0ull, mx = 0ull; \
        for (int q = 0; q < 32; ++q) { const unsigned long long v = __hip_atomic_load((gu64*)(p.ws + (128u << 20)) + q * 64 + (threadIdx.x & 63), RLX_AGENT); if (t1 - v < 10000ull) m = v < m ? v : m; } \
        for (int o = 1; o < 64; o <<= 1) { const unsigned long long v = __shfl_xor(m, o); m = v < m ? v : m; } \
        for (int q = 0; q < 32; ++q) { const unsigned long long v = __hip_atomic_load((gu64*)(p.ws + (128u << 20)) + 2048 + q * 64 + (threadIdx.x & 63), RLX_AGENT); if (v - m < 10000ull) mx = v > mx ? v : mx; } \
        for (int o = 1; o < 64; o <<= 1) { const unsigned long long v = __shfl_xor(mx, o); mx = v > mx ? v : mx; } \
        if (AVG) { unsigned sm = 0, cn = 0; \
            for (int q = 0; q < 32; ++q) { const unsigned long long v = __hip_atomic_load((gu64*)(p.ws + (128u << 20)) + 2048 + q * 64 + (threadIdx.x & 63), RLX_AGENT); if (v - m < 10000ull) { sm += (unsigned)(v - m); ++cn; } } \
            for (int o = 1; o < 64; o <<= 1) { sm += __shfl_xor(sm, o); cn += __shfl_xor(cn, o); } \
            mx = m + sm / max(cn, 1u); } \
        if (MEAS == 31) mx = t1; \
        const unsigned dl = MEAS == 99 ? 0u : min(__builtin_amdgcn_readfirstlane((unsigned)(mx - m)), 5000u) * 4u; \
        while (__builtin_amdgcn_s_memrealtime() < t1 + dl) __builtin_amdgcn_s_sleep(1); \
    } } while (0)
#define STAMP_FLUSH() do {} while (0)
__device__ __forceinline__ s16x4 tr4(unsigned a) { return __builtin_amdgcn_ds_read_tr16_b64_v4i16(LP(s16x4, a)); }
__device__ __forceinline__ bf16x8 cat8(s16x4 a, s16x4 b) { return __builtin_shufflevector(a, b, 0, 1, 2, 3, 4, 5, 6, 7); }
__device__ __forceinline__ bf16x8 tr_frag(unsigned a0, unsigned a1) { return cat8(tr4(a0), tr4(a1)); }
__device__ __forceinline__ unsigned swz(unsigned row) { return ((row & 3) << 2) | ((row >> 2) & 3); }
__device__ __forceinline__ unsigned off_b(unsigned row, unsigned ch) { return 256u * row + 16u * (ch ^ swz(row)); }
__device__ __forceinline__ unsigned tile_off(unsigned key, unsigned chunk) { return ((key >> 5) * 4 + (chunk >> 4)) * 8192u + off_b(key & 31, chunk & 15); }
__device__ __forceinline__ void wg_barrier() { asm volatile("s_waitcnt lgkmcnt(0)\n\ts_barrier" ::: "memory"); }
template <int NT> __device__ __forceinline__ void dma16(unsigned voff, const void* sbase, unsigned lds_dst) {
    if (NT) asm volatile("s_mov_b32 m0, %2\n\ts_nop 0\n\tglobal_load_lds_dwordx4 %0, %1 nt" :: "v"(voff), "s"(sbase), "s"(lds_dst) : "memory", "m0");
    else asm volatile("s_mov_b32 m0, %2\n\ts_nop 0\n\tglobal_load_lds_dwordx4 %0, %1" :: "v"(voff), "s"(sbase), "s"(lds_dst) : "memory", "m0");
}
template <int NT> __device__ __forceinline__ void dma16x(unsigned voff, unsigned vx, const void* sbase, unsigned lds_dst) {
    unsigned t;
#define DMAX(pol, c) asm volatile("s_mov_b32 m0, %3\n\tv_xor_b32 %0, %4, %1\n\tglobal_load_lds_dwordx4 %0, %2" pol : "=&v"(t) : "v"(voff), "s"(sbase), "s"(lds_dst), c(vx) : "memory", "m0")
    if (__builtin_constant_p(vx)) { if (NT) DMAX(" nt", "i"); else DMAX("", "i"); }
    else { if (NT) DMAX(" nt", "s"); else DMAX("", "s"); }
}
template <int NT> __device__ __forceinline__ void pf4(unsigned voff, const void* sbase, unsigned lds_dst) {
    if (NT) asm volatile("s_mov_b32 m0, %2\n\ts_nop 0\n\tglobal_load_lds_dword %0, %1 nt" :: "v"(voff), "s"(sbase), "s"(lds_dst) : "memory", "m0");
    else asm volatile("s_mov_b32 m0, %2\n\ts_nop 0\n\tglobal_load_lds_dword %0, %1" :: "v"(voff), "s"(sbase), "s"(lds_dst) : "memory", "m0");
}
__device__ __forceinline__ void pf4s(unsigned voff, const void* sbase, unsigned lds_dst) {
    asm volatile("s_mov_b32 m0, %2\n\ts_nop 0\n\tglobal_load_lds_dword %0, %1 sc1" :: "v"(voff), "s"(sbase), "s"(lds_dst) : "memory", "m0");
}
#define XGATE 300
#define SPINCAP (1u << 20)
constexpr unsigned kSpinCap = SPINCAP;
struct Params {
    const unsigned short* x;
    const unsigned short* ckv;
    const unsigned short* w_q;
    const unsigned short* w_uk;
    const unsigned short* w_uv;
    const unsigned short* w_o;
    const unsigned short* w1;
    const unsigned short* w2;
    const unsigned short* c;
    const unsigned short* w_mod;
    char* ws;
    unsigned short* out;
};
#define VMW(n) asm volatile("s_waitcnt vmcnt(%0)" :: "n"(n) : "memory")
#define PIN(x) asm volatile("" : "+v"(x))
#define PIN4(a, b, c, d) asm volatile("" : "+v"(a), "+v"(b), "+v"(c), "+v"(d) :: "memory")
#define MFMA16(a, b, c) __builtin_amdgcn_mfma_f32_16x16x32_bf16(a, b, c, 0, 0, 0)
#define MFMA32(a, b, c) __builtin_amdgcn_mfma_f32_32x32x16_bf16(a, b, c, 0, 0, 0)
#define BF8(x) __builtin_bit_cast(bf16x8, x)
#define ST16(v, off, soff) __builtin_amdgcn_raw_buffer_store_b128(v, rw, off, soff, 16)
#define LD16(off, soff) __builtin_amdgcn_raw_buffer_load_b128(rw, off, soff, 16)
#define SCL (0.08838834764831845f * 1.4426950408889634f)
#define BIGV 1.0e18f
#define QD 4
#define QE 4
#define L_SCR 131072
#define HS_LD 520

__global__ __launch_bounds__(512, 2) void k_main(Params p) {
    unsigned long long pc;
    asm volatile("s_getpc_b64 %0" : "=s"(pc));
    const int id = blockIdx.x, tid = threadIdx.x;
    int lane = tid & 63;
    const int wv = __builtin_amdgcn_readfirstlane(tid >> 6);
    int g = lane >> 4, nl = lane & 15;
    auto rederive = [&]() {
        asm volatile("v_mbcnt_lo_u32_b32 %0, -1, 0\n\tv_mbcnt_hi_u32_b32 %0, -1, %0" : "=v"(lane));
        g = lane >> 4; nl = lane & 15;
    };
    const int x = id & 7, j0 = id >> 3, j = JPERM && j0 < 16 ? 15 - j0 : j0;
    if (wv < 5 && j == 15) {
        asm volatile("s_mov_b32 m0, %2\n\ts_nop 0\n\tglobal_load_lds_dword %0, %1" :: "v"(min((unsigned)((wv * 64 + lane) * 128), (unsigned)CPFE)), "s"(pc), "s"(L_SCR + 31744) : "memory", "m0");
    }
    if ((j >> 2) == 3) {
        dma16<0>((unsigned)(((nl & 7) * 512 + (nl >> 3) * 32 + g * 8) * 2 + wv * 128), p.c, wv * 16384 + 8192);
        dma16<0>((unsigned)tid * 16u, p.x, wv * 16384 + 9216);
    }
    char* ws = p.ws;
    if (TLBW && j == 15 && wv == 5 && lane < 2) pf4s((unsigned)((x * 2 + lane) * 2097152), p.ckv, L_SCR + 31744);
    STAMP(0);
    const __amdgpu_buffer_rsrc_t rw = __builtin_amdgcn_make_buffer_rsrc(ws, 0, WS_END, 0x00020000);
    const u32x4 s4 = {SENT, SENT, SENT, SENT};
    unsigned long long cnt = 0ull;
    auto cnt_load = [&]() { asm volatile("global_load_dwordx2 %0, %1, %2 sc1" : "+v"(cnt) : "v"((unsigned)WS_EPOCH), "s"(ws) : "memory"); };
    unsigned tlo = 0u, thi = 0u;
    auto tag_take = [&]() {
        asm volatile("" : "+v"(cnt) :: "memory");
        const unsigned long long t = cnt + 1ull;
        tlo = __builtin_amdgcn_readfirstlane((unsigned)t); thi = __builtin_amdgcn_readfirstlane((unsigned)(t >> 32));
    };
    auto flag_put = [&](unsigned base, unsigned idx) {
        if (lane < 8) __builtin_amdgcn_raw_buffer_store_b64(u32x2{tlo, thi}, rw, base + lane * STR + idx * 8, 0, 16);
    };
    auto tagged_wait = [&](unsigned off) {
        u32x4 v;
#pragma unroll 1
        for (unsigned spins = 0; spins < kSpinCap; ++spins) {
            asm volatile("buffer_load_dwordx4 %0, %1, %2, 0 offen sc1\n\ts_waitcnt vmcnt(0)" : "=v"(v) : "v"(off), "s"(rw) : "memory");
            if (__all((v[2] == tlo) & (v[3] == thi))) break;
            __builtin_amdgcn_s_sleep(8);
        }
        return v;
    };
    auto flags_ok = [&](const u32x4& f) { return (f[0] == tlo) & (f[1] == thi) & (f[2] == tlo) & (f[3] == thi); };
    auto flags_wait = [&](unsigned off) {
        for (unsigned spins = 0;;) {
            u32x4 f;
            asm volatile("buffer_load_dwordx4 %0, %1, %2, 0 offen sc1\n\ts_waitcnt vmcnt(0)" : "=v"(f) : "v"(off), "s"(rw) : "memory");
            if (__all(flags_ok(f))) break;
            __builtin_amdgcn_s_sleep(16);
            if (++spins > kSpinCap) break;
            asm volatile("" ::: "memory");
        }
    };
    unsigned pcl, frl, frs;
    auto dma_piece = [&](const unsigned short* src, unsigned ldb, unsigned dst, int n, int nt) {
        const unsigned v0 = (lane >> 3) * ldb + pcl, v1 = v0 ^ 64u;
#pragma unroll
        for (int i = 0; i < n; ++i) {
            if (nt) dma16<1>((i & 1) ? v1 : v0, (const char*)src + (size_t)i * 8 * ldb, dst + i * 1024);
            else dma16<0>((i & 1) ? v1 : v0, (const char*)src + (size_t)i * 8 * ldb, dst + i * 1024);
        }
    };
    auto piece_frag = [&](unsigned base, int ks, int t) {
        const unsigned a = base + frl + ((t ^ frs) << 5) + ks * 4096;
        return tr_frag(a, a + 512);
    };
    const unsigned rgn = (unsigned)(wv * 16384);

    if (j < 16) {
        const unsigned long long t_start = __builtin_amdgcn_s_memrealtime();
        rederive();
        pcl = (unsigned)(((((lane & 7) >> 1) ^ ((lane >> 4) & 1)) * 32) + (lane & 1) * 16);
        frl = (unsigned)((g * 8 + (nl >> 2)) * 128 + (lane & 3) * 8); frs = (unsigned)((nl >> 3) | ((g & 1) << 1));
        const int th = x * 4 + (j & 3);
        if (j < 12) {
            const int qt = j >> 2, rot = qt * 5;
            const bool pub = wv < (qt < 2 ? 3 : 2);
            const unsigned qo = (unsigned)(WS_Q + (th & 7) * STR + ((nl & 7) * 4 + (th >> 3)) * 1024 + (qt * 192 + wv * 64 + g * 16 + (nl >> 3) * 8) * 2);
            const unsigned vb = (unsigned)(g * 8192 + ((nl ^ (g << 2)) << 4));
            const char* wuk = (const char*)(p.w_uk + th * 128) + (size_t)qt * 192 * 8192;
            const char* wq = (const char*)(p.w_q + th * 128) + (size_t)wv * 32768;
            const unsigned L_U = 114688;
            {
                const int k0 = qt < 2 ? wv * 6 : wv * 4;
#pragma unroll
                for (int i = 0; i < 6; ++i) {
                    if (i < 4 || qt < 2) dma16<NTM & 2>(vb ^ (((k0 + i) & 3) << 4), wuk + (size_t)(k0 + i) * 32768, L_U + (k0 + i) * 1024);
                    if (i == 4) VMW(4);
                }
            }
            const unsigned vq = vb ^ ((wv & 3) << 4);
            auto wq_dma = [&](int ps) { dma16<NTM & 2>(vq, wq + (size_t)((ps + rot) & 15) * 262144, ps * 8192 + wv * 1024); };
            bf16x8 uk[4][4], aq[16];
            u32x4 fi, hv[4];
            unsigned fio = (unsigned)(WS_FI + x * STR + (lane & 7) * 16), hsrc, hsrc2, ta0, ta1, tb0, tb1, qo2, hw0, hw1, hr, hr2, qr0, qr1, qw, thx = 0u;
            const unsigned L_HI = L_SCR, L_QI = L_SCR + 8192;
            auto h_load = [&]() {
                asm volatile("buffer_load_dwordx4 %0, %4, %6, 0 offen sc1\n\tbuffer_load_dwordx4 %1, %4, %6, 0 offen offset:1024 sc1\n\t"
                             "buffer_load_dwordx4 %2, %5, %6, 0 offen sc1\n\tbuffer_load_dwordx4 %3, %5, %6, 0 offen offset:1024 sc1"
                             : "+v"(hv[0]), "+v"(hv[1]), "+v"(hv[2]), "+v"(hv[3]) : "v"(hsrc), "v"(hsrc2), "s"(rw) : "memory");
            };
            auto h_bad = [&]() {
                PIN4(hv[0], hv[1], hv[2], hv[3]);
                unsigned bad = (hv[0][1] ^ tlo) | (hv[0][3] ^ thx) | (hv[1][1] ^ tlo) | (hv[1][3] ^ thx) | (hv[2][1] ^ tlo) | (hv[2][3] ^ thx) | (hv[3][1] ^ tlo) | (hv[3][3] ^ thx);
                PIN(bad);
                return bad;
            };
#pragma unroll
            for (int ps = 0; ps < 16; ++ps) {
                wq_dma(ps);
                if (ps == 4) cnt_load();
                if (ps == HP) { h_load(); asm volatile("buffer_load_dwordx4 %0, %1, %2, 0 offen sc1" : "=v"(fi) : "v"(fio), "s"(rw) : "memory"); }
                if (ps >= 4 && ps < 8) VMW(5); else if (ps >= HP && ps < HP + 4) VMW(9); else VMW(4);
                if (ps == 3) {
                    asm volatile("s_barrier" ::: "memory");
                    if (pub) {
#pragma unroll
                        for (int t = 0; t < 4; ++t)
#pragma unroll
                            for (int s = 0; s < 4; ++s) uk[t][s] = *LP(bf16x8, L_U + off_b(wv * 64 + (nl >> 2) * 16 + t * 4 + (nl & 3), s * 4 + g));
                    }
                    STAMP(1);
                }
                if (ps == 5) {
                    hsrc = (unsigned)(WS_H + x * STR + wv * 2048 + lane * 16); hsrc2 = hsrc + 16384;
                    ta0 = off_b(8 * g + (nl >> 2), 2 * wv + ((lane & 3) >> 1)) + 8 * (lane & 1); ta1 = off_b(8 * g + 4 + (nl >> 2), 2 * wv + ((lane & 3) >> 1)) + 8 * (lane & 1);
                    tb0 = ta0 + 65536; tb1 = ta1 + 65536; qo2 = qo;
                    hw0 = L_HI + ((2 * wv - rot) & 15) * 512 + (lane >> 4) * 128 + (lane & 7) * 16 + ((lane >> 3) & 1) * 8;
                    hw1 = L_HI + ((2 * wv + 1 - rot) & 15) * 512 + (lane >> 4) * 128 + (lane & 7) * 16 + ((lane >> 3) & 1) * 8;
                    hr2 = L_HI + g * 128 + (nl & 7) * 16 + (nl >> 3) * 512;
                    PIN(hr2);
                    qr0 = L_QI + g * 128 + (nl & 7) * 16 + (nl >> 3) * 2048; qr1 = qr0 ^ 2048u;
                    PIN(qr0); PIN(qr1);
                    hr = L_HI + g * 128 + (nl & 7) * 16; qw = L_QI + (wv >> 1) * 512 + ((wv & 1) * 2 + (g >> 1)) * 128 + (nl & 7) * 16 + (g & 1) * 8;
                    PIN4(hsrc, hsrc2, ta0, ta1); PIN4(tb0, tb1, qo2, hw0); PIN(hw1); PIN(qw);
                }
                if (ps == 8) { tag_take(); thx = tlo ^ thi ^ TAGC; }
                if (ps == 9) {
#pragma unroll
                    for (int t = 0; t < 4; ++t) PIN4(uk[t][0], uk[t][1], uk[t][2], uk[t][3]);
                    wg_barrier();
                    *LP(unsigned, L_QI + 2048 + wv * 256 + lane * 4) = 0u;
#pragma unroll
                    for (int q = 0; q < 6; ++q) aq[q] = tr_frag(ta0 + q * 8192, ta1 + q * 8192);
                }
                if (ps == 13) {
                    asm volatile("s_barrier" ::: "memory");
#pragma unroll
                    for (int q = 6; q < 10; ++q) aq[q] = q < 8 ? tr_frag(ta0 + q * 8192, ta1 + q * 8192) : tr_frag(tb0 + (q - 8) * 8192, tb1 + (q - 8) * 8192);
                }
            }
            STAMP(15);
            if (HP + 4 > 15) VMW(HP + 4 > 15 ? 15 - HP : 0);
            if (!__all(h_bad() == 0u)) {
                for (unsigned spins = 0;;) {
                    h_load();
                    VMW(0);
                    if (__all(h_bad() == 0u)) break;
                    __builtin_amdgcn_s_sleep(SLPH);
                    if (++spins > kSpinCap) break;
                }
            }
            {
                unsigned hh[4];
#pragma unroll
                for (int q = 0; q < 4; ++q) {
                    const unsigned t = hv[2 + (q >> 1)][(q & 1) * 2], sh = hv[q >> 1][(q & 1) * 2];
                    hh[q] = pk2(lo16(t) + lo16(sh), hi16(t) + hi16(sh));
                }
                *LP(u32x2, hw0) = u32x2{hh[0], hh[1]};
                *LP(u32x2, hw1) = u32x2{hh[2], hh[3]};
            }
            STAMP(3);
            asm volatile("s_waitcnt vmcnt(0) lgkmcnt(0)\n\ts_barrier" : "+v"(fi) :: "memory");
            STAMP(2);
            {
                bf16x8 hb[8];
#pragma unroll
                for (int q = 0; q < 5; ++q) hb[q] = *LP(bf16x8, hr2 + q * 1024);
#pragma unroll
                for (int ps = 10; ps < 16; ++ps) {
                    if (!(ps & 1)) hb[ps >> 1] = *LP(bf16x8, hr2 + (ps >> 1) * 1024);
                    aq[ps] = tr_frag(tb0 + (ps - 8) * 8192, tb1 + (ps - 8) * 8192);
                }
                __builtin_amdgcn_sched_barrier(0);
                f32x4 d0 = {0.f, 0.f, 0.f, 0.f}, d1 = {0.f, 0.f, 0.f, 0.f};
#pragma unroll
                for (int ps = 0; ps < 16; ps += 2) {
                    d0 = MFMA16(aq[ps], hb[ps >> 1], d0); d1 = MFMA16(aq[ps + 1], hb[ps >> 1], d1);
                    if (ps == 6) { unsigned bad = (fi[0] ^ tlo) | (fi[1] ^ thi) | (fi[2] ^ tlo) | (fi[3] ^ thi); PIN(bad); if (!__all(bad == 0u)) flags_wait(fio); }
                }
#pragma unroll
                for (int e = 0; e < 4; ++e) { float v = d0[e] + dppf<0x128>(d1[e]); asm("" : "+v"(v)); d0[e] = v; }
                if (nl < 8) *LP(u32x2, qw) = u32x2{pk2(d0[0], d0[1]), pk2(d0[2], d0[3])};
            }
            wg_barrier();
            if (pub) {
                bf16x8 qb[2][4];
#pragma unroll
                for (int s = 0; s < 4; ++s) { qb[0][s] = *LP(bf16x8, qr0 + s * 512); qb[1][s] = *LP(bf16x8, qr1 + s * 512); }
                __builtin_amdgcn_sched_barrier(0);
                f32x4 a = {0.f, 0.f, 0.f, 0.f}, b = a;
#pragma unroll
                for (int s = 0; s < 4; ++s) {
                    a = MFMA16(uk[0][s], qb[0][s], a); b = MFMA16(uk[1][s], qb[0][s], b);
                    a = MFMA16(uk[2][s], qb[1][s], a); b = MFMA16(uk[3][s], qb[1][s], b);
                }
                const u32x4 qv = {pk2s(a[0], a[1]), pk2s(a[2], a[3]), pk2s(b[0], b[1]), pk2s(b[2], b[3])};
                ST16(qv, qo2, 0);
            }
            STAMP(4);
            wg_barrier();
            if (LMPF && wv >= 3 && wv < 6) {
                const unsigned n = (unsigned)(j * 3 + wv - 3);
                if (n < 32) pf4<0>(((n & 7u) * 64 + lane) * 6144u + 2048u + (x * 4 + (n >> 3)) * 128u, p.w_mod, L_SCR + 31744);
            }
            if (QHELP && wv < 3 && j + 12 * wv < 32) {
                const int hh = j + 12 * wv;
#pragma unroll 1
                for (unsigned spins = 0; spins < 64; ++spins) {
                    __builtin_amdgcn_s_sleep(QHELP);
                    const u32x4 v = LD16((unsigned)(WS_Q + (hh & 7) * STR + (x * 4 + (hh >> 3)) * 1024 + lane * 16), 0);
                    if (__all(mx4(v, 0u) != SENT)) break;
                    asm volatile("" ::: "memory");
                }
                STAMP(6);
            }
        } else {
            const int cg = x * 2 + (j & 1);
            if (j < 14) {
                dma_piece(p.w_mod + (size_t)wv * 64 * 3072 + cg * 64, 6144, rgn, 8, NTM & 2);
                cnt_load();
                VMW(9);
            } else {
                cnt_load();
                ST16(s4, (unsigned)(WS_Q + wv * STR + cg * 2048 + lane * 16), 0); ST16(s4, (unsigned)(WS_Q + wv * STR + cg * 2048 + lane * 16), 1024);
                VMW(0);
                tag_take();
                wg_barrier();
                if (wv == 7) flag_put(WS_FI, cg);
                STAMP(15);
                if (j == 14) {
                    u32x2 best = {~0u, ~0u};
#pragma unroll
                    for (int r = 0; r < 2 * NCAL; ++r) {
                        const unsigned hf = (r ^ wv) & 1;
                        VMW(0);
                        const unsigned long long t0 = __builtin_amdgcn_s_memtime();
                        if (lane < 8) ST16(s4, WS_CAL + x * STR + hf * 8192 + (wv * 8 + r) * 128 + lane * 16, 0);
                        VMW(0);
                        const unsigned dt = (unsigned)(__builtin_amdgcn_s_memtime() - t0);
                        if (hf) best[1] = min(best[1], dt); else best[0] = min(best[0], dt);
                    }
                    *LP(u32x2, 16384 * 7 + 14336 + wv * 8) = best;
                    wg_barrier();
                    if (wv == 0 && lane < 8) {
                        u32x2 m = *LP(u32x2, 16384 * 7 + 14336 + lane * 8);
#pragma unroll
                        for (int o = 1; o < 8; o <<= 1) { m[0] = min(m[0], (unsigned)__shfl_xor(m[0], o)); m[1] = min(m[1], (unsigned)__shfl_xor(m[1], o)); }
                        ST16((u32x4{(unsigned)max(-1000, min(1000, (int)m[1] - (int)m[0])), 0u, tlo, thi}), (unsigned)(WS_NP + lane * STR + x * 16), 0);
                    }
                    if (wv == 0) {
                        const int d = (int)tagged_wait(WS_NP + x * STR + (lane & 7) * 16)[0];
                        int t = (lane & 4) ? -d : d;
                        t += __builtin_amdgcn_update_dpp(0, t, 0xB1, 0xf, 0xf, true);
                        t += __builtin_amdgcn_update_dpp(0, t, 0x4E, 0xf, 0xf, true);
                        t += __builtin_amdgcn_update_dpp(0, t, 0x141, 0xf, 0xf, true);
                        const bool n1 = abs(d) > 160 ? d < 0 : (t < 0) != ((lane & 4) != 0);
                        const unsigned mk = NEARP ? (unsigned)__ballot(n1) & 0xffu : 0u;
                        if (lane == 0) ST16((u32x4{mk, 0u, tlo, thi}), (unsigned)(WS_NM + x * STR), 0);
                    }
                    STAMP(9);
                }
            }
            const u16x8 cf = *LP(u16x8, rgn + 8192 + lane * 16), xa = *LP(u16x8, rgn + 9216 + lane * 16);
            u32x4 af[2];
#pragma unroll
            for (int q = 0; q < 4; ++q) {
                const float a = bf2f(cf[2 * q]), b = bf2f(cf[2 * q + 1]);
                af[0][q] = pk2(a * __builtin_amdgcn_rcpf(1.f + __expf(-a)), b * __builtin_amdgcn_rcpf(1.f + __expf(-b)));
                af[1][q] = (unsigned)__builtin_amdgcn_update_dpp(0, (int)af[0][q], 0x128, 0xf, 0xf, true);
            }
            const unsigned L_MS = L_SCR;
            unsigned wa[4], fr[4];
#pragma unroll
            for (int t = 0; t < 4; ++t) { wa[t] = L_MS + wv * 2048 + (nl & 7) * 256 + (((t * 4 + g) ^ (nl & 7)) << 4) + (nl >> 3) * 16384; fr[t] = rgn + frl + ((t ^ frs) << 5); }
            PIN4(wa[0], wa[1], wa[2], wa[3]); PIN4(fr[0], fr[1], fr[2], fr[3]); PIN(af[0]); PIN(af[1]);
            const unsigned L_XN = 12288;
            auto mod_mm = [&](bool ln) {
                VMW(0);
                bf16x8 f[2][4];
#pragma unroll
                for (int ks = 0; ks < 2; ++ks)
#pragma unroll
                    for (int t = 0; t < 4; ++t) f[ks][t] = tr_frag(fr[t] + ks * 4096, fr[t] + ks * 4096 + 512);
                __builtin_amdgcn_sched_barrier(0);
                f32x4 d[4];
#pragma unroll
                for (int t = 0; t < 4; ++t) d[t] = MFMA16(f[0][t], BF8(af[0]), (f32x4{0.f, 0.f, 0.f, 0.f}));
#pragma unroll
                for (int t = 0; t < 4; ++t) d[t] = MFMA16(f[1][t], BF8(af[1]), d[t]);
#pragma unroll
                for (int t = 0; t < 4; ++t) *LP(f32x4, wa[t]) = d[t];
                if (ln) {
                    float v[8];
#pragma unroll
                    for (int q = 0; q < 8; ++q) v[q] = bf2f(xa[q]);
                    const float p1 = ((v[0] + v[1]) + (v[2] + v[3])) + ((v[4] + v[5]) + (v[6] + v[7]));
                    const float p2 = ((v[0] * v[0] + v[1] * v[1]) + (v[2] * v[2] + v[3] * v[3])) + ((v[4] * v[4] + v[5] * v[5]) + (v[6] * v[6] + v[7] * v[7]));
                    float mb, rden;
                    ln_stats(p1, p2, mb, rden);
                    u32x4 r;
#pragma unroll
                    for (int q = 0; q < 4; ++q) { const unsigned d = pk2(v[2 * q] - mb, v[2 * q + 1] - mb); r[q] = pk2(lo16(d) * rden, hi16(d) * rden); }
                    *LP(u32x4, L_XN + wv * 16384 + lane * 16) = r;
                }
                wg_barrier();
            };
            auto sum8 = [&](unsigned a) {
                f32x4 u[8];
#pragma unroll
                for (int w2 = 0; w2 < 8; ++w2) u[w2] = *LP(f32x4, a + w2 * 2048);
                __builtin_amdgcn_sched_barrier(0);
                return ((u[0] + u[1]) + (u[2] + u[3])) + ((u[4] + u[5]) + (u[6] + u[7]));
            };
            if (j < 14) {
                const int hb = lane & 7, c4 = 2 * wv + (nl >> 3);
                unsigned hoff = (unsigned)(WS_H + g * STR + cg * 2048 + wv * 256 + nl * 16);
                unsigned sa = L_MS + hb * 256 + ((c4 ^ hb) << 4);
                unsigned xa2 = L_XN + hb * 16384 + (cg & 7) * 128 + c4 * 8;
                PIN(hoff); PIN(sa); PIN(xa2);
                STAMP(1);
                mod_mm(cg >= 8);
                STAMP(2);
                const u32x2 xn = *LP(u32x2, xa2);
                const f32x4 v = sum8(sa);
                tag_take();
                const unsigned thx = tlo ^ thi ^ TAGC;
                u32x2 r = {pk2(v[0], v[1]), pk2(v[2], v[3])};
                if (cg >= 8) {
#pragma unroll
                    for (int q = 0; q < 2; ++q) { const unsigned sc = pk2(1.f + lo16(r[q]), 1.f + hi16(r[q])); r[q] = pk2(lo16(xn[q]) * lo16(sc), hi16(xn[q]) * hi16(sc)); }
                }
                ST16((u32x4{r[0], tlo, r[1], thx}), hoff, 0);
                ST16((u32x4{r[0], tlo, r[1], thx}), hoff, 4 * STR);
                STAMP(5);
            }
            wg_barrier();
            while (__builtin_amdgcn_s_memrealtime() < t_start + LMD) __builtin_amdgcn_s_sleep(2);
            const int lg = x * 4 + (j & 3);
            dma_piece(p.w_mod + (size_t)wv * 64 * 3072 + 1024 + lg * 64, 6144, rgn, 8, NTM & 4);
            mod_mm(false);
            if (lg >= 8 && lg < 24) {
                if (wv == 0) {
                    const int b = lane >> 3, c8 = lane & 7;
                    const f32x4 v0 = sum8(L_MS + b * 256 + (((2 * c8) ^ b) << 4)), v1 = sum8(L_MS + b * 256 + (((2 * c8 + 1) ^ b) << 4));
                    ST16((u32x4{pk2(v0[0], v0[1]), pk2(v0[2], v0[3]), pk2(v1[0], v1[1]), pk2(v1[2], v1[3])}), (unsigned)(WS_MB + b * STR + (lg - 8) * 128 + c8 * 16), 0);
                }
            } else if (wv < 4) {
                const int b = wv * 2 + (lane >> 5), cp = lane & 31;
                f32x2_t u[8];
#pragma unroll
                for (int w2 = 0; w2 < 8; ++w2) u[w2] = *LP(f32x2_t, L_MS + w2 * 2048 + b * 256 + (((cp >> 1) ^ b) << 4) + (cp & 1) * 8);
                const f32x2_t v = ((u[0] + u[1]) + (u[2] + u[3])) + ((u[4] + u[5]) + (u[6] + u[7]));
                ST16((u32x4{__float_as_uint(rbf(v[0])), __float_as_uint(rbf(v[1])), tlo, thi}), (unsigned)(WS_MOD + b * STR + (lg * 32 + cp) * 16), 0);
            }
            STAMP(6);
        }
        const int bq = (j >> 2) & 1, dh = j >> 3, src = th * 2 + dh, rb = bq * 4 + (src >> 4), rc = src & 15;
        wg_barrier();
        if (TLBW > 1 && j == 0) {
            if (wv == 4 && lane < 5) pf4s((unsigned)((lane + 1) * 2097152 + 8064), ws, L_SCR + 31744);
            if (wv == 5 && lane < 2) { pf4s((unsigned)(lane * 2097152), p.w_uv, L_SCR + 31744); pf4s((unsigned)(lane * 2097152), p.w_o, L_SCR + 31744); }
            if (wv == 6 && lane < 1) { pf4s(0u, p.w1, L_SCR + 31744); pf4s(0u, p.w2, L_SCR + 31744); pf4s(0u, p.out, L_SCR + 31744); }
        }
        while (__builtin_amdgcn_s_memrealtime() < t_start + TWD - 110) __builtin_amdgcn_s_sleep(2);
        const unsigned npm = __builtin_amdgcn_readfirstlane(tagged_wait(WS_NM + x * STR)[0]), np = (npm >> x) & 1u;
        unsigned oo = (unsigned)(WS_OUTP + ((bq * 4 + (nl & 3)) * 16 + wv * 2 + (nl >> 3)) * 16384 + (src ^ wv) * 128 + ((nl >> 2) & 1) * 64 + g * 16) + ((~npm >> ((nl & 3) * 2 + (wv >> 2))) & 1u) * 8192u;
        const int rcol = rc * 32 + (lane & 7) * 4 + (lane >> 5) * 2;
        unsigned xdst = (unsigned)(WS_X1 + ((lane >> 3) & 3) * STR + (rb * 512 + rcol) * 2), xdst2 = xdst + 4 * STR + ((npm >> (4 + ((lane >> 3) & 3))) & 1u) * 8192u;
        xdst += ((npm >> ((lane >> 3) & 3)) & 1u) * 8192u;
        ST16(s4, oo, 0);
        if (wv == 7) { __builtin_amdgcn_raw_buffer_store_b32(SENT, rw, xdst, 0, 16); __builtin_amdgcn_raw_buffer_store_b32(SENT, rw, xdst2, 0, 16); }
        VMW(0);
        wg_barrier();
        if (wv == 0) flag_put(WS_FB, id);
        while (__builtin_amdgcn_s_memrealtime() < t_start + TWD) __builtin_amdgcn_s_sleep(2);
        asm volatile("s_barrier" ::: "memory");
        STAMP(7);
        if ((PFM & 8) && wv == 0 && lane < 32) pf4<NTM & 8>((unsigned)((j * 32 + lane) * 8192 + x * 1024 + ((3u - ((unsigned)(size_t)p.w_uv >> 7)) & 7u) * 128), p.w_uv, L_SCR + 31744);
        if (bq) dma_piece(p.w_o + (size_t)(th * 128 + dh * 64) * 512 + wv * 64, 1024, rgn + 8192, 8, NTM & 8);
        dma_piece(p.w_uv + ((size_t)wv * 64 * NH + th) * 128 + dh * 64, 8192, rgn, 8, NTM & 8);
        if (!bq) dma_piece(p.w_o + (size_t)(th * 128 + dh * 64) * 512 + wv * 64, 1024, rgn + 8192, 8, NTM & 8);
        u32x4 fb, gm;
        unsigned xg;
        VMW(4);
        if (wv == 7)
            asm volatile("buffer_load_dwordx4 %0, %3, %5, 0 offen sc1\n\tbuffer_load_dwordx4 %1, %4, %5, 0 offen sc1\n\tglobal_load_dword %2, %6, %7" : "=&v"(fb), "=&v"(gm), "=&v"(xg)
                         : "v"((unsigned)(WS_FB + x * STR + lane * 16)), "v"((unsigned)(WS_MOD + rb * STR + rcol * 8)), "s"(rw), "v"((unsigned)(rb * 512 + rcol) * 2u), "s"(p.x) : "memory");
        VMW(0);
        asm volatile("" : "+v"(fb), "+v"(gm), "+v"(xg));
        STAMP(8);
        bf16x8 fuv[2][4], fwo[2][4];
        unsigned pfa[4];
#pragma unroll
        for (int t = 0; t < 4; ++t) { pfa[t] = rgn + frl + ((t ^ frs) << 5); PIN(pfa[t]); }
#pragma unroll
        for (int ks = 0; ks < 2; ++ks)
#pragma unroll
            for (int t = 0; t < 4; ++t) {
                const unsigned a = rgn + (unsigned)((g * 4 + (nl >> 2)) * 128 + (lane & 3) * 8) + ((t ^ (unsigned)((nl >> 3) | (g & 2))) << 5) + ks * 4096;
                fuv[ks][t] = tr_frag(a, a + 2048);
                fwo[ks][t] = tr_frag(pfa[t] + 8192 + ks * 4096, pfa[t] + 8192 + ks * 4096 + 512);
            }
#pragma unroll
        for (int ks = 0; ks < 2; ++ks) { PIN4(fuv[ks][0], fuv[ks][1], fuv[ks][2], fuv[ks][3]); PIN4(fwo[ks][0], fwo[ks][1], fwo[ks][2], fwo[ks][3]); }
        const unsigned L_WGT = L_SCR, L_MSK = L_SCR + 256, L_RED = L_SCR + 1024;
        unsigned psrc = (unsigned)(WS_PART + (bq * 4 * 32 + th) * 32768 + (wv >> 2) * 16384 + (wv & 3) * 2048 + ((lane >> 3) ^ (th & 7)) * 128) + np * 8192u + pcl, psrc1 = (psrc ^ 64u) + 1024;
        unsigned psrc2 = psrc ^ 8192u, psrc3 = psrc1 ^ 8192u;
        PIN(psrc2); PIN(psrc3); PIN(xdst2);
        unsigned rw2 = L_RED + wv * 1024 + (nl & 3) * 256 + g * 16 + (YSEL ? (nl >> 2) * 64 : 0);
        unsigned wa0 = (nl & 3) == (g >> 1) ? L_WGT + (HILO ? nl & 4 : 0) * 32 + g * 16 : L_WGT + 272, wa1 = (nl & 3) == 2 + (g >> 1) ? L_WGT + (HILO ? nl & 4 : 0) * 32 + 64 + g * 16 : L_WGT + 272;
        unsigned cr = rgn + (nl & 3) * 128 + g * 16;
        PIN(cr);
        unsigned r2r = L_RED + lane * 16, cw = rgn + lane * 8;
        unsigned osrc = (unsigned)(WS_OUTP + (rb * 16 + rc) * 16384 + lane * 16) + (np ^ 1u) * 8192u;
        PIN(psrc); PIN(psrc1); PIN(rw2); PIN(wa0); PIN(wa1); PIN(r2r); PIN4(cw, oo, osrc, xdst);
        float mlm = 0.f, mll = 0.f;
        if (wv == 0) *LP(unsigned, L_WGT + 256 + lane * 4) = 0u;
        wg_barrier();
        const bool early = __all(flags_ok(fb) & (gm[2] == tlo) & (gm[3] == thi));
        u32x2 ga = {gm[0], gm[1]};
        PIN(ga);
        bf16x8 pa[2][4], wh[2];
        {
            u32x4 ml = {0u, 0u, ~tlo, ~thi};
            const unsigned mlsrc = (unsigned)(WS_ML + (bq * 4 + g) * STR + nl * 512 + th * 16) + ((npm >> (x & 6)) & 1u) * 8192u;
            unsigned long long done = 0ull;
            unsigned lm = L_MSK;
            PIN(lm);
#pragma unroll 1
            for (unsigned spins = 0; spins < kSpinCap; ++spins) {
                unsigned long long m;
                if (wv == 0) {
                    if ((ml[2] != tlo) | (ml[3] != thi)) ml = LD16(mlsrc, 0);
                    m = __ballot((ml[2] == tlo) & (ml[3] == thi));
                    unsigned long long f = m & (m >> 4);
                    f &= f >> 2; f &= f >> 1;
                    asm volatile("ds_write_b64 %0, %1" :: "v"(lm), "v"(f) : "memory");
                    m = m == ~0ull ? 0x0101010101010101ull : 0ull;
                } else {
                    unsigned long long t;
                    asm volatile("ds_read_b64 %0, %1\n\ts_waitcnt lgkmcnt(0)" : "=v"(t) : "v"(lm) : "memory");
                    m = ((unsigned long long)(unsigned)__builtin_amdgcn_readfirstlane((unsigned)(t >> 32)) << 32) | (unsigned)__builtin_amdgcn_readfirstlane((unsigned)t);
                    m &= 0x0101010101010101ull & ~done;
                }
                if (m) {
                    done |= m;
#define PLD(bit, off, v, base) "s_bitcmp1_b64 %0, " #bit "\n\ts_cbranch_scc0 1f\n\ts_add_u32 m0, %1, " #off "\n\ts_nop 0\n\tglobal_load_lds_dwordx4 " v ", " base " sc1\n1:\n\t"
                    asm volatile(PLD(0, 0, "%2", "%4") PLD(8, 1024, "%3", "%4") PLD(16, 2048, "%2", "%5") PLD(24, 3072, "%3", "%5")
                                 PLD(32, 4096, "%8", "%6") PLD(40, 5120, "%9", "%6") PLD(48, 6144, "%8", "%7") PLD(56, 7168, "%9", "%7")
                                 :: "s"(m), "s"(rgn), "v"(psrc), "v"(psrc1), "s"(ws), "s"(ws + 1048576), "s"(ws + 2097152), "s"(ws + 3145728), "v"(psrc2), "v"(psrc3) : "memory", "m0", "scc");
                    if (done == 0x0101010101010101ull) break;
                } else if (SLPM > 0 && wv == 0) __builtin_amdgcn_s_sleep(SLPM);
            }
            mlm = __uint_as_float(ml[0]); mll = __uint_as_float(ml[1]);
            STAMP(10);
            if (wv == 0) {
                float M = mlm;
                M = fmaxf(M, dppf<0xB1>(M)); M = fmaxf(M, dppf<0x4E>(M)); M = fmaxf(M, dppf<0x141>(M)); M = fmaxf(M, dppf<0x140>(M));
                const float e = __builtin_amdgcn_exp2f(mlm - M);
                const float w = e * __builtin_amdgcn_rcpf(sum16(e * mll));
                const unsigned short hi = f2bf(w);
                *LP(unsigned short, L_WGT + lane * 2) = hi;
                if (HILO) *LP(unsigned short, L_WGT + 128 + lane * 2) = f2bf(w - bf2f(hi));
            }
            wg_barrier();
            wh[0] = *LP(bf16x8, wa0); wh[1] = *LP(bf16x8, wa1);
            __builtin_amdgcn_sched_barrier(0);
            VMW(0);
#pragma unroll
            for (int ks = 0; ks < 2; ++ks)
#pragma unroll
                for (int t = 0; t < 4; ++t) pa[ks][t] = tr_frag(pfa[t] + ks * 4096, pfa[t] + ks * 4096 + 512);
        }
        STAMP(11);
        {
            f32x4 d[4];
#pragma unroll
            for (int t = 0; t < 4; ++t) d[t] = MFMA16(pa[0][t], wh[0], (f32x4{0.f, 0.f, 0.f, 0.f}));
#pragma unroll
            for (int t = 0; t < 4; ++t) d[t] = MFMA16(pa[1][t], wh[1], d[t]);
#pragma unroll
            for (int t = 0; t < 4; ++t)
#pragma unroll
                for (int e = 0; e < 4; ++e) if (HILO) { float v = d[t][e] + dppf<0x124>(d[t][e]); asm("" : "+v"(v)); d[t][e] = v; }
            const u32x4 o0 = {pk2(d[0][0], d[0][1]), pk2(d[0][2], d[0][3]), pk2(d[1][0], d[1][1]), pk2(d[1][2], d[1][3])};
            const u32x4 o1 = {pk2(d[2][0], d[2][1]), pk2(d[2][2], d[2][3]), pk2(d[3][0], d[3][1]), pk2(d[3][2], d[3][3])};
            f32x4 y[4];
#pragma unroll
            for (int t = 0; t < 4; ++t) y[t] = MFMA16(fuv[0][t], BF8(o0), (f32x4{0.f, 0.f, 0.f, 0.f}));
#pragma unroll
            for (int t = 0; t < 4; ++t) y[t] = MFMA16(fuv[1][t], BF8(o1), y[t]);
            if (YSEL) *LP(f32x4, rw2) = (nl & 8) ? ((nl & 4) ? y[3] : y[2]) : ((nl & 4) ? y[1] : y[0]);
            else if (nl < 4) {
#pragma unroll
                for (int t = 0; t < 4; ++t) *LP(f32x4, rw2 + t * 64) = y[t];
            }
        }
        wg_barrier();
        {
            f32x4 cs[8];
#pragma unroll
            for (int w2 = 0; w2 < 8; ++w2) cs[w2] = *LP(f32x4, r2r + w2 * 1024);
            const f32x4 c4 = ((cs[0] + cs[1]) + (cs[2] + cs[3])) + ((cs[4] + cs[5]) + (cs[6] + cs[7]));
            *LP(u32x2, cw) = u32x2{pk2(c4[0], c4[1]), pk2(c4[2], c4[3])};
            asm volatile("s_waitcnt lgkmcnt(0)" ::: "memory");
            const u32x4 cx0 = *LP(u32x4, cr), cx1 = *LP(u32x4, cr + 64);
            f32x4 o[4];
#pragma unroll
            for (int q = 0; q < 4; ++q) o[q] = MFMA16(fwo[0][q], BF8(cx0), (f32x4{0.f, 0.f, 0.f, 0.f}));
#pragma unroll
            for (int q = 0; q < 4; ++q) o[q] = MFMA16(fwo[1][q], BF8(cx1), o[q]);
            const f32x4 oa = (nl & 8) ? ((nl & 4) ? o[3] : o[2]) : ((nl & 4) ? o[1] : o[0]);
            const u32x4 sv = {nosent(__float_as_uint(oa[0])), nosent(__float_as_uint(oa[1])), nosent(__float_as_uint(oa[2])), nosent(__float_as_uint(oa[3]))};
            ST16(sv, oo, 0);
        }
        STAMP(12);
        wg_barrier();
        if (wv == 7) {
            if (!early) {
                flags_wait(WS_FB + x * STR + lane * 16);
                { const u32x4 v = tagged_wait(WS_MOD + rb * STR + rcol * 8); ga = u32x2{v[0], v[1]}; }
                PIN(ga);
            }
            u32x4 op[8];
            for (unsigned spins = 0;;) {
#pragma unroll
                for (int k = 0; k < 8; ++k) op[k] = LD16(osrc + (k & 3) * 1024, (k >> 2) * 4096);
                unsigned m = 0u;
#pragma unroll
                for (int k = 0; k < 8; ++k) m = mx4(op[k], m);
                if (__all(m != SENT)) break;
                if (++spins > kSpinCap) break;
                asm volatile("" ::: "memory");
            }
            STAMP(13);
            const f32x4 v = ((__builtin_bit_cast(f32x4, op[0]) + __builtin_bit_cast(f32x4, op[1])) + (__builtin_bit_cast(f32x4, op[2]) + __builtin_bit_cast(f32x4, op[3]))) +
                            ((__builtin_bit_cast(f32x4, op[4]) + __builtin_bit_cast(f32x4, op[5])) + (__builtin_bit_cast(f32x4, op[6]) + __builtin_bit_cast(f32x4, op[7])));
            float w[2];
#pragma unroll
            for (int q = 0; q < 2; ++q) {
                auto t = __builtin_amdgcn_permlane32_swap(__float_as_uint(v[q]), __float_as_uint(v[2 + q]), false, false);
                w[q] = __uint_as_float(t[0]) + __uint_as_float(t[1]);
                auto u = __builtin_amdgcn_permlane16_swap(__float_as_uint(w[q]), __float_as_uint(w[q]), false, false);
                w[q] = __uint_as_float(u[0]) + __uint_as_float(u[1]);
                w[q] += dppf<0x128>(w[q]);
            }
            unsigned t = pk2(w[0], w[1]);
            t = pk2(__uint_as_float(ga[0]) * lo16(t), __uint_as_float(ga[1]) * hi16(t));
            t = pk2s(lo16(xg) + lo16(t), hi16(xg) + hi16(t));
            __builtin_amdgcn_raw_buffer_store_b32(t, rw, xdst, 0, 16);
            __builtin_amdgcn_raw_buffer_store_b32(t, rw, xdst2, 0, 16);
            STAMP(14);
        }
        STAMP_FLUSH();
        return;
    }

    const int s = j - 16;
    const unsigned long long t_s = __builtin_amdgcn_s_memrealtime();
    if (wv == 0) cnt_load();
    {
        const unsigned ln = (unsigned)(s * 64 + lane);
        if ((PFM & 1) && wv == 0) pf4<NTM & 1>((ln & 511u) * 6144u + (x * 2 + (ln >> 9)) * 128u, p.w_mod, L_SCR);
        if ((PFM & 2) && wv == 1) {
            const unsigned short* w = s < 8 ? p.w_uk : p.w_q;
            pf4<NTM & 1>((ln & 511u) * 8192u + x * 1024u + ((3u - ((unsigned)(size_t)w >> 7)) & 7u) * 128u, w, L_SCR);
        }
        if ((PFM & 48) == 48) {
            const unsigned r0 = (unsigned)(s * 4 + (wv & 3)), row = PFS == 0 ? r0 * 8 + (lane >> 3) : PFS == 1 ? (lane >> 3) * 64 + r0 : (r0 >> 5) * 256 + (lane >> 3) * 32 + (r0 & 31);
            pf4<NTM & 1>(row * 8192u + x * 1024u + (lane & 7u) * 128u, wv < 4 ? p.w_uk : p.w_q, L_SCR);
        }
        else if ((PFM & 16) && wv >= 4) { const unsigned l2 = (unsigned)((s * 4 + (wv & 3)) * 64 + lane); pf4<NTM & 1>((l2 >> 3) * 8192u + x * 1024u + (l2 & 7u) * 128u, p.w_q, L_SCR); }
        else if ((PFM & 32) && wv >= 4) { const unsigned l2 = (unsigned)((s * 4 + (wv & 3)) * 64 + lane); pf4<NTM & 1>((l2 >> 3) * 8192u + x * 1024u + (l2 & 7u) * 128u, p.w_uk, L_SCR); }
        if ((PFM & 4) && (wv & 6) == 2) { const unsigned l2 = (unsigned)((s * 2 + (wv & 1)) * 64 + lane); pf4<NTM & 1>((l2 & 511u) * 8192u + (x * 4 + (l2 >> 9)) * 256u, p.w_q, L_SCR); }
    }
    const int ug = x * 7 + s;
    const unsigned vb = (unsigned)(g * 1024 + ((nl ^ (g << 2)) << 4)), lb = (unsigned)((wv >> 1) * 32768 + (wv & 1) * 4096);
    const char* ck = (const char*)p.ckv + ((size_t)x * SK + s * 256 + wv * 16) * 1024;
    const unsigned rot = ROTC ? (unsigned)s & 3u : 0u;
    const unsigned vbr = vb ^ (rot << 8);
    const char* ck2 = ck + 131072;
    auto ck_dma = [&](unsigned i) { dma16x<NTM & 16>(vbr, ((i & 3u) << 4) | (((i >> 2) & 3u) << 8) | ((i & 3u) << 12), (i >> 4) ? ck2 : ck, lb + ((i >> 2) & 3u) * 8192u + (i & 3u) * 1024u); };
    bf16x8 ka[16];
    s16x4 va[8][4];
    unsigned kad[4], vad[4], vad2[4];
    auto kq = [](unsigned u) { return (0x78u >> (2 * u)) & 3u; };
    auto ka_reads = [&](int cb) {
#pragma unroll
        for (int u = 0; u < 4; ++u) ka[cb * 4 + u] = *LP(bf16x8, kad[u] + cb * 8192);
    };
    auto va_reads = [&]() {
#pragma unroll
        for (int kk = 0; kk < 8; ++kk)
#pragma unroll
            for (int q = 0; q < 4; ++q) va[kk][q] = tr4((kk < 4 ? vad[q] : vad2[q]) + ((kk >> 1) & 1) * 32768 + (kk & 1) * 4096);
    };
    auto va_read1 = [&](int kk) {
#pragma unroll
        for (int q = 0; q < 4; ++q) va[kk][q] = tr4((kk < 4 ? vad[q] : vad2[q]) + ((kk >> 1) & 1) * 32768 + (kk & 1) * 4096);
    };
    auto va_pin = [&]() {
#pragma unroll
        for (int q = 0; q < 8; ++q) PIN4(va[q][0], va[q][1], va[q][2], va[q][3]);
    };
    u32x4 fa = {0u, 0u, 0u, 0u};
    const unsigned fao = (unsigned)(WS_FI + x * STR + (lane & 7) * 16);
    constexpr int LAST0 = GI + NPOST > 19 ? GI + NPOST : 19, LAST = LAST0 + NP2;
    const unsigned qsrc = (unsigned)(WS_Q + (wv & 1) * 4 * STR + (x * 4 + (wv >> 1)) * 1024) + (g ^ rot) * 256 + ((nl ^ ((wv * 4) & 15)) << 4);
    auto q_dma = [&](int k) {
        unsigned t;
        asm volatile("s_mov_b32 m0, %3\n\tv_xor_b32 %0, %4, %1\n\tglobal_load_lds_dwordx4 %0, %2 sc1" : "=&v"(t) : "v"(qsrc), "s"(ws), "s"(L_SCR + (wv * 4 + k) * 1024), "i"(k * (STR + 16)) : "memory", "m0");
    };
#pragma unroll
    for (int i = 0; i <= LAST0; ++i) {
        ck_dma(i);
        if (i == 3) {
#pragma unroll
            for (int u = 0; u < 4; ++u) {
                kad[u] = tile_off(wv * 16 + kq(nl >> 2) * 4 + (nl & 3), 4 * u + g);
                vad[u] = tile_off(8 * (lane >> 5) + 4 * (u & 1) + (nl >> 2), wv * 8 + 4 * (u >> 1) + 2 * (g & 1) + ((lane & 3) >> 1)) + 8 * (lane & 1);
                vad2[u] = vad[u] + 65536;
                PIN(kad[u]); PIN(vad[u]); PIN(vad2[u]);
            }
        }
        if (i > GI && i < GI + NPOST) { if (i < GI + 4) VMW(8); } else if (i == GI + NPOST) VMW(NPOST); else if (i < NTH) { if (i >= W0) VMW(W0); } else if (i >= 4) VMW(4);
        if (i == 15 && wv == 0) {
            PIN(fa);
            tag_take();
            if (!__all(flags_ok(fa))) flags_wait(fao);
        }
        if ((i & 3) == 3 && i >= 7 && i <= 19) {
            const int cb = (i - 7) >> 2;
            if (cb > 0) { PIN4(ka[cb * 4 - 4], ka[cb * 4 - 3], ka[cb * 4 - 2], ka[cb * 4 - 1]); va_pin(); }
            wg_barrier();
            ka_reads(cb);
            if ((wv >> 1) == cb) va_reads();
            STAMP(16 + cb);
        }
        if (i == 10 && wv == 0) asm volatile("buffer_load_dwordx4 %0, %1, %2, 0 offen sc1" : "+v"(fa) : "v"(fao), "s"(rw) : "memory");
        if (i == GI && TG > 0) { while (__builtin_amdgcn_s_memrealtime() < t_s + TG) __builtin_amdgcn_s_sleep(1); }
        if (i == GI && WG < 4) VMW(WG);
        if (i == GI) { q_dma(0); q_dma(1); q_dma(2); q_dma(3); asm volatile("s_barrier" ::: "memory"); }
    }
    const unsigned L_EXM = L_SCR - 1024, L_BIG = L_SCR + 512 + 128;
    unsigned nx = LAST + 1, npm = 0u;
    unsigned qad[4];
    {
        unsigned qck = L_SCR + wv * 4096 + lane * 16;
        u32x4 qb[4];
#pragma unroll
        for (int k = 0; k < 4; ++k) qb[k] = *LP(u32x4, qck + k * 1024);
#pragma unroll
        for (int i = LAST0 + 1; i <= LAST; ++i) ck_dma(i);
#pragma unroll
        for (int k = 0; k < 4; ++k) { qad[k] = L_SCR + nl * 1024 + (((4 * k + g) ^ nl) << 4); PIN(qad[k]); }
        STAMP(20);
        if (!__all(mx4(qb[0], mx4(qb[1], mx4(qb[2], mx4(qb[3], 0u)))) != SENT)) {
            for (unsigned spins = 0;;) {
#pragma unroll
                for (int k = 0; k < 4; ++k)
                    if (!__all(mx4(qb[k], 0u) != SENT)) q_dma(k);
                if (nx <= 25) {
#pragma unroll
                    for (int i = 0; i < 3; ++i) ck_dma(nx + i);
                    nx += 3;
                    VMW(3);
                } else {
                    for (; nx < 28; ++nx) ck_dma(nx);
                    VMW(0);
                }
#pragma unroll
                for (int k = 0; k < 4; ++k) qb[k] = *LP(u32x4, qck + k * 1024);
                if (__all(mx4(qb[0], mx4(qb[1], mx4(qb[2], mx4(qb[3], 0u)))) != SENT)) break;
                if (++spins > kSpinCap) break;
            }
        }
    }
    constexpr int NE = LAST + 1 + SL0 + 4 < 31 ? LAST + 1 + SL0 + 4 : 31;
    auto slot = [&](int i) { if (i < 28) { if (nx <= (unsigned)i) ck_dma(i); } else ck_dma(i); };
    const unsigned kg = kq(g);
    unsigned exw = L_EXM + ((nl + (lane >> 5) * 16) * 8 + wv) * 4, exr = L_EXM + nl * 32;
    unsigned pwr = L_SCR + nl * 1024 + 768 + (((2 * wv + (kg >> 1)) ^ nl) << 4) + (kg & 1) * 8, esw = L_SCR + nl * 1024 + 512 + ((wv ^ (nl & 7)) << 4) + g * 4;
    unsigned prd = L_SCR + (lane & 31) * 1024 + 768 + (((lane >> 5) ^ nl) << 4);
    PIN4(exw, exr, pwr, esw); PIN(prd);
    PIN4(ka[12], ka[13], ka[14], ka[15]); va_pin();
    wg_barrier();
    STAMP(21);
    bf16x8 qr[8][2];
    auto q_ld = [&](int ks, int hg) { return *LP(bf16x8, qad[ks & 3] + (ks >> 2) * 256 + hg * 16384); };
    auto p_ld = [&](int kk) { return *LP(bf16x8, prd ^ (kk << 5)); };
    float ls0, ls1, m0, m1;
    f32x4 sa, sb;
    f32x16 o[2];
    unsigned sw[4], srd, p0, mlo, esr, fbo;
    u32x2 tg;
    u32x4 nq;
    auto probs = [&](float (&e)[8]) {
#pragma unroll
        for (int r = 0; r < 4; ++r) { e[r] = __builtin_amdgcn_exp2f(fmaf(sa[r], SCL, -m0)); e[4 + r] = __builtin_amdgcn_exp2f(fmaf(sb[r], SCL, -m1)); }
        *LP(u32x2, pwr) = u32x2{pk2(e[0], e[1]), pk2(e[2], e[3])};
        *LP(u32x2, pwr + 16384) = u32x2{pk2(e[4], e[5]), pk2(e[6], e[7])};
    };
    auto wave_max = [&]() {
        const float a = fmaxf(fmaxf(sa[0], sa[1]), fmaxf(sa[2], sa[3])), b = fmaxf(fmaxf(sb[0], sb[1]), fmaxf(sb[2], sb[3]));
        auto u = __builtin_amdgcn_permlane32_swap(__float_as_uint(a), __float_as_uint(b), false, false);
        const float c = fmax3(__uint_as_float(u[0]), __uint_as_float(u[1]), __uint_as_float(u[1]));
        auto v = __builtin_amdgcn_permlane16_swap(__float_as_uint(c), __float_as_uint(c), false, false);
        return fmax3(__uint_as_float(v[0]), __uint_as_float(v[1]), __uint_as_float(v[1]));
    };
    auto max8 = [&](unsigned a) {
        const f32x4 a0 = *LP(f32x4, a), a1 = *LP(f32x4, a + 16);
        return fmax3(fmax3(a0[0], a0[1], a0[2]), fmax3(a0[3], a1[0], a1[1]), fmax3(a1[2], a1[3], a1[3])) * SCL;
    };
    {
        bf16x8 qs[8][2];
#pragma unroll
        for (int ks = 0; ks < 8; ++ks) { qr[ks][0] = q_ld(ks + 8, 0); qr[ks][1] = q_ld(ks + 8, 1); }
        __builtin_amdgcn_sched_barrier(0);
#pragma unroll
        for (int i = LAST + 1; i < LAST + 1 + SL0; ++i) if (i < NE) slot(i);
        sa = f32x4{0.f, 0.f, 0.f, 0.f}; sb = sa;
        f32x4 s2a = sa, s2b = sa;
        bf16x8 kb[4];
#pragma unroll
        for (int n = 0; n < 16; ++n) {
            const int ks = (n + 8) & 15;
            sa = MFMA16(ka[ks], ks < 8 ? qs[ks][0] : qr[ks - 8][0], sa);
            sb = MFMA16(ka[ks], ks < 8 ? qs[ks][1] : qr[ks - 8][1], sb);
            if (QKM && n == 4) { VMW(LAST + SL0 + 1 - 19); for (int u = 0; u < 4; ++u) kb[u] = *LP(bf16x8, kad[u]); }
            if (QKM && ks < 4) { s2a = MFMA16(kb[ks], qs[ks][0], s2a); s2b = MFMA16(kb[ks], qs[ks][1], s2b); }
            if (n >= 8 - QD && n < 16 - QD) { qs[n + QD - 8][0] = q_ld(n + QD - 8, 0); qs[n + QD - 8][1] = q_ld(n + QD - 8, 1); }
            if ((n & 3) == 3 && LAST + 1 + SL0 + (n >> 2) < NE) slot(LAST + 1 + SL0 + (n >> 2));
            __builtin_amdgcn_sched_barrier(0);
        }
        *LP(float, exw) = wave_max();
        wg_barrier();
        m0 = max8(exr); m1 = max8(exr + 512);
        VMW(NE - 24);
        if (!QKM) ka_reads(0);
        ka_reads(1);
#pragma unroll
        for (int ks = 4 * QKM; ks < 4 * QKM + QE; ++ks) { qs[ks][0] = q_ld(ks, 0); qs[ks][1] = q_ld(ks, 1); }
        __builtin_amdgcn_sched_barrier(0);
        float e[8];
        probs(e);
        ls0 = (e[0] + e[1]) + (e[2] + e[3]); ls1 = (e[4] + e[5]) + (e[6] + e[7]);
        PIN(ls0); PIN(ls1);
        if (wv == 0) *LP(u32x4, L_BIG) = u32x4{0u, 0u, tlo, thi};
        VMW(NE - 28);
        wg_barrier();
        STAMP(22);
        static_assert(NE >= 28, "");
        bf16x8 pf[8];
        tg = *LP(u32x2, L_BIG + 8);
#pragma unroll
        for (int i = NE; i < 32; ++i) slot(i);
        sa = s2a; sb = s2b;
        if (QKM) { pf[0] = p_ld(0); pf[1] = p_ld(1); }
        __builtin_amdgcn_sched_barrier(0);
#pragma unroll
        for (int ks = 4 * QKM; ks < 8; ++ks) {
            sa = MFMA16(ka[ks], qs[ks][0], sa); sb = MFMA16(ka[ks], qs[ks][1], sb);
            if (ks + QE < 8) { qs[ks + QE][0] = q_ld(ks + QE, 0); qs[ks + QE][1] = q_ld(ks + QE, 1); }
            else if (!QKM) pf[ks + QE - 8] = p_ld(ks + QE - 8);
            __builtin_amdgcn_sched_barrier(0);
        }
#pragma unroll
        for (int i = 0; i < 16; ++i) { o[0][i] = 0.f; o[1][i] = 0.f; }
#pragma unroll
        for (int kk = 0; kk < 4; ++kk) {
            if (kk == 0) {
#pragma unroll
                for (int q = QKM ? 2 : QE; q < 8; ++q) pf[q] = p_ld(q);
            }
            if (kk == 1) ka_reads(2);
            o[0] = MFMA32(cat8(va[kk][0], va[kk][1]), pf[kk], o[0]);
            o[1] = MFMA32(cat8(va[kk][2], va[kk][3]), pf[kk], o[1]);
            __builtin_amdgcn_sched_barrier(0);
        }
        PIN4(pf[4], pf[5], pf[6], pf[7]); PIN4(ka[8], ka[9], ka[10], ka[11]); PIN(tg);
#pragma unroll
        for (int kk = 4; kk < 8; ++kk) {
            if (wv < 6) { va_read1(kk - 4); if (kk > 4) va_read1(kk - 1); }
            sa = MFMA16(ka[kk + 4], qr[kk - 4][0], sa); sb = MFMA16(ka[kk + 4], qr[kk - 4][1], sb);
            o[0] = MFMA32(cat8(va[kk][0], va[kk][1]), pf[kk], o[0]);
            o[1] = MFMA32(cat8(va[kk][2], va[kk][3]), pf[kk], o[1]);
            __builtin_amdgcn_sched_barrier(0);
        }
        if (wv < 6) va_read1(7);
        tlo = __builtin_amdgcn_readfirstlane(tg[0]); thi = __builtin_amdgcn_readfirstlane(tg[1]);
        rederive();
        fbo = (unsigned)(WS_FB + x * STR + lane * 16);
        asm volatile("buffer_load_dwordx4 %0, off, %1, %2 sc1" : "=v"(nq) : "s"(rw), "s"(WS_NM + x * STR) : "memory");
        const unsigned head = lane & 31, r8 = lane >> 3, cw = wv ^ (rot * 2);
        srd = lb + r8 * 128 + (((lane & 7) ^ r8) << 4);
        sw[0] = lb + head * 128 + (((lane >> 5) ^ (head & 7)) << 4);
        sw[1] = sw[0] ^ 32u; sw[2] = sw[0] ^ 64u; sw[3] = sw[0] ^ 96u;
        p0 = (unsigned)(WS_PART + x * 1048576) + (cw >> 2) * 16384 + (cw & 3) * 2048 + r8 * 32768 + ((s ^ r8) * 8 + (lane & 7)) * 16;
        PIN4(sw[0], sw[1], sw[2], sw[3]); PIN(srd); PIN(p0); PIN(fbo);
    }
    STAMP(23);
    unsigned po[4];
    float mrun;
    {
        VMW(1);
        STAMP(24);
        ka_reads(3);
        asm volatile("" : "=v"(fa));
        if (wv == 0 && s < 11) asm volatile("buffer_load_dwordx4 %0, %1, %2, 0 offen sc1" : "+v"(fa) : "v"(fbo), "s"(rw) : "memory");
        __builtin_amdgcn_sched_barrier(0);
#pragma unroll
        for (int ks = 12; ks < 16; ++ks) { sa = MFMA16(ka[ks], qr[ks - 8][0], sa); sb = MFMA16(ka[ks], qr[ks - 8][1], sb); }
        bf16x8 pf[8];
#pragma unroll 1
        for (bool first = true;; first = false) {
            float e[8];
            probs(e);
            const float n0 = (e[0] + e[1]) + (e[2] + e[3]), n1 = (e[4] + e[5]) + (e[6] + e[7]);
            *LP(float, esw) = ls0 + n0; *LP(float, esw + 16384) = ls1 + n1;
            if (__builtin_expect(!(n0 + n1 <= BIGV), 0)) *LP(unsigned, L_BIG) = 1u;
            va_pin();
            wg_barrier();
            STAMP(25);
            PIN(prd);
            const unsigned big = *LP(unsigned, L_BIG);
            if ((wv >> 1) == 3) va_reads();
#pragma unroll
            for (int kk = 0; kk < 8; ++kk) { pf[kk] = p_ld(kk); __builtin_amdgcn_sched_barrier(0); }
            if (__builtin_expect(__builtin_amdgcn_readfirstlane(big) == 0 || !first, 1)) break;
            const unsigned ex2 = L_SCR + 512 + 160;
            PIN(sa); PIN(sb);
            rederive();
            *LP(float, ex2 + (nl + (lane >> 5) * 16) * 1024 + wv * 4) = wave_max();
            wg_barrier();
            const float x0 = fmaxf(max8(ex2 + nl * 1024), m0), x1 = fmaxf(max8(ex2 + nl * 1024 + 16384), m1);
            const float al0 = __builtin_amdgcn_exp2f(m0 - x0), al1 = __builtin_amdgcn_exp2f(m1 - x1), al = (lane & 16) ? al1 : al0;
            m0 = x0; m1 = x1; ls0 *= al0; ls1 *= al1;
            o[0] *= al; o[1] *= al;
        }
        mrun = (lane & 16) ? m1 : m0;
        auto stage = [&](int t2, int gp) {
            const unsigned lo0 = pk2(o[t2][8 * gp + 0], o[t2][8 * gp + 1]), lo1 = pk2(o[t2][8 * gp + 2], o[t2][8 * gp + 3]);
            const unsigned hi0 = pk2(o[t2][8 * gp + 4], o[t2][8 * gp + 5]), hi1 = pk2(o[t2][8 * gp + 6], o[t2][8 * gp + 7]);
            auto r0 = __builtin_amdgcn_permlane32_swap(lo0, hi0, false, false);
            auto r1 = __builtin_amdgcn_permlane32_swap(lo1, hi1, false, false);
            *LP(u32x4, sw[t2 * 2 + gp]) = u32x4{r0[0], r1[0], r0[1], r1[1]};
        };
#pragma unroll
        for (int kk = 0; kk < 8; ++kk) {
            o[0] = MFMA32(cat8(va[kk][0], va[kk][1]), pf[kk], o[0]);
            if (kk == 1) {
                asm volatile("s_waitcnt vmcnt(0)" : "+v"(nq) :: "memory");
                npm = __builtin_amdgcn_readfirstlane(nq[0]);
                if ((unsigned)__builtin_amdgcn_readfirstlane(nq[2]) != tlo || (unsigned)__builtin_amdgcn_readfirstlane(nq[3]) != thi) npm = __builtin_amdgcn_readfirstlane(tagged_wait(WS_NM + x * STR)[0]);
            }
            if (kk == 3 || kk == 5) {
                const unsigned v = (npm ^ ((x & 2) ? 0xffu : 0u)) >> (lane >> 5);
#pragma unroll
                for (int i = kk - 3; i < kk - 1; ++i) { po[i] = p0 + ((v >> (2 * i)) & 1u) * 8192u; PIN(po[i]); }
            }
            __builtin_amdgcn_sched_barrier(0);
        }
#pragma unroll
        for (int kk = 0; kk < 8; ++kk) {
            o[1] = MFMA32(cat8(va[kk][2], va[kk][3]), pf[kk], o[1]);
            if (kk == 2) { PIN(o[0]); stage(0, 0); }
            if (kk == 4) stage(0, 1);
            __builtin_amdgcn_sched_barrier(0);
        }
        stage(1, 0); stage(1, 1);
        asm volatile("s_waitcnt lgkmcnt(0)" ::: "memory");
        u32x4 w[4];
#pragma unroll
        for (int i = 0; i < 4; ++i) w[i] = *LP(u32x4, srd + i * 1024);
#pragma unroll
        for (int i = 0; i < 4; ++i) ST16(w[i], po[i], i * 262144);
        __builtin_amdgcn_sched_barrier(0);
        STAMP(26);
        u32x4 mlrec;
        if (wv == 0) {
            const unsigned head = lane & 31;
            esr = L_SCR + head * 1024 + 512 + ((head & 7) << 4);
            mlo = (unsigned)(WS_ML + x * STR + s * 512 + head * 16) + ((npm >> ((head >> 3) * 2)) & 1u) * 8192u;
            PIN(mlo);
            f32x4 b8[8];
#pragma unroll
            for (int q = 0; q < 8; ++q) b8[q] = *LP(f32x4, esr ^ (q << 4));
            const f32x4 bs = ((b8[0] + b8[1]) + (b8[2] + b8[3])) + ((b8[4] + b8[5]) + (b8[6] + b8[7]));
            mlrec = u32x4{__float_as_uint(mrun), __float_as_uint((bs[0] + bs[1]) + (bs[2] + bs[3])), tlo, thi};
            PIN(mlrec);
        }
        VMW(0);
        wg_barrier();
        if (wv == 0) {
            if (lane < 32) ST16(mlrec, mlo, 0);
            PIN(fa);
            if (s < 11 && !__all(flags_ok(fa))) flags_wait(fbo);
        }
        STAMP(27);
        wg_barrier();
    }
    const unsigned long long t_att = __builtin_amdgcn_s_memrealtime();
    rederive();
    while (__builtin_amdgcn_s_memrealtime() < t_att + (s >= 11 ? MD0 : MD1)) __builtin_amdgcn_s_sleep(2);
    if (s >= 11) {
        const unsigned n = (unsigned)((s - 11) * 8 + wv);
        if (n < 28) pf4<NTM & 32>((n * 64 + lane) * 1024u + x * 128u, p.w2, 0);
        { const unsigned l2 = n * 64 + lane; pf4<NTM & 32>((l2 / 5u) * (DFF * 2) + min(((unsigned)x * 448u & ~127u) + (l2 % 5u) * 128u, DFF * 2 - 4u), p.w1, 0); }
        if (wv == 0) flag_put(WS_FA, id - 128);
        STAMP_FLUSH();
        return;
    }
    const unsigned L_H2S = L_SCR, L_REDM = L_SCR + 16384;
    if (s < 7) {
        {
            const char* w1s = (const char*)(p.w1 + (size_t)wv * 64 * DFF + ug * 32);
#pragma unroll
            for (int i = 0; i < 4; ++i) dma16<NTM & 32>((unsigned)((lane >> 2) * DFF * 2 + (lane & 3) * 16), w1s + (size_t)i * 16 * DFF * 2, wv * 4096 + i * 1024);
        }
        const int b = wv;
        if (wv == 0) ST16(s4, (unsigned)(WS_U + x * STR + (lane >> 5) * 8192 + s * 512 + (lane & 31) * 16), 0);
        const u32x4 shf = LD16((unsigned)(WS_MB + b * STR + lane * 16), 0), scl = LD16((unsigned)(WS_MB + b * STR + 1024 + lane * 16), 0);
        VMW(0);
        if (wv == 0) flag_put(WS_FA, id - 128);
        wg_barrier();
        bf16x8 wk0[8], wk1[8];
        if (wv >= 6) {
#pragma unroll
            for (int q = 0; q < 8; ++q) {
                const unsigned a = (unsigned)(q * 4096 + (g * 8 + (nl >> 2)) * 64 + (wv & 1) * 32 + (lane & 3) * 8);
                wk0[q] = tr_frag(a, a + 256); wk1[q] = tr_frag(a + 2048, a + 2304);
            }
#pragma unroll
            for (int q = 0; q < 8; q += 2) PIN4(wk0[q], wk0[q + 1], wk1[q], wk1[q + 1]);
        }
        float scv[8], shv[8];
#pragma unroll
        for (int q = 0; q < 4; ++q) {
            scv[2 * q] = rbf(1.f + lo16(scl[q])); scv[2 * q + 1] = rbf(1.f + hi16(scl[q])); shv[2 * q] = lo16(shf[q]); shv[2 * q + 1] = hi16(shf[q]);
            PIN4(scv[2 * q], scv[2 * q + 1], shv[2 * q], shv[2 * q + 1]);
        }
        unsigned har = L_H2S + ((lane & 7) * HS_LD + g * 8 + (nl >> 3) * 32) * 2, haw = L_H2S + (b * HS_LD + lane * 8) * 2;
        unsigned udst = (unsigned)(WS_U + x * STR + (nl >> 3) * 8192 + (s * 2 + (wv & 1)) * 256 + ((nl & 7) * 16 + g * 4) * 2), xsrc = (unsigned)(WS_X1 + x * STR + (b * 512 + lane * 8) * 2) + ((npm >> x) & 1u) * 8192u;
        PIN4(har, haw, udst, xsrc);
        STAMP(28);
        while (__builtin_amdgcn_s_memrealtime() < t_att + XGATE) __builtin_amdgcn_s_sleep(2);
        u32x4 xv;
        for (unsigned spins = 0;;) {
            xv = LD16(xsrc, 0);
            if (__all(mx4(xv, 0u) != SENT)) break;
            __builtin_amdgcn_s_sleep(SLPX);
            if (++spins > kSpinCap) break;
            asm volatile("" ::: "memory");
        }
        STAMP(29);
        {
            f32x2_t v[4];
#pragma unroll
            for (int q = 0; q < 4; ++q) v[q] = f32x2_t{lo16(xv[q]), hi16(xv[q])};
            const f32x2_t p1 = (v[0] + v[1]) + (v[2] + v[3]), p2 = (v[0] * v[0] + v[1] * v[1]) + (v[2] * v[2] + v[3] * v[3]);
            float mb, rden;
            ln_stats(p1[0] + p1[1], p2[0] + p2[1], mb, rden);
            u32x4 hr;
#pragma unroll
            for (int q = 0; q < 4; ++q) {
                const f32x2_t h = ((v[q] - mb) * rden) * f32x2_t{scv[2 * q], scv[2 * q + 1]} + f32x2_t{shv[2 * q], shv[2 * q + 1]};
                hr[q] = pk2(h[0], h[1]);
            }
            *LP(u32x4, haw) = hr;
        }
        wg_barrier();
        if (wv >= 6) {
            bf16x8 af[8];
#pragma unroll
            for (int q = 0; q < 8; ++q) af[q] = *LP(bf16x8, har + q * 128);
            __builtin_amdgcn_sched_barrier(0);
            f32x4 d0 = {0.f, 0.f, 0.f, 0.f}, d1 = {0.f, 0.f, 0.f, 0.f};
#pragma unroll
            for (int q = 0; q < 8; ++q) { d0 = MFMA16(wk0[q], af[q], d0); d1 = MFMA16(wk1[q], af[q], d1); }
            const f32x4 own = nl < 8 ? d0 : d1;
#pragma unroll
            for (int e = 0; e < 4; ++e) { float v = own[e] + dppf<0x128>(own[e]); asm("" : "+v"(v)); d0[e] = fmaxf(v, 0.f); }
            const u32x2 uv = {pk2s(d0[0], d0[1]), pk2s(d0[2], d0[3])};
            __builtin_amdgcn_raw_buffer_store_b64(uv, rw, udst, 0, 16);
            STAMP(30);
        }
        STAMP_FLUSH();
        return;
    }
    {
        const int n0 = (x * 4 + (s - 7)) * 16;
        const char* mws = (const char*)(p.w2 + (size_t)wv * 224 * 512 + n0);
#pragma unroll
        for (int i = 0; i < 7; ++i) dma16<NTM & 32>((unsigned)((lane >> 1) * 1024 + (lane & 1) * 16), mws + i * 32768, rgn + i * 1024);
        const int fb = lane >> 3, fc = (lane & 7) * 2;
        u32x2 gb = {0u, 0u};
        if (wv == 0) {
            flag_put(WS_FA, id - 128);
            { const u32x4 v = tagged_wait(WS_MOD + fb * STR + (1536 + n0 + fc) * 8); gb = u32x2{v[0], v[1]}; }
            flags_wait(WS_FA + x * STR + lane * 16);
            if (id == 23 * 8 && lane == 0) __builtin_amdgcn_raw_buffer_store_b64(u32x2{tlo, thi}, rw, WS_EPOCH, 0, 16);
            PIN(gb);
        }
        VMW(0);
        bf16x8 wfr[7];
        const unsigned trl = (unsigned)(g * 8 + (nl >> 2)) * 32u + (unsigned)(lane & 3) * 8u;
#pragma unroll
        for (int ks = 0; ks < 7; ++ks) { wfr[ks] = tr_frag(rgn + ks * 1024 + trl, rgn + ks * 1024 + trl + 128); PIN(wfr[ks]); }
        unsigned usrc = (unsigned)(WS_U + wv * STR + (g >> 1) * 256 + (nl & 7) * 32 + (g & 1) * 16) + ((npm >> x) & 1u) * 8192u;
        unsigned rdw = L_REDM + wv * 512 + (nl & 7) * 64 + g * 16, rdr = L_REDM + lane * 8;
        unsigned short* outp = p.out + fb * 512 + n0 + fc;
        PIN(usrc); PIN(rdw); PIN(rdr); PIN(outp);
        STAMP(28);
        unsigned xg = 0u;
        if (wv == 0) {
            while (__builtin_amdgcn_s_memrealtime() < t_att + XGATE) __builtin_amdgcn_s_sleep(2);
            for (unsigned spins = 0;;) {
                xg = __builtin_amdgcn_raw_buffer_load_b32(rw, (unsigned)(WS_X1 + x * STR + (fb * 512 + n0 + fc) * 2) + ((npm >> x) & 1u) * 8192u, 0, 16);
                if (__all(xg != SENT)) break;
                __builtin_amdgcn_s_sleep(SLPD);
                if (++spins > kSpinCap) break;
                asm volatile("" ::: "memory");
            }
            __builtin_amdgcn_s_sleep(SLPG);
        }
        wg_barrier();
        u32x4 uf[7];
        for (unsigned spins = 0;;) {
#pragma unroll
            for (int ks = 0; ks < 7; ++ks) uf[ks] = LD16(usrc + ks * 512, 0);
            unsigned m = 0u;
#pragma unroll
            for (int ks = 0; ks < 7; ++ks) m = mx4(uf[ks], m);
            if (__all(m != SENT)) break;
            if (++spins > kSpinCap) break;
            asm volatile("" ::: "memory");
        }
        STAMP(29);
        f32x4 d = {0.f, 0.f, 0.f, 0.f}, d2 = {0.f, 0.f, 0.f, 0.f};
#pragma unroll
        for (int ks = 0; ks < 7; ++ks) {
            if (ks & 1) d2 = MFMA16(wfr[ks], BF8(uf[ks]), d2);
            else d = MFMA16(wfr[ks], BF8(uf[ks]), d);
        }
        *LP(f32x4, rdw) = d + d2;
        wg_barrier();
        if (wv == 0) {
            f32x2_t c[8];
#pragma unroll
            for (int w2 = 0; w2 < 8; ++w2) c[w2] = *LP(f32x2_t, rdr + w2 * 512);
            const f32x2_t sm = ((c[0] + c[1]) + (c[2] + c[3])) + ((c[4] + c[5]) + (c[6] + c[7]));
            unsigned t = pk2(sm[0], sm[1]);
            t = pk2(__uint_as_float(gb[0]) * lo16(t), __uint_as_float(gb[1]) * hi16(t));
            *(GAS unsigned*)outp = pk2(lo16(xg) + lo16(t), hi16(xg) + hi16(t));
            STAMP(31);
        }
        STAMP_FLUSH();
    }
}

extern "C" void kernel_launch(void* const* d_in, const int* in_sizes, int n_in,
                              void* d_out, int out_size, void* d_ws, size_t ws_size,
                              hipStream_t stream) {
    Params p;
    p.x = (const unsigned short*)d_in[0];
    p.ckv = (const unsigned short*)d_in[1];
    p.w_q = (const unsigned short*)d_in[2];
    p.w_uk = (const unsigned short*)d_in[3];
    p.w_uv = (const unsigned short*)d_in[4];
    p.w_o = (const unsigned short*)d_in[5];
    p.w1 = (const unsigned short*)d_in[7];
    p.w2 = (const unsigned short*)d_in[8];
    p.c = (const unsigned short*)d_in[9];
    p.w_mod = (const unsigned short*)d_in[10];
    p.ws = (char*)(((size_t)d_ws + 2097151) & ~(size_t)2097151);
    p.out = (unsigned short*)d_out;
    (void)hipFuncSetAttribute((const void*)k_main, hipFuncAttributeMaxDynamicSharedMemorySize, 163840);
    k_main<<<256, 512, 163840, stream>>>(p);
}
```

```cpp
#include <hip/hip_runtime.h>
#include <hip/hip_bf16.h>
#include <stdint.h>

#define NH 32
#define SK 4096
#define DFF 1792
#define GI 18
#define ROTC 1
#define PFS 2
#define XNO 0
#define CPJ 7
#define NTM 16
#define QHELP 4
#define HP 13
#define NTH 0
#define W0 2
#define SL0 1
#define LMPF 0
#define SLPB 20
#define MD0 0
#define MD1 0
#define NEARP 1
#define NCAL 2
#define TLBW 2
#define TG 520
#define JPERM 1
#define NPOST 3
#define XS3 1
#define WG 1
#define NP2 2
#define QKM 1
#define HILO 0
#define YSEL 1
#define WMT 0
#define SD 0
#define QDL 0
#define CNL 1
#define TLBA 1
#define CPW 0
#define TWD 570
#define LMD 250
#define PFM 57
#define CPFE 35500
#define SLPM 0
#define SLPD 12
#define SLPG 8
#define SLPX 1
#define SLPH 4

using u16x8 = __attribute__((ext_vector_type(8))) unsigned short;
using s16x4 = __attribute__((ext_vector_type(4))) short;
using bf16x8 = __attribute__((ext_vector_type(8))) short;
using f32x16 = __attribute__((ext_vector_type(16))) float;
using f32x4 = __attribute__((ext_vector_type(4))) float;
using u32x4 = __attribute__((ext_vector_type(4))) unsigned;
using u32x2 = __attribute__((ext_vector_type(2))) unsigned;

#define GAS __attribute__((address_space(1)))
#define LAS __attribute__((address_space(3)))
typedef GAS unsigned long long gu64;
#define RLX_AGENT __ATOMIC_RELAXED, __HIP_MEMORY_SCOPE_AGENT
#define SENT 0xffffffffu
#define TAGC 0x9e3779b9u

__device__ __forceinline__ float bf2f(unsigned short u) { return __uint_as_float(((unsigned)u) << 16); }
typedef __attribute__((ext_vector_type(2))) __bf16 bf16x2_t;
typedef __attribute__((ext_vector_type(2))) float f32x2_t;
__device__ __forceinline__ unsigned pk2(float a, float b) {
    f32x2_t v = {a, b};
    bf16x2_t r = __builtin_convertvector(v, bf16x2_t);
    return __builtin_bit_cast(unsigned, r);
}
__device__ __forceinline__ unsigned nosent(unsigned r) { return min(r, SENT - 1u); }
__device__ __forceinline__ unsigned pk2s(float a, float b) { return nosent(pk2(a, b)); }
__device__ __forceinline__ unsigned short f2bf(float f) { return (unsigned short)(pk2(f, 0.f) & 0xffffu); }
__device__ __forceinline__ float rbf(float f) { return __uint_as_float(pk2(f, 0.f) << 16); }
__device__ __forceinline__ float lo16(unsigned u) { return __uint_as_float(u << 16); }
__device__ __forceinline__ float hi16(unsigned u) { return __uint_as_float(u & 0xffff0000u); }
__device__ __forceinline__ unsigned umax3(unsigned a, unsigned b, unsigned c) { return max(max(a, b), c); }
__device__ __forceinline__ unsigned mx4(const u32x4& v, unsigned m) { return umax3(v[2], v[3], umax3(v[0], v[1], m)); }
__device__ __forceinline__ float fmax3(float a, float b, float c) { float r; asm("v_max3_f32 %0, %1, %2, %3" : "=v"(r) : "v"(a), "v"(b), "v"(c)); return r; }
template <int C> __device__ __forceinline__ float dppf(float v) { return __int_as_float(__builtin_amdgcn_update_dpp(0, __float_as_int(v), C, 0xf, 0xf, true)); }
__device__ __forceinline__ float sum16(float v) { v += dppf<0xB1>(v); v += dppf<0x4E>(v); v += dppf<0x141>(v); v += dppf<0x140>(v); return v; }

__device__ __forceinline__ void ln_stats(float p1, float p2, float& mb, float& rden) {
    auto t = __builtin_amdgcn_permlane32_swap(__float_as_uint(p1), __float_as_uint(p2), false, false);
    float a = sum16(__uint_as_float(t[0]) + __uint_as_float(t[1]));
    auto t2 = __builtin_amdgcn_permlane16_swap(__float_as_uint(a), __float_as_uint(a), false, false);
    a = (__uint_as_float(t2[0]) + __uint_as_float(t2[1])) * (1.f / 512.f);
    auto t3 = __builtin_amdgcn_permlane32_swap(__float_as_uint(a), __float_as_uint(a), false, false);
    const float m = __uint_as_float(t3[0]);
    const float var = rbf(fmaxf(fmaf(-m, m, __uint_as_float(t3[1])), 0.f));
    mb = rbf(m);
    rden = __builtin_amdgcn_rcpf(rbf(__builtin_amdgcn_sqrtf(rbf(var + 1e-5f))));
}
#define STR 262144
#define WS_Q 0
#define WS_H 32768
#define WS_X1 65536
#define WS_ML 81920
#define WS_MOD 98304
#define WS_MB 139264
#define WS_U 114688
#define WS_FI 131072
#define WS_FA 132096
#define WS_FB 133120
#define WS_NP 134144
#define WS_NM 134400
#define WS_EPOCH 135168
#define WS_CAL 147456
#define WS_OUTP 2097152
#define WS_PART 4194304
#define WS_END 12582912

#define LP(T, off) ((LAS T*)(size_t)(unsigned)(off))
#define STAMP(k) do {} while (0)
#define STAMP_FLUSH() do {} while (0)
#define STAMP0() do {} while (0)
__device__ __forceinline__ s16x4 tr4(unsigned a) { return __builtin_amdgcn_ds_read_tr16_b64_v4i16(LP(s16x4, a)); }
__device__ __forceinline__ bf16x8 cat8(s16x4 a, s16x4 b) { return __builtin_shufflevector(a, b, 0, 1, 2, 3, 4, 5, 6, 7); }
__device__ __forceinline__ bf16x8 tr_frag(unsigned a0, unsigned a1) { return cat8(tr4(a0), tr4(a1)); }
__device__ __forceinline__ unsigned swz(unsigned row) { return ((row & 3) << 2) | ((row >> 2) & 3); }
__device__ __forceinline__ unsigned off_b(unsigned row, unsigned ch) { return 256u * row + 16u * (ch ^ swz(row)); }
__device__ __forceinline__ unsigned tile_off(unsigned key, unsigned chunk) { return ((key >> 5) * 4 + (chunk >> 4)) * 8192u + off_b(key & 31, chunk & 15); }
__device__ __forceinline__ void wg_barrier() { asm volatile("s_waitcnt lgkmcnt(0)\n\ts_barrier" ::: "memory"); }
template <int NT> __device__ __forceinline__ void dma16(unsigned voff, const void* sbase, unsigned lds_dst) {
    if (NT) asm volatile("s_mov_b32 m0, %2\n\ts_nop 0\n\tglobal_load_lds_dwordx4 %0, %1 nt" :: "v"(voff), "s"(sbase), "s"(lds_dst) : "memory", "m0");
    else asm volatile("s_mov_b32 m0, %2\n\ts_nop 0\n\tglobal_load_lds_dwordx4 %0, %1" :: "v"(voff), "s"(sbase), "s"(lds_dst) : "memory", "m0");
}
template <int NT> __device__ __forceinline__ void dma16x(unsigned voff, unsigned vx, const void* sbase, unsigned lds_dst) {
    unsigned t;
#define DMAX(pol, c) asm volatile("s_mov_b32 m0, %3\n\tv_xor_b32 %0, %4, %1\n\tglobal_load_lds_dwordx4 %0, %2" pol : "=&v"(t) : "v"(voff), "s"(sbase), "s"(lds_dst), c(vx) : "memory", "m0")
    if (__builtin_constant_p(vx)) { if (NT) DMAX(" nt", "i"); else DMAX("", "i"); }
    else { if (NT) DMAX(" nt", "s"); else DMAX("", "s"); }
}
template <int NT> __device__ __forceinline__ void pf4(unsigned voff, const void* sbase, unsigned lds_dst) {
    if (NT) asm volatile("s_mov_b32 m0, %2\n\ts_nop 0\n\tglobal_load_lds_dword %0, %1 nt" :: "v"(voff), "s"(sbase), "s"(lds_dst) : "memory", "m0");
    else asm volatile("s_mov_b32 m0, %2\n\ts_nop 0\n\tglobal_load_lds_dword %0, %1" :: "v"(voff), "s"(sbase), "s"(lds_dst) : "memory", "m0");
}
__device__ __forceinline__ void pf4s(unsigned voff, const void* sbase, unsigned lds_dst) {
    asm volatile("s_mov_b32 m0, %2\n\ts_nop 0\n\tglobal_load_lds_dword %0, %1 sc1" :: "v"(voff), "s"(sbase), "s"(lds_dst) : "memory", "m0");
}
#define XGATE 300
#define SPINCAP (1u << 20)
constexpr unsigned kSpinCap = SPINCAP;
struct Params {
    const unsigned short* x;
    const unsigned short* ckv;
    const unsigned short* w_q;
    const unsigned short* w_uk;
    const unsigned short* w_uv;
    const unsigned short* w_o;
    const unsigned short* w1;
    const unsigned short* w2;
    const unsigned short* c;
    const unsigned short* w_mod;
    char* ws;
    unsigned short* out;
};
#define VMW(n) asm volatile("s_waitcnt vmcnt(%0)" :: "n"(n) : "memory")
#define PIN(x) asm volatile("" : "+v"(x))
#define PIN4(a, b, c, d) asm volatile("" : "+v"(a), "+v"(b), "+v"(c), "+v"(d) :: "memory")
#define MFMA16(a, b, c) __builtin_amdgcn_mfma_f32_16x16x32_bf16(a, b, c, 0, 0, 0)
#define MFMA32(a, b, c) __builtin_amdgcn_mfma_f32_32x32x16_bf16(a, b, c, 0, 0, 0)
#define BF8(x) __builtin_bit_cast(bf16x8, x)
#define ST16(v, off, soff) __builtin_amdgcn_raw_buffer_store_b128(v, rw, off, soff, 16)
#define LD16(off, soff) __builtin_amdgcn_raw_buffer_load_b128(rw, off, soff, 16)
#define SCL (0.08838834764831845f * 1.4426950408889634f)
#define BIGV 1.0e18f
#define QD 4
#define QE 4
#define L_SCR 131072
#define HS_LD 520

__global__ __launch_bounds__(512, 2) void k_main(Params p) {
    unsigned long long pc;
    asm volatile("s_getpc_b64 %0" : "=s"(pc));
    const int id = blockIdx.x, tid = threadIdx.x;
    int lane = tid & 63;
    const int wv = __builtin_amdgcn_readfirstlane(tid >> 6);
    int g = lane >> 4, nl = lane & 15;
    auto rederive = [&]() {
        asm volatile("v_mbcnt_lo_u32_b32 %0, -1, 0\n\tv_mbcnt_hi_u32_b32 %0, -1, %0" : "=v"(lane));
        g = lane >> 4; nl = lane & 15;
    };
    const int x = id & 7, j0 = id >> 3, j = JPERM && j0 < 16 ? 15 - j0 : j0;
    if (wv >= CPW && wv < CPW + 5 && j == 15) {
        asm volatile("s_mov_b32 m0, %2\n\ts_nop 0\n\tglobal_load_lds_dword %0, %1" :: "v"(min((unsigned)(((wv - CPW) * 64 + lane) * 128), (unsigned)CPFE)), "s"(pc), "s"(L_SCR + 31744) : "memory", "m0");
    }
    char* ws = p.ws;
    if (TLBA == 2) {
        if (j >= 14 && j < 16 && wv < 3 && lane < 2) {
            const unsigned short* b = j == 14 ? (wv == 0 ? (const unsigned short*)(ws + WS_CAL) : wv == 1 ? p.w_q : p.w_mod) : (wv == 0 ? p.ckv + (size_t)x * 2097152 : wv == 1 ? p.x : p.w_uk);
            pf4s((unsigned)((j == 15 && wv == 1) || (j == 14 && wv == 0) ? 0 : lane * 2097152), b, L_SCR + 31744);
        }
    }
    if ((j >> 2) == 3) {
        dma16<0>((unsigned)(((nl & 7) * 512 + (nl >> 3) * 32 + g * 8) * 2 + wv * 128), p.c, wv * 16384 + 8192);
        dma16<0>((unsigned)tid * 16u, p.x, wv * 16384 + 9216);
    }
    if (TLBA < 2 && TLBW && j == 15 && wv == 5 && lane < 2) pf4s((unsigned)((x * 2 + lane) * 2097152), p.ckv, L_SCR + 31744);
    if (TLBA == 1 && j == 14 && wv >= 4 && lane < 2) pf4s((unsigned)(lane * 2097152 + x * 128), wv == 4 ? p.w_mod : wv == 5 ? p.w_uk : wv == 6 ? p.w_q : (const unsigned short*)(ws + WS_CAL), L_SCR + 31744);
    STAMP0();
    const __amdgpu_buffer_rsrc_t rw = __builtin_amdgcn_make_buffer_rsrc(ws, 0, WS_END, 0x00020000);
    const u32x4 s4 = {SENT, SENT, SENT, SENT};
    unsigned long long cnt = 0ull;
    auto cnt_load = [&]() { asm volatile("global_load_dwordx2 %0, %1, %2 sc1" : "+v"(cnt) : "v"((unsigned)WS_EPOCH), "s"(ws) : "memory"); };
    unsigned tlo = 0u, thi = 0u;
    auto tag_take = [&]() {
        asm volatile("" : "+v"(cnt) :: "memory");
        const unsigned long long t = cnt + 1ull;
        tlo = __builtin_amdgcn_readfirstlane((unsigned)t); thi = __builtin_amdgcn_readfirstlane((unsigned)(t >> 32));
    };
    auto flag_put = [&](unsigned base, unsigned idx) {
        if (lane < 8) __builtin_amdgcn_raw_buffer_store_b64(u32x2{tlo, thi}, rw, base + lane * STR + idx * 8, 0, 16);
    };
    auto tagged_wait = [&](unsigned off) {
        u32x4 v;
#pragma unroll 1
        for (unsigned spins = 0; spins < kSpinCap; ++spins) {
            asm volatile("buffer_load_dwordx4 %0, %1, %2, 0 offen sc1\n\ts_waitcnt vmcnt(0)" : "=v"(v) : "v"(off), "s"(rw) : "memory");
            if (__all((v[2] == tlo) & (v[3] == thi))) break;
            __builtin_amdgcn_s_sleep(8);
        }
        return v;
    };
    auto flags_ok = [&](const u32x4& f) { return (f[0] == tlo) & (f[1] == thi) & (f[2] == tlo) & (f[3] == thi); };
    auto flags_wait = [&](unsigned off) {
        for (unsigned spins = 0;;) {
            u32x4 f;
            asm volatile("buffer_load_dwordx4 %0, %1, %2, 0 offen sc1\n\ts_waitcnt vmcnt(0)" : "=v"(f) : "v"(off), "s"(rw) : "memory");
            if (__all(flags_ok(f))) break;
            __builtin_amdgcn_s_sleep(16);
            if (++spins > kSpinCap) break;
            asm volatile("" ::: "memory");
        }
    };
    unsigned pcl, frl, frs;
    auto dma_piece = [&](const unsigned short* src, unsigned ldb, unsigned dst, int n, int nt) {
        const unsigned v0 = (lane >> 3) * ldb + pcl, v1 = v0 ^ 64u;
#pragma unroll
        for (int i = 0; i < n; ++i) {
            if (nt) dma16<1>((i & 1) ? v1 : v0, (const char*)src + (size_t)i * 8 * ldb, dst + i * 1024);
            else dma16<0>((i & 1) ? v1 : v0, (const char*)src + (size_t)i * 8 * ldb, dst + i * 1024);
        }
    };
    auto piece_frag = [&](unsigned base, int ks, int t) {
        const unsigned a = base + frl + ((t ^ frs) << 5) + ks * 4096;
        return tr_frag(a, a + 512);
    };
    const unsigned rgn = (unsigned)(wv * 16384);

    if (j < 16) {
        const unsigned long long t_start = __builtin_amdgcn_s_memrealtime();
        rederive();
        pcl = (unsigned)(((((lane & 7) >> 1) ^ ((lane >> 4) & 1)) * 32) + (lane & 1) * 16);
        frl = (unsigned)((g * 8 + (nl >> 2)) * 128 + (lane & 3) * 8); frs = (unsigned)((nl >> 3) | ((g & 1) << 1));
        const int th = x * 4 + (j & 3);
        if (j < 12) {
            const int qt = j >> 2, rot = qt * 5;
            const bool pub = wv < (qt < 2 ? 3 : 2);
            const unsigned qo = (unsigned)(WS_Q + (th & 7) * STR + ((nl & 7) * 4 + (th >> 3)) * 1024 + (qt * 192 + wv * 64 + g * 16 + (nl >> 3) * 8) * 2);
            const unsigned vb = (unsigned)(g * 8192 + ((nl ^ (g << 2)) << 4));
            const char* wuk = (const char*)(p.w_uk + th * 128) + (size_t)qt * 192 * 8192;
            const char* wq = (const char*)(p.w_q + th * 128) + (size_t)wv * 32768;
            const unsigned L_U = 114688;
            if (QDL) __builtin_amdgcn_s_sleep(QDL);
            {
                const int k0 = qt < 2 ? wv * 6 : wv * 4;
#pragma unroll
                for (int i = 0; i < 6; ++i) {
                    if (i < 4 || qt < 2) dma16<NTM & 2>(vb ^ (((k0 + i) & 3) << 4), wuk + (size_t)(k0 + i) * 32768, L_U + (k0 + i) * 1024);
                    if (i == 4) VMW(4);
                }
            }
            const unsigned vq = vb ^ ((wv & 3) << 4);
            auto wq_dma = [&](int ps) { dma16<NTM & 2>(vq, wq + (size_t)((ps + rot) & 15) * 262144, ps * 8192 + wv * 1024); };
            bf16x8 uk[4][4], aq[16];
            u32x4 fi, hv[4];
            unsigned fio = (unsigned)(WS_FI + x * STR + (lane & 7) * 16), hsrc, hsrc2, ta0, ta1, tb0, tb1, qo2, hw0, hw1, hr, hr2, qr0, qr1, qw, thx = 0u;
            const unsigned L_HI = L_SCR, L_QI = L_SCR + 8192;
            auto h_load = [&]() {
                asm volatile("buffer_load_dwordx4 %0, %4, %6, 0 offen sc1\n\tbuffer_load_dwordx4 %1, %4, %6, 0 offen offset:1024 sc1\n\t"
                             "buffer_load_dwordx4 %2, %5, %6, 0 offen sc1\n\tbuffer_load_dwordx4 %3, %5, %6, 0 offen offset:1024 sc1"
                             : "+v"(hv[0]), "+v"(hv[1]), "+v"(hv[2]), "+v"(hv[3]) : "v"(hsrc), "v"(hsrc2), "s"(rw) : "memory");
            };
            auto h_bad = [&]() {
                PIN4(hv[0], hv[1], hv[2], hv[3]);
                unsigned bad = (hv[0][1] ^ tlo) | (hv[0][3] ^ thx) | (hv[1][1] ^ tlo) | (hv[1][3] ^ thx) | (hv[2][1] ^ tlo) | (hv[2][3] ^ thx) | (hv[3][1] ^ tlo) | (hv[3][3] ^ thx);
                PIN(bad);
                return bad;
            };
#pragma unroll
            for (int ps = 0; ps < 16; ++ps) {
                wq_dma(ps);
                if (ps == 4) cnt_load();
                if (ps == HP) { h_load(); asm volatile("buffer_load_dwordx4 %0, %1, %2, 0 offen sc1" : "=v"(fi) : "v"(fio), "s"(rw) : "memory"); }
                if (ps >= 4 && ps < 8) VMW(5); else if (ps >= HP && ps < HP + 4) VMW(9); else VMW(4);
                if (ps == 3) {
                    asm volatile("s_barrier" ::: "memory");
                    if (pub) {
#pragma unroll
                        for (int t = 0; t < 4; ++t)
#pragma unroll
                            for (int s = 0; s < 4; ++s) uk[t][s] = *LP(bf16x8, L_U + off_b(wv * 64 + (nl >> 2) * 16 + t * 4 + (nl & 3), s * 4 + g));
                    }
                    STAMP(1);
                }
                if (ps == 5) {
                    hsrc = (unsigned)(WS_H + x * STR + wv * 2048 + lane * 16); hsrc2 = hsrc + 16384;
                    ta0 = off_b(8 * g + (nl >> 2), 2 * wv + ((lane & 3) >> 1)) + 8 * (lane & 1); ta1 = off_b(8 * g + 4 + (nl >> 2), 2 * wv + ((lane & 3) >> 1)) + 8 * (lane & 1);
                    tb0 = ta0 + 65536; tb1 = ta1 + 65536; qo2 = qo;
                    hw0 = L_HI + ((2 * wv - rot) & 15) * 512 + (lane >> 4) * 128 + (lane & 7) * 16 + ((lane >> 3) & 1) * 8;
                    hw1 = L_HI + ((2 * wv + 1 - rot) & 15) * 512 + (lane >> 4) * 128 + (lane & 7) * 16 + ((lane >> 3) & 1) * 8;
                    hr2 = L_HI + g * 128 + (nl & 7) * 16 + (nl >> 3) * 512;
                    PIN(hr2);
                    qr0 = L_QI + g * 128 + (nl & 7) * 16 + (nl >> 3) * 2048; qr1 = qr0 ^ 2048u;
                    PIN(qr0); PIN(qr1);
                    hr = L_HI + g * 128 + (nl & 7) * 16; qw = L_QI + (wv >> 1) * 512 + ((wv & 1) * 2 + (g >> 1)) * 128 + (nl & 7) * 16 + (g & 1) * 8;
                    PIN4(hsrc, hsrc2, ta0, ta1); PIN4(tb0, tb1, qo2, hw0); PIN(hw1); PIN(qw);
                }
                if (ps == 8) { tag_take(); thx = tlo ^ thi ^ TAGC; }
                if (ps == 9) {
#pragma unroll
                    for (int t = 0; t < 4; ++t) PIN4(uk[t][0], uk[t][1], uk[t][2], uk[t][3]);
                    wg_barrier();
                    *LP(unsigned, L_QI + 2048 + wv * 256 + lane * 4) = 0u;
#pragma unroll
                    for (int q = 0; q < 6; ++q) aq[q] = tr_frag(ta0 + q * 8192, ta1 + q * 8192);
                }
                if (ps == 13) {
                    asm volatile("s_barrier" ::: "memory");
#pragma unroll
                    for (int q = 6; q < 10; ++q) aq[q] = q < 8 ? tr_frag(ta0 + q * 8192, ta1 + q * 8192) : tr_frag(tb0 + (q - 8) * 8192, tb1 + (q - 8) * 8192);
                }
            }
            STAMP(15);
            if (HP + 4 > 15) VMW(HP + 4 > 15 ? 15 - HP : 0);
            if (!__all(h_bad() == 0u)) {
                for (unsigned spins = 0;;) {
                    h_load();
                    VMW(0);
                    if (__all(h_bad() == 0u)) break;
                    __builtin_amdgcn_s_sleep(SLPH);
                    if (++spins > kSpinCap) break;
                }
            }
            {
                unsigned hh[4];
#pragma unroll
                for (int q = 0; q < 4; ++q) {
                    const unsigned t = hv[2 + (q >> 1)][(q & 1) * 2], sh = hv[q >> 1][(q & 1) * 2];
                    hh[q] = pk2(lo16(t) + lo16(sh), hi16(t) + hi16(sh));
                }
                *LP(u32x2, hw0) = u32x2{hh[0], hh[1]};
                *LP(u32x2, hw1) = u32x2{hh[2], hh[3]};
            }
            STAMP(3);
            asm volatile("s_waitcnt vmcnt(0) lgkmcnt(0)\n\ts_barrier" : "+v"(fi) :: "memory");
            STAMP(2);
            {
                bf16x8 hb[8];
#pragma unroll
                for (int q = 0; q < 5; ++q) hb[q] = *LP(bf16x8, hr2 + q * 1024);
#pragma unroll
                for (int ps = 10; ps < 16; ++ps) {
                    if (!(ps & 1)) hb[ps >> 1] = *LP(bf16x8, hr2 + (ps >> 1) * 1024);
                    aq[ps] = tr_frag(tb0 + (ps - 8) * 8192, tb1 + (ps - 8) * 8192);
                }
                __builtin_amdgcn_sched_barrier(0);
                f32x4 d0 = {0.f, 0.f, 0.f, 0.f}, d1 = {0.f, 0.f, 0.f, 0.f};
#pragma unroll
                for (int ps = 0; ps < 16; ps += 2) {
                    d0 = MFMA16(aq[ps], hb[ps >> 1], d0); d1 = MFMA16(aq[ps + 1], hb[ps >> 1], d1);
                    if (ps == 6) { unsigned bad = (fi[0] ^ tlo) | (fi[1] ^ thi) | (fi[2] ^ tlo) | (fi[3] ^ thi); PIN(bad); if (!__all(bad == 0u)) flags_wait(fio); }
                }
#pragma unroll
                for (int e = 0; e < 4; ++e) { float v = d0[e] + dppf<0x128>(d1[e]); asm("" : "+v"(v)); d0[e] = v; }
                if (nl < 8) *LP(u32x2, qw) = u32x2{pk2(d0[0], d0[1]), pk2(d0[2], d0[3])};
            }
            wg_barrier();
            if (pub) {
                bf16x8 qb[2][4];
#pragma unroll
                for (int s = 0; s < 4; ++s) { qb[0][s] = *LP(bf16x8, qr0 + s * 512); qb[1][s] = *LP(bf16x8, qr1 + s * 512); }
                __builtin_amdgcn_sched_barrier(0);
                f32x4 a = {0.f, 0.f, 0.f, 0.f}, b = a;
#pragma unroll
                for (int s = 0; s < 4; ++s) {
                    a = MFMA16(uk[0][s], qb[0][s], a); b = MFMA16(uk[1][s], qb[0][s], b);
                    a = MFMA16(uk[2][s], qb[1][s], a); b = MFMA16(uk[3][s], qb[1][s], b);
                }
                const u32x4 qv = {pk2s(a[0], a[1]), pk2s(a[2], a[3]), pk2s(b[0], b[1]), pk2s(b[2], b[3])};
                ST16(qv, qo2, 0);
            }
            STAMP(4);
            wg_barrier();
            if (LMPF && wv >= 3 && wv < 6) {
                const unsigned n = (unsigned)(j * 3 + wv - 3);
                if (n < 32) pf4<0>(((n & 7u) * 64 + lane) * 6144u + 2048u + (x * 4 + (n >> 3)) * 128u, p.w_mod, L_SCR + 31744);
            }
            if (QHELP && wv < 3 && j + 12 * wv < 32) {
                const int hh = j + 12 * wv;
#pragma unroll 1
                for (unsigned spins = 0; spins < 64; ++spins) {
                    __builtin_amdgcn_s_sleep(QHELP);
                    const u32x4 v = LD16((unsigned)(WS_Q + (hh & 7) * STR + (x * 4 + (hh >> 3)) * 1024 + lane * 16), 0);
                    if (__all(mx4(v, 0u) != SENT)) break;
                    asm volatile("" ::: "memory");
                }
                STAMP(6);
            }
        } else {
            const int cg = x * 2 + (j & 1);
            if (j < 14) {
                dma_piece(p.w_mod + (size_t)wv * 64 * 3072 + cg * 64, 6144, rgn, 8, NTM & 2);
                cnt_load();
                VMW(9);
            } else {
                cnt_load();
                ST16(s4, (unsigned)(WS_Q + wv * STR + cg * 2048 + lane * 16), 0); ST16(s4, (unsigned)(WS_Q + wv * STR + cg * 2048 + lane * 16), 1024);
                VMW(0);
                tag_take();
                wg_barrier();
                if (wv == 7) flag_put(WS_FI, cg);
                STAMP(15);
                if (j == 14) {
                    u32x2 best = {~0u, ~0u};
#pragma unroll
                    for (int r = 0; r < 2 * NCAL; ++r) {
                        const unsigned hf = (r ^ wv) & 1;
                        VMW(0);
                        const unsigned long long t0 = __builtin_amdgcn_s_memtime();
                        if (lane < 8) ST16(s4, WS_CAL + x * STR + hf * 8192 + (wv * 8 + r) * 128 + lane * 16, 0);
                        VMW(0);
                        const unsigned dt = (unsigned)(__builtin_amdgcn_s_memtime() - t0);
                        if (hf) best[1] = min(best[1], dt); else best[0] = min(best[0], dt);
                    }
                    *LP(u32x2, 16384 * 7 + 14336 + wv * 8) = best;
                    wg_barrier();
                    if (wv == 0 && lane < 8) {
                        u32x2 m = *LP(u32x2, 16384 * 7 + 14336 + lane * 8);
#pragma unroll
                        for (int o = 1; o < 8; o <<= 1) { m[0] = min(m[0], (unsigned)__shfl_xor(m[0], o)); m[1] = min(m[1], (unsigned)__shfl_xor(m[1], o)); }
                        ST16((u32x4{(unsigned)max(-1000, min(1000, (int)m[1] - (int)m[0])), 0u, tlo, thi}), (unsigned)(WS_NP + lane * STR + x * 16), 0);
                    }
                    if (wv == 0) {
                        const int d = (int)tagged_wait(WS_NP + x * STR + (lane & 7) * 16)[0];
                        int t = (lane & 4) ? -d : d;
                        t += __builtin_amdgcn_update_dpp(0, t, 0xB1, 0xf, 0xf, true);
                        t += __builtin_amdgcn_update_dpp(0, t, 0x4E, 0xf, 0xf, true);
                        t += __builtin_amdgcn_update_dpp(0, t, 0x141, 0xf, 0xf, true);
                        const bool n1 = abs(d) > 160 ? d < 0 : (t < 0) != ((lane & 4) != 0);
                        const unsigned mk = NEARP ? (unsigned)__ballot(n1) & 0xffu : 0u;
                        if (lane == 0) ST16((u32x4{mk, 0u, tlo, thi}), (unsigned)(WS_NM + x * STR), 0);
                    }
                    STAMP(9);
                }
            }
            const u16x8 cf = *LP(u16x8, rgn + 8192 + lane * 16), xa = *LP(u16x8, rgn + 9216 + lane * 16);
            u32x4 af[2];
#pragma unroll
            for (int q = 0; q < 4; ++q) {
                const float a = bf2f(cf[2 * q]), b = bf2f(cf[2 * q + 1]);
                af[0][q] = pk2(a * __builtin_amdgcn_rcpf(1.f + __expf(-a)), b * __builtin_amdgcn_rcpf(1.f + __expf(-b)));
                af[1][q] = (unsigned)__builtin_amdgcn_update_dpp(0, (int)af[0][q], 0x128, 0xf, 0xf, true);
            }
            const unsigned L_MS = L_SCR;
            unsigned wa[4], fr[4];
#pragma unroll
            for (int t = 0; t < 4; ++t) { wa[t] = L_MS + wv * 2048 + (nl & 7) * 256 + (((t * 4 + g) ^ (nl & 7)) << 4) + (nl >> 3) * 16384; fr[t] = rgn + frl + ((t ^ frs) << 5); }
            PIN4(wa[0], wa[1], wa[2], wa[3]); PIN4(fr[0], fr[1], fr[2], fr[3]); PIN(af[0]); PIN(af[1]);
            const unsigned L_XN = 12288;
            auto mod_mm = [&](bool ln) {
                VMW(0);
                bf16x8 f[2][4];
#pragma unroll
                for (int ks = 0; ks < 2; ++ks)
#pragma unroll
                    for (int t = 0; t < 4; ++t) f[ks][t] = tr_frag(fr[t] + ks * 4096, fr[t] + ks * 4096 + 512);
                __builtin_amdgcn_sched_barrier(0);
                f32x4 d[4];
#pragma unroll
                for (int t = 0; t < 4; ++t) d[t] = MFMA16(f[0][t], BF8(af[0]), (f32x4{0.f, 0.f, 0.f, 0.f}));
#pragma unroll
                for (int t = 0; t < 4; ++t) d[t] = MFMA16(f[1][t], BF8(af[1]), d[t]);
#pragma unroll
                for (int t = 0; t < 4; ++t) *LP(f32x4, wa[t]) = d[t];
                if (ln) {
                    float v[8];
#pragma unroll
                    for (int q = 0; q < 8; ++q) v[q] = bf2f(xa[q]);
                    const float p1 = ((v[0] + v[1]) + (v[2] + v[3])) + ((v[4] + v[5]) + (v[6] + v[7]));
                    const float p2 = ((v[0] * v[0] + v[1] * v[1]) + (v[2] * v[2] + v[3] * v[3])) + ((v[4] * v[4] + v[5] * v[5]) + (v[6] * v[6] + v[7] * v[7]));
                    float mb, rden;
                    ln_stats(p1, p2, mb, rden);
                    u32x4 r;
#pragma unroll
                    for (int q = 0; q < 4; ++q) { const unsigned d = pk2(v[2 * q] - mb, v[2 * q + 1] - mb); r[q] = pk2(lo16(d) * rden, hi16(d) * rden); }
                    *LP(u32x4, L_XN + wv * 16384 + lane * 16) = r;
                }
                wg_barrier();
            };
            auto sum8 = [&](unsigned a) {
                f32x4 u[8];
#pragma unroll
                for (int w2 = 0; w2 < 8; ++w2) u[w2] = *LP(f32x4, a + w2 * 2048);
                __builtin_amdgcn_sched_barrier(0);
                return ((u[0] + u[1]) + (u[2] + u[3])) + ((u[4] + u[5]) + (u[6] + u[7]));
            };
            if (j < 14) {
                const int hb = lane & 7, c4 = 2 * wv + (nl >> 3);
                unsigned hoff = (unsigned)(WS_H + g * STR + cg * 2048 + wv * 256 + nl * 16);
                unsigned sa = L_MS + hb * 256 + ((c4 ^ hb) << 4);
                unsigned xa2 = L_XN + hb * 16384 + (cg & 7) * 128 + c4 * 8;
                PIN(hoff); PIN(sa); PIN(xa2);
                STAMP(1);
                mod_mm(cg >= 8);
                STAMP(2);
                const u32x2 xn = *LP(u32x2, xa2);
                const f32x4 v = sum8(sa);
                tag_take();
                const unsigned thx = tlo ^ thi ^ TAGC;
                u32x2 r = {pk2(v[0], v[1]), pk2(v[2], v[3])};
                if (cg >= 8) {
#pragma unroll
                    for (int q = 0; q < 2; ++q) { const unsigned sc = pk2(1.f + lo16(r[q]), 1.f + hi16(r[q])); r[q] = pk2(lo16(xn[q]) * lo16(sc), hi16(xn[q]) * hi16(sc)); }
                }
                ST16((u32x4{r[0], tlo, r[1], thx}), hoff, 0);
                ST16((u32x4{r[0], tlo, r[1], thx}), hoff, 4 * STR);
                STAMP(5);
            }
            wg_barrier();
            while (__builtin_amdgcn_s_memrealtime() < t_start + LMD) __builtin_amdgcn_s_sleep(2);
            const int lg = x * 4 + (j & 3);
            dma_piece(p.w_mod + (size_t)wv * 64 * 3072 + 1024 + lg * 64, 6144, rgn, 8, NTM & 4);
            mod_mm(false);
            if (lg >= 8 && lg < 24) {
                if (wv == 0) {
                    const int b = lane >> 3, c8 = lane & 7;
                    const f32x4 v0 = sum8(L_MS + b * 256 + (((2 * c8) ^ b) << 4)), v1 = sum8(L_MS + b * 256 + (((2 * c8 + 1) ^ b) << 4));
                    ST16((u32x4{pk2(v0[0], v0[1]), pk2(v0[2], v0[3]), pk2(v1[0], v1[1]), pk2(v1[2], v1[3])}), (unsigned)(WS_MB + b * STR + (lg - 8) * 128 + c8 * 16), 0);
                }
            } else if (wv < 4) {
                const int b = wv * 2 + (lane >> 5), cp = lane & 31;
                f32x2_t u[8];
#pragma unroll
                for (int w2 = 0; w2 < 8; ++w2) u[w2] = *LP(f32x2_t, L_MS + w2 * 2048 + b * 256 + (((cp >> 1) ^ b) << 4) + (cp & 1) * 8);
                const f32x2_t v = ((u[0] + u[1]) + (u[2] + u[3])) + ((u[4] + u[5]) + (u[6] + u[7]));
                ST16((u32x4{__float_as_uint(rbf(v[0])), __float_as_uint(rbf(v[1])), tlo, thi}), (unsigned)(WS_MOD + b * STR + (lg * 32 + cp) * 16), 0);
            }
            STAMP(6);
        }
        const int bq = (j >> 2) & 1, dh = j >> 3, src = th * 2 + dh, rb = bq * 4 + (src >> 4), rc = src & 15;
        wg_barrier();
        if (TLBW > 1 && j == 0) {
            if (wv == 4 && lane < 5) pf4s((unsigned)((lane + 1) * 2097152 + 8064), ws, L_SCR + 31744);
            if (wv == 5 && lane < 2) { pf4s((unsigned)(lane * 2097152), p.w_uv, L_SCR + 31744); pf4s((unsigned)(lane * 2097152), p.w_o, L_SCR + 31744); }
            if (wv == 6 && lane < 1) { pf4s(0u, p.w1, L_SCR + 31744); pf4s(0u, p.w2, L_SCR + 31744); pf4s(0u, p.out, L_SCR + 31744); }
        }
        while (__builtin_amdgcn_s_memrealtime() < t_start + TWD - 110) __builtin_amdgcn_s_sleep(2);
        const unsigned npm = __builtin_amdgcn_readfirstlane(tagged_wait(WS_NM + x * STR)[0]), np = (npm >> x) & 1u;
        unsigned oo = (unsigned)(WS_OUTP + ((bq * 4 + (nl & 3)) * 16 + wv * 2 + (nl >> 3)) * 16384 + (src ^ wv) * 128 + ((nl >> 2) & 1) * 64 + g * 16) + ((~npm >> ((nl & 3) * 2 + (wv >> 2))) & 1u) * 8192u;
        const int rcol = rc * 32 + (lane & 7) * 4 + (lane >> 5) * 2;
        unsigned xdst = (unsigned)(WS_X1 + ((lane >> 3) & 3) * STR + (rb * 512 + rcol) * 2), xdst2 = xdst + 4 * STR + ((npm >> (4 + ((lane >> 3) & 3))) & 1u) * 8192u;
        xdst += ((npm >> ((lane >> 3) & 3)) & 1u) * 8192u;
        ST16(s4, oo, 0);
        if (wv == 7) { __builtin_amdgcn_raw_buffer_store_b32(SENT, rw, xdst, 0, 16); __builtin_amdgcn_raw_buffer_store_b32(SENT, rw, xdst2, 0, 16); }
        VMW(0);
        wg_barrier();
        if (wv == 0) flag_put(WS_FB, id);
        while (__builtin_amdgcn_s_memrealtime() < t_start + TWD) __builtin_amdgcn_s_sleep(2);
        asm volatile("s_barrier" ::: "memory");
        STAMP(7);
        if ((PFM & 8) && wv == 0 && lane < 32) pf4<NTM & 8>((unsigned)((j * 32 + lane) * 8192 + x * 1024 + ((3u - ((unsigned)(size_t)p.w_uv >> 7)) & 7u) * 128), p.w_uv, L_SCR + 31744);
        if (bq) dma_piece(p.w_o + (size_t)(th * 128 + dh * 64) * 512 + wv * 64, 1024, rgn + 8192, 8, NTM & 8);
        dma_piece(p.w_uv + ((size_t)wv * 64 * NH + th) * 128 + dh * 64, 8192, rgn, 8, NTM & 8);
        if (!bq) dma_piece(p.w_o + (size_t)(th * 128 + dh * 64) * 512 + wv * 64, 1024, rgn + 8192, 8, NTM & 8);
        u32x4 fb, gm;
        unsigned xg;
        VMW(4);
        if (wv == 7)
            asm volatile("buffer_load_dwordx4 %0, %3, %5, 0 offen sc1\n\tbuffer_load_dwordx4 %1, %4, %5, 0 offen sc1\n\tglobal_load_dword %2, %6, %7" : "=&v"(fb), "=&v"(gm), "=&v"(xg)
                         : "v"((unsigned)(WS_FB + x * STR + lane * 16)), "v"((unsigned)(WS_MOD + rb * STR + rcol * 8)), "s"(rw), "v"((unsigned)(rb * 512 + rcol) * 2u), "s"(p.x) : "memory");
        VMW(0);
        asm volatile("" : "+v"(fb), "+v"(gm), "+v"(xg));
        STAMP(8);
        bf16x8 fuv[2][4], fwo[2][4];
        unsigned pfa[4];
#pragma unroll
        for (int t = 0; t < 4; ++t) { pfa[t] = rgn + frl + ((t ^ frs) << 5); PIN(pfa[t]); }
#pragma unroll
        for (int ks = 0; ks < 2; ++ks)
#pragma unroll
            for (int t = 0; t < 4; ++t) {
                const unsigned a = rgn + (unsigned)((g * 4 + (nl >> 2)) * 128 + (lane & 3) * 8) + ((t ^ (unsigned)((nl >> 3) | (g & 2))) << 5) + ks * 4096;
                fuv[ks][t] = tr_frag(a, a + 2048);
                fwo[ks][t] = tr_frag(pfa[t] + 8192 + ks * 4096, pfa[t] + 8192 + ks * 4096 + 512);
            }
#pragma unroll
        for (int ks = 0; ks < 2; ++ks) { PIN4(fuv[ks][0], fuv[ks][1], fuv[ks][2], fuv[ks][3]); PIN4(fwo[ks][0], fwo[ks][1], fwo[ks][2], fwo[ks][3]); }
        const unsigned L_WGT = L_SCR, L_MSK = L_SCR + 256, L_RED = L_SCR + 1024;
        unsigned psrc = (unsigned)(WS_PART + (bq * 4 * 32 + th) * 32768 + (wv >> 2) * 16384 + (wv & 3) * 2048 + ((lane >> 3) ^ (th & 7)) * 128) + np * 8192u + pcl, psrc1 = (psrc ^ 64u) + 1024;
        unsigned psrc2 = psrc ^ 8192u, psrc3 = psrc1 ^ 8192u;
        PIN(psrc2); PIN(psrc3); PIN(xdst2);
        unsigned rw2 = L_RED + wv * 1024 + (nl & 3) * 256 + g * 16 + (YSEL ? (nl >> 2) * 64 : 0);
        unsigned wa0 = (nl & 3) == (g >> 1) ? L_WGT + (HILO ? nl & 4 : 0) * 32 + g * 16 : L_WGT + 272, wa1 = (nl & 3) == 2 + (g >> 1) ? L_WGT + (HILO ? nl & 4 : 0) * 32 + 64 + g * 16 : L_WGT + 272;
        unsigned cr = rgn + (nl & 3) * 128 + g * 16;
        PIN(cr);
        unsigned r2r = L_RED + lane * 16, cw = rgn + lane * 8;
        unsigned osrc = (unsigned)(WS_OUTP + (rb * 16 + rc) * 16384 + lane * 16) + (np ^ 1u) * 8192u;
        PIN(psrc); PIN(psrc1); PIN(rw2); PIN(wa0); PIN(wa1); PIN(r2r); PIN4(cw, oo, osrc, xdst);
        float mlm = 0.f, mll = 0.f;
        if (wv == 0) *LP(unsigned, L_WGT + 256 + lane * 4) = 0u;
        wg_barrier();
        const bool early = __all(flags_ok(fb) & (gm[2] == tlo) & (gm[3] == thi));
        u32x2 ga = {gm[0], gm[1]};
        PIN(ga);
        bf16x8 pa[2][4], wh[2];
        {
            u32x4 ml = {0u, 0u, ~tlo, ~thi};
            const unsigned mlsrc = (unsigned)(WS_ML + (bq * 4 + g) * STR + nl * 512 + th * 16) + ((npm >> (x & 6)) & 1u) * 8192u;
            unsigned long long done = 0ull;
            unsigned lm = L_MSK;
            PIN(lm);
#pragma unroll 1
            for (unsigned spins = 0; spins < kSpinCap; ++spins) {
                unsigned long long m;
                if (wv == 0) {
                    if ((ml[2] != tlo) | (ml[3] != thi)) ml = LD16(mlsrc, 0);
                    m = __ballot((ml[2] == tlo) & (ml[3] == thi));
                    unsigned long long f = m & (m >> 4);
                    f &= f >> 2; f &= f >> 1;
                    asm volatile("ds_write_b64 %0, %1" :: "v"(lm), "v"(f) : "memory");
                    m = m == ~0ull ? 0x0101010101010101ull : 0ull;
                } else {
                    unsigned long long t;
                    asm volatile("ds_read_b64 %0, %1\n\ts_waitcnt lgkmcnt(0)" : "=v"(t) : "v"(lm) : "memory");
                    m = ((unsigned long long)(unsigned)__builtin_amdgcn_readfirstlane((unsigned)(t >> 32)) << 32) | (unsigned)__builtin_amdgcn_readfirstlane((unsigned)t);
                    m &= 0x0101010101010101ull & ~done;
                }
                if (m) {
                    done |= m;
#define PLD(bit, off, v, base) "s_bitcmp1_b64 %0, " #bit "\n\ts_cbranch_scc0 1f\n\ts_add_u32 m0, %1, " #off "\n\ts_nop 0\n\tglobal_load_lds_dwordx4 " v ", " base " sc1\n1:\n\t"
                    asm volatile(PLD(0, 0, "%2", "%4") PLD(8, 1024, "%3", "%4") PLD(16, 2048, "%2", "%5") PLD(24, 3072, "%3", "%5")
                                 PLD(32, 4096, "%8", "%6") PLD(40, 5120, "%9", "%6") PLD(48, 6144, "%8", "%7") PLD(56, 7168, "%9", "%7")
                                 :: "s"(m), "s"(rgn), "v"(psrc), "v"(psrc1), "s"(ws), "s"(ws + 1048576), "s"(ws + 2097152), "s"(ws + 3145728), "v"(psrc2), "v"(psrc3) : "memory", "m0", "scc");
                    if (done == 0x0101010101010101ull) break;
                } else if (SLPM > 0 && wv == 0) __builtin_amdgcn_s_sleep(SLPM);
            }
            mlm = __uint_as_float(ml[0]); mll = __uint_as_float(ml[1]);
            STAMP(10);
            if (wv == 0) {
                float M = mlm;
                M = fmaxf(M, dppf<0xB1>(M)); M = fmaxf(M, dppf<0x4E>(M)); M = fmaxf(M, dppf<0x141>(M)); M = fmaxf(M, dppf<0x140>(M));
                const float e = __builtin_amdgcn_exp2f(mlm - M);
                const float w = e * __builtin_amdgcn_rcpf(sum16(e * mll));
                const unsigned short hi = f2bf(w);
                *LP(unsigned short, L_WGT + lane * 2) = hi;
                if (HILO) *LP(unsigned short, L_WGT + 128 + lane * 2) = f2bf(w - bf2f(hi));
            }
            wg_barrier();
            wh[0] = *LP(bf16x8, wa0); wh[1] = *LP(bf16x8, wa1);
            __builtin_amdgcn_sched_barrier(0);
            VMW(0);
#pragma unroll
            for (int ks = 0; ks < 2; ++ks)
#pragma unroll
                for (int t = 0; t < 4; ++t) pa[ks][t] = tr_frag(pfa[t] + ks * 4096, pfa[t] + ks * 4096 + 512);
        }
        STAMP(11);
        {
            f32x4 d[4];
#pragma unroll
            for (int t = 0; t < 4; ++t) d[t] = MFMA16(pa[0][t], wh[0], (f32x4{0.f, 0.f, 0.f, 0.f}));
#pragma unroll
            for (int t = 0; t < 4; ++t) d[t] = MFMA16(pa[1][t], wh[1], d[t]);
#pragma unroll
            for (int t = 0; t < 4; ++t)
#pragma unroll
                for (int e = 0; e < 4; ++e) if (HILO) { float v = d[t][e] + dppf<0x124>(d[t][e]); asm("" : "+v"(v)); d[t][e] = v; }
            const u32x4 o0 = {pk2(d[0][0], d[0][1]), pk2(d[0][2], d[0][3]), pk2(d[1][0], d[1][1]), pk2(d[1][2], d[1][3])};
            const u32x4 o1 = {pk2(d[2][0], d[2][1]), pk2(d[2][2], d[2][3]), pk2(d[3][0], d[3][1]), pk2(d[3][2], d[3][3])};
            f32x4 y[4];
#pragma unroll
            for (int t = 0; t < 4; ++t) y[t] = MFMA16(fuv[0][t], BF8(o0), (f32x4{0.f, 0.f, 0.f, 0.f}));
#pragma unroll
            for (int t = 0; t < 4; ++t) y[t] = MFMA16(fuv[1][t], BF8(o1), y[t]);
            if (YSEL) *LP(f32x4, rw2) = (nl & 8) ? ((nl & 4) ? y[3] : y[2]) : ((nl & 4) ? y[1] : y[0]);
            else if (nl < 4) {
#pragma unroll
                for (int t = 0; t < 4; ++t) *LP(f32x4, rw2 + t * 64) = y[t];
            }
        }
        wg_barrier();
        {
            f32x4 cs[8];
#pragma unroll
            for (int w2 = 0; w2 < 8; ++w2) cs[w2] = *LP(f32x4, r2r + w2 * 1024);
            const f32x4 c4 = ((cs[0] + cs[1]) + (cs[2] + cs[3])) + ((cs[4] + cs[5]) + (cs[6] + cs[7]));
            *LP(u32x2, cw) = u32x2{pk2(c4[0], c4[1]), pk2(c4[2], c4[3])};
            asm volatile("s_waitcnt lgkmcnt(0)" ::: "memory");
            const u32x4 cx0 = *LP(u32x4, cr), cx1 = *LP(u32x4, cr + 64);
            f32x4 o[4];
#pragma unroll
            for (int q = 0; q < 4; ++q) o[q] = MFMA16(fwo[0][q], BF8(cx0), (f32x4{0.f, 0.f, 0.f, 0.f}));
#pragma unroll
            for (int q = 0; q < 4; ++q) o[q] = MFMA16(fwo[1][q], BF8(cx1), o[q]);
            const f32x4 oa = (nl & 8) ? ((nl & 4) ? o[3] : o[2]) : ((nl & 4) ? o[1] : o[0]);
            const u32x4 sv = {nosent(__float_as_uint(oa[0])), nosent(__float_as_uint(oa[1])), nosent(__float_as_uint(oa[2])), nosent(__float_as_uint(oa[3]))};
            ST16(sv, oo, 0);
        }
        STAMP(12);
        wg_barrier();
        if (wv == 7) {
            if (!early) {
                flags_wait(WS_FB + x * STR + lane * 16);
                { const u32x4 v = tagged_wait(WS_MOD + rb * STR + rcol * 8); ga = u32x2{v[0], v[1]}; }
                PIN(ga);
            }
            u32x4 op[8];
            for (unsigned spins = 0;;) {
#pragma unroll
                for (int k = 0; k < 8; ++k) op[k] = LD16(osrc + (k & 3) * 1024, (k >> 2) * 4096);
                unsigned m = 0u;
#pragma unroll
                for (int k = 0; k < 8; ++k) m = mx4(op[k], m);
                if (__all(m != SENT)) break;
                if (++spins > kSpinCap) break;
                asm volatile("" ::: "memory");
            }
            STAMP(13);
            const f32x4 v = ((__builtin_bit_cast(f32x4, op[0]) + __builtin_bit_cast(f32x4, op[1])) + (__builtin_bit_cast(f32x4, op[2]) + __builtin_bit_cast(f32x4, op[3]))) +
                            ((__builtin_bit_cast(f32x4, op[4]) + __builtin_bit_cast(f32x4, op[5])) + (__builtin_bit_cast(f32x4, op[6]) + __builtin_bit_cast(f32x4, op[7])));
            float w[2];
#pragma unroll
            for (int q = 0; q < 2; ++q) {
                auto t = __builtin_amdgcn_permlane32_swap(__float_as_uint(v[q]), __float_as_uint(v[2 + q]), false, false);
                w[q] = __uint_as_float(t[0]) + __uint_as_float(t[1]);
                auto u = __builtin_amdgcn_permlane16_swap(__float_as_uint(w[q]), __float_as_uint(w[q]), false, false);
                w[q] = __uint_as_float(u[0]) + __uint_as_float(u[1]);
                w[q] += dppf<0x128>(w[q]);
            }
            unsigned t = pk2(w[0], w[1]);
            t = pk2(__uint_as_float(ga[0]) * lo16(t), __uint_as_float(ga[1]) * hi16(t));
            t = pk2s(lo16(xg) + lo16(t), hi16(xg) + hi16(t));
            __builtin_amdgcn_raw_buffer_store_b32(t, rw, xdst, 0, 16);
            __builtin_amdgcn_raw_buffer_store_b32(t, rw, xdst2, 0, 16);
            STAMP(14);
        }
        STAMP_FLUSH();
        return;
    }

    const int s = j - 16;
    const unsigned long long t_s = __builtin_amdgcn_s_memrealtime();
    if (!CNL && wv == 0) cnt_load();
    {
        const unsigned ln = (unsigned)(s * 64 + lane);
        if ((PFM & 1) && wv == 0) pf4<NTM & 1>((ln & 511u) * 6144u + (x * 2 + (ln >> 9)) * 128u, p.w_mod, L_SCR);
        if ((PFM & 2) && wv == 1) {
            const unsigned short* w = s < 8 ? p.w_uk : p.w_q;
            pf4<NTM & 1>((ln & 511u) * 8192u + x * 1024u + ((3u - ((unsigned)(size_t)w >> 7)) & 7u) * 128u, w, L_SCR);
        }
        if (SD) __builtin_amdgcn_s_sleep(SD);
        if ((PFM & 48) == 48) {
            const unsigned r0 = (unsigned)(s * 4 + (wv & 3)), row = PFS == 0 ? r0 * 8 + (lane >> 3) : PFS == 1 ? (lane >> 3) * 64 + r0 : (r0 >> 5) * 256 + (lane >> 3) * 32 + (r0 & 31);
            pf4<NTM & 1>(row * 8192u + x * 1024u + (lane & 7u) * 128u, wv < 4 ? p.w_uk : p.w_q, L_SCR);
        }
        else if ((PFM & 16) && wv >= 4) { const unsigned l2 = (unsigned)((s * 4 + (wv & 3)) * 64 + lane); pf4<NTM & 1>((l2 >> 3) * 8192u + x * 1024u + (l2 & 7u) * 128u, p.w_q, L_SCR); }
        else if ((PFM & 32) && wv >= 4) { const unsigned l2 = (unsigned)((s * 4 + (wv & 3)) * 64 + lane); pf4<NTM & 1>((l2 >> 3) * 8192u + x * 1024u + (l2 & 7u) * 128u, p.w_uk, L_SCR); }
        if ((PFM & 4) && (wv & 6) == 2) { const unsigned l2 = (unsigned)((s * 2 + (wv & 1)) * 64 + lane); pf4<NTM & 1>((l2 & 511u) * 8192u + (x * 4 + (l2 >> 9)) * 256u, p.w_q, L_SCR); }
    }
    const int ug = x * 7 + s;
    const unsigned vb = (unsigned)(g * 1024 + ((nl ^ (g << 2)) << 4)), lb = (unsigned)((wv >> 1) * 32768 + (wv & 1) * 4096);
    const char* ck = (const char*)p.ckv + ((size_t)x * SK + s * 256 + wv * 16) * 1024;
    const unsigned rot = ROTC ? (unsigned)s & 3u : 0u;
    const unsigned vbr = vb ^ (rot << 8);
    const char* ck2 = ck + 131072;
    auto ck_dma = [&](unsigned i) { dma16x<NTM & 16>(vbr, ((i & 3u) << 4) | (((i >> 2) & 3u) << 8) | ((i & 3u) << 12), (i >> 4) ? ck2 : ck, lb + ((i >> 2) & 3u) * 8192u + (i & 3u) * 1024u); };
    bf16x8 ka[16];
    s16x4 va[8][4];
    unsigned kad[4], vad[4], vad2[4];
    auto kq = [](unsigned u) { return (0x78u >> (2 * u)) & 3u; };
    auto ka_reads = [&](int cb) {
#pragma unroll
        for (int u = 0; u < 4; ++u) ka[cb * 4 + u] = *LP(bf16x8, kad[u] + cb * 8192);
    };
    auto va_reads = [&]() {
#pragma unroll
        for (int kk = 0; kk < 8; ++kk)
#pragma unroll
            for (int q = 0; q < 4; ++q) va[kk][q] = tr4((kk < 4 ? vad[q] : vad2[q]) + ((kk >> 1) & 1) * 32768 + (kk & 1) * 4096);
    };
    auto va_read1 = [&](int kk) {
#pragma unroll
        for (int q = 0; q < 4; ++q) va[kk][q] = tr4((kk < 4 ? vad[q] : vad2[q]) + ((kk >> 1) & 1) * 32768 + (kk & 1) * 4096);
    };
    auto va_pin = [&]() {
#pragma unroll
        for (int q = 0; q < 8; ++q) PIN4(va[q][0], va[q][1], va[q][2], va[q][3]);
    };
    u32x4 fa = {0u, 0u, 0u, 0u};
    const unsigned fao = (unsigned)(WS_FI + x * STR + (lane & 7) * 16);
    constexpr int LAST0 = GI + NPOST, LAST = LAST0 + NP2;
    static_assert(GI - WG >= 15 && GI < 20 && LAST >= 19 && LAST < 28, "");
    const unsigned qsrc = (unsigned)(WS_Q + (wv & 1) * 4 * STR + (x * 4 + (wv >> 1)) * 1024) + (g ^ rot) * 256 + ((nl ^ ((wv * 4) & 15)) << 4);
    auto q_dma = [&](int k) {
        unsigned t;
        asm volatile("s_mov_b32 m0, %3\n\tv_xor_b32 %0, %4, %1\n\tglobal_load_lds_dwordx4 %0, %2 sc1" : "=&v"(t) : "v"(qsrc), "s"(ws), "s"(L_SCR + (wv * 4 + k) * 1024), "i"(k * (STR + 16)) : "memory", "m0");
    };
#pragma unroll
    for (int i = 0; i <= GI; ++i) {
        ck_dma(i);
        if (CNL && i == 5 && wv == 0) cnt_load();
        if (i == 3) {
#pragma unroll
            for (int u = 0; u < 4; ++u) {
                kad[u] = tile_off(wv * 16 + kq(nl >> 2) * 4 + (nl & 3), 4 * u + g);
                vad[u] = tile_off(8 * (lane >> 5) + 4 * (u & 1) + (nl >> 2), wv * 8 + 4 * (u >> 1) + 2 * (g & 1) + ((lane & 3) >> 1)) + 8 * (lane & 1);
                vad2[u] = vad[u] + 65536;
                PIN(kad[u]); PIN(vad[u]); PIN(vad2[u]);
            }
        }
        if (i >= 4) VMW(4);
        if (i == 15 && wv == 0) {
            PIN(fa);
            tag_take();
            if (!__all(flags_ok(fa))) flags_wait(fao);
        }
        if ((i & 3) == 3 && i >= 7 && i <= 15) {
            const int cb = (i - 7) >> 2;
            if (cb > 0) { PIN4(ka[cb * 4 - 4], ka[cb * 4 - 3], ka[cb * 4 - 2], ka[cb * 4 - 1]); va_pin(); }
            wg_barrier();
            ka_reads(cb);
            if ((wv >> 1) == cb) va_reads();
            STAMP(16 + cb);
        }
        if (i == 10 && wv == 0) asm volatile("buffer_load_dwordx4 %0, %1, %2, 0 offen sc1" : "+v"(fa) : "v"(fao), "s"(rw) : "memory");
    }
    PIN4(ka[8], ka[9], ka[10], ka[11]); va_pin();
    while (__builtin_amdgcn_s_memrealtime() < t_s + TG) __builtin_amdgcn_s_sleep(1);
    VMW(WG);
    q_dma(0); q_dma(1); q_dma(2); q_dma(3);
    asm volatile("s_barrier" ::: "memory");
    STAMP(19);
#pragma unroll
    for (int i = GI + 1; i <= LAST0; ++i) ck_dma(i);
    ka_reads(3);
    if ((wv >> 1) == 3) va_reads();
    VMW(NPOST);
    const unsigned L_EXM = L_SCR - 1024, L_BIG = L_SCR + 512 + 128;
    unsigned nx = LAST + 1, npm = 0u;
    unsigned qad[4];
    {
        unsigned qck = L_SCR + wv * 4096 + lane * 16;
        u32x4 qb[4];
#pragma unroll
        for (int k = 0; k < 4; ++k) qb[k] = *LP(u32x4, qck + k * 1024);
#pragma unroll
        for (int i = LAST0 + 1; i <= LAST; ++i) ck_dma(i);
#pragma unroll
        for (int k = 0; k < 4; ++k) { qad[k] = L_SCR + nl * 1024 + (((4 * k + g) ^ nl) << 4); PIN(qad[k]); }
        STAMP(20);
        if (!__all(mx4(qb[0], mx4(qb[1], mx4(qb[2], mx4(qb[3], 0u)))) != SENT)) {
            for (unsigned spins = 0;;) {
#pragma unroll
                for (int k = 0; k < 4; ++k)
                    if (!__all(mx4(qb[k], 0u) != SENT)) q_dma(k);
                if (nx <= 25) {
#pragma unroll
                    for (int i = 0; i < 3; ++i) ck_dma(nx + i);
                    nx += 3;
                    VMW(3);
                } else {
                    for (; nx < 28; ++nx) ck_dma(nx);
                    VMW(0);
                }
#pragma unroll
                for (int k = 0; k < 4; ++k) qb[k] = *LP(u32x4, qck + k * 1024);
                if (__all(mx4(qb[0], mx4(qb[1], mx4(qb[2], mx4(qb[3], 0u)))) != SENT)) break;
                if (++spins > kSpinCap) break;
            }
        }
    }
    constexpr int N0 = LAST + 1 + SL0, NE = N0 + 6 < 31 ? N0 + 6 : 31;
    auto slot = [&](int i) { if (i < 28) { if (nx <= (unsigned)i) ck_dma(i); } else ck_dma(i); };
    const unsigned kg = kq(g);
    unsigned exw = L_EXM + ((nl + (lane >> 5) * 16) * 8 + wv) * 4, exr = L_EXM + nl * 32;
    unsigned pwr = L_SCR + nl * 1024 + 768 + (((2 * wv + (kg >> 1)) ^ nl) << 4) + (kg & 1) * 8, esw = L_SCR + nl * 1024 + 512 + ((wv ^ (nl & 7)) << 4) + g * 4;
    unsigned prd = L_SCR + (lane & 31) * 1024 + 768 + (((lane >> 5) ^ nl) << 4);
    PIN4(exw, exr, pwr, esw); PIN(prd);
    PIN4(ka[12], ka[13], ka[14], ka[15]); va_pin();
    wg_barrier();
    STAMP(21);
    bf16x8 qr[8][2];
    auto q_ld = [&](int ks, int hg) { return *LP(bf16x8, qad[ks & 3] + (ks >> 2) * 256 + hg * 16384); };
    auto p_ld = [&](int kk) { return *LP(bf16x8, prd ^ (kk << 5)); };
    float ls0, ls1, m0, m1;
    f32x4 sa, sb;
    f32x16 o[2];
    unsigned sw[4], srd, p0, mlo, esr, fbo;
    u32x2 tg;
    u32x4 nq;
    auto probs = [&](float (&e)[8]) {
#pragma unroll
        for (int r = 0; r < 4; ++r) { e[r] = __builtin_amdgcn_exp2f(fmaf(sa[r], SCL, -m0)); e[4 + r] = __builtin_amdgcn_exp2f(fmaf(sb[r], SCL, -m1)); }
        *LP(u32x2, pwr) = u32x2{pk2(e[0], e[1]), pk2(e[2], e[3])};
        *LP(u32x2, pwr + 16384) = u32x2{pk2(e[4], e[5]), pk2(e[6], e[7])};
    };
    auto wave_max = [&]() {
        const float a = fmaxf(fmaxf(sa[0], sa[1]), fmaxf(sa[2], sa[3])), b = fmaxf(fmaxf(sb[0], sb[1]), fmaxf(sb[2], sb[3]));
        auto u = __builtin_amdgcn_permlane32_swap(__float_as_uint(a), __float_as_uint(b), false, false);
        const float c = fmax3(__uint_as_float(u[0]), __uint_as_float(u[1]), __uint_as_float(u[1]));
        auto v = __builtin_amdgcn_permlane16_swap(__float_as_uint(c), __float_as_uint(c), false, false);
        return fmax3(__uint_as_float(v[0]), __uint_as_float(v[1]), __uint_as_float(v[1]));
    };
    auto max8 = [&](unsigned a) {
        const f32x4 a0 = *LP(f32x4, a), a1 = *LP(f32x4, a + 16);
        return fmax3(fmax3(a0[0], a0[1], a0[2]), fmax3(a0[3], a1[0], a1[1]), fmax3(a1[2], a1[3], a1[3])) * SCL;
    };
    {
        bf16x8 qs[8][2];
#pragma unroll
        for (int ks = 0; ks < 8; ++ks) { qr[ks][0] = q_ld(ks + 8, 0); qr[ks][1] = q_ld(ks + 8, 1); }
        __builtin_amdgcn_sched_barrier(0);
#pragma unroll
        for (int i = LAST + 1; i < LAST + 1 + SL0; ++i) if (i < NE) slot(i);
        sa = f32x4{0.f, 0.f, 0.f, 0.f}; sb = sa;
        f32x4 s2a = sa, s2b = sa;
        bf16x8 kb[4];
#pragma unroll
        for (int n = 0; n < 16; ++n) {
            const int ks = (n + 8) & 15;
            sa = MFMA16(ka[ks], ks < 8 ? qs[ks][0] : qr[ks - 8][0], sa);
            sb = MFMA16(ka[ks], ks < 8 ? qs[ks][1] : qr[ks - 8][1], sb);
            if (QKM && n == 4) { VMW(LAST + SL0 + 1 - 19); for (int u = 0; u < 4; ++u) kb[u] = *LP(bf16x8, kad[u]); }
            if (QKM && ks < 4) { s2a = MFMA16(kb[ks], qs[ks][0], s2a); s2b = MFMA16(kb[ks], qs[ks][1], s2b); }
            if (n >= 8 - QD && n < 16 - QD) { qs[n + QD - 8][0] = q_ld(n + QD - 8, 0); qs[n + QD - 8][1] = q_ld(n + QD - 8, 1); }
            if ((n & 3) == 3 && LAST + 1 + SL0 + (n >> 2) < NE) slot(LAST + 1 + SL0 + (n >> 2));
            __builtin_amdgcn_sched_barrier(0);
        }
        *LP(float, exw) = wave_max();
        if (N0 + 4 < NE) slot(N0 + 4);
        wg_barrier();
        m0 = max8(exr); m1 = max8(exr + 512);
#pragma unroll
        for (int ks = 4; ks < 8; ++ks) { qs[ks][0] = q_ld(ks, 0); qs[ks][1] = q_ld(ks, 1); }
        __builtin_amdgcn_sched_barrier(0);
        float e[8];
        probs(e);
        ls0 = (e[0] + e[1]) + (e[2] + e[3]); ls1 = (e[4] + e[5]) + (e[6] + e[7]);
        PIN(ls0); PIN(ls1);
        if (wv == 0) *LP(u32x4, L_BIG) = u32x4{0u, 0u, tlo, thi};
        if (N0 + 5 < NE) slot(N0 + 5);
        VMW(NE - 24);
        ka_reads(1);
        asm volatile("s_waitcnt lgkmcnt(4)\n\ts_barrier" ::: "memory");
        STAMP(22);
        static_assert(NE >= 28 && QKM == 1, "");
        bf16x8 pf[8];
        tg = *LP(u32x2, L_BIG + 8);
        pf[0] = p_ld(0); pf[1] = p_ld(1);
        slot(NE);
        sa = s2a; sb = s2b;
        __builtin_amdgcn_sched_barrier(0);
#pragma unroll
        for (int ks = 4; ks < 8; ++ks) { sa = MFMA16(ka[ks], qs[ks][0], sa); sb = MFMA16(ka[ks], qs[ks][1], sb); }
        __builtin_amdgcn_sched_barrier(0);
#pragma unroll
        for (int i = 0; i < 16; ++i) { o[0][i] = 0.f; o[1][i] = 0.f; }
#pragma unroll
        for (int kk = 0; kk < 8; ++kk) {
            if (kk == 0) {
#pragma unroll
                for (int q = 2; q < 8; ++q) pf[q] = p_ld(q);
            }
            if (kk == 2) { PIN4(pf[2], pf[3], pf[4], pf[5]); PIN(pf[6]); PIN(pf[7]); PIN(tg); }
            if (kk >= 2 && wv < 4) { va_read1(kk - 2); if (kk == 7) va_read1(6); }
            o[0] = MFMA32(cat8(va[kk][0], va[kk][1]), pf[kk], o[0]);
            o[1] = MFMA32(cat8(va[kk][2], va[kk][3]), pf[kk], o[1]);
            if ((kk & 1) && NE + 1 + (kk >> 1) < 32) slot(NE + 1 + (kk >> 1));
            __builtin_amdgcn_sched_barrier(0);
        }
        if (wv < 4) va_read1(7);
        VMW(4);
        ka_reads(2);
        tlo = __builtin_amdgcn_readfirstlane(tg[0]); thi = __builtin_amdgcn_readfirstlane(tg[1]);
        rederive();
        fbo = (unsigned)(WS_FB + x * STR + lane * 16);
        asm volatile("buffer_load_dwordx4 %0, off, %1, %2 sc1" : "=v"(nq) : "s"(rw), "s"(WS_NM + x * STR) : "memory");
        const unsigned head = lane & 31, r8 = lane >> 3, cw = wv ^ (rot * 2);
        srd = lb + r8 * 128 + (((lane & 7) ^ r8) << 4);
        sw[0] = lb + head * 128 + (((lane >> 5) ^ (head & 7)) << 4);
        sw[1] = sw[0] ^ 32u; sw[2] = sw[0] ^ 64u; sw[3] = sw[0] ^ 96u;
        p0 = (unsigned)(WS_PART + x * 1048576) + (cw >> 2) * 16384 + (cw & 3) * 2048 + r8 * 32768 + ((s ^ r8) * 8 + (lane & 7)) * 16;
        PIN4(sw[0], sw[1], sw[2], sw[3]); PIN(srd); PIN(p0); PIN(fbo);
#pragma unroll
        for (int ks = 8; ks < 12; ++ks) { sa = MFMA16(ka[ks], qr[ks - 8][0], sa); sb = MFMA16(ka[ks], qr[ks - 8][1], sb); }
    }
    STAMP(23);
    unsigned po[4];
    float mrun;
    {
        VMW(1);
        STAMP(24);
        ka_reads(3);
        asm volatile("" : "=v"(fa));
        if (wv == 0 && s < 11) asm volatile("buffer_load_dwordx4 %0, %1, %2, 0 offen sc1" : "+v"(fa) : "v"(fbo), "s"(rw) : "memory");
        __builtin_amdgcn_sched_barrier(0);
#pragma unroll
        for (int ks = 12; ks < 16; ++ks) { sa = MFMA16(ka[ks], qr[ks - 8][0], sa); sb = MFMA16(ka[ks], qr[ks - 8][1], sb); }
        bf16x8 pf[8];
#pragma unroll 1
        for (bool first = true;; first = false) {
            float e[8];
            probs(e);
            const float n0 = (e[0] + e[1]) + (e[2] + e[3]), n1 = (e[4] + e[5]) + (e[6] + e[7]);
            *LP(float, esw) = ls0 + n0; *LP(float, esw + 16384) = ls1 + n1;
            if (__builtin_expect(!(n0 + n1 <= BIGV), 0)) *LP(unsigned, L_BIG) = 1u;
            va_pin();
            wg_barrier();
            STAMP(25);
            PIN(prd);
            const unsigned big = *LP(unsigned, L_BIG);
            if (wv >= 4) va_reads();
#pragma unroll
            for (int kk = 0; kk < 8; ++kk) { pf[kk] = p_ld(kk); __builtin_amdgcn_sched_barrier(0); }
            if (__builtin_expect(__builtin_amdgcn_readfirstlane(big) == 0 || !first, 1)) break;
            const unsigned ex2 = L_SCR + 512 + 160;
            PIN(sa); PIN(sb);
            rederive();
            *LP(float, ex2 + (nl + (lane >> 5) * 16) * 1024 + wv * 4) = wave_max();
            wg_barrier();
            const float x0 = fmaxf(max8(ex2 + nl * 1024), m0), x1 = fmaxf(max8(ex2 + nl * 1024 + 16384), m1);
            const float al0 = __builtin_amdgcn_exp2f(m0 - x0), al1 = __builtin_amdgcn_exp2f(m1 - x1), al = (lane & 16) ? al1 : al0;
            m0 = x0; m1 = x1; ls0 *= al0; ls1 *= al1;
            o[0] *= al; o[1] *= al;
        }
        mrun = (lane & 16) ? m1 : m0;
        auto stage = [&](int t2, int gp) {
            const unsigned lo0 = pk2(o[t2][8 * gp + 0], o[t2][8 * gp + 1]), lo1 = pk2(o[t2][8 * gp + 2], o[t2][8 * gp + 3]);
            const unsigned hi0 = pk2(o[t2][8 * gp + 4], o[t2][8 * gp + 5]), hi1 = pk2(o[t2][8 * gp + 6], o[t2][8 * gp + 7]);
            auto r0 = __builtin_amdgcn_permlane32_swap(lo0, hi0, false, false);
            auto r1 = __builtin_amdgcn_permlane32_swap(lo1, hi1, false, false);
            *LP(u32x4, sw[t2 * 2 + gp]) = u32x4{r0[0], r1[0], r0[1], r1[1]};
        };
#pragma unroll
        for (int kk = 0; kk < 8; ++kk) {
            o[0] = MFMA32(cat8(va[kk][0], va[kk][1]), pf[kk], o[0]);
            if (kk == 1) {
                asm volatile("s_waitcnt vmcnt(0)" : "+v"(nq) :: "memory");
                npm = __builtin_amdgcn_readfirstlane(nq[0]);
                if ((unsigned)__builtin_amdgcn_readfirstlane(nq[2]) != tlo || (unsigned)__builtin_amdgcn_readfirstlane(nq[3]) != thi) npm = __builtin_amdgcn_readfirstlane(tagged_wait(WS_NM + x * STR)[0]);
            }
            if (kk == 3 || kk == 5) {
                const unsigned v = (npm ^ ((x & 2) ? 0xffu : 0u)) >> (lane >> 5);
#pragma unroll
                for (int i = kk - 3; i < kk - 1; ++i) { po[i] = p0 + ((v >> (2 * i)) & 1u) * 8192u; PIN(po[i]); }
            }
            __builtin_amdgcn_sched_barrier(0);
        }
#pragma unroll
        for (int kk = 0; kk < 8; ++kk) {
            o[1] = MFMA32(cat8(va[kk][2], va[kk][3]), pf[kk], o[1]);
            if (kk == 2) { PIN(o[0]); stage(0, 0); }
            if (kk == 4) stage(0, 1);
            __builtin_amdgcn_sched_barrier(0);
        }
        stage(1, 0); stage(1, 1);
        asm volatile("s_waitcnt lgkmcnt(0)" ::: "memory");
        u32x4 w[4];
#pragma unroll
        for (int i = 0; i < 4; ++i) w[i] = *LP(u32x4, srd + i * 1024);
#pragma unroll
        for (int i = 0; i < 4; ++i) ST16(w[i], po[i], i * 262144);
        __builtin_amdgcn_sched_barrier(0);
        STAMP(26);
        u32x4 mlrec;
        if (wv == 0) {
            const unsigned head = lane & 31;
            esr = L_SCR + head * 1024 + 512 + ((head & 7) << 4);
            mlo = (unsigned)(WS_ML + x * STR + s * 512 + head * 16) + ((npm >> ((head >> 3) * 2)) & 1u) * 8192u;
            PIN(mlo);
            f32x4 b8[8];
#pragma unroll
            for (int q = 0; q < 8; ++q) b8[q] = *LP(f32x4, esr ^ (q << 4));
            const f32x4 bs = ((b8[0] + b8[1]) + (b8[2] + b8[3])) + ((b8[4] + b8[5]) + (b8[6] + b8[7]));
            mlrec = u32x4{__float_as_uint(mrun), __float_as_uint((bs[0] + bs[1]) + (bs[2] + bs[3])), tlo, thi};
            PIN(mlrec);
        }
        VMW(0);
        wg_barrier();
        if (wv == 0) {
            if (lane < 32) ST16(mlrec, mlo, 0);
            PIN(fa);
            if (s < 11 && !__all(flags_ok(fa))) flags_wait(fbo);
        }
        STAMP(27);
        wg_barrier();
    }
    const unsigned long long t_att = __builtin_amdgcn_s_memrealtime();
    rederive();
    while (__builtin_amdgcn_s_memrealtime() < t_att + (s >= 11 ? MD0 : MD1)) __builtin_amdgcn_s_sleep(2);
    if (s >= 11) {
        const unsigned n = (unsigned)((s - 11) * 8 + wv);
        if (n < 28) pf4<NTM & 32>((n * 64 + lane) * 1024u + x * 128u, p.w2, 0);
        { const unsigned l2 = n * 64 + lane; pf4<NTM & 32>((l2 / 5u) * (DFF * 2) + min(((unsigned)x * 448u & ~127u) + (l2 % 5u) * 128u, DFF * 2 - 4u), p.w1, 0); }
        if (wv == 0) flag_put(WS_FA, id - 128);
        STAMP_FLUSH();
        return;
    }
    const unsigned L_H2S = L_SCR, L_REDM = L_SCR + 16384;
    if (s < 7) {
        {
            const char* w1s = (const char*)(p.w1 + (size_t)wv * 64 * DFF + ug * 32);
#pragma unroll
            for (int i = 0; i < 4; ++i) dma16<NTM & 32>((unsigned)((lane >> 2) * DFF * 2 + (lane & 3) * 16), w1s + (size_t)i * 16 * DFF * 2, wv * 4096 + i * 1024);
        }
        const int b = wv;
        if (wv == 0) ST16(s4, (unsigned)(WS_U + x * STR + (lane >> 5) * 8192 + s * 512 + (lane & 31) * 16), 0);
        const u32x4 shf = LD16((unsigned)(WS_MB + b * STR + lane * 16), 0), scl = LD16((unsigned)(WS_MB + b * STR + 1024 + lane * 16), 0);
        VMW(0);
        if (wv == 0) flag_put(WS_FA, id - 128);
        wg_barrier();
        bf16x8 wk0[8], wk1[8];
        if (wv >= 6) {
#pragma unroll
            for (int q = 0; q < 8; ++q) {
                const unsigned a = (unsigned)(q * 4096 + (g * 8 + (nl >> 2)) * 64 + (wv & 1) * 32 + (lane & 3) * 8);
                wk0[q] = tr_frag(a, a + 256); wk1[q] = tr_frag(a + 2048, a + 2304);
            }
#pragma unroll
            for (int q = 0; q < 8; q += 2) PIN4(wk0[q], wk0[q + 1], wk1[q], wk1[q + 1]);
        }
        float scv[8], shv[8];
#pragma unroll
        for (int q = 0; q < 4; ++q) {
            scv[2 * q] = rbf(1.f + lo16(scl[q])); scv[2 * q + 1] = rbf(1.f + hi16(scl[q])); shv[2 * q] = lo16(shf[q]); shv[2 * q + 1] = hi16(shf[q]);
            PIN4(scv[2 * q], scv[2 * q + 1], shv[2 * q], shv[2 * q + 1]);
        }
        unsigned har = L_H2S + ((lane & 7) * HS_LD + g * 8 + (nl >> 3) * 32) * 2, haw = L_H2S + (b * HS_LD + lane * 8) * 2;
        unsigned udst = (unsigned)(WS_U + x * STR + (nl >> 3) * 8192 + (s * 2 + (wv & 1)) * 256 + ((nl & 7) * 16 + g * 4) * 2), xsrc = (unsigned)(WS_X1 + x * STR + (b * 512 + lane * 8) * 2) + ((npm >> x) & 1u) * 8192u;
        PIN4(har, haw, udst, xsrc);
        STAMP(28);
        while (__builtin_amdgcn_s_memrealtime() < t_att + XGATE) __builtin_amdgcn_s_sleep(2);
        u32x4 xv;
        for (unsigned spins = 0;;) {
            xv = LD16(xsrc, 0);
            if (__all(mx4(xv, 0u) != SENT)) break;
            __builtin_amdgcn_s_sleep(SLPX);
            if (++spins > kSpinCap) break;
            asm volatile("" ::: "memory");
        }
        STAMP(29);
        {
            f32x2_t v[4];
#pragma unroll
            for (int q = 0; q < 4; ++q) v[q] = f32x2_t{lo16(xv[q]), hi16(xv[q])};
            const f32x2_t p1 = (v[0] + v[1]) + (v[2] + v[3]), p2 = (v[0] * v[0] + v[1] * v[1]) + (v[2] * v[2] + v[3] * v[3]);
            float mb, rden;
            ln_stats(p1[0] + p1[1], p2[0] + p2[1], mb, rden);
            u32x4 hr;
#pragma unroll
            for (int q = 0; q < 4; ++q) {
                const f32x2_t h = ((v[q] - mb) * rden) * f32x2_t{scv[2 * q], scv[2 * q + 1]} + f32x2_t{shv[2 * q], shv[2 * q + 1]};
                hr[q] = pk2(h[0], h[1]);
            }
            *LP(u32x4, haw) = hr;
        }
        wg_barrier();
        if (wv >= 6) {
            bf16x8 af[8];
#pragma unroll
            for (int q = 0; q < 8; ++q) af[q] = *LP(bf16x8, har + q * 128);
            __builtin_amdgcn_sched_barrier(0);
            f32x4 d0 = {0.f, 0.f, 0.f, 0.f}, d1 = {0.f, 0.f, 0.f, 0.f};
#pragma unroll
            for (int q = 0; q < 8; ++q) { d0 = MFMA16(wk0[q], af[q], d0); d1 = MFMA16(wk1[q], af[q], d1); }
            const f32x4 own = nl < 8 ? d0 : d1;
#pragma unroll
            for (int e = 0; e < 4; ++e) { float v = own[e] + dppf<0x128>(own[e]); asm("" : "+v"(v)); d0[e] = fmaxf(v, 0.f); }
            const u32x2 uv = {pk2s(d0[0], d0[1]), pk2s(d0[2], d0[3])};
            __builtin_amdgcn_raw_buffer_store_b64(uv, rw, udst, 0, 16);
            STAMP(30);
        }
        STAMP_FLUSH();
        return;
    }
    {
        const int n0 = (x * 4 + (s - 7)) * 16;
        const char* mws = (const char*)(p.w2 + (size_t)wv * 224 * 512 + n0);
#pragma unroll
        for (int i = 0; i < 7; ++i) dma16<NTM & 32>((unsigned)((lane >> 1) * 1024 + (lane & 1) * 16), mws + i * 32768, rgn + i * 1024);
        const int fb = lane >> 3, fc = (lane & 7) * 2;
        u32x2 gb = {0u, 0u};
        if (wv == 0) {
            flag_put(WS_FA, id - 128);
            { const u32x4 v = tagged_wait(WS_MOD + fb * STR + (1536 + n0 + fc) * 8); gb = u32x2{v[0], v[1]}; }
            flags_wait(WS_FA + x * STR + lane * 16);
            if (id == 23 * 8 && lane == 0) __builtin_amdgcn_raw_buffer_store_b64(u32x2{tlo, thi}, rw, WS_EPOCH, 0, 16);
            PIN(gb);
        }
        VMW(0);
        bf16x8 wfr[7];
        const unsigned trl = (unsigned)(g * 8 + (nl >> 2)) * 32u + (unsigned)(lane & 3) * 8u;
#pragma unroll
        for (int ks = 0; ks < 7; ++ks) { wfr[ks] = tr_frag(rgn + ks * 1024 + trl, rgn + ks * 1024 + trl + 128); PIN(wfr[ks]); }
        unsigned usrc = (unsigned)(WS_U + wv * STR + (g >> 1) * 256 + (nl & 7) * 32 + (g & 1) * 16) + ((npm >> x) & 1u) * 8192u;
        unsigned rdw = L_REDM + wv * 512 + (nl & 7) * 64 + g * 16, rdr = L_REDM + lane * 8;
        unsigned short* outp = p.out + fb * 512 + n0 + fc;
        PIN(usrc); PIN(rdw); PIN(rdr); PIN(outp);
        STAMP(28);
        unsigned xg = 0u;
        if (wv == 0) {
            while (__builtin_amdgcn_s_memrealtime() < t_att + XGATE) __builtin_amdgcn_s_sleep(2);
            for (unsigned spins = 0;;) {
                xg = __builtin_amdgcn_raw_buffer_load_b32(rw, (unsigned)(WS_X1 + x * STR + (fb * 512 + n0 + fc) * 2) + ((npm >> x) & 1u) * 8192u, 0, 16);
                if (__all(xg != SENT)) break;
                __builtin_amdgcn_s_sleep(SLPD);
                if (++spins > kSpinCap) break;
                asm volatile("" ::: "memory");
            }
            __builtin_amdgcn_s_sleep(SLPG);
        }
        wg_barrier();
        u32x4 uf[7];
        for (unsigned spins = 0;;) {
#pragma unroll
            for (int ks = 0; ks < 7; ++ks) uf[ks] = LD16(usrc + ks * 512, 0);
            unsigned m = 0u;
#pragma unroll
            for (int ks = 0; ks < 7; ++ks) m = mx4(uf[ks], m);
            if (__all(m != SENT)) break;
            if (++spins > kSpinCap) break;
            asm volatile("" ::: "memory");
        }
        STAMP(29);
        f32x4 d = {0.f, 0.f, 0.f, 0.f}, d2 = {0.f, 0.f, 0.f, 0.f};
#pragma unroll
        for (int ks = 0; ks < 7; ++ks) {
            if (ks & 1) d2 = MFMA16(wfr[ks], BF8(uf[ks]), d2);
            else d = MFMA16(wfr[ks], BF8(uf[ks]), d);
        }
        *LP(f32x4, rdw) = d + d2;
        wg_barrier();
        if (wv == 0) {
            f32x2_t c[8];
#pragma unroll
            for (int w2 = 0; w2 < 8; ++w2) c[w2] = *LP(f32x2_t, rdr + w2 * 512);
            const f32x2_t sm = ((c[0] + c[1]) + (c[2] + c[3])) + ((c[4] + c[5]) + (c[6] + c[7]));
            unsigned t = pk2(sm[0], sm[1]);
            t = pk2(__uint_as_float(gb[0]) * lo16(t), __uint_as_float(gb[1]) * hi16(t));
            *(GAS unsigned*)outp = pk2(lo16(xg) + lo16(t), hi16(xg) + hi16(t));
            STAMP(31);
        }
        STAMP_FLUSH();
    }
}

extern "C" void kernel_launch(void* const* d_in, const int* in_sizes, int n_in,
                              void* d_out, int out_size, void* d_ws, size_t ws_size,
                              hipStream_t stream) {
    Params p;
    p.x = (const unsigned short*)d_in[0];
    p.ckv = (const unsigned short*)d_in[1];
    p.w_q = (const unsigned short*)d_in[2];
    p.w_uk = (const unsigned short*)d_in[3];
    p.w_uv = (const unsigned short*)d_in[4];
    p.w_o = (const unsigned short*)d_in[5];
    p.w1 = (const unsigned short*)d_in[7];
    p.w2 = (const unsigned short*)d_in[8];
    p.c = (const unsigned short*)d_in[9];
    p.w_mod = (const unsigned short*)d_in[10];
    p.ws = (char*)(((size_t)d_ws + 2097151) & ~(size_t)2097151);
    p.out = (unsigned short*)d_out;
    (void)hipFuncSetAttribute((const void*)k_main, hipFuncAttributeMaxDynamicSharedMemorySize, 163840);
    k_main<<<256, 512, 163840, stream>>>(p);
}
```

```cpp
#define TG 520
#include <hip/hip_runtime.h>
#include <hip/hip_bf16.h>
#include <stdint.h>

#define NH 32
#define SK 4096
#define DFF 1792
#define GI 18
#define ROTC 1
#define PFS 2
#define XNO 0
#define CPJ 7
#define NTM 16
#define QHELP 4
#define HP 13
#define NTH 0
#define W0 2
#define SL0 1
#define LMPF 0
#define SLPB 20
#define MD0 0
#define MD1 0
#define NEARP 1
#define NCAL 2
#define TLBW 2
#define JPERM 1
#define NPOST 3
#define XS3 1
#define WG 1
#define NP2 2
#define QKM 1
#define HILO 0
#define YSEL 1
#define WMT 0
#define SD 0
#define QDL 0
#define CNL 1
#define TLBA 1
#define CPW 0
#define TWD 570
#define LMD 250
#define PFM 57
#define CPFE 35500
#define SLPM 0
#define SLPD 12
#define SLPG 8
#define SLPX 1
#define SLPH 4

using u16x8 = __attribute__((ext_vector_type(8))) unsigned short;
using s16x4 = __attribute__((ext_vector_type(4))) short;
using bf16x8 = __attribute__((ext_vector_type(8))) short;
using f32x16 = __attribute__((ext_vector_type(16))) float;
using f32x4 = __attribute__((ext_vector_type(4))) float;
using u32x4 = __attribute__((ext_vector_type(4))) unsigned;
using u32x2 = __attribute__((ext_vector_type(2))) unsigned;

#define GAS __attribute__((address_space(1)))
#define LAS __attribute__((address_space(3)))
typedef GAS unsigned long long gu64;
#define RLX_AGENT __ATOMIC_RELAXED, __HIP_MEMORY_SCOPE_AGENT
#define SENT 0xffffffffu
#define TAGC 0x9e3779b9u

__device__ __forceinline__ float bf2f(unsigned short u) { return __uint_as_float(((unsigned)u) << 16); }
typedef __attribute__((ext_vector_type(2))) __bf16 bf16x2_t;
typedef __attribute__((ext_vector_type(2))) float f32x2_t;
__device__ __forceinline__ unsigned pk2(float a, float b) {
    f32x2_t v = {a, b};
    bf16x2_t r = __builtin_convertvector(v, bf16x2_t);
    return __builtin_bit_cast(unsigned, r);
}
__device__ __forceinline__ unsigned nosent(unsigned r) { return min(r, SENT - 1u); }
__device__ __forceinline__ unsigned pk2s(float a, float b) { return nosent(pk2(a, b)); }
__device__ __forceinline__ unsigned short f2bf(float f) { return (unsigned short)(pk2(f, 0.f) & 0xffffu); }
__device__ __forceinline__ float rbf(float f) { return __uint_as_float(pk2(f, 0.f) << 16); }
__device__ __forceinline__ float lo16(unsigned u) { return __uint_as_float(u << 16); }
__device__ __forceinline__ float hi16(unsigned u) { return __uint_as_float(u & 0xffff0000u); }
__device__ __forceinline__ unsigned umax3(unsigned a, unsigned b, unsigned c) { return max(max(a, b), c); }
__device__ __forceinline__ unsigned mx4(const u32x4& v, unsigned m) { return umax3(v[2], v[3], umax3(v[0], v[1], m)); }
__device__ __forceinline__ float fmax3(float a, float b, float c) { float r; asm("v_max3_f32 %0, %1, %2, %3" : "=v"(r) : "v"(a), "v"(b), "v"(c)); return r; }
template <int C> __device__ __forceinline__ float dppf(float v) { return __int_as_float(__builtin_amdgcn_update_dpp(0, __float_as_int(v), C, 0xf, 0xf, true)); }
__device__ __forceinline__ float sum16(float v) { v += dppf<0xB1>(v); v += dppf<0x4E>(v); v += dppf<0x141>(v); v += dppf<0x140>(v); return v; }

__device__ __forceinline__ void ln_stats(float p1, float p2, float& mb, float& rden) {
    auto t = __builtin_amdgcn_permlane32_swap(__float_as_uint(p1), __float_as_uint(p2), false, false);
    float a = sum16(__uint_as_float(t[0]) + __uint_as_float(t[1]));
    auto t2 = __builtin_amdgcn_permlane16_swap(__float_as_uint(a), __float_as_uint(a), false, false);
    a = (__uint_as_float(t2[0]) + __uint_as_float(t2[1])) * (1.f / 512.f);
    auto t3 = __builtin_amdgcn_permlane32_swap(__float_as_uint(a), __float_as_uint(a), false, false);
    const float m = __uint_as_float(t3[0]);
    const float var = rbf(fmaxf(fmaf(-m, m, __uint_as_float(t3[1])), 0.f));
    mb = rbf(m);
    rden = __builtin_amdgcn_rcpf(rbf(__builtin_amdgcn_sqrtf(rbf(var + 1e-5f))));
}
#define STR 262144
#define WS_Q 0
#define WS_H 32768
#define WS_X1 65536
#define WS_ML 81920
#define WS_MOD 98304
#define WS_MB 139264
#define WS_U 114688
#define WS_FI 131072
#define WS_FA 132096
#define WS_FB 133120
#define WS_NP 134144
#define WS_NM 134400
#define WS_EPOCH 135168
#define WS_CAL 147456
#define WS_OUTP 2097152
#define WS_PART 4194304
#define WS_END 12582912

#define LP(T, off) ((LAS T*)(size_t)(unsigned)(off))
#define STAMP(k) do {} while (0)
#define STAMP_FLUSH() do {} while (0)
#define STAMP0() do {} while (0)
__device__ __forceinline__ s16x4 tr4(unsigned a) { return __builtin_amdgcn_ds_read_tr16_b64_v4i16(LP(s16x4, a)); }
__device__ __forceinline__ bf16x8 cat8(s16x4 a, s16x4 b) { return __builtin_shufflevector(a, b, 0, 1, 2, 3, 4, 5, 6, 7); }
__device__ __forceinline__ bf16x8 tr_frag(unsigned a0, unsigned a1) { return cat8(tr4(a0), tr4(a1)); }
__device__ __forceinline__ unsigned swz(unsigned row) { return ((row & 3) << 2) | ((row >> 2) & 3); }
__device__ __forceinline__ unsigned off_b(unsigned row, unsigned ch) { return 256u * row + 16u * (ch ^ swz(row)); }
__device__ __forceinline__ unsigned tile_off(unsigned key, unsigned chunk) { return ((key >> 5) * 4 + (chunk >> 4)) * 8192u + off_b(key & 31, chunk & 15); }
__device__ __forceinline__ void wg_barrier() { asm volatile("s_waitcnt lgkmcnt(0)\n\ts_barrier" ::: "memory"); }
template <int NT> __device__ __forceinline__ void dma16(unsigned voff, const void* sbase, unsigned lds_dst) {
    if (NT) asm volatile("s_mov_b32 m0, %2\n\ts_nop 0\n\tglobal_load_lds_dwordx4 %0, %1 nt" :: "v"(voff), "s"(sbase), "s"(lds_dst) : "memory", "m0");
    else asm volatile("s_mov_b32 m0, %2\n\ts_nop 0\n\tglobal_load_lds_dwordx4 %0, %1" :: "v"(voff), "s"(sbase), "s"(lds_dst) : "memory", "m0");
}
template <int NT> __device__ __forceinline__ void dma16x(unsigned voff, unsigned vx, const void* sbase, unsigned lds_dst) {
    unsigned t;
#define DMAX(pol, c) asm volatile("s_mov_b32 m0, %3\n\tv_xor_b32 %0, %4, %1\n\tglobal_load_lds_dwordx4 %0, %2" pol : "=&v"(t) : "v"(voff), "s"(sbase), "s"(lds_dst), c(vx) : "memory", "m0")
    if (__builtin_constant_p(vx)) { if (NT) DMAX(" nt", "i"); else DMAX("", "i"); }
    else { if (NT) DMAX(" nt", "s"); else DMAX("", "s"); }
}
template <int NT> __device__ __forceinline__ void pf4(unsigned voff, const void* sbase, unsigned lds_dst) {
    if (NT) asm volatile("s_mov_b32 m0, %2\n\ts_nop 0\n\tglobal_load_lds_dword %0, %1 nt" :: "v"(voff), "s"(sbase), "s"(lds_dst) : "memory", "m0");
    else asm volatile("s_mov_b32 m0, %2\n\ts_nop 0\n\tglobal_load_lds_dword %0, %1" :: "v"(voff), "s"(sbase), "s"(lds_dst) : "memory", "m0");
}
__device__ __forceinline__ void pf4s(unsigned voff, const void* sbase, unsigned lds_dst) {
    asm volatile("s_mov_b32 m0, %2\n\ts_nop 0\n\tglobal_load_lds_dword %0, %1 sc1" :: "v"(voff), "s"(sbase), "s"(lds_dst) : "memory", "m0");
}
#define XGATE 300
#define SPINCAP (1u << 20)
constexpr unsigned kSpinCap = SPINCAP;
struct Params {
    const unsigned short* x;
    const unsigned short* ckv;
    const unsigned short* w_q;
    const unsigned short* w_uk;
    const unsigned short* w_uv;
    const unsigned short* w_o;
    const unsigned short* w1;
    const unsigned short* w2;
    const unsigned short* c;
    const unsigned short* w_mod;
    char* ws;
    unsigned short* out;
};
#define VMW(n) asm volatile("s_waitcnt vmcnt(%0)" :: "n"(n) : "memory")
#define PIN(x) asm volatile("" : "+v"(x))
#define PIN4(a, b, c, d) asm volatile("" : "+v"(a), "+v"(b), "+v"(c), "+v"(d) :: "memory")
#define MFMA16(a, b, c) __builtin_amdgcn_mfma_f32_16x16x32_bf16(a, b, c, 0, 0, 0)
#define MFMA32(a, b, c) __builtin_amdgcn_mfma_f32_32x32x16_bf16(a, b, c, 0, 0, 0)
#define BF8(x) __builtin_bit_cast(bf16x8, x)
#define ST16(v, off, soff) __builtin_amdgcn_raw_buffer_store_b128(v, rw, off, soff, 16)
#define LD16(off, soff) __builtin_amdgcn_raw_buffer_load_b128(rw, off, soff, 16)
#define SCL (0.08838834764831845f * 1.4426950408889634f)
#define BIGV 1.0e18f
#define QD 4
#define QE 4
#define L_SCR 131072
#define HS_LD 520

__global__ __launch_bounds__(512, 2) void k_main(Params p) {
    unsigned long long pc;
    asm volatile("s_getpc_b64 %0" : "=s"(pc));
    const int id = blockIdx.x, tid = threadIdx.x;
    int lane = tid & 63;
    const int wv = __builtin_amdgcn_readfirstlane(tid >> 6);
    int g = lane >> 4, nl = lane & 15;
    auto rederive = [&]() {
        asm volatile("v_mbcnt_lo_u32_b32 %0, -1, 0\n\tv_mbcnt_hi_u32_b32 %0, -1, %0" : "=v"(lane));
        g = lane >> 4; nl = lane & 15;
    };
    const int x = id & 7, j0 = id >> 3, j = JPERM && j0 < 16 ? 15 - j0 : j0;
    if (wv >= CPW && wv < CPW + 5 && j == 15) {
        asm volatile("s_mov_b32 m0, %2\n\ts_nop 0\n\tglobal_load_lds_dword %0, %1" :: "v"(min((unsigned)(((wv - CPW) * 64 + lane) * 128), (unsigned)CPFE)), "s"(pc), "s"(L_SCR + 31744) : "memory", "m0");
    }
    char* ws = p.ws;
    if (TLBA == 2) {
        if (j >= 14 && j < 16 && wv < 3 && lane < 2) {
            const unsigned short* b = j == 14 ? (wv == 0 ? (const unsigned short*)(ws + WS_CAL) : wv == 1 ? p.w_q : p.w_mod) : (wv == 0 ? p.ckv + (size_t)x * 2097152 : wv == 1 ? p.x : p.w_uk);
            pf4s((unsigned)((j == 15 && wv == 1) || (j == 14 && wv == 0) ? 0 : lane * 2097152), b, L_SCR + 31744);
        }
    }
    if ((j >> 2) == 3) {
        dma16<0>((unsigned)(((nl & 7) * 512 + (nl >> 3) * 32 + g * 8) * 2 + wv * 128), p.c, wv * 16384 + 8192);
        dma16<0>((unsigned)tid * 16u, p.x, wv * 16384 + 9216);
    }
    if (TLBA < 2 && TLBW && j == 15 && wv == 5 && lane < 2) pf4s((unsigned)((x * 2 + lane) * 2097152), p.ckv, L_SCR + 31744);
    if (TLBA == 1 && j == 14 && wv >= 4 && lane < 2) pf4s((unsigned)(lane * 2097152 + x * 128), wv == 4 ? p.w_mod : wv == 5 ? p.w_uk : wv == 6 ? p.w_q : (const unsigned short*)(ws + WS_CAL), L_SCR + 31744);
    STAMP0();
    const __amdgpu_buffer_rsrc_t rw = __builtin_amdgcn_make_buffer_rsrc(ws, 0, WS_END, 0x00020000);
    const u32x4 s4 = {SENT, SENT, SENT, SENT};
    unsigned long long cnt = 0ull;
    auto cnt_load = [&]() { asm volatile("global_load_dwordx2 %0, %1, %2 sc1" : "+v"(cnt) : "v"((unsigned)WS_EPOCH), "s"(ws) : "memory"); };
    unsigned tlo = 0u, thi = 0u;
    auto tag_take = [&]() {
        asm volatile("" : "+v"(cnt) :: "memory");
        const unsigned long long t = cnt + 1ull;
        tlo = __builtin_amdgcn_readfirstlane((unsigned)t); thi = __builtin_amdgcn_readfirstlane((unsigned)(t >> 32));
    };
    auto flag_put = [&](unsigned base, unsigned idx) {
        if (lane < 8) __builtin_amdgcn_raw_buffer_store_b64(u32x2{tlo, thi}, rw, base + lane * STR + idx * 8, 0, 16);
    };
    auto tagged_wait = [&](unsigned off) {
        u32x4 v;
#pragma unroll 1
        for (unsigned spins = 0; spins < kSpinCap; ++spins) {
            asm volatile("buffer_load_dwordx4 %0, %1, %2, 0 offen sc1\n\ts_waitcnt vmcnt(0)" : "=v"(v) : "v"(off), "s"(rw) : "memory");
            if (__all((v[2] == tlo) & (v[3] == thi))) break;
            __builtin_amdgcn_s_sleep(8);
        }
        return v;
    };
    auto flags_ok = [&](const u32x4& f) { return (f[0] == tlo) & (f[1] == thi) & (f[2] == tlo) & (f[3] == thi); };
    auto flags_wait = [&](unsigned off) {
        for (unsigned spins = 0;;) {
            u32x4 f;
            asm volatile("buffer_load_dwordx4 %0, %1, %2, 0 offen sc1\n\ts_waitcnt vmcnt(0)" : "=v"(f) : "v"(off), "s"(rw) : "memory");
            if (__all(flags_ok(f))) break;
            __builtin_amdgcn_s_sleep(16);
            if (++spins > kSpinCap) break;
            asm volatile("" ::: "memory");
        }
    };
    unsigned pcl, frl, frs;
    auto dma_piece = [&](const unsigned short* src, unsigned ldb, unsigned dst, int n, int nt) {
        const unsigned v0 = (lane >> 3) * ldb + pcl, v1 = v0 ^ 64u;
#pragma unroll
        for (int i = 0; i < n; ++i) {
            if (nt) dma16<1>((i & 1) ? v1 : v0, (const char*)src + (size_t)i * 8 * ldb, dst + i * 1024);
            else dma16<0>((i & 1) ? v1 : v0, (const char*)src + (size_t)i * 8 * ldb, dst + i * 1024);
        }
    };
    auto piece_frag = [&](unsigned base, int ks, int t) {
        const unsigned a = base + frl + ((t ^ frs) << 5) + ks * 4096;
        return tr_frag(a, a + 512);
    };
    const unsigned rgn = (unsigned)(wv * 16384);

    if (j < 16) {
        const unsigned long long t_start = __builtin_amdgcn_s_memrealtime();
        rederive();
        pcl = (unsigned)(((((lane & 7) >> 1) ^ ((lane >> 4) & 1)) * 32) + (lane & 1) * 16);
        frl = (unsigned)((g * 8 + (nl >> 2)) * 128 + (lane & 3) * 8); frs = (unsigned)((nl >> 3) | ((g & 1) << 1));
        const int th = x * 4 + (j & 3);
        if (j < 12) {
            const int qt = j >> 2, rot = qt * 5;
            const bool pub = wv < (qt < 2 ? 3 : 2);
            const unsigned qo = (unsigned)(WS_Q + (th & 7) * STR + ((nl & 7) * 4 + (th >> 3)) * 1024 + (qt * 192 + wv * 64 + g * 16 + (nl >> 3) * 8) * 2);
            const unsigned vb = (unsigned)(g * 8192 + ((nl ^ (g << 2)) << 4));
            const char* wuk = (const char*)(p.w_uk + th * 128) + (size_t)qt * 192 * 8192;
            const char* wq = (const char*)(p.w_q + th * 128) + (size_t)wv * 32768;
            const unsigned L_U = 114688;
            if (QDL) __builtin_amdgcn_s_sleep(QDL);
            {
                const int k0 = qt < 2 ? wv * 6 : wv * 4;
#pragma unroll
                for (int i = 0; i < 6; ++i) {
                    if (i < 4 || qt < 2) dma16<NTM & 2>(vb ^ (((k0 + i) & 3) << 4), wuk + (size_t)(k0 + i) * 32768, L_U + (k0 + i) * 1024);
                    if (i == 4) VMW(4);
                }
            }
            const unsigned vq = vb ^ ((wv & 3) << 4);
            auto wq_dma = [&](int ps) { dma16<NTM & 2>(vq, wq + (size_t)((ps + rot) & 15) * 262144, ps * 8192 + wv * 1024); };
            bf16x8 uk[4][4], aq[16];
            u32x4 fi, hv[4];
            unsigned fio = (unsigned)(WS_FI + x * STR + (lane & 7) * 16), hsrc, hsrc2, ta0, ta1, tb0, tb1, qo2, hw0, hw1, hr, hr2, qr0, qr1, qw, thx = 0u;
            const unsigned L_HI = L_SCR, L_QI = L_SCR + 8192;
            auto h_load = [&]() {
                asm volatile("buffer_load_dwordx4 %0, %4, %6, 0 offen sc1\n\tbuffer_load_dwordx4 %1, %4, %6, 0 offen offset:1024 sc1\n\t"
                             "buffer_load_dwordx4 %2, %5, %6, 0 offen sc1\n\tbuffer_load_dwordx4 %3, %5, %6, 0 offen offset:1024 sc1"
                             : "+v"(hv[0]), "+v"(hv[1]), "+v"(hv[2]), "+v"(hv[3]) : "v"(hsrc), "v"(hsrc2), "s"(rw) : "memory");
            };
            auto h_bad = [&]() {
                PIN4(hv[0], hv[1], hv[2], hv[3]);
                unsigned bad = (hv[0][1] ^ tlo) | (hv[0][3] ^ thx) | (hv[1][1] ^ tlo) | (hv[1][3] ^ thx) | (hv[2][1] ^ tlo) | (hv[2][3] ^ thx) | (hv[3][1] ^ tlo) | (hv[3][3] ^ thx);
                PIN(bad);
                return bad;
            };
#pragma unroll
            for (int ps = 0; ps < 16; ++ps) {
                wq_dma(ps);
                if (ps == 4) cnt_load();
                if (ps == HP) { h_load(); asm volatile("buffer_load_dwordx4 %0, %1, %2, 0 offen sc1" : "=v"(fi) : "v"(fio), "s"(rw) : "memory"); }
                if (ps >= 4 && ps < 8) VMW(5); else if (ps >= HP && ps < HP + 4) VMW(9); else VMW(4);
                if (ps == 3) {
                    asm volatile("s_barrier" ::: "memory");
                    if (pub) {
#pragma unroll
                        for (int t = 0; t < 4; ++t)
#pragma unroll
                            for (int s = 0; s < 4; ++s) uk[t][s] = *LP(bf16x8, L_U + off_b(wv * 64 + (nl >> 2) * 16 + t * 4 + (nl & 3), s * 4 + g));
                    }
                    STAMP(1);
                }
                if (ps == 5) {
                    hsrc = (unsigned)(WS_H + x * STR + wv * 2048 + lane * 16); hsrc2 = hsrc + 16384;
                    ta0 = off_b(8 * g + (nl >> 2), 2 * wv + ((lane & 3) >> 1)) + 8 * (lane & 1); ta1 = off_b(8 * g + 4 + (nl >> 2), 2 * wv + ((lane & 3) >> 1)) + 8 * (lane & 1);
                    tb0 = ta0 + 65536; tb1 = ta1 + 65536; qo2 = qo;
                    hw0 = L_HI + ((2 * wv - rot) & 15) * 512 + (lane >> 4) * 128 + (lane & 7) * 16 + ((lane >> 3) & 1) * 8;
                    hw1 = L_HI + ((2 * wv + 1 - rot) & 15) * 512 + (lane >> 4) * 128 + (lane & 7) * 16 + ((lane >> 3) & 1) * 8;
                    hr2 = L_HI + g * 128 + (nl & 7) * 16 + (nl >> 3) * 512;
                    PIN(hr2);
                    qr0 = L_QI + g * 128 + (nl & 7) * 16 + (nl >> 3) * 2048; qr1 = qr0 ^ 2048u;
                    PIN(qr0); PIN(qr1);
                    hr = L_HI + g * 128 + (nl & 7) * 16; qw = L_QI + (wv >> 1) * 512 + ((wv & 1) * 2 + (g >> 1)) * 128 + (nl & 7) * 16 + (g & 1) * 8;
                    PIN4(hsrc, hsrc2, ta0, ta1); PIN4(tb0, tb1, qo2, hw0); PIN(hw1); PIN(qw);
                }
                if (ps == 8) { tag_take(); thx = tlo ^ thi ^ TAGC; }
                if (ps == 9) {
#pragma unroll
                    for (int t = 0; t < 4; ++t) PIN4(uk[t][0], uk[t][1], uk[t][2], uk[t][3]);
                    wg_barrier();
                    *LP(unsigned, L_QI + 2048 + wv * 256 + lane * 4) = 0u;
#pragma unroll
                    for (int q = 0; q < 6; ++q) aq[q] = tr_frag(ta0 + q * 8192, ta1 + q * 8192);
                }
                if (ps == 13) {
                    asm volatile("s_barrier" ::: "memory");
#pragma unroll
                    for (int q = 6; q < 10; ++q) aq[q] = q < 8 ? tr_frag(ta0 + q * 8192, ta1 + q * 8192) : tr_frag(tb0 + (q - 8) * 8192, tb1 + (q - 8) * 8192);
                }
            }
            STAMP(15);
            if (HP + 4 > 15) VMW(HP + 4 > 15 ? 15 - HP : 0);
            if (!__all(h_bad() == 0u)) {
                for (unsigned spins = 0;;) {
                    h_load();
                    VMW(0);
                    if (__all(h_bad() == 0u)) break;
                    __builtin_amdgcn_s_sleep(SLPH);
                    if (++spins > kSpinCap) break;
                }
            }
            {
                unsigned hh[4];
#pragma unroll
                for (int q = 0; q < 4; ++q) {
                    const unsigned t = hv[2 + (q >> 1)][(q & 1) * 2], sh = hv[q >> 1][(q & 1) * 2];
                    hh[q] = pk2(lo16(t) + lo16(sh), hi16(t) + hi16(sh));
                }
                *LP(u32x2, hw0) = u32x2{hh[0], hh[1]};
                *LP(u32x2, hw1) = u32x2{hh[2], hh[3]};
            }
            STAMP(3);
            asm volatile("s_waitcnt vmcnt(0) lgkmcnt(0)\n\ts_barrier" : "+v"(fi) :: "memory");
            STAMP(2);
            {
                bf16x8 hb[8];
#pragma unroll
                for (int q = 0; q < 5; ++q) hb[q] = *LP(bf16x8, hr2 + q * 1024);
#pragma unroll
                for (int ps = 10; ps < 16; ++ps) {
                    if (!(ps & 1)) hb[ps >> 1] = *LP(bf16x8, hr2 + (ps >> 1) * 1024);
                    aq[ps] = tr_frag(tb0 + (ps - 8) * 8192, tb1 + (ps - 8) * 8192);
                }
                __builtin_amdgcn_sched_barrier(0);
                f32x4 d0 = {0.f, 0.f, 0.f, 0.f}, d1 = {0.f, 0.f, 0.f, 0.f};
#pragma unroll
                for (int ps = 0; ps < 16; ps += 2) {
                    d0 = MFMA16(aq[ps], hb[ps >> 1], d0); d1 = MFMA16(aq[ps + 1], hb[ps >> 1], d1);
                    if (ps == 6) { unsigned bad = (fi[0] ^ tlo) | (fi[1] ^ thi) | (fi[2] ^ tlo) | (fi[3] ^ thi); PIN(bad); if (!__all(bad == 0u)) flags_wait(fio); }
                }
#pragma unroll
                for (int e = 0; e < 4; ++e) { float v = d0[e] + dppf<0x128>(d1[e]); asm("" : "+v"(v)); d0[e] = v; }
                if (nl < 8) *LP(u32x2, qw) = u32x2{pk2(d0[0], d0[1]), pk2(d0[2], d0[3])};
            }
            wg_barrier();
            if (pub) {
                bf16x8 qb[2][4];
#pragma unroll
                for (int s = 0; s < 4; ++s) { qb[0][s] = *LP(bf16x8, qr0 + s * 512); qb[1][s] = *LP(bf16x8, qr1 + s * 512); }
                __builtin_amdgcn_sched_barrier(0);
                f32x4 a = {0.f, 0.f, 0.f, 0.f}, b = a;
#pragma unroll
                for (int s = 0; s < 4; ++s) {
                    a = MFMA16(uk[0][s], qb[0][s], a); b = MFMA16(uk[1][s], qb[0][s], b);
                    a = MFMA16(uk[2][s], qb[1][s], a); b = MFMA16(uk[3][s], qb[1][s], b);
                }
                const u32x4 qv = {pk2s(a[0], a[1]), pk2s(a[2], a[3]), pk2s(b[0], b[1]), pk2s(b[2], b[3])};
                ST16(qv, qo2, 0);
            }
            STAMP(4);
            wg_barrier();
            if (LMPF && wv >= 3 && wv < 6) {
                const unsigned n = (unsigned)(j * 3 + wv - 3);
                if (n < 32) pf4<0>(((n & 7u) * 64 + lane) * 6144u + 2048u + (x * 4 + (n >> 3)) * 128u, p.w_mod, L_SCR + 31744);
            }
            if (QHELP && wv < 3 && j + 12 * wv < 32) {
                const int hh = j + 12 * wv;
#pragma unroll 1
                for (unsigned spins = 0; spins < 64; ++spins) {
                    __builtin_amdgcn_s_sleep(QHELP);
                    const u32x4 v = LD16((unsigned)(WS_Q + (hh & 7) * STR + (x * 4 + (hh >> 3)) * 1024 + lane * 16), 0);
                    if (__all(mx4(v, 0u) != SENT)) break;
                    asm volatile("" ::: "memory");
                }
                STAMP(6);
            }
        } else {
            const int cg = x * 2 + (j & 1);
            if (j < 14) {
                dma_piece(p.w_mod + (size_t)wv * 64 * 3072 + cg * 64, 6144, rgn, 8, NTM & 2);
                cnt_load();
                VMW(9);
            } else {
                cnt_load();
                ST16(s4, (unsigned)(WS_Q + wv * STR + cg * 2048 + lane * 16), 0); ST16(s4, (unsigned)(WS_Q + wv * STR + cg * 2048 + lane * 16), 1024);
                VMW(0);
                tag_take();
                wg_barrier();
                if (wv == 7) flag_put(WS_FI, cg);
                STAMP(15);
                if (j == 14) {
                    u32x2 best = {~0u, ~0u};
#pragma unroll
                    for (int r = 0; r < 2 * NCAL; ++r) {
                        const unsigned hf = (r ^ wv) & 1;
                        VMW(0);
                        const unsigned long long t0 = __builtin_amdgcn_s_memtime();
                        if (lane < 8) ST16(s4, WS_CAL + x * STR + hf * 8192 + (wv * 8 + r) * 128 + lane * 16, 0);
                        VMW(0);
                        const unsigned dt = (unsigned)(__builtin_amdgcn_s_memtime() - t0);
                        if (hf) best[1] = min(best[1], dt); else best[0] = min(best[0], dt);
                    }
                    *LP(u32x2, 16384 * 7 + 14336 + wv * 8) = best;
                    wg_barrier();
                    if (wv == 0 && lane < 8) {
                        u32x2 m = *LP(u32x2, 16384 * 7 + 14336 + lane * 8);
#pragma unroll
                        for (int o = 1; o < 8; o <<= 1) { m[0] = min(m[0], (unsigned)__shfl_xor(m[0], o)); m[1] = min(m[1], (unsigned)__shfl_xor(m[1], o)); }
                        ST16((u32x4{(unsigned)max(-1000, min(1000, (int)m[1] - (int)m[0])), 0u, tlo, thi}), (unsigned)(WS_NP + lane * STR + x * 16), 0);
                    }
                    if (wv == 0) {
                        const int d = (int)tagged_wait(WS_NP + x * STR + (lane & 7) * 16)[0];
                        int t = (lane & 4) ? -d : d;
                        t += __builtin_amdgcn_update_dpp(0, t, 0xB1, 0xf, 0xf, true);
                        t += __builtin_amdgcn_update_dpp(0, t, 0x4E, 0xf, 0xf, true);
                        t += __builtin_amdgcn_update_dpp(0, t, 0x141, 0xf, 0xf, true);
                        const bool n1 = abs(d) > 160 ? d < 0 : (t < 0) != ((lane & 4) != 0);
                        const unsigned mk = NEARP ? (unsigned)__ballot(n1) & 0xffu : 0u;
                        if (lane == 0) ST16((u32x4{mk, 0u, tlo, thi}), (unsigned)(WS_NM + x * STR), 0);
                    }
                    STAMP(9);
                }
            }
            const u16x8 cf = *LP(u16x8, rgn + 8192 + lane * 16), xa = *LP(u16x8, rgn + 9216 + lane * 16);
            u32x4 af[2];
#pragma unroll
            for (int q = 0; q < 4; ++q) {
                const float a = bf2f(cf[2 * q]), b = bf2f(cf[2 * q + 1]);
                af[0][q] = pk2(a * __builtin_amdgcn_rcpf(1.f + __expf(-a)), b * __builtin_amdgcn_rcpf(1.f + __expf(-b)));
                af[1][q] = (unsigned)__builtin_amdgcn_update_dpp(0, (int)af[0][q], 0x128, 0xf, 0xf, true);
            }
            const unsigned L_MS = L_SCR;
            unsigned wa[4], fr[4];
#pragma unroll
            for (int t = 0; t < 4; ++t) { wa[t] = L_MS + wv * 2048 + (nl & 7) * 256 + (((t * 4 + g) ^ (nl & 7)) << 4) + (nl >> 3) * 16384; fr[t] = rgn + frl + ((t ^ frs) << 5); }
            PIN4(wa[0], wa[1], wa[2], wa[3]); PIN4(fr[0], fr[1], fr[2], fr[3]); PIN(af[0]); PIN(af[1]);
            const unsigned L_XN = 12288;
            auto mod_mm = [&](bool ln) {
                VMW(0);
                bf16x8 f[2][4];
#pragma unroll
                for (int ks = 0; ks < 2; ++ks)
#pragma unroll
                    for (int t = 0; t < 4; ++t) f[ks][t] = tr_frag(fr[t] + ks * 4096, fr[t] + ks * 4096 + 512);
                __builtin_amdgcn_sched_barrier(0);
                f32x4 d[4];
#pragma unroll
                for (int t = 0; t < 4; ++t) d[t] = MFMA16(f[0][t], BF8(af[0]), (f32x4{0.f, 0.f, 0.f, 0.f}));
#pragma unroll
                for (int t = 0; t < 4; ++t) d[t] = MFMA16(f[1][t], BF8(af[1]), d[t]);
#pragma unroll
                for (int t = 0; t < 4; ++t) *LP(f32x4, wa[t]) = d[t];
                if (ln) {
                    float v[8];
#pragma unroll
                    for (int q = 0; q < 8; ++q) v[q] = bf2f(xa[q]);
                    const float p1 = ((v[0] + v[1]) + (v[2] + v[3])) + ((v[4] + v[5]) + (v[6] + v[7]));
                    const float p2 = ((v[0] * v[0] + v[1] * v[1]) + (v[2] * v[2] + v[3] * v[3])) + ((v[4] * v[4] + v[5] * v[5]) + (v[6] * v[6] + v[7] * v[7]));
                    float mb, rden;
                    ln_stats(p1, p2, mb, rden);
                    u32x4 r;
#pragma unroll
                    for (int q = 0; q < 4; ++q) { const unsigned d = pk2(v[2 * q] - mb, v[2 * q + 1] - mb); r[q] = pk2(lo16(d) * rden, hi16(d) * rden); }
                    *LP(u32x4, L_XN + wv * 16384 + lane * 16) = r;
                }
                wg_barrier();
            };
            auto sum8 = [&](unsigned a) {
                f32x4 u[8];
#pragma unroll
                for (int w2 = 0; w2 < 8; ++w2) u[w2] = *LP(f32x4, a + w2 * 2048);
                __builtin_amdgcn_sched_barrier(0);
                return ((u[0] + u[1]) + (u[2] + u[3])) + ((u[4] + u[5]) + (u[6] + u[7]));
            };
            if (j < 14) {
                const int hb = lane & 7, c4 = 2 * wv + (nl >> 3);
                unsigned hoff = (unsigned)(WS_H + g * STR + cg * 2048 + wv * 256 + nl * 16);
                unsigned sa = L_MS + hb * 256 + ((c4 ^ hb) << 4);
                unsigned xa2 = L_XN + hb * 16384 + (cg & 7) * 128 + c4 * 8;
                PIN(hoff); PIN(sa); PIN(xa2);
                STAMP(1);
                mod_mm(cg >= 8);
                STAMP(2);
                const u32x2 xn = *LP(u32x2, xa2);
                const f32x4 v = sum8(sa);
                tag_take();
                const unsigned thx = tlo ^ thi ^ TAGC;
                u32x2 r = {pk2(v[0], v[1]), pk2(v[2], v[3])};
                if (cg >= 8) {
#pragma unroll
                    for (int q = 0; q < 2; ++q) { const unsigned sc = pk2(1.f + lo16(r[q]), 1.f + hi16(r[q])); r[q] = pk2(lo16(xn[q]) * lo16(sc), hi16(xn[q]) * hi16(sc)); }
                }
                ST16((u32x4{r[0], tlo, r[1], thx}), hoff, 0);
                ST16((u32x4{r[0], tlo, r[1], thx}), hoff, 4 * STR);
                STAMP(5);
            }
            wg_barrier();
            while (__builtin_amdgcn_s_memrealtime() < t_start + LMD) __builtin_amdgcn_s_sleep(2);
            const int lg = x * 4 + (j & 3);
            dma_piece(p.w_mod + (size_t)wv * 64 * 3072 + 1024 + lg * 64, 6144, rgn, 8, NTM & 4);
            mod_mm(false);
            if (lg >= 8 && lg < 24) {
                if (wv == 0) {
                    const int b = lane >> 3, c8 = lane & 7;
                    const f32x4 v0 = sum8(L_MS + b * 256 + (((2 * c8) ^ b) << 4)), v1 = sum8(L_MS + b * 256 + (((2 * c8 + 1) ^ b) << 4));
                    ST16((u32x4{pk2(v0[0], v0[1]), pk2(v0[2], v0[3]), pk2(v1[0], v1[1]), pk2(v1[2], v1[3])}), (unsigned)(WS_MB + b * STR + (lg - 8) * 128 + c8 * 16), 0);
                }
            } else if (wv < 4) {
                const int b = wv * 2 + (lane >> 5), cp = lane & 31;
                f32x2_t u[8];
#pragma unroll
                for (int w2 = 0; w2 < 8; ++w2) u[w2] = *LP(f32x2_t, L_MS + w2 * 2048 + b * 256 + (((cp >> 1) ^ b) << 4) + (cp & 1) * 8);
                const f32x2_t v = ((u[0] + u[1]) + (u[2] + u[3])) + ((u[4] + u[5]) + (u[6] + u[7]));
                ST16((u32x4{__float_as_uint(rbf(v[0])), __float_as_uint(rbf(v[1])), tlo, thi}), (unsigned)(WS_MOD + b * STR + (lg * 32 + cp) * 16), 0);
            }
            STAMP(6);
        }
        const int bq = (j >> 2) & 1, dh = j >> 3, src = th * 2 + dh, rb = bq * 4 + (src >> 4), rc = src & 15;
        wg_barrier();
        if (TLBW > 1 && j == 0) {
            if (wv == 4 && lane < 5) pf4s((unsigned)((lane + 1) * 2097152 + 8064), ws, L_SCR + 31744);
            if (wv == 5 && lane < 2) { pf4s((unsigned)(lane * 2097152), p.w_uv, L_SCR + 31744); pf4s((unsigned)(lane * 2097152), p.w_o, L_SCR + 31744); }
            if (wv == 6 && lane < 1) { pf4s(0u, p.w1, L_SCR + 31744); pf4s(0u, p.w2, L_SCR + 31744); pf4s(0u, p.out, L_SCR + 31744); }
        }
        while (__builtin_amdgcn_s_memrealtime() < t_start + TWD - 110) __builtin_amdgcn_s_sleep(2);
        const unsigned npm = __builtin_amdgcn_readfirstlane(tagged_wait(WS_NM + x * STR)[0]), np = (npm >> x) & 1u;
        unsigned oo = (unsigned)(WS_OUTP + ((bq * 4 + (nl & 3)) * 16 + wv * 2 + (nl >> 3)) * 16384 + (src ^ wv) * 128 + ((nl >> 2) & 1) * 64 + g * 16) + ((~npm >> ((nl & 3) * 2 + (wv >> 2))) & 1u) * 8192u;
        const int rcol = rc * 32 + (lane & 7) * 4 + (lane >> 5) * 2;
        unsigned xdst = (unsigned)(WS_X1 + ((lane >> 3) & 3) * STR + (rb * 512 + rcol) * 2), xdst2 = xdst + 4 * STR + ((npm >> (4 + ((lane >> 3) & 3))) & 1u) * 8192u;
        xdst += ((npm >> ((lane >> 3) & 3)) & 1u) * 8192u;
        ST16(s4, oo, 0);
        if (wv == 7) { __builtin_amdgcn_raw_buffer_store_b32(SENT, rw, xdst, 0, 16); __builtin_amdgcn_raw_buffer_store_b32(SENT, rw, xdst2, 0, 16); }
        VMW(0);
        wg_barrier();
        if (wv == 0) flag_put(WS_FB, id);
        while (__builtin_amdgcn_s_memrealtime() < t_start + TWD) __builtin_amdgcn_s_sleep(2);
        asm volatile("s_barrier" ::: "memory");
        STAMP(7);
        if ((PFM & 8) && wv == 0 && lane < 32) pf4<NTM & 8>((unsigned)((j * 32 + lane) * 8192 + x * 1024 + ((3u - ((unsigned)(size_t)p.w_uv >> 7)) & 7u) * 128), p.w_uv, L_SCR + 31744);
        if (bq) dma_piece(p.w_o + (size_t)(th * 128 + dh * 64) * 512 + wv * 64, 1024, rgn + 8192, 8, NTM & 8);
        dma_piece(p.w_uv + ((size_t)wv * 64 * NH + th) * 128 + dh * 64, 8192, rgn, 8, NTM & 8);
        if (!bq) dma_piece(p.w_o + (size_t)(th * 128 + dh * 64) * 512 + wv * 64, 1024, rgn + 8192, 8, NTM & 8);
        u32x4 fb, gm;
        unsigned xg;
        VMW(4);
        if (wv == 7)
            asm volatile("buffer_load_dwordx4 %0, %3, %5, 0 offen sc1\n\tbuffer_load_dwordx4 %1, %4, %5, 0 offen sc1\n\tglobal_load_dword %2, %6, %7" : "=&v"(fb), "=&v"(gm), "=&v"(xg)
                         : "v"((unsigned)(WS_FB + x * STR + lane * 16)), "v"((unsigned)(WS_MOD + rb * STR + rcol * 8)), "s"(rw), "v"((unsigned)(rb * 512 + rcol) * 2u), "s"(p.x) : "memory");
        VMW(0);
        asm volatile("" : "+v"(fb), "+v"(gm), "+v"(xg));
        STAMP(8);
        bf16x8 fuv[2][4], fwo[2][4];
        unsigned pfa[4];
#pragma unroll
        for (int t = 0; t < 4; ++t) { pfa[t] = rgn + frl + ((t ^ frs) << 5); PIN(pfa[t]); }
#pragma unroll
        for (int ks = 0; ks < 2; ++ks)
#pragma unroll
            for (int t = 0; t < 4; ++t) {
                const unsigned a = rgn + (unsigned)((g * 4 + (nl >> 2)) * 128 + (lane & 3) * 8) + ((t ^ (unsigned)((nl >> 3) | (g & 2))) << 5) + ks * 4096;
                fuv[ks][t] = tr_frag(a, a + 2048);
                fwo[ks][t] = tr_frag(pfa[t] + 8192 + ks * 4096, pfa[t] + 8192 + ks * 4096 + 512);
            }
#pragma unroll
        for (int ks = 0; ks < 2; ++ks) { PIN4(fuv[ks][0], fuv[ks][1], fuv[ks][2], fuv[ks][3]); PIN4(fwo[ks][0], fwo[ks][1], fwo[ks][2], fwo[ks][3]); }
        const unsigned L_WGT = L_SCR, L_MSK = L_SCR + 256, L_RED = L_SCR + 1024;
        unsigned psrc = (unsigned)(WS_PART + (bq * 4 * 32 + th) * 32768 + (wv >> 2) * 16384 + (wv & 3) * 2048 + ((lane >> 3) ^ (th & 7)) * 128) + np * 8192u + pcl, psrc1 = (psrc ^ 64u) + 1024;
        unsigned psrc2 = psrc ^ 8192u, psrc3 = psrc1 ^ 8192u;
        PIN(psrc2); PIN(psrc3); PIN(xdst2);
        unsigned rw2 = L_RED + wv * 1024 + (nl & 3) * 256 + g * 16 + (YSEL ? (nl >> 2) * 64 : 0);
        unsigned wa0 = (nl & 3) == (g >> 1) ? L_WGT + (HILO ? nl & 4 : 0) * 32 + g * 16 : L_WGT + 272, wa1 = (nl & 3) == 2 + (g >> 1) ? L_WGT + (HILO ? nl & 4 : 0) * 32 + 64 + g * 16 : L_WGT + 272;
        unsigned cr = rgn + (nl & 3) * 128 + g * 16;
        PIN(cr);
        unsigned r2r = L_RED + lane * 16, cw = rgn + lane * 8;
        unsigned osrc = (unsigned)(WS_OUTP + (rb * 16 + rc) * 16384 + lane * 16) + (np ^ 1u) * 8192u;
        PIN(psrc); PIN(psrc1); PIN(rw2); PIN(wa0); PIN(wa1); PIN(r2r); PIN4(cw, oo, osrc, xdst);
        float mlm = 0.f, mll = 0.f;
        if (wv == 0) *LP(unsigned, L_WGT + 256 + lane * 4) = 0u;
        wg_barrier();
        const bool early = __all(flags_ok(fb) & (gm[2] == tlo) & (gm[3] == thi));
        u32x2 ga = {gm[0], gm[1]};
        PIN(ga);
        bf16x8 pa[2][4], wh[2];
        {
            u32x4 ml = {0u, 0u, ~tlo, ~thi};
            const unsigned mlsrc = (unsigned)(WS_ML + (bq * 4 + g) * STR + nl * 512 + th * 16) + ((npm >> (x & 6)) & 1u) * 8192u;
            unsigned long long done = 0ull;
            unsigned lm = L_MSK;
            PIN(lm);
#pragma unroll 1
            for (unsigned spins = 0; spins < kSpinCap; ++spins) {
                unsigned long long m;
                if (wv == 0) {
                    if ((ml[2] != tlo) | (ml[3] != thi)) ml = LD16(mlsrc, 0);
                    m = __ballot((ml[2] == tlo) & (ml[3] == thi));
                    unsigned long long f = m & (m >> 4);
                    f &= f >> 2; f &= f >> 1;
                    asm volatile("ds_write_b64 %0, %1" :: "v"(lm), "v"(f) : "memory");
                    m = m == ~0ull ? 0x0101010101010101ull : 0ull;
                } else {
                    unsigned long long t;
                    asm volatile("ds_read_b64 %0, %1\n\ts_waitcnt lgkmcnt(0)" : "=v"(t) : "v"(lm) : "memory");
                    m = ((unsigned long long)(unsigned)__builtin_amdgcn_readfirstlane((unsigned)(t >> 32)) << 32) | (unsigned)__builtin_amdgcn_readfirstlane((unsigned)t);
                    m &= 0x0101010101010101ull & ~done;
                }
                if (m) {
                    done |= m;
#define PLD(bit, off, v, base) "s_bitcmp1_b64 %0, " #bit "\n\ts_cbranch_scc0 1f\n\ts_add_u32 m0, %1, " #off "\n\ts_nop 0\n\tglobal_load_lds_dwordx4 " v ", " base " sc1\n1:\n\t"
                    asm volatile(PLD(0, 0, "%2", "%4") PLD(8, 1024, "%3", "%4") PLD(16, 2048, "%2", "%5") PLD(24, 3072, "%3", "%5")
                                 PLD(32, 4096, "%8", "%6") PLD(40, 5120, "%9", "%6") PLD(48, 6144, "%8", "%7") PLD(56, 7168, "%9", "%7")
                                 :: "s"(m), "s"(rgn), "v"(psrc), "v"(psrc1), "s"(ws), "s"(ws + 1048576), "s"(ws + 2097152), "s"(ws + 3145728), "v"(psrc2), "v"(psrc3) : "memory", "m0", "scc");
                    if (done == 0x0101010101010101ull) break;
                } else if (SLPM > 0 && wv == 0) __builtin_amdgcn_s_sleep(SLPM);
            }
            mlm = __uint_as_float(ml[0]); mll = __uint_as_float(ml[1]);
            STAMP(10);
            if (wv == 0) {
                float M = mlm;
                M = fmaxf(M, dppf<0xB1>(M)); M = fmaxf(M, dppf<0x4E>(M)); M = fmaxf(M, dppf<0x141>(M)); M = fmaxf(M, dppf<0x140>(M));
                const float e = __builtin_amdgcn_exp2f(mlm - M);
                const float w = e * __builtin_amdgcn_rcpf(sum16(e * mll));
                const unsigned short hi = f2bf(w);
                *LP(unsigned short, L_WGT + lane * 2) = hi;
                if (HILO) *LP(unsigned short, L_WGT + 128 + lane * 2) = f2bf(w - bf2f(hi));
            }
            wg_barrier();
            wh[0] = *LP(bf16x8, wa0); wh[1] = *LP(bf16x8, wa1);
            __builtin_amdgcn_sched_barrier(0);
            VMW(0);
#pragma unroll
            for (int ks = 0; ks < 2; ++ks)
#pragma unroll
                for (int t = 0; t < 4; ++t) pa[ks][t] = tr_frag(pfa[t] + ks * 4096, pfa[t] + ks * 4096 + 512);
        }
        STAMP(11);
        {
            f32x4 d[4];
#pragma unroll
            for (int t = 0; t < 4; ++t) d[t] = MFMA16(pa[0][t], wh[0], (f32x4{0.f, 0.f, 0.f, 0.f}));
#pragma unroll
            for (int t = 0; t < 4; ++t) d[t] = MFMA16(pa[1][t], wh[1], d[t]);
#pragma unroll
            for (int t = 0; t < 4; ++t)
#pragma unroll
                for (int e = 0; e < 4; ++e) if (HILO) { float v = d[t][e] + dppf<0x124>(d[t][e]); asm("" : "+v"(v)); d[t][e] = v; }
            const u32x4 o0 = {pk2(d[0][0], d[0][1]), pk2(d[0][2], d[0][3]), pk2(d[1][0], d[1][1]), pk2(d[1][2], d[1][3])};
            const u32x4 o1 = {pk2(d[2][0], d[2][1]), pk2(d[2][2], d[2][3]), pk2(d[3][0], d[3][1]), pk2(d[3][2], d[3][3])};
            f32x4 y[4];
#pragma unroll
            for (int t = 0; t < 4; ++t) y[t] = MFMA16(fuv[0][t], BF8(o0), (f32x4{0.f, 0.f, 0.f, 0.f}));
#pragma unroll
            for (int t = 0; t < 4; ++t) y[t] = MFMA16(fuv[1][t], BF8(o1), y[t]);
            if (YSEL) *LP(f32x4, rw2) = (nl & 8) ? ((nl & 4) ? y[3] : y[2]) : ((nl & 4) ? y[1] : y[0]);
            else if (nl < 4) {
#pragma unroll
                for (int t = 0; t < 4; ++t) *LP(f32x4, rw2 + t * 64) = y[t];
            }
        }
        wg_barrier();
        {
            f32x4 cs[8];
#pragma unroll
            for (int w2 = 0; w2 < 8; ++w2) cs[w2] = *LP(f32x4, r2r + w2 * 1024);
            const f32x4 c4 = ((cs[0] + cs[1]) + (cs[2] + cs[3])) + ((cs[4] + cs[5]) + (cs[6] + cs[7]));
            *LP(u32x2, cw) = u32x2{pk2(c4[0], c4[1]), pk2(c4[2], c4[3])};
            asm volatile("s_waitcnt lgkmcnt(0)" ::: "memory");
            const u32x4 cx0 = *LP(u32x4, cr), cx1 = *LP(u32x4, cr + 64);
            f32x4 o[4];
#pragma unroll
            for (int q = 0; q < 4; ++q) o[q] = MFMA16(fwo[0][q], BF8(cx0), (f32x4{0.f, 0.f, 0.f, 0.f}));
#pragma unroll
            for (int q = 0; q < 4; ++q) o[q] = MFMA16(fwo[1][q], BF8(cx1), o[q]);
            const f32x4 oa = (nl & 8) ? ((nl & 4) ? o[3] : o[2]) : ((nl & 4) ? o[1] : o[0]);
            const u32x4 sv = {nosent(__float_as_uint(oa[0])), nosent(__float_as_uint(oa[1])), nosent(__float_as_uint(oa[2])), nosent(__float_as_uint(oa[3]))};
            ST16(sv, oo, 0);
        }
        STAMP(12);
        wg_barrier();
        if (wv == 7) {
            if (!early) {
                flags_wait(WS_FB + x * STR + lane * 16);
                { const u32x4 v = tagged_wait(WS_MOD + rb * STR + rcol * 8); ga = u32x2{v[0], v[1]}; }
                PIN(ga);
            }
            u32x4 op[8];
            for (unsigned spins = 0;;) {
#pragma unroll
                for (int k = 0; k < 8; ++k) op[k] = LD16(osrc + (k & 3) * 1024, (k >> 2) * 4096);
                unsigned m = 0u;
#pragma unroll
                for (int k = 0; k < 8; ++k) m = mx4(op[k], m);
                if (__all(m != SENT)) break;
                if (++spins > kSpinCap) break;
                asm volatile("" ::: "memory");
            }
            STAMP(13);
            const f32x4 v = ((__builtin_bit_cast(f32x4, op[0]) + __builtin_bit_cast(f32x4, op[1])) + (__builtin_bit_cast(f32x4, op[2]) + __builtin_bit_cast(f32x4, op[3]))) +
                            ((__builtin_bit_cast(f32x4, op[4]) + __builtin_bit_cast(f32x4, op[5])) + (__builtin_bit_cast(f32x4, op[6]) + __builtin_bit_cast(f32x4, op[7])));
            float w[2];
#pragma unroll
            for (int q = 0; q < 2; ++q) {
                auto t = __builtin_amdgcn_permlane32_swap(__float_as_uint(v[q]), __float_as_uint(v[2 + q]), false, false);
                w[q] = __uint_as_float(t[0]) + __uint_as_float(t[1]);
                auto u = __builtin_amdgcn_permlane16_swap(__float_as_uint(w[q]), __float_as_uint(w[q]), false, false);
                w[q] = __uint_as_float(u[0]) + __uint_as_float(u[1]);
                w[q] += dppf<0x128>(w[q]);
            }
            unsigned t = pk2(w[0], w[1]);
            t = pk2(__uint_as_float(ga[0]) * lo16(t), __uint_as_float(ga[1]) * hi16(t));
            t = pk2s(lo16(xg) + lo16(t), hi16(xg) + hi16(t));
            __builtin_amdgcn_raw_buffer_store_b32(t, rw, xdst, 0, 16);
            __builtin_amdgcn_raw_buffer_store_b32(t, rw, xdst2, 0, 16);
            STAMP(14);
        }
        STAMP_FLUSH();
        return;
    }

    const int s = j - 16;
    const unsigned long long t_s = __builtin_amdgcn_s_memrealtime();
    if (!CNL && wv == 0) cnt_load();
    {
        const unsigned ln = (unsigned)(s * 64 + lane);
        if ((PFM & 1) && wv == 0) pf4<NTM & 1>((ln & 511u) * 6144u + (x * 2 + (ln >> 9)) * 128u, p.w_mod, L_SCR);
        if ((PFM & 2) && wv == 1) {
            const unsigned short* w = s < 8 ? p.w_uk : p.w_q;
            pf4<NTM & 1>((ln & 511u) * 8192u + x * 1024u + ((3u - ((unsigned)(size_t)w >> 7)) & 7u) * 128u, w, L_SCR);
        }
        if (SD) __builtin_amdgcn_s_sleep(SD);
        if ((PFM & 48) == 48) {
            const unsigned r0 = (unsigned)(s * 4 + (wv & 3)), row = PFS == 0 ? r0 * 8 + (lane >> 3) : PFS == 1 ? (lane >> 3) * 64 + r0 : (r0 >> 5) * 256 + (lane >> 3) * 32 + (r0 & 31);
            pf4<NTM & 1>(row * 8192u + x * 1024u + (lane & 7u) * 128u, wv < 4 ? p.w_uk : p.w_q, L_SCR);
        }
        else if ((PFM & 16) && wv >= 4) { const unsigned l2 = (unsigned)((s * 4 + (wv & 3)) * 64 + lane); pf4<NTM & 1>((l2 >> 3) * 8192u + x * 1024u + (l2 & 7u) * 128u, p.w_q, L_SCR); }
        else if ((PFM & 32) && wv >= 4) { const unsigned l2 = (unsigned)((s * 4 + (wv & 3)) * 64 + lane); pf4<NTM & 1>((l2 >> 3) * 8192u + x * 1024u + (l2 & 7u) * 128u, p.w_uk, L_SCR); }
        if ((PFM & 4) && (wv & 6) == 2) { const unsigned l2 = (unsigned)((s * 2 + (wv & 1)) * 64 + lane); pf4<NTM & 1>((l2 & 511u) * 8192u + (x * 4 + (l2 >> 9)) * 256u, p.w_q, L_SCR); }
    }
    const int ug = x * 7 + s;
    const unsigned vb = (unsigned)(g * 1024 + ((nl ^ (g << 2)) << 4)), lb = (unsigned)((wv >> 1) * 32768 + (wv & 1) * 4096);
    const char* ck = (const char*)p.ckv + ((size_t)x * SK + s * 256 + wv * 16) * 1024;
    const unsigned rot = ROTC ? (unsigned)s & 3u : 0u;
    const unsigned vbr = vb ^ (rot << 8);
    const char* ck2 = ck + 131072;
    auto ck_dma = [&](unsigned i) { dma16x<NTM & 16>(vbr, ((i & 3u) << 4) | (((i >> 2) & 3u) << 8) | ((i & 3u) << 12), (i >> 4) ? ck2 : ck, lb + ((i >> 2) & 3u) * 8192u + (i & 3u) * 1024u); };
    bf16x8 ka[16];
    s16x4 va[8][4];
    unsigned kad[4], vad[4], vad2[4];
    auto kq = [](unsigned u) { return (0x78u >> (2 * u)) & 3u; };
    auto ka_reads = [&](int cb) {
#pragma unroll
        for (int u = 0; u < 4; ++u) ka[cb * 4 + u] = *LP(bf16x8, kad[u] + cb * 8192);
    };
    auto va_reads = [&]() {
#pragma unroll
        for (int kk = 0; kk < 8; ++kk)
#pragma unroll
            for (int q = 0; q < 4; ++q) va[kk][q] = tr4((kk < 4 ? vad[q] : vad2[q]) + ((kk >> 1) & 1) * 32768 + (kk & 1) * 4096);
    };
    auto va_read1 = [&](int kk) {
#pragma unroll
        for (int q = 0; q < 4; ++q) va[kk][q] = tr4((kk < 4 ? vad[q] : vad2[q]) + ((kk >> 1) & 1) * 32768 + (kk & 1) * 4096);
    };
    auto va_pin = [&]() {
#pragma unroll
        for (int q = 0; q < 8; ++q) PIN4(va[q][0], va[q][1], va[q][2], va[q][3]);
    };
    u32x4 fa = {0u, 0u, 0u, 0u};
    const unsigned fao = (unsigned)(WS_FI + x * STR + (lane & 7) * 16);
    constexpr int LAST0 = GI + NPOST, LAST = LAST0 + NP2;
    static_assert(GI - WG >= 15 && GI < 20 && LAST >= 19 && LAST < 28, "");
    const unsigned qsrc = (unsigned)(WS_Q + (wv & 1) * 4 * STR + (x * 4 + (wv >> 1)) * 1024) + (g ^ rot) * 256 + ((nl ^ ((wv * 4) & 15)) << 4);
    auto q_dma = [&](int k) {
        unsigned t;
        asm volatile("s_mov_b32 m0, %3\n\tv_xor_b32 %0, %4, %1\n\tglobal_load_lds_dwordx4 %0, %2 sc1" : "=&v"(t) : "v"(qsrc), "s"(ws), "s"(L_SCR + (wv * 4 + k) * 1024), "i"(k * (STR + 16)) : "memory", "m0");
    };
#pragma unroll
    for (int i = 0; i <= GI; ++i) {
        ck_dma(i);
        if (CNL && i == 5 && wv == 0) cnt_load();
        if (i == 3) {
#pragma unroll
            for (int u = 0; u < 4; ++u) {
                kad[u] = tile_off(wv * 16 + kq(nl >> 2) * 4 + (nl & 3), 4 * u + g);
                vad[u] = tile_off(8 * (lane >> 5) + 4 * (u & 1) + (nl >> 2), wv * 8 + 4 * (u >> 1) + 2 * (g & 1) + ((lane & 3) >> 1)) + 8 * (lane & 1);
                vad2[u] = vad[u] + 65536;
                PIN(kad[u]); PIN(vad[u]); PIN(vad2[u]);
            }
        }
        if (i >= 4) VMW(4);
        if (i == 15 && wv == 0) {
            PIN(fa);
            tag_take();
            if (!__all(flags_ok(fa))) flags_wait(fao);
        }
        if ((i & 3) == 3 && i >= 7 && i <= 15) {
            const int cb = (i - 7) >> 2;
            if (cb > 0) { PIN4(ka[cb * 4 - 4], ka[cb * 4 - 3], ka[cb * 4 - 2], ka[cb * 4 - 1]); va_pin(); }
            wg_barrier();
            ka_reads(cb);
            if ((wv >> 1) == cb) va_reads();
            STAMP(16 + cb);
        }
        if (i == 10 && wv == 0) asm volatile("buffer_load_dwordx4 %0, %1, %2, 0 offen sc1" : "+v"(fa) : "v"(fao), "s"(rw) : "memory");
    }
    PIN4(ka[8], ka[9], ka[10], ka[11]); va_pin();
    while (__builtin_amdgcn_s_memrealtime() < t_s + TG) __builtin_amdgcn_s_sleep(1);
    VMW(WG);
    q_dma(0); q_dma(1); q_dma(2); q_dma(3);
    asm volatile("s_barrier" ::: "memory");
    STAMP(19);
#pragma unroll
    for (int i = GI + 1; i <= LAST0; ++i) ck_dma(i);
    ka_reads(3);
    if ((wv >> 1) == 3) va_reads();
    VMW(NPOST);
    const unsigned L_EXM = L_SCR - 1024, L_BIG = L_SCR + 512 + 128;
    unsigned nx = LAST + 1, npm = 0u;
    unsigned qad[4], retried = 0u;
    {
        unsigned qck = L_SCR + wv * 4096 + lane * 16;
        u32x4 qb[4];
#pragma unroll
        for (int k = 0; k < 4; ++k) qb[k] = *LP(u32x4, qck + k * 1024);
#pragma unroll
        for (int i = LAST0 + 1; i <= LAST; ++i) ck_dma(i);
#pragma unroll
        for (int k = 0; k < 4; ++k) { qad[k] = L_SCR + nl * 1024 + (((4 * k + g) ^ nl) << 4); PIN(qad[k]); }
        STAMP(20);
        if (!__all(mx4(qb[0], mx4(qb[1], mx4(qb[2], mx4(qb[3], 0u)))) != SENT)) {
            retried = 1u;
            for (unsigned spins = 0;;) {
#pragma unroll
                for (int k = 0; k < 4; ++k)
                    if (!__all(mx4(qb[k], 0u) != SENT)) q_dma(k);
                if (nx <= 25) {
#pragma unroll
                    for (int i = 0; i < 3; ++i) ck_dma(nx + i);
                    nx += 3;
                    VMW(3);
                } else {
                    for (; nx < 28; ++nx) ck_dma(nx);
                    VMW(0);
                }
#pragma unroll
                for (int k = 0; k < 4; ++k) qb[k] = *LP(u32x4, qck + k * 1024);
                if (__all(mx4(qb[0], mx4(qb[1], mx4(qb[2], mx4(qb[3], 0u)))) != SENT)) break;
                if (++spins > kSpinCap) break;
            }
        }
    }
    constexpr int N0 = LAST + 1 + SL0, NE = N0 + 6 < 31 ? N0 + 6 : 31;
    auto slot = [&](int i) { if (i < 28) { if (nx <= (unsigned)i) ck_dma(i); } else ck_dma(i); };
    const unsigned kg = kq(g);
    unsigned exw = L_EXM + ((nl + (lane >> 5) * 16) * 8 + wv) * 4, exr = L_EXM + nl * 32;
    unsigned pwr = L_SCR + nl * 1024 + 768 + (((2 * wv + (kg >> 1)) ^ nl) << 4) + (kg & 1) * 8, esw = L_SCR + nl * 1024 + 512 + ((wv ^ (nl & 7)) << 4) + g * 4;
    unsigned prd = L_SCR + (lane & 31) * 1024 + 768 + (((lane >> 5) ^ nl) << 4);
    PIN4(exw, exr, pwr, esw); PIN(prd);
    PIN4(ka[12], ka[13], ka[14], ka[15]); va_pin();
    wg_barrier();
    STAMP(21);
    bf16x8 qr[8][2];
    auto q_ld = [&](int ks, int hg) { return *LP(bf16x8, qad[ks & 3] + (ks >> 2) * 256 + hg * 16384); };
    auto p_ld = [&](int kk) { return *LP(bf16x8, prd ^ (kk << 5)); };
    float ls0, ls1, m0, m1;
    f32x4 sa, sb;
    f32x16 o[2];
    unsigned sw[4], srd, p0, mlo, esr, fbo;
    u32x2 tg;
    u32x4 nq;
    auto probs = [&](float (&e)[8]) {
#pragma unroll
        for (int r = 0; r < 4; ++r) { e[r] = __builtin_amdgcn_exp2f(fmaf(sa[r], SCL, -m0)); e[4 + r] = __builtin_amdgcn_exp2f(fmaf(sb[r], SCL, -m1)); }
        *LP(u32x2, pwr) = u32x2{pk2(e[0], e[1]), pk2(e[2], e[3])};
        *LP(u32x2, pwr + 16384) = u32x2{pk2(e[4], e[5]), pk2(e[6], e[7])};
    };
    auto wave_max = [&]() {
        const float a = fmaxf(fmaxf(sa[0], sa[1]), fmaxf(sa[2], sa[3])), b = fmaxf(fmaxf(sb[0], sb[1]), fmaxf(sb[2], sb[3]));
        auto u = __builtin_amdgcn_permlane32_swap(__float_as_uint(a), __float_as_uint(b), false, false);
        const float c = fmax3(__uint_as_float(u[0]), __uint_as_float(u[1]), __uint_as_float(u[1]));
        auto v = __builtin_amdgcn_permlane16_swap(__float_as_uint(c), __float_as_uint(c), false, false);
        return fmax3(__uint_as_float(v[0]), __uint_as_float(v[1]), __uint_as_float(v[1]));
    };
    auto max8 = [&](unsigned a) {
        const f32x4 a0 = *LP(f32x4, a), a1 = *LP(f32x4, a + 16);
        return fmax3(fmax3(a0[0], a0[1], a0[2]), fmax3(a0[3], a1[0], a1[1]), fmax3(a1[2], a1[3], a1[3])) * SCL;
    };
    {
        bf16x8 qs[8][2];
#pragma unroll
        for (int ks = 0; ks < 8; ++ks) { qr[ks][0] = q_ld(ks + 8, 0); qr[ks][1] = q_ld(ks + 8, 1); }
        __builtin_amdgcn_sched_barrier(0);
#pragma unroll
        for (int i = LAST + 1; i < LAST + 1 + SL0; ++i) if (i < NE) slot(i);
        sa = f32x4{0.f, 0.f, 0.f, 0.f}; sb = sa;
        f32x4 s2a = sa, s2b = sa;
        bf16x8 kb[4];
#pragma unroll
        for (int n = 0; n < 16; ++n) {
            const int ks = (n + 8) & 15;
            sa = MFMA16(ka[ks], ks < 8 ? qs[ks][0] : qr[ks - 8][0], sa);
            sb = MFMA16(ka[ks], ks < 8 ? qs[ks][1] : qr[ks - 8][1], sb);
            if (QKM && n == 4) { VMW(LAST + SL0 + 1 - 19); for (int u = 0; u < 4; ++u) kb[u] = *LP(bf16x8, kad[u]); }
            if (QKM && ks < 4) { s2a = MFMA16(kb[ks], qs[ks][0], s2a); s2b = MFMA16(kb[ks], qs[ks][1], s2b); }
            if (n >= 8 - QD && n < 16 - QD) { qs[n + QD - 8][0] = q_ld(n + QD - 8, 0); qs[n + QD - 8][1] = q_ld(n + QD - 8, 1); }
            if ((n & 3) == 3 && LAST + 1 + SL0 + (n >> 2) < NE) slot(LAST + 1 + SL0 + (n >> 2));
            __builtin_amdgcn_sched_barrier(0);
        }
        *LP(float, exw) = wave_max();
        if (N0 + 4 < NE) slot(N0 + 4);
        wg_barrier();
        m0 = max8(exr); m1 = max8(exr + 512);
#pragma unroll
        for (int ks = 4; ks < 8; ++ks) { qs[ks][0] = q_ld(ks, 0); qs[ks][1] = q_ld(ks, 1); }
        __builtin_amdgcn_sched_barrier(0);
        float e[8];
        probs(e);
        ls0 = (e[0] + e[1]) + (e[2] + e[3]); ls1 = (e[4] + e[5]) + (e[6] + e[7]);
        PIN(ls0); PIN(ls1);
        if (wv == 0) *LP(u32x4, L_BIG) = u32x4{0u, 0u, tlo, thi};
        if (N0 + 5 < NE) slot(N0 + 5);
        VMW(NE - 24);
        ka_reads(1);
        asm volatile("s_waitcnt lgkmcnt(4)\n\ts_barrier" ::: "memory");
        STAMP(22);
        static_assert(NE >= 28 && QKM == 1, "");
        bf16x8 pf[8];
        tg = *LP(u32x2, L_BIG + 8);
        pf[0] = p_ld(0); pf[1] = p_ld(1);
        slot(NE);
        sa = s2a; sb = s2b;
        __builtin_amdgcn_sched_barrier(0);
#pragma unroll
        for (int ks = 4; ks < 8; ++ks) { sa = MFMA16(ka[ks], qs[ks][0], sa); sb = MFMA16(ka[ks], qs[ks][1], sb); }
        __builtin_amdgcn_sched_barrier(0);
#pragma unroll
        for (int i = 0; i < 16; ++i) { o[0][i] = 0.f; o[1][i] = 0.f; }
#pragma unroll
        for (int kk = 0; kk < 8; ++kk) {
            if (kk == 0) {
#pragma unroll
                for (int q = 2; q < 8; ++q) pf[q] = p_ld(q);
            }
            if (kk == 2) { PIN4(pf[2], pf[3], pf[4], pf[5]); PIN(pf[6]); PIN(pf[7]); PIN(tg); }
            if (kk >= 2 && wv < 4) { va_read1(kk - 2); if (kk == 7) va_read1(6); }
            o[0] = MFMA32(cat8(va[kk][0], va[kk][1]), pf[kk], o[0]);
            o[1] = MFMA32(cat8(va[kk][2], va[kk][3]), pf[kk], o[1]);
            if ((kk & 1) && NE + 1 + (kk >> 1) < 32) slot(NE + 1 + (kk >> 1));
            __builtin_amdgcn_sched_barrier(0);
        }
        if (wv < 4) va_read1(7);
        VMW(4);
        ka_reads(2);
        tlo = __builtin_amdgcn_readfirstlane(tg[0]); thi = __builtin_amdgcn_readfirstlane(tg[1]);
        rederive();
        fbo = (unsigned)(WS_FB + x * STR + lane * 16);
        asm volatile("buffer_load_dwordx4 %0, off, %1, %2 sc1" : "=v"(nq) : "s"(rw), "s"(WS_NM + x * STR) : "memory");
        const unsigned head = lane & 31, r8 = lane >> 3, cw = wv ^ (rot * 2);
        srd = lb + r8 * 128 + (((lane & 7) ^ r8) << 4);
        sw[0] = lb + head * 128 + (((lane >> 5) ^ (head & 7)) << 4);
        sw[1] = sw[0] ^ 32u; sw[2] = sw[0] ^ 64u; sw[3] = sw[0] ^ 96u;
        p0 = (unsigned)(WS_PART + x * 1048576) + (cw >> 2) * 16384 + (cw & 3) * 2048 + r8 * 32768 + ((s ^ r8) * 8 + (lane & 7)) * 16;
        PIN4(sw[0], sw[1], sw[2], sw[3]); PIN(srd); PIN(p0); PIN(fbo);
#pragma unroll
        for (int ks = 8; ks < 12; ++ks) { sa = MFMA16(ka[ks], qr[ks - 8][0], sa); sb = MFMA16(ka[ks], qr[ks - 8][1], sb); }
    }
    STAMP(23);
    unsigned po[4];
    float mrun;
    {
        VMW(1);
        STAMP(24);
        ka_reads(3);
        asm volatile("" : "=v"(fa));
        if (wv == 0 && s < 11) asm volatile("buffer_load_dwordx4 %0, %1, %2, 0 offen sc1" : "+v"(fa) : "v"(fbo), "s"(rw) : "memory");
        __builtin_amdgcn_sched_barrier(0);
#pragma unroll
        for (int ks = 12; ks < 16; ++ks) { sa = MFMA16(ka[ks], qr[ks - 8][0], sa); sb = MFMA16(ka[ks], qr[ks - 8][1], sb); }
        bf16x8 pf[8];
#pragma unroll 1
        for (bool first = true;; first = false) {
            float e[8];
            probs(e);
            const float n0 = (e[0] + e[1]) + (e[2] + e[3]), n1 = (e[4] + e[5]) + (e[6] + e[7]);
            *LP(float, esw) = ls0 + n0; *LP(float, esw + 16384) = ls1 + n1;
            if (__builtin_expect(!(n0 + n1 <= BIGV), 0)) *LP(unsigned, L_BIG) = 1u;
            va_pin();
            wg_barrier();
            STAMP(25);
            PIN(prd);
            const unsigned big = *LP(unsigned, L_BIG);
            if (wv >= 4) va_reads();
#pragma unroll
            for (int kk = 0; kk < 8; ++kk) { pf[kk] = p_ld(kk); __builtin_amdgcn_sched_barrier(0); }
            if (__builtin_expect(__builtin_amdgcn_readfirstlane(big) == 0 || !first, 1)) break;
            const unsigned ex2 = L_SCR + 512 + 160;
            PIN(sa); PIN(sb);
            rederive();
            *LP(float, ex2 + (nl + (lane >> 5) * 16) * 1024 + wv * 4) = wave_max();
            wg_barrier();
            const float x0 = fmaxf(max8(ex2 + nl * 1024), m0), x1 = fmaxf(max8(ex2 + nl * 1024 + 16384), m1);
            const float al0 = __builtin_amdgcn_exp2f(m0 - x0), al1 = __builtin_amdgcn_exp2f(m1 - x1), al = (lane & 16) ? al1 : al0;
            m0 = x0; m1 = x1; ls0 *= al0; ls1 *= al1;
            o[0] *= al; o[1] *= al;
        }
        mrun = (lane & 16) ? m1 : m0;
        auto stage = [&](int t2, int gp) {
            const unsigned lo0 = pk2(o[t2][8 * gp + 0], o[t2][8 * gp + 1]), lo1 = pk2(o[t2][8 * gp + 2], o[t2][8 * gp + 3]);
            const unsigned hi0 = pk2(o[t2][8 * gp + 4], o[t2][8 * gp + 5]), hi1 = pk2(o[t2][8 * gp + 6], o[t2][8 * gp + 7]);
            auto r0 = __builtin_amdgcn_permlane32_swap(lo0, hi0, false, false);
            auto r1 = __builtin_amdgcn_permlane32_swap(lo1, hi1, false, false);
            *LP(u32x4, sw[t2 * 2 + gp]) = u32x4{r0[0], r1[0], r0[1], r1[1]};
        };
#pragma unroll
        for (int kk = 0; kk < 8; ++kk) {
            o[0] = MFMA32(cat8(va[kk][0], va[kk][1]), pf[kk], o[0]);
            if (kk == 1) {
                asm volatile("s_waitcnt vmcnt(0)" : "+v"(nq) :: "memory");
                npm = __builtin_amdgcn_readfirstlane(nq[0]);
                if ((unsigned)__builtin_amdgcn_readfirstlane(nq[2]) != tlo || (unsigned)__builtin_amdgcn_readfirstlane(nq[3]) != thi) npm = __builtin_amdgcn_readfirstlane(tagged_wait(WS_NM + x * STR)[0]);
            }
            if (kk == 3 || kk == 5) {
                const unsigned v = (npm ^ ((x & 2) ? 0xffu : 0u)) >> (lane >> 5);
#pragma unroll
                for (int i = kk - 3; i < kk - 1; ++i) { po[i] = p0 + ((v >> (2 * i)) & 1u) * 8192u; PIN(po[i]); }
            }
            __builtin_amdgcn_sched_barrier(0);
        }
#pragma unroll
        for (int kk = 0; kk < 8; ++kk) {
            o[1] = MFMA32(cat8(va[kk][2], va[kk][3]), pf[kk], o[1]);
            if (kk == 2) { PIN(o[0]); stage(0, 0); }
            if (kk == 4) stage(0, 1);
            __builtin_amdgcn_sched_barrier(0);
        }
        stage(1, 0); stage(1, 1);
        asm volatile("s_waitcnt lgkmcnt(0)" ::: "memory");
        u32x4 w[4];
#pragma unroll
        for (int i = 0; i < 4; ++i) w[i] = *LP(u32x4, srd + i * 1024);
#pragma unroll
        for (int i = 0; i < 4; ++i) ST16(w[i], po[i], i * 262144);
        __builtin_amdgcn_sched_barrier(0);
        STAMP(26);
        u32x4 mlrec;
        if (wv == 0) {
            const unsigned head = lane & 31;
            esr = L_SCR + head * 1024 + 512 + ((head & 7) << 4);
            mlo = (unsigned)(WS_ML + x * STR + s * 512 + head * 16) + ((npm >> ((head >> 3) * 2)) & 1u) * 8192u;
            PIN(mlo);
            f32x4 b8[8];
#pragma unroll
            for (int q = 0; q < 8; ++q) b8[q] = *LP(f32x4, esr ^ (q << 4));
            const f32x4 bs = ((b8[0] + b8[1]) + (b8[2] + b8[3])) + ((b8[4] + b8[5]) + (b8[6] + b8[7]));
            mlrec = u32x4{__float_as_uint(mrun), __float_as_uint((bs[0] + bs[1]) + (bs[2] + bs[3])), tlo, thi};
            PIN(mlrec);
        }
        VMW(0);
        wg_barrier();
        if (wv == 0) {
            if (lane < 32) ST16(mlrec, mlo, 0);
            PIN(fa);
            if (s < 11 && !__all(flags_ok(fa))) flags_wait(fbo);
        }
        STAMP(27);
        wg_barrier();
    }
    const unsigned long long t_att = __builtin_amdgcn_s_memrealtime();
    __hip_atomic_store((GAS unsigned*)(p.ws + (128u << 20)) + (id - 128) * 8 + wv, retried, RLX_AGENT);
    rederive();
    while (__builtin_amdgcn_s_memrealtime() < t_att + (s >= 11 ? MD0 : MD1)) __builtin_amdgcn_s_sleep(2);
    if (s >= 11) {
        const unsigned n = (unsigned)((s - 11) * 8 + wv);
        if (n < 28) pf4<NTM & 32>((n * 64 + lane) * 1024u + x * 128u, p.w2, 0);
        { const unsigned l2 = n * 64 + lane; pf4<NTM & 32>((l2 / 5u) * (DFF * 2) + min(((unsigned)x * 448u & ~127u) + (l2 % 5u) * 128u, DFF * 2 - 4u), p.w1, 0); }
        if (wv == 0) flag_put(WS_FA, id - 128);
        STAMP_FLUSH();
        return;
    }
    const unsigned L_H2S = L_SCR, L_REDM = L_SCR + 16384;
    if (s < 7) {
        {
            const char* w1s = (const char*)(p.w1 + (size_t)wv * 64 * DFF + ug * 32);
#pragma unroll
            for (int i = 0; i < 4; ++i) dma16<NTM & 32>((unsigned)((lane >> 2) * DFF * 2 + (lane & 3) * 16), w1s + (size_t)i * 16 * DFF * 2, wv * 4096 + i * 1024);
        }
        const int b = wv;
        if (wv == 0) ST16(s4, (unsigned)(WS_U + x * STR + (lane >> 5) * 8192 + s * 512 + (lane & 31) * 16), 0);
        const u32x4 shf = LD16((unsigned)(WS_MB + b * STR + lane * 16), 0), scl = LD16((unsigned)(WS_MB + b * STR + 1024 + lane * 16), 0);
        VMW(0);
        if (wv == 0) flag_put(WS_FA, id - 128);
        wg_barrier();
        bf16x8 wk0[8], wk1[8];
        if (wv >= 6) {
#pragma unroll
            for (int q = 0; q < 8; ++q) {
                const unsigned a = (unsigned)(q * 4096 + (g * 8 + (nl >> 2)) * 64 + (wv & 1) * 32 + (lane & 3) * 8);
                wk0[q] = tr_frag(a, a + 256); wk1[q] = tr_frag(a + 2048, a + 2304);
            }
#pragma unroll
            for (int q = 0; q < 8; q += 2) PIN4(wk0[q], wk0[q + 1], wk1[q], wk1[q + 1]);
        }
        float scv[8], shv[8];
#pragma unroll
        for (int q = 0; q < 4; ++q) {
            scv[2 * q] = rbf(1.f + lo16(scl[q])); scv[2 * q + 1] = rbf(1.f + hi16(scl[q])); shv[2 * q] = lo16(shf[q]); shv[2 * q + 1] = hi16(shf[q]);
            PIN4(scv[2 * q], scv[2 * q + 1], shv[2 * q], shv[2 * q + 1]);
        }
        unsigned har = L_H2S + ((lane & 7) * HS_LD + g * 8 + (nl >> 3) * 32) * 2, haw = L_H2S + (b * HS_LD + lane * 8) * 2;
        unsigned udst = (unsigned)(WS_U + x * STR + (nl >> 3) * 8192 + (s * 2 + (wv & 1)) * 256 + ((nl & 7) * 16 + g * 4) * 2), xsrc = (unsigned)(WS_X1 + x * STR + (b * 512 + lane * 8) * 2) + ((npm >> x) & 1u) * 8192u;
        PIN4(har, haw, udst, xsrc);
        STAMP(28);
        while (__builtin_amdgcn_s_memrealtime() < t_att + XGATE) __builtin_amdgcn_s_sleep(2);
        u32x4 xv;
        for (unsigned spins = 0;;) {
            xv = LD16(xsrc, 0);
            if (__all(mx4(xv, 0u) != SENT)) break;
            __builtin_amdgcn_s_sleep(SLPX);
            if (++spins > kSpinCap) break;
            asm volatile("" ::: "memory");
        }
        STAMP(29);
        {
            f32x2_t v[4];
#pragma unroll
            for (int q = 0; q < 4; ++q) v[q] = f32x2_t{lo16(xv[q]), hi16(xv[q])};
            const f32x2_t p1 = (v[0] + v[1]) + (v[2] + v[3]), p2 = (v[0] * v[0] + v[1] * v[1]) + (v[2] * v[2] + v[3] * v[3]);
            float mb, rden;
            ln_stats(p1[0] + p1[1], p2[0] + p2[1], mb, rden);
            u32x4 hr;
#pragma unroll
            for (int q = 0; q < 4; ++q) {
                const f32x2_t h = ((v[q] - mb) * rden) * f32x2_t{scv[2 * q], scv[2 * q + 1]} + f32x2_t{shv[2 * q], shv[2 * q + 1]};
                hr[q] = pk2(h[0], h[1]);
            }
            *LP(u32x4, haw) = hr;
        }
        wg_barrier();
        if (wv >= 6) {
            bf16x8 af[8];
#pragma unroll
            for (int q = 0; q < 8; ++q) af[q] = *LP(bf16x8, har + q * 128);
            __builtin_amdgcn_sched_barrier(0);
            f32x4 d0 = {0.f, 0.f, 0.f, 0.f}, d1 = {0.f, 0.f, 0.f, 0.f};
#pragma unroll
            for (int q = 0; q < 8; ++q) { d0 = MFMA16(wk0[q], af[q], d0); d1 = MFMA16(wk1[q], af[q], d1); }
            const f32x4 own = nl < 8 ? d0 : d1;
#pragma unroll
            for (int e = 0; e < 4; ++e) { float v = own[e] + dppf<0x128>(own[e]); asm("" : "+v"(v)); d0[e] = fmaxf(v, 0.f); }
            const u32x2 uv = {pk2s(d0[0], d0[1]), pk2s(d0[2], d0[3])};
            __builtin_amdgcn_raw_buffer_store_b64(uv, rw, udst, 0, 16);
            STAMP(30);
        }
        STAMP_FLUSH();
        return;
    }
    {
        const int n0 = (x * 4 + (s - 7)) * 16;
        const char* mws = (const char*)(p.w2 + (size_t)wv * 224 * 512 + n0);
#pragma unroll
        for (int i = 0; i < 7; ++i) dma16<NTM & 32>((unsigned)((lane >> 1) * 1024 + (lane & 1) * 16), mws + i * 32768, rgn + i * 1024);
        const int fb = lane >> 3, fc = (lane & 7) * 2;
        u32x2 gb = {0u, 0u};
        if (wv == 0) {
            flag_put(WS_FA, id - 128);
            { const u32x4 v = tagged_wait(WS_MOD + fb * STR + (1536 + n0 + fc) * 8); gb = u32x2{v[0], v[1]}; }
            flags_wait(WS_FA + x * STR + lane * 16);
            if (id == 23 * 8 && lane == 0) __builtin_amdgcn_raw_buffer_store_b64(u32x2{tlo, thi}, rw, WS_EPOCH, 0, 16);
            PIN(gb);
        }
        VMW(0);
        bf16x8 wfr[7];
        const unsigned trl = (unsigned)(g * 8 + (nl >> 2)) * 32u + (unsigned)(lane & 3) * 8u;
#pragma unroll
        for (int ks = 0; ks < 7; ++ks) { wfr[ks] = tr_frag(rgn + ks * 1024 + trl, rgn + ks * 1024 + trl + 128); PIN(wfr[ks]); }
        unsigned usrc = (unsigned)(WS_U + wv * STR + (g >> 1) * 256 + (nl & 7) * 32 + (g & 1) * 16) + ((npm >> x) & 1u) * 8192u;
        unsigned rdw = L_REDM + wv * 512 + (nl & 7) * 64 + g * 16, rdr = L_REDM + lane * 8;
        unsigned short* outp = p.out + fb * 512 + n0 + fc;
        PIN(usrc); PIN(rdw); PIN(rdr); PIN(outp);
        STAMP(28);
        unsigned xg = 0u;
        if (wv == 0) {
            while (__builtin_amdgcn_s_memrealtime() < t_att + XGATE) __builtin_amdgcn_s_sleep(2);
            for (unsigned spins = 0;;) {
                xg = __builtin_amdgcn_raw_buffer_load_b32(rw, (unsigned)(WS_X1 + x * STR + (fb * 512 + n0 + fc) * 2) + ((npm >> x) & 1u) * 8192u, 0, 16);
                if (__all(xg != SENT)) break;
                __builtin_amdgcn_s_sleep(SLPD);
                if (++spins > kSpinCap) break;
                asm volatile("" ::: "memory");
            }
            __builtin_amdgcn_s_sleep(SLPG);
        }
        wg_barrier();
        u32x4 uf[7];
        for (unsigned spins = 0;;) {
#pragma unroll
            for (int ks = 0; ks < 7; ++ks) uf[ks] = LD16(usrc + ks * 512, 0);
            unsigned m = 0u;
#pragma unroll
            for (int ks = 0; ks < 7; ++ks) m = mx4(uf[ks], m);
            if (__all(m != SENT)) break;
            if (++spins > kSpinCap) break;
            asm volatile("" ::: "memory");
        }
        STAMP(29);
        f32x4 d = {0.f, 0.f, 0.f, 0.f}, d2 = {0.f, 0.f, 0.f, 0.f};
#pragma unroll
        for (int ks = 0; ks < 7; ++ks) {
            if (ks & 1) d2 = MFMA16(wfr[ks], BF8(uf[ks]), d2);
            else d = MFMA16(wfr[ks], BF8(uf[ks]), d);
        }
        *LP(f32x4, rdw) = d + d2;
        wg_barrier();
        if (wv == 0) {
            f32x2_t c[8];
#pragma unroll
            for (int w2 = 0; w2 < 8; ++w2) c[w2] = *LP(f32x2_t, rdr + w2 * 512);
            const f32x2_t sm = ((c[0] + c[1]) + (c[2] + c[3])) + ((c[4] + c[5]) + (c[6] + c[7]));
            unsigned t = pk2(sm[0], sm[1]);
            t = pk2(__uint_as_float(gb[0]) * lo16(t), __uint_as_float(gb[1]) * hi16(t));
            *(GAS unsigned*)outp = pk2(lo16(xg) + lo16(t), hi16(xg) + hi16(t));
            { const unsigned long long t1 = __builtin_amdgcn_s_memrealtime(); unsigned sm = 0;
              for (int q = 0; q < 2; ++q) { unsigned any = 0; for (int w = 0; w < 8; ++w) any |= __hip_atomic_load((GAS unsigned*)(p.ws + (128u << 20)) + (q * 64 + lane) * 8 + w, RLX_AGENT); sm += any & 1u; }
              for (int o = 1; o < 64; o <<= 1) sm += __shfl_xor(sm, o);
              const unsigned dl = __builtin_amdgcn_readfirstlane(sm) * 50u + 500u;
              while (__builtin_amdgcn_s_memrealtime() < t1 + dl) __builtin_amdgcn_s_sleep(1); }
        }
        STAMP_FLUSH();
    }
}

extern "C" void kernel_launch(void* const* d_in, const int* in_sizes, int n_in,
                              void* d_out, int out_size, void* d_ws, size_t ws_size,
                              hipStream_t stream) {
    Params p;
    p.x = (const unsigned short*)d_in[0];
    p.ckv = (const unsigned short*)d_in[1];
    p.w_q = (const unsigned short*)d_in[2];
    p.w_uk = (const unsigned short*)d_in[3];
    p.w_uv = (const unsigned short*)d_in[4];
    p.w_o = (const unsigned short*)d_in[5];
    p.w1 = (const unsigned short*)d_in[7];
    p.w2 = (const unsigned short*)d_in[8];
    p.c = (const unsigned short*)d_in[9];
    p.w_mod = (const unsigned short*)d_in[10];
    p.ws = (char*)(((size_t)d_ws + 2097151) & ~(size_t)2097151);
    p.out = (unsigned short*)d_out;
    (void)hipFuncSetAttribute((const void*)k_main, hipFuncAttributeMaxDynamicSharedMemorySize, 163840);
    k_main<<<256, 512, 163840, stream>>>(p);
}
```

```cpp
#include <hip/hip_runtime.h>
#include <hip/hip_bf16.h>
#include <stdint.h>

#define NH 32
#define SK 4096
#define DFF 1792
#define GI 18
#define ROTC 1
#define PFS 2
#define XNO 0
#define CPJ 7
#define NTM 16
#define QHELP 4
#define HP 13
#define NTH 0
#define W0 2
#define SL0 1
#define LMPF 0
#define SLPB 20
#define MD0 0
#define MD1 0
#define NEARP 1
#define NCAL 2
#define TLBW 2
#define TG 480
#define JPERM 1
#define NPOST 3
#define XS3 1
#define WG 1
#define NP2 2
#define QKM 1
#define HILO 0
#define YSEL 1
#define WMT 0
#define SD 0
#define QDL 0
#define CNL 1
#define TLBA 1
#define CPW 0
#define SLPR 10
#define TK 5
#define TWD 570
#define LMD 250
#define PFM 57
#define CPFE 36600
#define SLPM 0
#define SLPD 2
#define SLPG 18
#define SLPX 1
#define SLPH 4

using u16x8 = __attribute__((ext_vector_type(8))) unsigned short;
using s16x4 = __attribute__((ext_vector_type(4))) short;
using bf16x8 = __attribute__((ext_vector_type(8))) short;
using f32x16 = __attribute__((ext_vector_type(16))) float;
using f32x4 = __attribute__((ext_vector_type(4))) float;
using u32x4 = __attribute__((ext_vector_type(4))) unsigned;
using u32x2 = __attribute__((ext_vector_type(2))) unsigned;

#define GAS __attribute__((address_space(1)))
#define LAS __attribute__((address_space(3)))
typedef GAS unsigned long long gu64;
#define RLX_AGENT __ATOMIC_RELAXED, __HIP_MEMORY_SCOPE_AGENT
#define SENT 0xffffffffu
#define TAGC 0x9e3779b9u

__device__ __forceinline__ float bf2f(unsigned short u) { return __uint_as_float(((unsigned)u) << 16); }
typedef __attribute__((ext_vector_type(2))) __bf16 bf16x2_t;
typedef __attribute__((ext_vector_type(2))) float f32x2_t;
__device__ __forceinline__ unsigned pk2(float a, float b) {
    f32x2_t v = {a, b};
    bf16x2_t r = __builtin_convertvector(v, bf16x2_t);
    return __builtin_bit_cast(unsigned, r);
}
__device__ __forceinline__ unsigned nosent(unsigned r) { return min(r, SENT - 1u); }
__device__ __forceinline__ unsigned pk2s(float a, float b) { return nosent(pk2(a, b)); }
__device__ __forceinline__ unsigned short f2bf(float f) { return (unsigned short)(pk2(f, 0.f) & 0xffffu); }
__device__ __forceinline__ float rbf(float f) { return __uint_as_float(pk2(f, 0.f) << 16); }
__device__ __forceinline__ float lo16(unsigned u) { return __uint_as_float(u << 16); }
__device__ __forceinline__ float hi16(unsigned u) { return __uint_as_float(u & 0xffff0000u); }
__device__ __forceinline__ unsigned umax3(unsigned a, unsigned b, unsigned c) { return max(max(a, b), c); }
__device__ __forceinline__ unsigned mx4(const u32x4& v, unsigned m) { return umax3(v[2], v[3], umax3(v[0], v[1], m)); }
__device__ __forceinline__ float fmax3(float a, float b, float c) { float r; asm("v_max3_f32 %0, %1, %2, %3" : "=v"(r) : "v"(a), "v"(b), "v"(c)); return r; }
template <int C> __device__ __forceinline__ float dppf(float v) { return __int_as_float(__builtin_amdgcn_update_dpp(0, __float_as_int(v), C, 0xf, 0xf, true)); }
__device__ __forceinline__ float sum16(float v) { v += dppf<0xB1>(v); v += dppf<0x4E>(v); v += dppf<0x141>(v); v += dppf<0x140>(v); return v; }

__device__ __forceinline__ void ln_stats(float p1, float p2, float& mb, float& rden) {
    auto t = __builtin_amdgcn_permlane32_swap(__float_as_uint(p1), __float_as_uint(p2), false, false);
    float a = sum16(__uint_as_float(t[0]) + __uint_as_float(t[1]));
    auto t2 = __builtin_amdgcn_permlane16_swap(__float_as_uint(a), __float_as_uint(a), false, false);
    a = (__uint_as_float(t2[0]) + __uint_as_float(t2[1])) * (1.f / 512.f);
    auto t3 = __builtin_amdgcn_permlane32_swap(__float_as_uint(a), __float_as_uint(a), false, false);
    const float m = __uint_as_float(t3[0]);
    const float var = rbf(fmaxf(fmaf(-m, m, __uint_as_float(t3[1])), 0.f));
    mb = rbf(m);
    rden = __builtin_amdgcn_rcpf(rbf(__builtin_amdgcn_sqrtf(rbf(var + 1e-5f))));
}
#define STR 262144
#define WS_Q 0
#define WS_H 32768
#define WS_X1 65536
#define WS_ML 81920
#define WS_MOD 98304
#define WS_MB 139264
#define WS_U 114688
#define WS_FI 131072
#define WS_FA 132096
#define WS_FB 133120
#define WS_NP 134144
#define WS_NM 134400
#define WS_EPOCH 135168
#define WS_CAL 147456
#define WS_OUTP 2097152
#define WS_PART 4194304
#define WS_END 12582912

#define LP(T, off) ((LAS T*)(size_t)(unsigned)(off))
#define STAMP(k) do {} while (0)
#define STAMP_FLUSH() do {} while (0)
#define STAMP0() do {} while (0)
__device__ __forceinline__ s16x4 tr4(unsigned a) { return __builtin_amdgcn_ds_read_tr16_b64_v4i16(LP(s16x4, a)); }
__device__ __forceinline__ bf16x8 cat8(s16x4 a, s16x4 b) { return __builtin_shufflevector(a, b, 0, 1, 2, 3, 4, 5, 6, 7); }
__device__ __forceinline__ bf16x8 tr_frag(unsigned a0, unsigned a1) { return cat8(tr4(a0), tr4(a1)); }
__device__ __forceinline__ unsigned swz(unsigned row) { return ((row & 3) << 2) | ((row >> 2) & 3); }
__device__ __forceinline__ unsigned off_b(unsigned row, unsigned ch) { return 256u * row + 16u * (ch ^ swz(row)); }
__device__ __forceinline__ unsigned tile_off(unsigned key, unsigned chunk) { return ((key >> 5) * 4 + (chunk >> 4)) * 8192u + off_b(key & 31, chunk & 15); }
__device__ __forceinline__ void wg_barrier() { asm volatile("s_waitcnt lgkmcnt(0)\n\ts_barrier" ::: "memory"); }
template <int NT> __device__ __forceinline__ void dma16(unsigned voff, const void* sbase, unsigned lds_dst) {
    if (NT) asm volatile("s_mov_b32 m0, %2\n\ts_nop 0\n\tglobal_load_lds_dwordx4 %0, %1 nt" :: "v"(voff), "s"(sbase), "s"(lds_dst) : "memory", "m0");
    else asm volatile("s_mov_b32 m0, %2\n\ts_nop 0\n\tglobal_load_lds_dwordx4 %0, %1" :: "v"(voff), "s"(sbase), "s"(lds_dst) : "memory", "m0");
}
template <int NT> __device__ __forceinline__ void dma16x(unsigned voff, unsigned vx, const void* sbase, unsigned lds_dst) {
    unsigned t;
#define DMAX(pol, c) asm volatile("s_mov_b32 m0, %3\n\tv_xor_b32 %0, %4, %1\n\tglobal_load_lds_dwordx4 %0, %2" pol : "=&v"(t) : "v"(voff), "s"(sbase), "s"(lds_dst), c(vx) : "memory", "m0")
    if (__builtin_constant_p(vx)) { if (NT) DMAX(" nt", "i"); else DMAX("", "i"); }
    else { if (NT) DMAX(" nt", "s"); else DMAX("", "s"); }
}
template <int NT> __device__ __forceinline__ void pf4(unsigned voff, const void* sbase, unsigned lds_dst) {
    if (NT) asm volatile("s_mov_b32 m0, %2\n\ts_nop 0\n\tglobal_load_lds_dword %0, %1 nt" :: "v"(voff), "s"(sbase), "s"(lds_dst) : "memory", "m0");
    else asm volatile("s_mov_b32 m0, %2\n\ts_nop 0\n\tglobal_load_lds_dword %0, %1" :: "v"(voff), "s"(sbase), "s"(lds_dst) : "memory", "m0");
}
__device__ __forceinline__ void pf4s(unsigned voff, const void* sbase, unsigned lds_dst) {
    asm volatile("s_mov_b32 m0, %2\n\ts_nop 0\n\tglobal_load_lds_dword %0, %1 sc1" :: "v"(voff), "s"(sbase), "s"(lds_dst) : "memory", "m0");
}
#define XGATE 300
#define SPINCAP (1u << 20)
constexpr unsigned kSpinCap = SPINCAP;
struct Params {
    const unsigned short* x;
    const unsigned short* ckv;
    const unsigned short* w_q;
    const unsigned short* w_uk;
    const unsigned short* w_uv;
    const unsigned short* w_o;
    const unsigned short* w1;
    const unsigned short* w2;
    const unsigned short* c;
    const unsigned short* w_mod;
    char* ws;
    unsigned short* out;
};
#define VMW(n) asm volatile("s_waitcnt vmcnt(%0)" :: "n"(n) : "memory")
#define PIN(x) asm volatile("" : "+v"(x))
#define PIN4(a, b, c, d) asm volatile("" : "+v"(a), "+v"(b), "+v"(c), "+v"(d) :: "memory")
#define MFMA16(a, b, c) __builtin_amdgcn_mfma_f32_16x16x32_bf16(a, b, c, 0, 0, 0)
#define MFMA32(a, b, c) __builtin_amdgcn_mfma_f32_32x32x16_bf16(a, b, c, 0, 0, 0)
#define BF8(x) __builtin_bit_cast(bf16x8, x)
#define ST16(v, off, soff) __builtin_amdgcn_raw_buffer_store_b128(v, rw, off, soff, 16)
#define LD16(off, soff) __builtin_amdgcn_raw_buffer_load_b128(rw, off, soff, 16)
#define SCL (0.08838834764831845f * 1.4426950408889634f)
#define BIGV 1.0e18f
#define QD 4
#define QE 4
#define L_SCR 131072
#define HS_LD 520

__global__ __launch_bounds__(512, 2) void k_main(Params p) {
    unsigned long long pc;
    asm volatile("s_getpc_b64 %0" : "=s"(pc));
    const int id = blockIdx.x, tid = threadIdx.x;
    int lane = tid & 63;
    const int wv = __builtin_amdgcn_readfirstlane(tid >> 6);
    int g = lane >> 4, nl = lane & 15;
    auto rederive = [&]() {
        asm volatile("v_mbcnt_lo_u32_b32 %0, -1, 0\n\tv_mbcnt_hi_u32_b32 %0, -1, %0" : "=v"(lane));
        g = lane >> 4; nl = lane & 15;
    };
    const int x = id & 7, j0 = id >> 3, j = JPERM && j0 < 16 ? 15 - j0 : j0;
    if (wv >= CPW && wv < CPW + 5 && j == 15) {
        asm volatile("s_mov_b32 m0, %2\n\ts_nop 0\n\tglobal_load_lds_dword %0, %1" :: "v"(min((unsigned)(((wv - CPW) * 64 + lane) * 128), (unsigned)CPFE)), "s"(pc), "s"(L_SCR + 31744) : "memory", "m0");
    }
    char* ws = p.ws;
    if (TLBA == 2) {
        if (j >= 14 && j < 16 && wv < 3 && lane < 2) {
            const unsigned short* b = j == 14 ? (wv == 0 ? (const unsigned short*)(ws + WS_CAL) : wv == 1 ? p.w_q : p.w_mod) : (wv == 0 ? p.ckv + (size_t)x * 2097152 : wv == 1 ? p.x : p.w_uk);
            pf4s((unsigned)((j == 15 && wv == 1) || (j == 14 && wv == 0) ? 0 : lane * 2097152), b, L_SCR + 31744);
        }
    }
    if ((j >> 2) == 3) {
        dma16<0>((unsigned)(((nl & 7) * 512 + (nl >> 3) * 32 + g * 8) * 2 + wv * 128), p.c, wv * 16384 + 8192);
        dma16<0>((unsigned)tid * 16u, p.x, wv * 16384 + 9216);
    }
    if (TLBA < 2 && TLBW && j == 15 && wv == 5 && lane < 2) pf4s((unsigned)((x * 2 + lane) * 2097152), p.ckv, L_SCR + 31744);
    if (TLBA == 1 && j == 14 && wv >= 4 && lane < 2) pf4s((unsigned)(lane * 2097152 + x * 128), wv == 4 ? p.w_mod : wv == 5 ? p.w_uk : wv == 6 ? p.w_q : (const unsigned short*)(ws + WS_CAL), L_SCR + 31744);
    STAMP0();
    const __amdgpu_buffer_rsrc_t rw = __builtin_amdgcn_make_buffer_rsrc(ws, 0, WS_END, 0x00020000);
    const u32x4 s4 = {SENT, SENT, SENT, SENT};
    unsigned long long cnt = 0ull;
    auto cnt_load = [&]() { asm volatile("global_load_dwordx2 %0, %1, %2 sc1" : "+v"(cnt) : "v"((unsigned)WS_EPOCH), "s"(ws) : "memory"); };
    unsigned tlo = 0u, thi = 0u;
    auto tag_take = [&]() {
        asm volatile("" : "+v"(cnt) :: "memory");
        const unsigned long long t = cnt + 1ull;
        tlo = __builtin_amdgcn_readfirstlane((unsigned)t); thi = __builtin_amdgcn_readfirstlane((unsigned)(t >> 32));
    };
    auto flag_put = [&](unsigned base, unsigned idx) {
        if (lane < 8) __builtin_amdgcn_raw_buffer_store_b64(u32x2{tlo, thi}, rw, base + lane * STR + idx * 8, 0, 16);
    };
    auto tagged_wait = [&](unsigned off) {
        u32x4 v;
#pragma unroll 1
        for (unsigned spins = 0; spins < kSpinCap; ++spins) {
            asm volatile("buffer_load_dwordx4 %0, %1, %2, 0 offen sc1\n\ts_waitcnt vmcnt(0)" : "=v"(v) : "v"(off), "s"(rw) : "memory");
            if (__all((v[2] == tlo) & (v[3] == thi))) break;
            __builtin_amdgcn_s_sleep(8);
        }
        return v;
    };
    auto flags_ok = [&](const u32x4& f) { return (f[0] == tlo) & (f[1] == thi) & (f[2] == tlo) & (f[3] == thi); };
    auto flags_wait = [&](unsigned off) {
        for (unsigned spins = 0;;) {
            u32x4 f;
            asm volatile("buffer_load_dwordx4 %0, %1, %2, 0 offen sc1\n\ts_waitcnt vmcnt(0)" : "=v"(f) : "v"(off), "s"(rw) : "memory");
            if (__all(flags_ok(f))) break;
            __builtin_amdgcn_s_sleep(16);
            if (++spins > kSpinCap) break;
            asm volatile("" ::: "memory");
        }
    };
    unsigned pcl, frl, frs;
    auto dma_piece = [&](const unsigned short* src, unsigned ldb, unsigned dst, int n, int nt) {
        const unsigned v0 = (lane >> 3) * ldb + pcl, v1 = v0 ^ 64u;
#pragma unroll
        for (int i = 0; i < n; ++i) {
            if (nt) dma16<1>((i & 1) ? v1 : v0, (const char*)src + (size_t)i * 8 * ldb, dst + i * 1024);
            else dma16<0>((i & 1) ? v1 : v0, (const char*)src + (size_t)i * 8 * ldb, dst + i * 1024);
        }
    };
    auto piece_frag = [&](unsigned base, int ks, int t) {
        const unsigned a = base + frl + ((t ^ frs) << 5) + ks * 4096;
        return tr_frag(a, a + 512);
    };
    const unsigned rgn = (unsigned)(wv * 16384);

    if (j < 16) {
        const unsigned long long t_start = __builtin_amdgcn_s_memrealtime();
        rederive();
        pcl = (unsigned)(((((lane & 7) >> 1) ^ ((lane >> 4) & 1)) * 32) + (lane & 1) * 16);
        frl = (unsigned)((g * 8 + (nl >> 2)) * 128 + (lane & 3) * 8); frs = (unsigned)((nl >> 3) | ((g & 1) << 1));
        const int th = x * 4 + (j & 3);
        if (j < 12) {
            const int qt = j >> 2, rot = qt * 5;
            const bool pub = wv < (qt < 2 ? 3 : 2);
            const unsigned qo = (unsigned)(WS_Q + (th & 7) * STR + ((nl & 7) * 4 + (th >> 3)) * 1024 + (qt * 192 + wv * 64 + g * 16 + (nl >> 3) * 8) * 2);
            const unsigned vb = (unsigned)(g * 8192 + ((nl ^ (g << 2)) << 4));
            const char* wuk = (const char*)(p.w_uk + th * 128) + (size_t)qt * 192 * 8192;
            const char* wq = (const char*)(p.w_q + th * 128) + (size_t)wv * 32768;
            const unsigned L_U = 114688;
            if (QDL) __builtin_amdgcn_s_sleep(QDL);
            {
                const int k0 = qt < 2 ? wv * 6 : wv * 4;
#pragma unroll
                for (int i = 0; i < 6; ++i) {
                    if (i < 4 || qt < 2) dma16<NTM & 2>(vb ^ (((k0 + i) & 3) << 4), wuk + (size_t)(k0 + i) * 32768, L_U + (k0 + i) * 1024);
                    if (i == 4) VMW(4);
                }
            }
            const unsigned vq = vb ^ ((wv & 3) << 4);
            auto wq_dma = [&](int ps) { dma16<NTM & 2>(vq, wq + (size_t)((ps + rot) & 15) * 262144, ps * 8192 + wv * 1024); };
            bf16x8 uk[4][4], aq[16];
            u32x4 fi, hv[4];
            unsigned fio = (unsigned)(WS_FI + x * STR + (lane & 7) * 16), hsrc, hsrc2, ta0, ta1, tb0, tb1, qo2, hw0, hw1, hr, hr2, qr0, qr1, qw, thx = 0u;
            const unsigned L_HI = L_SCR, L_QI = L_SCR + 8192;
            auto h_load = [&]() {
                asm volatile("buffer_load_dwordx4 %0, %4, %6, 0 offen sc1\n\tbuffer_load_dwordx4 %1, %4, %6, 0 offen offset:1024 sc1\n\t"
                             "buffer_load_dwordx4 %2, %5, %6, 0 offen sc1\n\tbuffer_load_dwordx4 %3, %5, %6, 0 offen offset:1024 sc1"
                             : "+v"(hv[0]), "+v"(hv[1]), "+v"(hv[2]), "+v"(hv[3]) : "v"(hsrc), "v"(hsrc2), "s"(rw) : "memory");
            };
            auto h_bad = [&]() {
                PIN4(hv[0], hv[1], hv[2], hv[3]);
                unsigned bad = (hv[0][1] ^ tlo) | (hv[0][3] ^ thx) | (hv[1][1] ^ tlo) | (hv[1][3] ^ thx) | (hv[2][1] ^ tlo) | (hv[2][3] ^ thx) | (hv[3][1] ^ tlo) | (hv[3][3] ^ thx);
                PIN(bad);
                return bad;
            };
#pragma unroll
            for (int ps = 0; ps < 16; ++ps) {
                wq_dma(ps);
                if (ps == 4) cnt_load();
                if (ps == HP) { h_load(); asm volatile("buffer_load_dwordx4 %0, %1, %2, 0 offen sc1" : "=v"(fi) : "v"(fio), "s"(rw) : "memory"); }
                if (ps >= 4 && ps < 8) VMW(5); else if (ps >= HP && ps < HP + 4) VMW(9); else VMW(4);
                if (ps == 3) {
                    asm volatile("s_barrier" ::: "memory");
                    if (pub) {
#pragma unroll
                        for (int t = 0; t < 4; ++t)
#pragma unroll
                            for (int s = 0; s < 4; ++s) uk[t][s] = *LP(bf16x8, L_U + off_b(wv * 64 + (nl >> 2) * 16 + t * 4 + (nl & 3), s * 4 + g));
                    }
                    STAMP(1);
                }
                if (ps == 5) {
                    hsrc = (unsigned)(WS_H + x * STR + wv * 2048 + lane * 16); hsrc2 = hsrc + 16384;
                    ta0 = off_b(8 * g + (nl >> 2), 2 * wv + ((lane & 3) >> 1)) + 8 * (lane & 1); ta1 = off_b(8 * g + 4 + (nl >> 2), 2 * wv + ((lane & 3) >> 1)) + 8 * (lane & 1);
                    tb0 = ta0 + 65536; tb1 = ta1 + 65536; qo2 = qo;
                    hw0 = L_HI + ((2 * wv - rot) & 15) * 512 + (lane >> 4) * 128 + (lane & 7) * 16 + ((lane >> 3) & 1) * 8;
                    hw1 = L_HI + ((2 * wv + 1 - rot) & 15) * 512 + (lane >> 4) * 128 + (lane & 7) * 16 + ((lane >> 3) & 1) * 8;
                    hr2 = L_HI + g * 128 + (nl & 7) * 16 + (nl >> 3) * 512;
                    PIN(hr2);
                    qr0 = L_QI + g * 128 + (nl & 7) * 16 + (nl >> 3) * 2048; qr1 = qr0 ^ 2048u;
                    PIN(qr0); PIN(qr1);
                    hr = L_HI + g * 128 + (nl & 7) * 16; qw = L_QI + (wv >> 1) * 512 + ((wv & 1) * 2 + (g >> 1)) * 128 + (nl & 7) * 16 + (g & 1) * 8;
                    PIN4(hsrc, hsrc2, ta0, ta1); PIN4(tb0, tb1, qo2, hw0); PIN(hw1); PIN(qw);
                }
                if (ps == 8) { tag_take(); thx = tlo ^ thi ^ TAGC; }
                if (ps == 9) {
#pragma unroll
                    for (int t = 0; t < 4; ++t) PIN4(uk[t][0], uk[t][1], uk[t][2], uk[t][3]);
                    wg_barrier();
                    *LP(unsigned, L_QI + 2048 + wv * 256 + lane * 4) = 0u;
#pragma unroll
                    for (int q = 0; q < 6; ++q) aq[q] = tr_frag(ta0 + q * 8192, ta1 + q * 8192);
                }
                if (ps == 13) {
                    asm volatile("s_barrier" ::: "memory");
#pragma unroll
                    for (int q = 6; q < 10; ++q) aq[q] = q < 8 ? tr_frag(ta0 + q * 8192, ta1 + q * 8192) : tr_frag(tb0 + (q - 8) * 8192, tb1 + (q - 8) * 8192);
                }
            }
            STAMP(15);
            if (HP + 4 > 15) VMW(HP + 4 > 15 ? 15 - HP : 0);
            if (!__all(h_bad() == 0u)) {
                for (unsigned spins = 0;;) {
                    h_load();
                    VMW(0);
                    if (__all(h_bad() == 0u)) break;
                    __builtin_amdgcn_s_sleep(SLPH);
                    if (++spins > kSpinCap) break;
                }
            }
            {
                unsigned hh[4];
#pragma unroll
                for (int q = 0; q < 4; ++q) {
                    const unsigned t = hv[2 + (q >> 1)][(q & 1) * 2], sh = hv[q >> 1][(q & 1) * 2];
                    hh[q] = pk2(lo16(t) + lo16(sh), hi16(t) + hi16(sh));
                }
                *LP(u32x2, hw0) = u32x2{hh[0], hh[1]};
                *LP(u32x2, hw1) = u32x2{hh[2], hh[3]};
            }
            STAMP(3);
            asm volatile("s_waitcnt vmcnt(0) lgkmcnt(0)\n\ts_barrier" : "+v"(fi) :: "memory");
            STAMP(2);
            {
                bf16x8 hb[8];
#pragma unroll
                for (int q = 0; q < 5; ++q) hb[q] = *LP(bf16x8, hr2 + q * 1024);
#pragma unroll
                for (int ps = 10; ps < 16; ++ps) {
                    if (!(ps & 1)) hb[ps >> 1] = *LP(bf16x8, hr2 + (ps >> 1) * 1024);
                    aq[ps] = tr_frag(tb0 + (ps - 8) * 8192, tb1 + (ps - 8) * 8192);
                }
                __builtin_amdgcn_sched_barrier(0);
                f32x4 d0 = {0.f, 0.f, 0.f, 0.f}, d1 = {0.f, 0.f, 0.f, 0.f};
#pragma unroll
                for (int ps = 0; ps < 16; ps += 2) {
                    d0 = MFMA16(aq[ps], hb[ps >> 1], d0); d1 = MFMA16(aq[ps + 1], hb[ps >> 1], d1);
                    if (ps == 6) { unsigned bad = (fi[0] ^ tlo) | (fi[1] ^ thi) | (fi[2] ^ tlo) | (fi[3] ^ thi); PIN(bad); if (!__all(bad == 0u)) flags_wait(fio); }
                }
#pragma unroll
                for (int e = 0; e < 4; ++e) { float v = d0[e] + dppf<0x128>(d1[e]); asm("" : "+v"(v)); d0[e] = v; }
                if (nl < 8) *LP(u32x2, qw) = u32x2{pk2(d0[0], d0[1]), pk2(d0[2], d0[3])};
            }
            wg_barrier();
            if (pub) {
                bf16x8 qb[2][4];
#pragma unroll
                for (int s = 0; s < 4; ++s) { qb[0][s] = *LP(bf16x8, qr0 + s * 512); qb[1][s] = *LP(bf16x8, qr1 + s * 512); }
                __builtin_amdgcn_sched_barrier(0);
                f32x4 a = {0.f, 0.f, 0.f, 0.f}, b = a;
#pragma unroll
                for (int s = 0; s < 4; ++s) {
                    a = MFMA16(uk[0][s], qb[0][s], a); b = MFMA16(uk[1][s], qb[0][s], b);
                    a = MFMA16(uk[2][s], qb[1][s], a); b = MFMA16(uk[3][s], qb[1][s], b);
                }
                const u32x4 qv = {pk2s(a[0], a[1]), pk2s(a[2], a[3]), pk2s(b[0], b[1]), pk2s(b[2], b[3])};
                ST16(qv, qo2, 0);
            }
            STAMP(4);
            wg_barrier();
            if (LMPF && wv >= 3 && wv < 6) {
                const unsigned n = (unsigned)(j * 3 + wv - 3);
                if (n < 32) pf4<0>(((n & 7u) * 64 + lane) * 6144u + 2048u + (x * 4 + (n >> 3)) * 128u, p.w_mod, L_SCR + 31744);
            }
            if (QHELP && wv < 3 && j + 12 * wv < 32) {
                const int hh = j + 12 * wv;
#pragma unroll 1
                for (unsigned spins = 0; spins < 64; ++spins) {
                    __builtin_amdgcn_s_sleep(QHELP);
                    const u32x4 v = LD16((unsigned)(WS_Q + (hh & 7) * STR + (x * 4 + (hh >> 3)) * 1024 + lane * 16), 0);
                    if (__all(mx4(v, 0u) != SENT)) break;
                    asm volatile("" ::: "memory");
                }
                STAMP(6);
            }
        } else {
            const int cg = x * 2 + (j & 1);
            if (j < 14) {
                dma_piece(p.w_mod + (size_t)wv * 64 * 3072 + cg * 64, 6144, rgn, 8, NTM & 2);
                cnt_load();
                VMW(9);
            } else {
                cnt_load();
                ST16(s4, (unsigned)(WS_Q + wv * STR + cg * 2048 + lane * 16), 0); ST16(s4, (unsigned)(WS_Q + wv * STR + cg * 2048 + lane * 16), 1024);
                VMW(0);
                tag_take();
                wg_barrier();
                if (wv == 7) flag_put(WS_FI, cg);
                STAMP(15);
                if (j == 14) {
                    u32x2 best = {~0u, ~0u};
#pragma unroll
                    for (int r = 0; r < 2 * NCAL; ++r) {
                        const unsigned hf = (r ^ wv) & 1;
                        VMW(0);
                        const unsigned long long t0 = __builtin_amdgcn_s_memtime();
                        if (lane < 8) ST16(s4, WS_CAL + x * STR + hf * 8192 + (wv * 8 + r) * 128 + lane * 16, 0);
                        VMW(0);
                        const unsigned dt = (unsigned)(__builtin_amdgcn_s_memtime() - t0);
                        if (hf) best[1] = min(best[1], dt); else best[0] = min(best[0], dt);
                    }
                    *LP(u32x2, 16384 * 7 + 14336 + wv * 8) = best;
                    wg_barrier();
                    if (wv == 0 && lane < 8) {
                        u32x2 m = *LP(u32x2, 16384 * 7 + 14336 + lane * 8);
#pragma unroll
                        for (int o = 1; o < 8; o <<= 1) { m[0] = min(m[0], (unsigned)__shfl_xor(m[0], o)); m[1] = min(m[1], (unsigned)__shfl_xor(m[1], o)); }
                        ST16((u32x4{(unsigned)max(-1000, min(1000, (int)m[1] - (int)m[0])), 0u, tlo, thi}), (unsigned)(WS_NP + lane * STR + x * 16), 0);
                    }
                    if (wv == 0) {
                        const int d = (int)tagged_wait(WS_NP + x * STR + (lane & 7) * 16)[0];
                        int t = (lane & 4) ? -d : d;
                        t += __builtin_amdgcn_update_dpp(0, t, 0xB1, 0xf, 0xf, true);
                        t += __builtin_amdgcn_update_dpp(0, t, 0x4E, 0xf, 0xf, true);
                        t += __builtin_amdgcn_update_dpp(0, t, 0x141, 0xf, 0xf, true);
                        const bool n1 = abs(d) > 160 ? d < 0 : (t < 0) != ((lane & 4) != 0);
                        const unsigned mk = NEARP ? (unsigned)__ballot(n1) & 0xffu : 0u;
                        if (lane == 0) ST16((u32x4{mk, 0u, tlo, thi}), (unsigned)(WS_NM + x * STR), 0);
                    }
                    STAMP(9);
                }
            }
            const u16x8 cf = *LP(u16x8, rgn + 8192 + lane * 16), xa = *LP(u16x8, rgn + 9216 + lane * 16);
            u32x4 af[2];
#pragma unroll
            for (int q = 0; q < 4; ++q) {
                const float a = bf2f(cf[2 * q]), b = bf2f(cf[2 * q + 1]);
                af[0][q] = pk2(a * __builtin_amdgcn_rcpf(1.f + __expf(-a)), b * __builtin_amdgcn_rcpf(1.f + __expf(-b)));
                af[1][q] = (unsigned)__builtin_amdgcn_update_dpp(0, (int)af[0][q], 0x128, 0xf, 0xf, true);
            }
            const unsigned L_MS = L_SCR;
            unsigned wa[4], fr[4];
#pragma unroll
            for (int t = 0; t < 4; ++t) { wa[t] = L_MS + wv * 2048 + (nl & 7) * 256 + (((t * 4 + g) ^ (nl & 7)) << 4) + (nl >> 3) * 16384; fr[t] = rgn + frl + ((t ^ frs) << 5); }
            PIN4(wa[0], wa[1], wa[2], wa[3]); PIN4(fr[0], fr[1], fr[2], fr[3]); PIN(af[0]); PIN(af[1]);
            const unsigned L_XN = 12288;
            auto mod_mm = [&](bool ln) {
                VMW(0);
                bf16x8 f[2][4];
#pragma unroll
                for (int ks = 0; ks < 2; ++ks)
#pragma unroll
                    for (int t = 0; t < 4; ++t) f[ks][t] = tr_frag(fr[t] + ks * 4096, fr[t] + ks * 4096 + 512);
                __builtin_amdgcn_sched_barrier(0);
                f32x4 d[4];
#pragma unroll
                for (int t = 0; t < 4; ++t) d[t] = MFMA16(f[0][t], BF8(af[0]), (f32x4{0.f, 0.f, 0.f, 0.f}));
#pragma unroll
                for (int t = 0; t < 4; ++t) d[t] = MFMA16(f[1][t], BF8(af[1]), d[t]);
#pragma unroll
                for (int t = 0; t < 4; ++t) *LP(f32x4, wa[t]) = d[t];
                if (ln) {
                    float v[8];
#pragma unroll
                    for (int q = 0; q < 8; ++q) v[q] = bf2f(xa[q]);
                    const float p1 = ((v[0] + v[1]) + (v[2] + v[3])) + ((v[4] + v[5]) + (v[6] + v[7]));
                    const float p2 = ((v[0] * v[0] + v[1] * v[1]) + (v[2] * v[2] + v[3] * v[3])) + ((v[4] * v[4] + v[5] * v[5]) + (v[6] * v[6] + v[7] * v[7]));
                    float mb, rden;
                    ln_stats(p1, p2, mb, rden);
                    u32x4 r;
#pragma unroll
                    for (int q = 0; q < 4; ++q) { const unsigned d = pk2(v[2 * q] - mb, v[2 * q + 1] - mb); r[q] = pk2(lo16(d) * rden, hi16(d) * rden); }
                    *LP(u32x4, L_XN + wv * 16384 + lane * 16) = r;
                }
                wg_barrier();
            };
            auto sum8 = [&](unsigned a) {
                f32x4 u[8];
#pragma unroll
                for (int w2 = 0; w2 < 8; ++w2) u[w2] = *LP(f32x4, a + w2 * 2048);
                __builtin_amdgcn_sched_barrier(0);
                return ((u[0] + u[1]) + (u[2] + u[3])) + ((u[4] + u[5]) + (u[6] + u[7]));
            };
            if (j < 14) {
                const int hb = lane & 7, c4 = 2 * wv + (nl >> 3);
                unsigned hoff = (unsigned)(WS_H + g * STR + cg * 2048 + wv * 256 + nl * 16);
                unsigned sa = L_MS + hb * 256 + ((c4 ^ hb) << 4);
                unsigned xa2 = L_XN + hb * 16384 + (cg & 7) * 128 + c4 * 8;
                PIN(hoff); PIN(sa); PIN(xa2);
                STAMP(1);
                mod_mm(cg >= 8);
                STAMP(2);
                const u32x2 xn = *LP(u32x2, xa2);
                const f32x4 v = sum8(sa);
                tag_take();
                const unsigned thx = tlo ^ thi ^ TAGC;
                u32x2 r = {pk2(v[0], v[1]), pk2(v[2], v[3])};
                if (cg >= 8) {
#pragma unroll
                    for (int q = 0; q < 2; ++q) { const unsigned sc = pk2(1.f + lo16(r[q]), 1.f + hi16(r[q])); r[q] = pk2(lo16(xn[q]) * lo16(sc), hi16(xn[q]) * hi16(sc)); }
                }
                ST16((u32x4{r[0], tlo, r[1], thx}), hoff, 0);
                ST16((u32x4{r[0], tlo, r[1], thx}), hoff, 4 * STR);
                STAMP(5);
            }
            wg_barrier();
            while (__builtin_amdgcn_s_memrealtime() < t_start + LMD) __builtin_amdgcn_s_sleep(2);
            const int lg = x * 4 + (j & 3);
            dma_piece(p.w_mod + (size_t)wv * 64 * 3072 + 1024 + lg * 64, 6144, rgn, 8, NTM & 4);
            mod_mm(false);
            if (lg >= 8 && lg < 24) {
                if (wv == 0) {
                    const int b = lane >> 3, c8 = lane & 7;
                    const f32x4 v0 = sum8(L_MS + b * 256 + (((2 * c8) ^ b) << 4)), v1 = sum8(L_MS + b * 256 + (((2 * c8 + 1) ^ b) << 4));
                    ST16((u32x4{pk2(v0[0], v0[1]), pk2(v0[2], v0[3]), pk2(v1[0], v1[1]), pk2(v1[2], v1[3])}), (unsigned)(WS_MB + b * STR + (lg - 8) * 128 + c8 * 16), 0);
                }
            } else if (wv < 4) {
                const int b = wv * 2 + (lane >> 5), cp = lane & 31;
                f32x2_t u[8];
#pragma unroll
                for (int w2 = 0; w2 < 8; ++w2) u[w2] = *LP(f32x2_t, L_MS + w2 * 2048 + b * 256 + (((cp >> 1) ^ b) << 4) + (cp & 1) * 8);
                const f32x2_t v = ((u[0] + u[1]) + (u[2] + u[3])) + ((u[4] + u[5]) + (u[6] + u[7]));
                ST16((u32x4{__float_as_uint(rbf(v[0])), __float_as_uint(rbf(v[1])), tlo, thi}), (unsigned)(WS_MOD + b * STR + (lg * 32 + cp) * 16), 0);
            }
            STAMP(6);
        }
        const int bq = (j >> 2) & 1, dh = j >> 3, src = th * 2 + dh, rb = bq * 4 + (src >> 4), rc = src & 15;
        wg_barrier();
        if (TLBW > 1 && j == 0) {
            if (wv == 4 && lane < 5) pf4s((unsigned)((lane + 1) * 2097152 + 8064), ws, L_SCR + 31744);
            if (wv == 5 && lane < 2) { pf4s((unsigned)(lane * 2097152), p.w_uv, L_SCR + 31744); pf4s((unsigned)(lane * 2097152), p.w_o, L_SCR + 31744); }
            if (wv == 6 && lane < 1) { pf4s(0u, p.w1, L_SCR + 31744); pf4s(0u, p.w2, L_SCR + 31744); pf4s(0u, p.out, L_SCR + 31744); }
        }
        while (__builtin_amdgcn_s_memrealtime() < t_start + TWD - 110) __builtin_amdgcn_s_sleep(2);
        const unsigned npm = __builtin_amdgcn_readfirstlane(tagged_wait(WS_NM + x * STR)[0]), np = (npm >> x) & 1u;
        unsigned oo = (unsigned)(WS_OUTP + ((bq * 4 + (nl & 3)) * 16 + wv * 2 + (nl >> 3)) * 16384 + (src ^ wv) * 128 + ((nl >> 2) & 1) * 64 + g * 16) + ((~npm >> ((nl & 3) * 2 + (wv >> 2))) & 1u) * 8192u;
        const int rcol = rc * 32 + (lane & 7) * 4 + (lane >> 5) * 2;
        unsigned xdst = (unsigned)(WS_X1 + ((lane >> 3) & 3) * STR + (rb * 512 + rcol) * 2), xdst2 = xdst + 4 * STR + ((npm >> (4 + ((lane >> 3) & 3))) & 1u) * 8192u;
        xdst += ((npm >> ((lane >> 3) & 3)) & 1u) * 8192u;
        ST16(s4, oo, 0);
        if (wv == 7) { __builtin_amdgcn_raw_buffer_store_b32(SENT, rw, xdst, 0, 16); __builtin_amdgcn_raw_buffer_store_b32(SENT, rw, xdst2, 0, 16); }
        VMW(0);
        wg_barrier();
        if (wv == 0) flag_put(WS_FB, id);
        while (__builtin_amdgcn_s_memrealtime() < t_start + TWD) __builtin_amdgcn_s_sleep(2);
        asm volatile("s_barrier" ::: "memory");
        STAMP(7);
        if ((PFM & 8) && wv == 0 && lane < 32) pf4<NTM & 8>((unsigned)((j * 32 + lane) * 8192 + x * 1024 + ((3u - ((unsigned)(size_t)p.w_uv >> 7)) & 7u) * 128), p.w_uv, L_SCR + 31744);
        if (bq) dma_piece(p.w_o + (size_t)(th * 128 + dh * 64) * 512 + wv * 64, 1024, rgn + 8192, 8, NTM & 8);
        dma_piece(p.w_uv + ((size_t)wv * 64 * NH + th) * 128 + dh * 64, 8192, rgn, 8, NTM & 8);
        if (!bq) dma_piece(p.w_o + (size_t)(th * 128 + dh * 64) * 512 + wv * 64, 1024, rgn + 8192, 8, NTM & 8);
        u32x4 fb, gm;
        unsigned xg;
        VMW(4);
        if (wv == 7)
            asm volatile("buffer_load_dwordx4 %0, %3, %5, 0 offen sc1\n\tbuffer_load_dwordx4 %1, %4, %5, 0 offen sc1\n\tglobal_load_dword %2, %6, %7" : "=&v"(fb), "=&v"(gm), "=&v"(xg)
                         : "v"((unsigned)(WS_FB + x * STR + lane * 16)), "v"((unsigned)(WS_MOD + rb * STR + rcol * 8)), "s"(rw), "v"((unsigned)(rb * 512 + rcol) * 2u), "s"(p.x) : "memory");
        VMW(0);
        asm volatile("" : "+v"(fb), "+v"(gm), "+v"(xg));
        STAMP(8);
        bf16x8 fuv[2][4], fwo[2][4];
        unsigned pfa[4];
#pragma unroll
        for (int t = 0; t < 4; ++t) { pfa[t] = rgn + frl + ((t ^ frs) << 5); PIN(pfa[t]); }
#pragma unroll
        for (int ks = 0; ks < 2; ++ks)
#pragma unroll
            for (int t = 0; t < 4; ++t) {
                const unsigned a = rgn + (unsigned)((g * 4 + (nl >> 2)) * 128 + (lane & 3) * 8) + ((t ^ (unsigned)((nl >> 3) | (g & 2))) << 5) + ks * 4096;
                fuv[ks][t] = tr_frag(a, a + 2048);
                fwo[ks][t] = tr_frag(pfa[t] + 8192 + ks * 4096, pfa[t] + 8192 + ks * 4096 + 512);
            }
#pragma unroll
        for (int ks = 0; ks < 2; ++ks) { PIN4(fuv[ks][0], fuv[ks][1], fuv[ks][2], fuv[ks][3]); PIN4(fwo[ks][0], fwo[ks][1], fwo[ks][2], fwo[ks][3]); }
        const unsigned L_WGT = L_SCR, L_MSK = L_SCR + 256, L_RED = L_SCR + 1024;
        unsigned psrc = (unsigned)(WS_PART + (bq * 4 * 32 + th) * 32768 + (wv >> 2) * 16384 + (wv & 3) * 2048 + ((lane >> 3) ^ (th & 7)) * 128) + np * 8192u + pcl, psrc1 = (psrc ^ 64u) + 1024;
        unsigned psrc2 = psrc ^ 8192u, psrc3 = psrc1 ^ 8192u;
        PIN(psrc2); PIN(psrc3); PIN(xdst2);
        unsigned rw2 = L_RED + wv * 1024 + (nl & 3) * 256 + g * 16 + (YSEL ? (nl >> 2) * 64 : 0);
        unsigned wa0 = (nl & 3) == (g >> 1) ? L_WGT + (HILO ? nl & 4 : 0) * 32 + g * 16 : L_WGT + 272, wa1 = (nl & 3) == 2 + (g >> 1) ? L_WGT + (HILO ? nl & 4 : 0) * 32 + 64 + g * 16 : L_WGT + 272;
        unsigned cr = rgn + (nl & 3) * 128 + g * 16;
        PIN(cr);
        unsigned r2r = L_RED + lane * 16, cw = rgn + lane * 8;
        unsigned osrc = (unsigned)(WS_OUTP + (rb * 16 + rc) * 16384 + lane * 16) + (np ^ 1u) * 8192u;
        PIN(psrc); PIN(psrc1); PIN(rw2); PIN(wa0); PIN(wa1); PIN(r2r); PIN4(cw, oo, osrc, xdst);
        float mlm = 0.f, mll = 0.f;
        if (wv == 0) *LP(unsigned, L_WGT + 256 + lane * 4) = 0u;
        wg_barrier();
        const bool early = __all(flags_ok(fb) & (gm[2] == tlo) & (gm[3] == thi));
        u32x2 ga = {gm[0], gm[1]};
        PIN(ga);
        bf16x8 pa[2][4], wh[2];
        {
            u32x4 ml = {0u, 0u, ~tlo, ~thi};
            const unsigned mlsrc = (unsigned)(WS_ML + (bq * 4 + g) * STR + nl * 512 + th * 16) + ((npm >> (x & 6)) & 1u) * 8192u;
            unsigned long long done = 0ull;
            unsigned lm = L_MSK;
            PIN(lm);
#define PLD(bit, off, v, base) "s_bitcmp1_b64 %0, " #bit "\n\ts_cbranch_scc0 1f\n\ts_add_u32 m0, %1, " #off "\n\ts_nop 0\n\tglobal_load_lds_dwordx4 " v ", " base " sc1\n1:\n\t"
            auto send = [&](unsigned long long m) {
                asm volatile(PLD(0, 0, "%2", "%4") PLD(8, 1024, "%3", "%4") PLD(16, 2048, "%2", "%5") PLD(24, 3072, "%3", "%5")
                             PLD(32, 4096, "%8", "%6") PLD(40, 5120, "%9", "%6") PLD(48, 6144, "%8", "%7") PLD(56, 7168, "%9", "%7")
                             :: "s"(m), "s"(rgn), "v"(psrc), "v"(psrc1), "s"(ws), "s"(ws + 1048576), "s"(ws + 2097152), "s"(ws + 3145728), "v"(psrc2), "v"(psrc3) : "memory", "m0", "scc");
            };
            if (wv == 0) {
#pragma unroll 1
                for (unsigned spins = 0; spins < kSpinCap; ++spins) {
                    if ((ml[2] != tlo) | (ml[3] != thi)) ml = LD16(mlsrc, 0);
                    const unsigned long long m = __ballot((ml[2] == tlo) & (ml[3] == thi));
                    if (m == ~0ull) break;
                    unsigned long long f = m & (m >> 4);
                    f &= f >> 2; f &= f >> 1;
                    asm volatile("ds_write_b64 %0, %1" :: "v"(lm), "v"(f) : "memory");
                }
                asm volatile("ds_write_b64 %0, %1" :: "v"(lm), "v"(~0ull) : "memory");
            } else {
#pragma unroll 1
                for (unsigned spins = 0; spins < kSpinCap && __popcll(done) < TK; ++spins) {
                    unsigned long long t;
                    asm volatile("ds_read_b64 %0, %1\n\ts_waitcnt lgkmcnt(0)" : "=v"(t) : "v"(lm) : "memory");
                    const unsigned long long m = (((unsigned long long)(unsigned)__builtin_amdgcn_readfirstlane((unsigned)(t >> 32)) << 32) | (unsigned)__builtin_amdgcn_readfirstlane((unsigned)t)) & 0x0101010101010101ull & ~done;
                    if (m) { done |= m; send(m); }
                }
            }
            asm volatile("s_barrier" ::: "memory");
            send(~done);
            mlm = __uint_as_float(ml[0]); mll = __uint_as_float(ml[1]);
            STAMP(10);
            if (wv == 0) {
                float M = mlm;
                M = fmaxf(M, dppf<0xB1>(M)); M = fmaxf(M, dppf<0x4E>(M)); M = fmaxf(M, dppf<0x141>(M)); M = fmaxf(M, dppf<0x140>(M));
                const float e = __builtin_amdgcn_exp2f(mlm - M);
                const float w = e * __builtin_amdgcn_rcpf(sum16(e * mll));
                const unsigned short hi = f2bf(w);
                *LP(unsigned short, L_WGT + lane * 2) = hi;
                if (HILO) *LP(unsigned short, L_WGT + 128 + lane * 2) = f2bf(w - bf2f(hi));
            }
            wg_barrier();
            wh[0] = *LP(bf16x8, wa0); wh[1] = *LP(bf16x8, wa1);
            __builtin_amdgcn_sched_barrier(0);
            VMW(0);
#pragma unroll
            for (int ks = 0; ks < 2; ++ks)
#pragma unroll
                for (int t = 0; t < 4; ++t) pa[ks][t] = tr_frag(pfa[t] + ks * 4096, pfa[t] + ks * 4096 + 512);
        }
        STAMP(11);
        {
            f32x4 d[4];
#pragma unroll
            for (int t = 0; t < 4; ++t) d[t] = MFMA16(pa[0][t], wh[0], (f32x4{0.f, 0.f, 0.f, 0.f}));
#pragma unroll
            for (int t = 0; t < 4; ++t) d[t] = MFMA16(pa[1][t], wh[1], d[t]);
#pragma unroll
            for (int t = 0; t < 4; ++t)
#pragma unroll
                for (int e = 0; e < 4; ++e) if (HILO) { float v = d[t][e] + dppf<0x124>(d[t][e]); asm("" : "+v"(v)); d[t][e] = v; }
            const u32x4 o0 = {pk2(d[0][0], d[0][1]), pk2(d[0][2], d[0][3]), pk2(d[1][0], d[1][1]), pk2(d[1][2], d[1][3])};
            const u32x4 o1 = {pk2(d[2][0], d[2][1]), pk2(d[2][2], d[2][3]), pk2(d[3][0], d[3][1]), pk2(d[3][2], d[3][3])};
            f32x4 y[4];
#pragma unroll
            for (int t = 0; t < 4; ++t) y[t] = MFMA16(fuv[0][t], BF8(o0), (f32x4{0.f, 0.f, 0.f, 0.f}));
#pragma unroll
            for (int t = 0; t < 4; ++t) y[t] = MFMA16(fuv[1][t], BF8(o1), y[t]);
            if (YSEL) *LP(f32x4, rw2) = (nl & 8) ? ((nl & 4) ? y[3] : y[2]) : ((nl & 4) ? y[1] : y[0]);
            else if (nl < 4) {
#pragma unroll
                for (int t = 0; t < 4; ++t) *LP(f32x4, rw2 + t * 64) = y[t];
            }
        }
        wg_barrier();
        {
            f32x4 cs[8];
#pragma unroll
            for (int w2 = 0; w2 < 8; ++w2) cs[w2] = *LP(f32x4, r2r + w2 * 1024);
            const f32x4 c4 = ((cs[0] + cs[1]) + (cs[2] + cs[3])) + ((cs[4] + cs[5]) + (cs[6] + cs[7]));
            *LP(u32x2, cw) = u32x2{pk2(c4[0], c4[1]), pk2(c4[2], c4[3])};
            asm volatile("s_waitcnt lgkmcnt(0)" ::: "memory");
            const u32x4 cx0 = *LP(u32x4, cr), cx1 = *LP(u32x4, cr + 64);
            f32x4 o[4];
#pragma unroll
            for (int q = 0; q < 4; ++q) o[q] = MFMA16(fwo[0][q], BF8(cx0), (f32x4{0.f, 0.f, 0.f, 0.f}));
#pragma unroll
            for (int q = 0; q < 4; ++q) o[q] = MFMA16(fwo[1][q], BF8(cx1), o[q]);
            const f32x4 oa = (nl & 8) ? ((nl & 4) ? o[3] : o[2]) : ((nl & 4) ? o[1] : o[0]);
            const u32x4 sv = {nosent(__float_as_uint(oa[0])), nosent(__float_as_uint(oa[1])), nosent(__float_as_uint(oa[2])), nosent(__float_as_uint(oa[3]))};
            ST16(sv, oo, 0);
        }
        STAMP(12);
        wg_barrier();
        if (wv == 7) {
            if (!early) {
                flags_wait(WS_FB + x * STR + lane * 16);
                { const u32x4 v = tagged_wait(WS_MOD + rb * STR + rcol * 8); ga = u32x2{v[0], v[1]}; }
                PIN(ga);
            }
            __builtin_amdgcn_s_sleep(SLPR);
            u32x4 op[8];
            for (unsigned spins = 0;;) {
#pragma unroll
                for (int k = 0; k < 8; ++k) op[k] = LD16(osrc + (k & 3) * 1024, (k >> 2) * 4096);
                unsigned m = 0u;
#pragma unroll
                for (int k = 0; k < 8; ++k) m = mx4(op[k], m);
                if (__all(m != SENT)) break;
                if (++spins > kSpinCap) break;
                asm volatile("" ::: "memory");
            }
            STAMP(13);
            const f32x4 v = ((__builtin_bit_cast(f32x4, op[0]) + __builtin_bit_cast(f32x4, op[1])) + (__builtin_bit_cast(f32x4, op[2]) + __builtin_bit_cast(f32x4, op[3]))) +
                            ((__builtin_bit_cast(f32x4, op[4]) + __builtin_bit_cast(f32x4, op[5])) + (__builtin_bit_cast(f32x4, op[6]) + __builtin_bit_cast(f32x4, op[7])));
            float w[2];
#pragma unroll
            for (int q = 0; q < 2; ++q) {
                auto t = __builtin_amdgcn_permlane32_swap(__float_as_uint(v[q]), __float_as_uint(v[2 + q]), false, false);
                w[q] = __uint_as_float(t[0]) + __uint_as_float(t[1]);
                auto u = __builtin_amdgcn_permlane16_swap(__float_as_uint(w[q]), __float_as_uint(w[q]), false, false);
                w[q] = __uint_as_float(u[0]) + __uint_as_float(u[1]);
                w[q] += dppf<0x128>(w[q]);
            }
            unsigned t = pk2(w[0], w[1]);
            t = pk2(__uint_as_float(ga[0]) * lo16(t), __uint_as_float(ga[1]) * hi16(t));
            t = pk2s(lo16(xg) + lo16(t), hi16(xg) + hi16(t));
            __builtin_amdgcn_raw_buffer_store_b32(t, rw, xdst, 0, 16);
            __builtin_amdgcn_raw_buffer_store_b32(t, rw, xdst2, 0, 16);
            STAMP(14);
        }
        STAMP_FLUSH();
        return;
    }

    const int s = j - 16;
    const unsigned long long t_s = __builtin_amdgcn_s_memrealtime();
    if (!CNL && wv == 0) cnt_load();
    {
        const unsigned ln = (unsigned)(s * 64 + lane);
        if ((PFM & 1) && wv == 0) pf4<NTM & 1>((ln & 511u) * 6144u + (x * 2 + (ln >> 9)) * 128u, p.w_mod, L_SCR);
        if ((PFM & 2) && wv == 1) {
            const unsigned short* w = s < 8 ? p.w_uk : p.w_q;
            pf4<NTM & 1>((ln & 511u) * 8192u + x * 1024u + ((3u - ((unsigned)(size_t)w >> 7)) & 7u) * 128u, w, L_SCR);
        }
        if (SD) __builtin_amdgcn_s_sleep(SD);
        if ((PFM & 48) == 48) {
            const unsigned r0 = (unsigned)(s * 4 + (wv & 3)), row = PFS == 0 ? r0 * 8 + (lane >> 3) : PFS == 1 ? (lane >> 3) * 64 + r0 : (r0 >> 5) * 256 + (lane >> 3) * 32 + (r0 & 31);
            pf4<NTM & 1>(row * 8192u + x * 1024u + (lane & 7u) * 128u, wv < 4 ? p.w_uk : p.w_q, L_SCR);
        }
        else if ((PFM & 16) && wv >= 4) { const unsigned l2 = (unsigned)((s * 4 + (wv & 3)) * 64 + lane); pf4<NTM & 1>((l2 >> 3) * 8192u + x * 1024u + (l2 & 7u) * 128u, p.w_q, L_SCR); }
        else if ((PFM & 32) && wv >= 4) { const unsigned l2 = (unsigned)((s * 4 + (wv & 3)) * 64 + lane); pf4<NTM & 1>((l2 >> 3) * 8192u + x * 1024u + (l2 & 7u) * 128u, p.w_uk, L_SCR); }
        if ((PFM & 4) && (wv & 6) == 2) { const unsigned l2 = (unsigned)((s * 2 + (wv & 1)) * 64 + lane); pf4<NTM & 1>((l2 & 511u) * 8192u + (x * 4 + (l2 >> 9)) * 256u, p.w_q, L_SCR); }
    }
    const int ug = x * 7 + s;
    const unsigned vb = (unsigned)(g * 1024 + ((nl ^ (g << 2)) << 4)), lb = (unsigned)((wv >> 1) * 32768 + (wv & 1) * 4096);
    const char* ck = (const char*)p.ckv + ((size_t)x * SK + s * 256 + wv * 16) * 1024;
    const unsigned rot = ROTC ? (unsigned)s & 3u : 0u;
    const unsigned vbr = vb ^ (rot << 8);
    const char* ck2 = ck + 131072;
    auto ck_dma = [&](unsigned i) { dma16x<NTM & 16>(vbr, ((i & 3u) << 4) | (((i >> 2) & 3u) << 8) | ((i & 3u) << 12), (i >> 4) ? ck2 : ck, lb + ((i >> 2) & 3u) * 8192u + (i & 3u) * 1024u); };
    bf16x8 ka[16];
    s16x4 va[8][4];
    unsigned kad[4], vad[4], vad2[4];
    auto kq = [](unsigned u) { return (0x78u >> (2 * u)) & 3u; };
    auto ka_reads = [&](int cb) {
#pragma unroll
        for (int u = 0; u < 4; ++u) ka[cb * 4 + u] = *LP(bf16x8, kad[u] + cb * 8192);
    };
    auto va_reads = [&]() {
#pragma unroll
        for (int kk = 0; kk < 8; ++kk)
#pragma unroll
            for (int q = 0; q < 4; ++q) va[kk][q] = tr4((kk < 4 ? vad[q] : vad2[q]) + ((kk >> 1) & 1) * 32768 + (kk & 1) * 4096);
    };
    auto va_read1 = [&](int kk) {
#pragma unroll
        for (int q = 0; q < 4; ++q) va[kk][q] = tr4((kk < 4 ? vad[q] : vad2[q]) + ((kk >> 1) & 1) * 32768 + (kk & 1) * 4096);
    };
    auto va_pin = [&]() {
#pragma unroll
        for (int q = 0; q < 8; ++q) PIN4(va[q][0], va[q][1], va[q][2], va[q][3]);
    };
    u32x4 fa = {0u, 0u, 0u, 0u};
    const unsigned fao = (unsigned)(WS_FI + x * STR + (lane & 7) * 16);
    constexpr int LAST0 = GI + NPOST, LAST = LAST0 + NP2;
    static_assert(GI - WG >= 15 && GI < 20 && LAST >= 19 && LAST < 28, "");
    const unsigned qsrc = (unsigned)(WS_Q + (wv & 1) * 4 * STR + (x * 4 + (wv >> 1)) * 1024) + (g ^ rot) * 256 + ((nl ^ ((wv * 4) & 15)) << 4);
    auto q_dma = [&](int k) {
        unsigned t;
        asm volatile("s_mov_b32 m0, %3\n\tv_xor_b32 %0, %4, %1\n\tglobal_load_lds_dwordx4 %0, %2 sc1" : "=&v"(t) : "v"(qsrc), "s"(ws), "s"(L_SCR + (wv * 4 + k) * 1024), "i"(k * (STR + 16)) : "memory", "m0");
    };
#pragma unroll
    for (int i = 0; i <= GI; ++i) {
        ck_dma(i);
        if (CNL && i == 5 && wv == 0) cnt_load();
        if (i == 3) {
#pragma unroll
            for (int u = 0; u < 4; ++u) {
                kad[u] = tile_off(wv * 16 + kq(nl >> 2) * 4 + (nl & 3), 4 * u + g);
                vad[u] = tile_off(8 * (lane >> 5) + 4 * (u & 1) + (nl >> 2), wv * 8 + 4 * (u >> 1) + 2 * (g & 1) + ((lane & 3) >> 1)) + 8 * (lane & 1);
                vad2[u] = vad[u] + 65536;
                PIN(kad[u]); PIN(vad[u]); PIN(vad2[u]);
            }
        }
        if (i >= 4) VMW(4);
        if (i == 15 && wv == 0) {
            PIN(fa);
            tag_take();
            if (!__all(flags_ok(fa))) flags_wait(fao);
        }
        if ((i & 3) == 3 && i >= 7 && i <= 15) {
            const int cb = (i - 7) >> 2;
            if (cb > 0) { PIN4(ka[cb * 4 - 4], ka[cb * 4 - 3], ka[cb * 4 - 2], ka[cb * 4 - 1]); va_pin(); }
            wg_barrier();
            ka_reads(cb);
            if ((wv >> 1) == cb) va_reads();
            STAMP(16 + cb);
        }
        if (i == 10 && wv == 0) asm volatile("buffer_load_dwordx4 %0, %1, %2, 0 offen sc1" : "+v"(fa) : "v"(fao), "s"(rw) : "memory");
    }
    PIN4(ka[8], ka[9], ka[10], ka[11]); va_pin();
    while (__builtin_amdgcn_s_memrealtime() < t_s + TG) __builtin_amdgcn_s_sleep(1);
    VMW(WG);
    q_dma(0); q_dma(1); q_dma(2); q_dma(3);
    asm volatile("s_barrier" ::: "memory");
    STAMP(19);
#pragma unroll
    for (int i = GI + 1; i <= LAST0; ++i) ck_dma(i);
    ka_reads(3);
    if ((wv >> 1) == 3) va_reads();
    VMW(NPOST);
    const unsigned L_EXM = L_SCR - 1024, L_BIG = L_SCR + 512 + 128;
    unsigned nx = LAST + 1, npm = 0u;
    unsigned qad[4];
    {
        unsigned qck = L_SCR + wv * 4096 + lane * 16;
        u32x4 qb[4];
#pragma unroll
        for (int k = 0; k < 4; ++k) qb[k] = *LP(u32x4, qck + k * 1024);
#pragma unroll
        for (int i = LAST0 + 1; i <= LAST; ++i) ck_dma(i);
#pragma unroll
        for (int k = 0; k < 4; ++k) { qad[k] = L_SCR + nl * 1024 + (((4 * k + g) ^ nl) << 4); PIN(qad[k]); }
        STAMP(20);
        if (!__all(mx4(qb[0], mx4(qb[1], mx4(qb[2], mx4(qb[3], 0u)))) != SENT)) {
            for (unsigned spins = 0;;) {
#pragma unroll
                for (int k = 0; k < 4; ++k)
                    if (!__all(mx4(qb[k], 0u) != SENT)) q_dma(k);
                if (nx <= 25) {
#pragma unroll
                    for (int i = 0; i < 3; ++i) ck_dma(nx + i);
                    nx += 3;
                    VMW(3);
                } else {
                    for (; nx < 28; ++nx) ck_dma(nx);
                    VMW(0);
                }
#pragma unroll
                for (int k = 0; k < 4; ++k) qb[k] = *LP(u32x4, qck + k * 1024);
                if (__all(mx4(qb[0], mx4(qb[1], mx4(qb[2], mx4(qb[3], 0u)))) != SENT)) break;
                if (++spins > kSpinCap) break;
            }
        }
    }
    constexpr int N0 = LAST + 1 + SL0, NE = N0 + 6 < 31 ? N0 + 6 : 31;
    auto slot = [&](int i) { if (i < 28) { if (nx <= (unsigned)i) ck_dma(i); } else ck_dma(i); };
    const unsigned kg = kq(g);
    unsigned exw = L_EXM + ((nl + (lane >> 5) * 16) * 8 + wv) * 4, exr = L_EXM + nl * 32;
    unsigned pwr = L_SCR + nl * 1024 + 768 + (((2 * wv + (kg >> 1)) ^ nl) << 4) + (kg & 1) * 8, esw = L_SCR + nl * 1024 + 512 + ((wv ^ (nl & 7)) << 4) + g * 4;
    unsigned prd = L_SCR + (lane & 31) * 1024 + 768 + (((lane >> 5) ^ nl) << 4);
    PIN4(exw, exr, pwr, esw); PIN(prd);
    PIN4(ka[12], ka[13], ka[14], ka[15]); va_pin();
    wg_barrier();
    STAMP(21);
    bf16x8 qr[8][2];
    auto q_ld = [&](int ks, int hg) { return *LP(bf16x8, qad[ks & 3] + (ks >> 2) * 256 + hg * 16384); };
    auto p_ld = [&](int kk) { return *LP(bf16x8, prd ^ (kk << 5)); };
    float ls0, ls1, m0, m1;
    f32x4 sa, sb;
    f32x16 o[2];
    unsigned sw[4], srd, p0, mlo, esr, fbo;
    u32x2 tg;
    u32x4 nq;
    auto probs = [&](float (&e)[8]) {
#pragma unroll
        for (int r = 0; r < 4; ++r) { e[r] = __builtin_amdgcn_exp2f(fmaf(sa[r], SCL, -m0)); e[4 + r] = __builtin_amdgcn_exp2f(fmaf(sb[r], SCL, -m1)); }
        *LP(u32x2, pwr) = u32x2{pk2(e[0], e[1]), pk2(e[2], e[3])};
        *LP(u32x2, pwr + 16384) = u32x2{pk2(e[4], e[5]), pk2(e[6], e[7])};
    };
    auto wave_max = [&]() {
        const float a = fmaxf(fmaxf(sa[0], sa[1]), fmaxf(sa[2], sa[3])), b = fmaxf(fmaxf(sb[0], sb[1]), fmaxf(sb[2], sb[3]));
        auto u = __builtin_amdgcn_permlane32_swap(__float_as_uint(a), __float_as_uint(b), false, false);
        const float c = fmax3(__uint_as_float(u[0]), __uint_as_float(u[1]), __uint_as_float(u[1]));
        auto v = __builtin_amdgcn_permlane16_swap(__float_as_uint(c), __float_as_uint(c), false, false);
        return fmax3(__uint_as_float(v[0]), __uint_as_float(v[1]), __uint_as_float(v[1]));
    };
    auto max8 = [&](unsigned a) {
        const f32x4 a0 = *LP(f32x4, a), a1 = *LP(f32x4, a + 16);
        return fmax3(fmax3(a0[0], a0[1], a0[2]), fmax3(a0[3], a1[0], a1[1]), fmax3(a1[2], a1[3], a1[3])) * SCL;
    };
    {
        bf16x8 qs[8][2];
#pragma unroll
        for (int ks = 0; ks < 8; ++ks) { qr[ks][0] = q_ld(ks + 8, 0); qr[ks][1] = q_ld(ks + 8, 1); }
        __builtin_amdgcn_sched_barrier(0);
#pragma unroll
        for (int i = LAST + 1; i < LAST + 1 + SL0; ++i) if (i < NE) slot(i);
        sa = f32x4{0.f, 0.f, 0.f, 0.f}; sb = sa;
        f32x4 s2a = sa, s2b = sa;
        bf16x8 kb[4];
#pragma unroll
        for (int n = 0; n < 16; ++n) {
            const int ks = (n + 8) & 15;
            sa = MFMA16(ka[ks], ks < 8 ? qs[ks][0] : qr[ks - 8][0], sa);
            sb = MFMA16(ka[ks], ks < 8 ? qs[ks][1] : qr[ks - 8][1], sb);
            if (QKM && n == 4) { VMW(LAST + SL0 + 1 - 19); for (int u = 0; u < 4; ++u) kb[u] = *LP(bf16x8, kad[u]); }
            if (QKM && ks < 4) { s2a = MFMA16(kb[ks], qs[ks][0], s2a); s2b = MFMA16(kb[ks], qs[ks][1], s2b); }
            if (n >= 8 - QD && n < 16 - QD) { qs[n + QD - 8][0] = q_ld(n + QD - 8, 0); qs[n + QD - 8][1] = q_ld(n + QD - 8, 1); }
            if ((n & 3) == 3 && LAST + 1 + SL0 + (n >> 2) < NE) slot(LAST + 1 + SL0 + (n >> 2));
            __builtin_amdgcn_sched_barrier(0);
        }
        *LP(float, exw) = wave_max();
        if (N0 + 4 < NE) slot(N0 + 4);
        wg_barrier();
        m0 = max8(exr); m1 = max8(exr + 512);
#pragma unroll
        for (int ks = 4; ks < 8; ++ks) { qs[ks][0] = q_ld(ks, 0); qs[ks][1] = q_ld(ks, 1); }
        __builtin_amdgcn_sched_barrier(0);
        float e[8];
        probs(e);
        ls0 = (e[0] + e[1]) + (e[2] + e[3]); ls1 = (e[4] + e[5]) + (e[6] + e[7]);
        PIN(ls0); PIN(ls1);
        if (wv == 0) *LP(u32x4, L_BIG) = u32x4{0u, 0u, tlo, thi};
        if (N0 + 5 < NE) slot(N0 + 5);
        VMW(NE - 24);
        ka_reads(1);
        asm volatile("s_waitcnt lgkmcnt(4)\n\ts_barrier" ::: "memory");
        STAMP(22);
        static_assert(NE >= 28 && QKM == 1, "");
        bf16x8 pf[8];
        tg = *LP(u32x2, L_BIG + 8);
        pf[0] = p_ld(0); pf[1] = p_ld(1);
        slot(NE);
        sa = s2a; sb = s2b;
        __builtin_amdgcn_sched_barrier(0);
#pragma unroll
        for (int ks = 4; ks < 8; ++ks) { sa = MFMA16(ka[ks], qs[ks][0], sa); sb = MFMA16(ka[ks], qs[ks][1], sb); }
        __builtin_amdgcn_sched_barrier(0);
#pragma unroll
        for (int i = 0; i < 16; ++i) { o[0][i] = 0.f; o[1][i] = 0.f; }
#pragma unroll
        for (int kk = 0; kk < 8; ++kk) {
            if (kk == 0) {
#pragma unroll
                for (int q = 2; q < 8; ++q) pf[q] = p_ld(q);
            }
            if (kk == 2) { PIN4(pf[2], pf[3], pf[4], pf[5]); PIN(pf[6]); PIN(pf[7]); PIN(tg); }
            if (kk >= 2 && wv < 4) { va_read1(kk - 2); if (kk == 7) va_read1(6); }
            o[0] = MFMA32(cat8(va[kk][0], va[kk][1]), pf[kk], o[0]);
            o[1] = MFMA32(cat8(va[kk][2], va[kk][3]), pf[kk], o[1]);
            if ((kk & 1) && NE + 1 + (kk >> 1) < 32) slot(NE + 1 + (kk >> 1));
            __builtin_amdgcn_sched_barrier(0);
        }
        if (wv < 4) va_read1(7);
        VMW(4);
        ka_reads(2);
        tlo = __builtin_amdgcn_readfirstlane(tg[0]); thi = __builtin_amdgcn_readfirstlane(tg[1]);
        rederive();
        fbo = (unsigned)(WS_FB + x * STR + lane * 16);
        asm volatile("buffer_load_dwordx4 %0, off, %1, %2 sc1" : "=v"(nq) : "s"(rw), "s"(WS_NM + x * STR) : "memory");
        const unsigned head = lane & 31, r8 = lane >> 3, cw = wv ^ (rot * 2);
        srd = lb + r8 * 128 + (((lane & 7) ^ r8) << 4);
        sw[0] = lb + head * 128 + (((lane >> 5) ^ (head & 7)) << 4);
        sw[1] = sw[0] ^ 32u; sw[2] = sw[0] ^ 64u; sw[3] = sw[0] ^ 96u;
        p0 = (unsigned)(WS_PART + x * 1048576) + (cw >> 2) * 16384 + (cw & 3) * 2048 + r8 * 32768 + ((s ^ r8) * 8 + (lane & 7)) * 16;
        PIN4(sw[0], sw[1], sw[2], sw[3]); PIN(srd); PIN(p0); PIN(fbo);
#pragma unroll
        for (int ks = 8; ks < 12; ++ks) { sa = MFMA16(ka[ks], qr[ks - 8][0], sa); sb = MFMA16(ka[ks], qr[ks - 8][1], sb); }
    }
    STAMP(23);
    unsigned po[4];
    float mrun;
    {
        VMW(1);
        STAMP(24);
        ka_reads(3);
        asm volatile("" : "=v"(fa));
        if (wv == 0 && s < 11) asm volatile("buffer_load_dwordx4 %0, %1, %2, 0 offen sc1" : "+v"(fa) : "v"(fbo), "s"(rw) : "memory");
        __builtin_amdgcn_sched_barrier(0);
#pragma unroll
        for (int ks = 12; ks < 16; ++ks) { sa = MFMA16(ka[ks], qr[ks - 8][0], sa); sb = MFMA16(ka[ks], qr[ks - 8][1], sb); }
        bf16x8 pf[8];
#pragma unroll 1
        for (bool first = true;; first = false) {
            float e[8];
            probs(e);
            const float n0 = (e[0] + e[1]) + (e[2] + e[3]), n1 = (e[4] + e[5]) + (e[6] + e[7]);
            *LP(float, esw) = ls0 + n0; *LP(float, esw + 16384) = ls1 + n1;
            if (__builtin_expect(!(n0 + n1 <= BIGV), 0)) *LP(unsigned, L_BIG) = 1u;
            va_pin();
            wg_barrier();
            STAMP(25);
            PIN(prd);
            const unsigned big = *LP(unsigned, L_BIG);
            if (wv >= 4) va_reads();
#pragma unroll
            for (int kk = 0; kk < 8; ++kk) { pf[kk] = p_ld(kk); __builtin_amdgcn_sched_barrier(0); }
            if (__builtin_expect(__builtin_amdgcn_readfirstlane(big) == 0 || !first, 1)) break;
            const unsigned ex2 = L_SCR + 512 + 160;
            PIN(sa); PIN(sb);
            rederive();
            *LP(float, ex2 + (nl + (lane >> 5) * 16) * 1024 + wv * 4) = wave_max();
            wg_barrier();
            const float x0 = fmaxf(max8(ex2 + nl * 1024), m0), x1 = fmaxf(max8(ex2 + nl * 1024 + 16384), m1);
            const float al0 = __builtin_amdgcn_exp2f(m0 - x0), al1 = __builtin_amdgcn_exp2f(m1 - x1), al = (lane & 16) ? al1 : al0;
            m0 = x0; m1 = x1; ls0 *= al0; ls1 *= al1;
            o[0] *= al; o[1] *= al;
        }
        mrun = (lane & 16) ? m1 : m0;
        auto stage = [&](int t2, int gp) {
            const unsigned lo0 = pk2(o[t2][8 * gp + 0], o[t2][8 * gp + 1]), lo1 = pk2(o[t2][8 * gp + 2], o[t2][8 * gp + 3]);
            const unsigned hi0 = pk2(o[t2][8 * gp + 4], o[t2][8 * gp + 5]), hi1 = pk2(o[t2][8 * gp + 6], o[t2][8 * gp + 7]);
            auto r0 = __builtin_amdgcn_permlane32_swap(lo0, hi0, false, false);
            auto r1 = __builtin_amdgcn_permlane32_swap(lo1, hi1, false, false);
            *LP(u32x4, sw[t2 * 2 + gp]) = u32x4{r0[0], r1[0], r0[1], r1[1]};
        };
#pragma unroll
        for (int kk = 0; kk < 8; ++kk) {
            o[0] = MFMA32(cat8(va[kk][0], va[kk][1]), pf[kk], o[0]);
            if (kk == 1) {
                asm volatile("s_waitcnt vmcnt(0)" : "+v"(nq) :: "memory");
                npm = __builtin_amdgcn_readfirstlane(nq[0]);
                if ((unsigned)__builtin_amdgcn_readfirstlane(nq[2]) != tlo || (unsigned)__builtin_amdgcn_readfirstlane(nq[3]) != thi) npm = __builtin_amdgcn_readfirstlane(tagged_wait(WS_NM + x * STR)[0]);
            }
            if (kk == 3 || kk == 5) {
                const unsigned v = (npm ^ ((x & 2) ? 0xffu : 0u)) >> (lane >> 5);
#pragma unroll
                for (int i = kk - 3; i < kk - 1; ++i) { po[i] = p0 + ((v >> (2 * i)) & 1u) * 8192u; PIN(po[i]); }
            }
            __builtin_amdgcn_sched_barrier(0);
        }
#pragma unroll
        for (int kk = 0; kk < 8; ++kk) {
            o[1] = MFMA32(cat8(va[kk][2], va[kk][3]), pf[kk], o[1]);
            if (kk == 2) { PIN(o[0]); stage(0, 0); }
            if (kk == 4) stage(0, 1);
            __builtin_amdgcn_sched_barrier(0);
        }
        stage(1, 0); stage(1, 1);
        asm volatile("s_waitcnt lgkmcnt(0)" ::: "memory");
        u32x4 w[4];
#pragma unroll
        for (int i = 0; i < 4; ++i) w[i] = *LP(u32x4, srd + i * 1024);
#pragma unroll
        for (int i = 0; i < 4; ++i) ST16(w[i], po[i], i * 262144);
        __builtin_amdgcn_sched_barrier(0);
        STAMP(26);
        u32x4 mlrec;
        if (wv == 0) {
            const unsigned head = lane & 31;
            esr = L_SCR + head * 1024 + 512 + ((head & 7) << 4);
            mlo = (unsigned)(WS_ML + x * STR + s * 512 + head * 16) + ((npm >> ((head >> 3) * 2)) & 1u) * 8192u;
            PIN(mlo);
            f32x4 b8[8];
#pragma unroll
            for (int q = 0; q < 8; ++q) b8[q] = *LP(f32x4, esr ^ (q << 4));
            const f32x4 bs = ((b8[0] + b8[1]) + (b8[2] + b8[3])) + ((b8[4] + b8[5]) + (b8[6] + b8[7]));
            mlrec = u32x4{__float_as_uint(mrun), __float_as_uint((bs[0] + bs[1]) + (bs[2] + bs[3])), tlo, thi};
            PIN(mlrec);
        }
        VMW(0);
        wg_barrier();
        if (wv == 0) {
            if (lane < 32) ST16(mlrec, mlo, 0);
            PIN(fa);
            if (s < 11 && !__all(flags_ok(fa))) flags_wait(fbo);
        }
        STAMP(27);
        wg_barrier();
    }
    const unsigned long long t_att = __builtin_amdgcn_s_memrealtime();
    rederive();
    while (__builtin_amdgcn_s_memrealtime() < t_att + (s >= 11 ? MD0 : MD1)) __builtin_amdgcn_s_sleep(2);
    if (s >= 11) {
        const unsigned n = (unsigned)((s - 11) * 8 + wv);
        if (n < 28) pf4<NTM & 32>((n * 64 + lane) * 1024u + x * 128u, p.w2, 0);
        { const unsigned l2 = n * 64 + lane; pf4<NTM & 32>((l2 / 5u) * (DFF * 2) + min(((unsigned)x * 448u & ~127u) + (l2 % 5u) * 128u, DFF * 2 - 4u), p.w1, 0); }
        if (wv == 0) flag_put(WS_FA, id - 128);
        STAMP_FLUSH();
        return;
    }
    const unsigned L_H2S = L_SCR, L_REDM = L_SCR + 16384;
    if (s < 7) {
        {
            const char* w1s = (const char*)(p.w1 + (size_t)wv * 64 * DFF + ug * 32);
#pragma unroll
            for (int i = 0; i < 4; ++i) dma16<NTM & 32>((unsigned)((lane >> 2) * DFF * 2 + (lane & 3) * 16), w1s + (size_t)i * 16 * DFF * 2, wv * 4096 + i * 1024);
        }
        const int b = wv;
        if (wv == 0) ST16(s4, (unsigned)(WS_U + x * STR + (lane >> 5) * 8192 + s * 512 + (lane & 31) * 16), 0);
        const u32x4 shf = LD16((unsigned)(WS_MB + b * STR + lane * 16), 0), scl = LD16((unsigned)(WS_MB + b * STR + 1024 + lane * 16), 0);
        VMW(0);
        if (wv == 0) flag_put(WS_FA, id - 128);
        wg_barrier();
        bf16x8 wk0[8], wk1[8];
        if (wv >= 6) {
#pragma unroll
            for (int q = 0; q < 8; ++q) {
                const unsigned a = (unsigned)(q * 4096 + (g * 8 + (nl >> 2)) * 64 + (wv & 1) * 32 + (lane & 3) * 8);
                wk0[q] = tr_frag(a, a + 256); wk1[q] = tr_frag(a + 2048, a + 2304);
            }
#pragma unroll
            for (int q = 0; q < 8; q += 2) PIN4(wk0[q], wk0[q + 1], wk1[q], wk1[q + 1]);
        }
        float scv[8], shv[8];
#pragma unroll
        for (int q = 0; q < 4; ++q) {
            scv[2 * q] = rbf(1.f + lo16(scl[q])); scv[2 * q + 1] = rbf(1.f + hi16(scl[q])); shv[2 * q] = lo16(shf[q]); shv[2 * q + 1] = hi16(shf[q]);
            PIN4(scv[2 * q], scv[2 * q + 1], shv[2 * q], shv[2 * q + 1]);
        }
        unsigned har = L_H2S + ((lane & 7) * HS_LD + g * 8 + (nl >> 3) * 32) * 2, haw = L_H2S + (b * HS_LD + lane * 8) * 2;
        unsigned udst = (unsigned)(WS_U + x * STR + (g & 1) * 8192 + (s * 2 + (wv & 1)) * 256 + (nl & 7) * 32 + (g >> 1) * 16), xsrc = (unsigned)(WS_X1 + x * STR + (b * 512 + lane * 8) * 2) + ((npm >> x) & 1u) * 8192u;
        PIN4(har, haw, udst, xsrc);
        STAMP(28);
        while (__builtin_amdgcn_s_memrealtime() < t_att + XGATE) __builtin_amdgcn_s_sleep(2);
        u32x4 xv;
        for (unsigned spins = 0;;) {
            xv = LD16(xsrc, 0);
            if (__all(mx4(xv, 0u) != SENT)) break;
            __builtin_amdgcn_s_sleep(SLPX);
            if (++spins > kSpinCap) break;
            asm volatile("" ::: "memory");
        }
        STAMP(29);
        {
            f32x2_t v[4];
#pragma unroll
            for (int q = 0; q < 4; ++q) v[q] = f32x2_t{lo16(xv[q]), hi16(xv[q])};
            const f32x2_t p1 = (v[0] + v[1]) + (v[2] + v[3]), p2 = (v[0] * v[0] + v[1] * v[1]) + (v[2] * v[2] + v[3] * v[3]);
            float mb, rden;
            ln_stats(p1[0] + p1[1], p2[0] + p2[1], mb, rden);
            u32x4 hr;
#pragma unroll
            for (int q = 0; q < 4; ++q) {
                const f32x2_t h = ((v[q] - mb) * rden) * f32x2_t{scv[2 * q], scv[2 * q + 1]} + f32x2_t{shv[2 * q], shv[2 * q + 1]};
                hr[q] = pk2(h[0], h[1]);
            }
            *LP(u32x4, haw) = hr;
        }
        wg_barrier();
        if (wv >= 6) {
            bf16x8 af[8];
#pragma unroll
            for (int q = 0; q < 8; ++q) af[q] = *LP(bf16x8, har + q * 128);
            __builtin_amdgcn_sched_barrier(0);
            f32x4 d0 = {0.f, 0.f, 0.f, 0.f}, d1 = {0.f, 0.f, 0.f, 0.f};
#pragma unroll
            for (int q = 0; q < 8; ++q) { d0 = MFMA16(wk0[q], af[q], d0); d1 = MFMA16(wk1[q], af[q], d1); }
            const f32x4 own = nl < 8 ? d0 : d1;
#pragma unroll
            for (int e = 0; e < 4; ++e) { float v = own[e] + dppf<0x128>(own[e]); asm("" : "+v"(v)); d0[e] = fmaxf(v, 0.f); }
            const unsigned u0 = pk2s(d0[0], d0[1]), u1 = pk2s(d0[2], d0[3]);
            auto r0 = __builtin_amdgcn_permlane16_swap(u0, u0, false, false);
            auto r1 = __builtin_amdgcn_permlane16_swap(u1, u1, false, false);
            if (nl < 8) ST16((u32x4{r0[0], r1[0], r0[1], r1[1]}), udst, 0);
            STAMP(30);
        }
        STAMP_FLUSH();
        return;
    }
    {
        const int n0 = (x * 4 + (s - 7)) * 16;
        const char* mws = (const char*)(p.w2 + (size_t)wv * 224 * 512 + n0);
#pragma unroll
        for (int i = 0; i < 7; ++i) dma16<NTM & 32>((unsigned)((lane >> 1) * 1024 + (lane & 1) * 16), mws + i * 32768, rgn + i * 1024);
        const int fb = (lane >> 2) & 7, fc = (lane & 3) * 4 + (lane >> 5) * 2;
        u32x2 gb = {0u, 0u};
        if (wv == 0) {
            flag_put(WS_FA, id - 128);
            { const u32x4 v = tagged_wait(WS_MOD + fb * STR + (1536 + n0 + fc) * 8); gb = u32x2{v[0], v[1]}; }
            flags_wait(WS_FA + x * STR + lane * 16);
            if (id == 23 * 8 && lane == 0) __builtin_amdgcn_raw_buffer_store_b64(u32x2{tlo, thi}, rw, WS_EPOCH, 0, 16);
            PIN(gb);
        }
        VMW(0);
        bf16x8 wfr[7];
        const unsigned trl = (unsigned)(g * 8 + (nl >> 2)) * 32u + (unsigned)(lane & 3) * 8u;
#pragma unroll
        for (int ks = 0; ks < 7; ++ks) { wfr[ks] = tr_frag(rgn + ks * 1024 + trl, rgn + ks * 1024 + trl + 128); PIN(wfr[ks]); }
        unsigned usrc = (unsigned)(WS_U + wv * STR + (g >> 1) * 256 + (nl & 7) * 32 + (g & 1) * 16) + ((npm >> x) & 1u) * 8192u, usrc3 = usrc + 3072;
        usrc += (nl >> 3) * 512;
        PIN(usrc3);
        unsigned rdw = L_REDM + (wv * 2 + (nl >> 3)) * 512 + (nl & 7) * 64 + g * 16, rdr = L_REDM + (lane >> 5) * 4096 + (lane & 31) * 16;
        unsigned short* outp = p.out + fb * 512 + n0 + fc;
        PIN(usrc); PIN(rdw); PIN(rdr); PIN(outp);
        STAMP(28);
        wg_barrier();
        while (__builtin_amdgcn_s_memrealtime() < t_att + XGATE) __builtin_amdgcn_s_sleep(2);
        const unsigned x1b = (unsigned)(WS_X1 + x * STR) + ((npm >> x) & 1u) * 8192u;
        for (unsigned spins = 0;;) {
            const u32x4 xv = LD16(x1b + wv * 1024 + lane * 16, 0);
            if (__all(mx4(xv, 0u) != SENT)) break;
            __builtin_amdgcn_s_sleep(SLPD);
            if (++spins > kSpinCap) break;
            asm volatile("" ::: "memory");
        }
        wg_barrier();
        unsigned xg = 0u;
        if (wv == 0) { xg = __builtin_amdgcn_raw_buffer_load_b32(rw, x1b + (fb * 512 + n0 + fc) * 2, 0, 16); PIN(xg); } else __builtin_amdgcn_s_sleep(5);
        __builtin_amdgcn_s_sleep(SLPG);
        u32x4 uf[4];
        for (unsigned spins = 0;;) {
#pragma unroll
            for (int q = 0; q < 4; ++q) uf[q] = LD16(q < 3 ? usrc + q * 1024 : usrc3, 0);
            if (__all(mx4(uf[0], mx4(uf[1], mx4(uf[2], mx4(uf[3], 0u)))) != SENT)) break;
            if (++spins > kSpinCap) break;
            asm volatile("" ::: "memory");
        }
        STAMP(29);
        f32x4 d = {0.f, 0.f, 0.f, 0.f}, d2 = {0.f, 0.f, 0.f, 0.f};
#pragma unroll
        for (int ks = 0; ks < 7; ++ks) {
            if (ks & 1) d2 = MFMA16(wfr[ks], BF8(uf[ks >> 1]), d2);
            else d = MFMA16(wfr[ks], BF8(uf[ks >> 1]), d);
        }
        *LP(f32x4, rdw) = nl < 8 ? d : d2;
        wg_barrier();
        if (wv == 0) {
            f32x4 c[8];
#pragma unroll
            for (int w2 = 0; w2 < 8; ++w2) c[w2] = *LP(f32x4, rdr + w2 * 512);
            const f32x4 s4v = ((c[0] + c[1]) + (c[2] + c[3])) + ((c[4] + c[5]) + (c[6] + c[7]));
            f32x2_t sm;
#pragma unroll
            for (int q = 0; q < 2; ++q) { auto t2 = __builtin_amdgcn_permlane32_swap(__float_as_uint(s4v[q]), __float_as_uint(s4v[2 + q]), false, false); sm[q] = __uint_as_float(t2[0]) + __uint_as_float(t2[1]); }
            unsigned t = pk2(sm[0], sm[1]);
            t = pk2(__uint_as_float(gb[0]) * lo16(t), __uint_as_float(gb[1]) * hi16(t));
            *(GAS unsigned*)outp = pk2(lo16(xg) + lo16(t), hi16(xg) + hi16(t));
            STAMP(31);
        }
        STAMP_FLUSH();
    }
}

extern "C" void kernel_launch(void* const* d_in, const int* in_sizes, int n_in,
                              void* d_out, int out_size, void* d_ws, size_t ws_size,
                              hipStream_t stream) {
    Params p;
    p.x = (const unsigned short*)d_in[0];
    p.ckv = (const unsigned short*)d_in[1];
    p.w_q = (const unsigned short*)d_in[2];
    p.w_uk = (const unsigned short*)d_in[3];
    p.w_uv = (const unsigned short*)d_in[4];
    p.w_o = (const unsigned short*)d_in[5];
    p.w1 = (const unsigned short*)d_in[7];
    p.w2 = (const unsigned short*)d_in[8];
    p.c = (const unsigned short*)d_in[9];
    p.w_mod = (const unsigned short*)d_in[10];
    p.ws = (char*)(((size_t)d_ws + 2097151) & ~(size_t)2097151);
    p.out = (unsigned short*)d_out;
    (void)hipFuncSetAttribute((const void*)k_main, hipFuncAttributeMaxDynamicSharedMemorySize, 163840);
    k_main<<<256, 512, 163840, stream>>>(p);
}
```

```cpp
#include <hip/hip_runtime.h>
#include <hip/hip_bf16.h>
#include <stdint.h>

#define NH 32
#define SK 4096
#define DFF 1792
#define GI 18
#define WG 1
#define NPOST 3
#define NP2 1
#define SL0 1
#define HP 13
#define QD 4
#define TK 5
#define NCAL 2
#define TG 430
#define TL 480
#define LMD 250
#define TWD 570
#define XGATE 300
#define CPFE 36000
#define QHELP 4
#define SLPH 4
#define SLPQ 2
#define SLPR 10
#define SLPX 1
#define SLPD 2
#define SLPG 18

using u16x8 = __attribute__((ext_vector_type(8))) unsigned short;
using s16x4 = __attribute__((ext_vector_type(4))) short;
using bf16x8 = __attribute__((ext_vector_type(8))) short;
using f32x16 = __attribute__((ext_vector_type(16))) float;
using f32x4 = __attribute__((ext_vector_type(4))) float;
using u32x4 = __attribute__((ext_vector_type(4))) unsigned;
using u32x2 = __attribute__((ext_vector_type(2))) unsigned;

#define GAS __attribute__((address_space(1)))
#define LAS __attribute__((address_space(3)))
typedef GAS unsigned long long gu64;
#define RLX_AGENT __ATOMIC_RELAXED, __HIP_MEMORY_SCOPE_AGENT
#define SENT 0xffffffffu
#define TAGC 0x9e3779b9u

__device__ __forceinline__ float bf2f(unsigned short u) { return __uint_as_float(((unsigned)u) << 16); }
typedef __attribute__((ext_vector_type(2))) __bf16 bf16x2_t;
typedef __attribute__((ext_vector_type(2))) float f32x2_t;
__device__ __forceinline__ unsigned pk2(float a, float b) {
    f32x2_t v = {a, b};
    bf16x2_t r = __builtin_convertvector(v, bf16x2_t);
    return __builtin_bit_cast(unsigned, r);
}
__device__ __forceinline__ unsigned nosent(unsigned r) { return min(r, SENT - 1u); }
__device__ __forceinline__ unsigned pk2s(float a, float b) { return nosent(pk2(a, b)); }
__device__ __forceinline__ unsigned short f2bf(float f) { return (unsigned short)(pk2(f, 0.f) & 0xffffu); }
__device__ __forceinline__ float rbf(float f) { return __uint_as_float(pk2(f, 0.f) << 16); }
__device__ __forceinline__ float lo16(unsigned u) { return __uint_as_float(u << 16); }
__device__ __forceinline__ float hi16(unsigned u) { return __uint_as_float(u & 0xffff0000u); }
__device__ __forceinline__ unsigned umax3(unsigned a, unsigned b, unsigned c) { return max(max(a, b), c); }
__device__ __forceinline__ unsigned mx4(const u32x4& v, unsigned m) { return umax3(v[2], v[3], umax3(v[0], v[1], m)); }
__device__ __forceinline__ float fmax3(float a, float b, float c) { float r; asm("v_max3_f32 %0, %1, %2, %3" : "=v"(r) : "v"(a), "v"(b), "v"(c)); return r; }
template <int C> __device__ __forceinline__ float dppf(float v) { return __int_as_float(__builtin_amdgcn_update_dpp(0, __float_as_int(v), C, 0xf, 0xf, true)); }
__device__ __forceinline__ float sum16(float v) { v += dppf<0xB1>(v); v += dppf<0x4E>(v); v += dppf<0x141>(v); v += dppf<0x140>(v); return v; }

__device__ __forceinline__ void ln_stats(float p1, float p2, float& mb, float& rden) {
    auto t = __builtin_amdgcn_permlane32_swap(__float_as_uint(p1), __float_as_uint(p2), false, false);
    float a = sum16(__uint_as_float(t[0]) + __uint_as_float(t[1]));
    auto t2 = __builtin_amdgcn_permlane16_swap(__float_as_uint(a), __float_as_uint(a), false, false);
    a = (__uint_as_float(t2[0]) + __uint_as_float(t2[1])) * (1.f / 512.f);
    auto t3 = __builtin_amdgcn_permlane32_swap(__float_as_uint(a), __float_as_uint(a), false, false);
    const float m = __uint_as_float(t3[0]);
    const float var = rbf(fmaxf(fmaf(-m, m, __uint_as_float(t3[1])), 0.f));
    mb = rbf(m);
    rden = __builtin_amdgcn_rcpf(rbf(__builtin_amdgcn_sqrtf(rbf(var + 1e-5f))));
}
#define STR 262144
#define WS_Q 0
#define WS_H 32768
#define WS_X1 65536
#define WS_ML 81920
#define WS_MOD 98304
#define WS_MB 139264
#define WS_U 114688
#define WS_FI 131072
#define WS_FA 132096
#define WS_FB 133120
#define WS_NP 134144
#define WS_NM 134400
#define WS_EPOCH 135168
#define WS_CAL 147456
#define WS_OUTP 2097152
#define WS_PART 4194304
#define WS_END 12582912

#define LP(T, off) ((LAS T*)(size_t)(unsigned)(off))
#define STAMP(k) do {} while (0)
#define STAMP_FLUSH() do {} while (0)
#define STAMP0() do {} while (0)
__device__ __forceinline__ s16x4 tr4(unsigned a) { return __builtin_amdgcn_ds_read_tr16_b64_v4i16(LP(s16x4, a)); }
__device__ __forceinline__ bf16x8 cat8(s16x4 a, s16x4 b) { return __builtin_shufflevector(a, b, 0, 1, 2, 3, 4, 5, 6, 7); }
__device__ __forceinline__ bf16x8 tr_frag(unsigned a0, unsigned a1) { return cat8(tr4(a0), tr4(a1)); }
__device__ __forceinline__ unsigned swz(unsigned row) { return ((row & 3) << 2) | ((row >> 2) & 3); }
__device__ __forceinline__ unsigned off_b(unsigned row, unsigned ch) { return 256u * row + 16u * (ch ^ swz(row)); }
__device__ __forceinline__ unsigned tile_off(unsigned key, unsigned chunk) { return ((key >> 5) * 4 + (chunk >> 4)) * 8192u + off_b(key & 31, chunk & 15); }
__device__ __forceinline__ void wg_barrier() { asm volatile("s_waitcnt lgkmcnt(0)\n\ts_barrier" ::: "memory"); }
__device__ __forceinline__ void dma16(unsigned voff, const void* sbase, unsigned lds_dst) {
    asm volatile("s_mov_b32 m0, %2\n\ts_nop 0\n\tglobal_load_lds_dwordx4 %0, %1" :: "v"(voff), "s"(sbase), "s"(lds_dst) : "memory", "m0");
}
__device__ __forceinline__ void dma16x(unsigned voff, unsigned vx, const void* sbase, unsigned lds_dst) {
    unsigned t;
    asm volatile("s_mov_b32 m0, %3\n\tv_xor_b32 %0, %4, %1\n\tglobal_load_lds_dwordx4 %0, %2 nt" : "=&v"(t) : "v"(voff), "s"(sbase), "s"(lds_dst), "i"(vx) : "memory", "m0");
}
__device__ __forceinline__ void pf4(unsigned voff, const void* sbase, unsigned lds_dst) {
    asm volatile("s_mov_b32 m0, %2\n\ts_nop 0\n\tglobal_load_lds_dword %0, %1" :: "v"(voff), "s"(sbase), "s"(lds_dst) : "memory", "m0");
}
__device__ __forceinline__ void pf4s(unsigned voff, const void* sbase, unsigned lds_dst) {
    asm volatile("s_mov_b32 m0, %2\n\ts_nop 0\n\tglobal_load_lds_dword %0, %1 sc1" :: "v"(voff), "s"(sbase), "s"(lds_dst) : "memory", "m0");
}
#define SPINCAP (1u << 20)
constexpr unsigned kSpinCap = SPINCAP;
struct Params {
    const unsigned short* x;
    const unsigned short* ckv;
    const unsigned short* w_q;
    const unsigned short* w_uk;
    const unsigned short* w_uv;
    const unsigned short* w_o;
    const unsigned short* w1;
    const unsigned short* w2;
    const unsigned short* c;
    const unsigned short* w_mod;
    char* ws;
    unsigned short* out;
};
#define VMW(n) asm volatile("s_waitcnt vmcnt(%0)" :: "n"(n) : "memory")
#define PIN(x) asm volatile("" : "+v"(x))
#define PIN4(a, b, c, d) asm volatile("" : "+v"(a), "+v"(b), "+v"(c), "+v"(d) :: "memory")
#define MFMA16(a, b, c) __builtin_amdgcn_mfma_f32_16x16x32_bf16(a, b, c, 0, 0, 0)
#define MFMA32(a, b, c) __builtin_amdgcn_mfma_f32_32x32x16_bf16(a, b, c, 0, 0, 0)
#define BF8(x) __builtin_bit_cast(bf16x8, x)
#define ST16(v, off, soff) __builtin_amdgcn_raw_buffer_store_b128(v, rw, off, soff, 16)
#define LD16(off, soff) __builtin_amdgcn_raw_buffer_load_b128(rw, off, soff, 16)
#define SCL (0.08838834764831845f * 1.4426950408889634f)
#define BIGV 1.0e18f
#define L_SCR 131072
#define HS_LD 520

__global__ __launch_bounds__(512, 2) void k_main(Params p) {
    unsigned long long pc;
    asm volatile("s_getpc_b64 %0" : "=s"(pc));
    const int id = blockIdx.x, tid = threadIdx.x;
    int lane = tid & 63;
    const int wv = __builtin_amdgcn_readfirstlane(tid >> 6);
    int g = lane >> 4, nl = lane & 15;
    auto rederive = [&]() {
        asm volatile("v_mbcnt_lo_u32_b32 %0, -1, 0\n\tv_mbcnt_hi_u32_b32 %0, -1, %0" : "=v"(lane));
        g = lane >> 4; nl = lane & 15;
    };
    const int x = id & 7, j0 = id >> 3, j = j0 < 16 ? 15 - j0 : j0;
    if (wv < 5 && j == 15) {
        asm volatile("s_mov_b32 m0, %2\n\ts_nop 0\n\tglobal_load_lds_dword %0, %1" :: "v"(min((unsigned)((wv * 64 + lane) * 128), (unsigned)CPFE)), "s"(pc), "s"(L_SCR + 31744) : "memory", "m0");
    }
    char* ws = p.ws;
    if ((j >> 2) == 3) {
        dma16((unsigned)(((nl & 7) * 512 + (nl >> 3) * 32 + g * 8) * 2 + wv * 128), p.c, wv * 16384 + 8192);
        dma16((unsigned)tid * 16u, p.x, wv * 16384 + 9216);
    }
    if (j == 15 && wv == 5 && lane < 2) pf4s((unsigned)((x * 2 + lane) * 2097152), p.ckv, L_SCR + 31744);
    if (j == 14 && wv >= 4 && lane < 2) pf4s((unsigned)(lane * 2097152 + x * 128), wv == 4 ? p.w_mod : wv == 5 ? p.w_uk : wv == 6 ? p.w_q : (const unsigned short*)(ws + WS_CAL), L_SCR + 31744);
    STAMP0();
    const __amdgpu_buffer_rsrc_t rw = __builtin_amdgcn_make_buffer_rsrc(ws, 0, WS_END, 0x00020000);
    const u32x4 s4 = {SENT, SENT, SENT, SENT};
    unsigned long long cnt = 0ull;
    auto cnt_load = [&]() { asm volatile("global_load_dwordx2 %0, %1, %2 sc1" : "+v"(cnt) : "v"((unsigned)WS_EPOCH), "s"(ws) : "memory"); };
    unsigned tlo = 0u, thi = 0u;
    auto tag_take = [&]() {
        asm volatile("" : "+v"(cnt) :: "memory");
        const unsigned long long t = cnt + 1ull;
        tlo = __builtin_amdgcn_readfirstlane((unsigned)t); thi = __builtin_amdgcn_readfirstlane((unsigned)(t >> 32));
    };
    auto flag_put = [&](unsigned base, unsigned idx) {
        if (lane < 8) __builtin_amdgcn_raw_buffer_store_b64(u32x2{tlo, thi}, rw, base + lane * STR + idx * 8, 0, 16);
    };
    auto tagged_wait = [&](unsigned off) {
        u32x4 v;
#pragma unroll 1
        for (unsigned spins = 0; spins < kSpinCap; ++spins) {
            asm volatile("buffer_load_dwordx4 %0, %1, %2, 0 offen sc1\n\ts_waitcnt vmcnt(0)" : "=v"(v) : "v"(off), "s"(rw) : "memory");
            if (__all((v[2] == tlo) & (v[3] == thi))) break;
            __builtin_amdgcn_s_sleep(8);
        }
        return v;
    };
    auto flags_ok = [&](const u32x4& f) { return (f[0] == tlo) & (f[1] == thi) & (f[2] == tlo) & (f[3] == thi); };
    auto flags_wait = [&](unsigned off) {
        for (unsigned spins = 0;;) {
            u32x4 f;
            asm volatile("buffer_load_dwordx4 %0, %1, %2, 0 offen sc1\n\ts_waitcnt vmcnt(0)" : "=v"(f) : "v"(off), "s"(rw) : "memory");
            if (__all(flags_ok(f))) break;
            __builtin_amdgcn_s_sleep(16);
            if (++spins > kSpinCap) break;
            asm volatile("" ::: "memory");
        }
    };
    unsigned pcl, frl, frs;
    auto dma_piece = [&](const unsigned short* src, unsigned ldb, unsigned dst) {
        const unsigned v0 = (lane >> 3) * ldb + pcl, v1 = v0 ^ 64u;
#pragma unroll
        for (int i = 0; i < 8; ++i) dma16((i & 1) ? v1 : v0, (const char*)src + (size_t)i * 8 * ldb, dst + i * 1024);
    };
    const unsigned rgn = (unsigned)(wv * 16384);

    if (j < 16) {
        const unsigned long long t_start = __builtin_amdgcn_s_memrealtime();
        rederive();
        pcl = (unsigned)(((((lane & 7) >> 1) ^ ((lane >> 4) & 1)) * 32) + (lane & 1) * 16);
        frl = (unsigned)((g * 8 + (nl >> 2)) * 128 + (lane & 3) * 8); frs = (unsigned)((nl >> 3) | ((g & 1) << 1));
        const int th = x * 4 + (j & 3);
        if (j < 12) {
            const int qt = j >> 2, rot = qt * 5;
            const bool pub = wv < (qt < 2 ? 3 : 2);
            const unsigned qo = (unsigned)(WS_Q + (th & 7) * STR + ((nl & 7) * 4 + (th >> 3)) * 1024 + (qt * 192 + wv * 64 + g * 16 + (nl >> 3) * 8) * 2);
            const unsigned vb = (unsigned)(g * 8192 + ((nl ^ (g << 2)) << 4));
            const char* wuk = (const char*)(p.w_uk + th * 128) + (size_t)qt * 192 * 8192;
            const char* wq = (const char*)(p.w_q + th * 128) + (size_t)wv * 32768;
            const unsigned L_U = 114688;
            {
                const int k0 = qt < 2 ? wv * 6 : wv * 4;
#pragma unroll
                for (int i = 0; i < 6; ++i) {
                    if (i < 4 || qt < 2) dma16(vb ^ (((k0 + i) & 3) << 4), wuk + (size_t)(k0 + i) * 32768, L_U + (k0 + i) * 1024);
                    if (i == 4) VMW(4);
                }
            }
            const unsigned vq = vb ^ ((wv & 3) << 4);
            auto wq_dma = [&](int ps) { dma16(vq, wq + (size_t)((ps + rot) & 15) * 262144, ps * 8192 + wv * 1024); };
            bf16x8 uk[4][4], aq[16];
            u32x4 fi, hv[4];
            unsigned fio = (unsigned)(WS_FI + x * STR + (lane & 7) * 16), hsrc, hsrc2, ta0, ta1, tb0, tb1, qo2, hw0, hw1, hr2, qr0, qr1, qw, thx = 0u;
            const unsigned L_HI = L_SCR, L_QI = L_SCR + 8192;
            auto h_load = [&]() {
                asm volatile("buffer_load_dwordx4 %0, %4, %6, 0 offen sc1\n\tbuffer_load_dwordx4 %1, %4, %6, 0 offen offset:1024 sc1\n\t"
                             "buffer_load_dwordx4 %2, %5, %6, 0 offen sc1\n\tbuffer_load_dwordx4 %3, %5, %6, 0 offen offset:1024 sc1"
                             : "+v"(hv[0]), "+v"(hv[1]), "+v"(hv[2]), "+v"(hv[3]) : "v"(hsrc), "v"(hsrc2), "s"(rw) : "memory");
            };
            auto h_bad = [&]() {
                PIN4(hv[0], hv[1], hv[2], hv[3]);
                unsigned bad = (hv[0][1] ^ tlo) | (hv[0][3] ^ thx) | (hv[1][1] ^ tlo) | (hv[1][3] ^ thx) | (hv[2][1] ^ tlo) | (hv[2][3] ^ thx) | (hv[3][1] ^ tlo) | (hv[3][3] ^ thx);
                PIN(bad);
                return bad;
            };
#pragma unroll
            for (int ps = 0; ps < 16; ++ps) {
                wq_dma(ps);
                if (ps == 4) cnt_load();
                if (ps == HP) { h_load(); asm volatile("buffer_load_dwordx4 %0, %1, %2, 0 offen sc1" : "=v"(fi) : "v"(fio), "s"(rw) : "memory"); }
                if (ps >= 4 && ps < 8) VMW(5); else if (ps >= HP && ps < HP + 4) VMW(9); else VMW(4);
                if (ps == 3) {
                    asm volatile("s_barrier" ::: "memory");
                    if (pub) {
#pragma unroll
                        for (int t = 0; t < 4; ++t)
#pragma unroll
                            for (int s = 0; s < 4; ++s) uk[t][s] = *LP(bf16x8, L_U + off_b(wv * 64 + (nl >> 2) * 16 + t * 4 + (nl & 3), s * 4 + g));
                    }
                    STAMP(1);
                }
                if (ps == 5) {
                    hsrc = (unsigned)(WS_H + x * STR + wv * 2048 + lane * 16); hsrc2 = hsrc + 16384;
                    ta0 = off_b(8 * g + (nl >> 2), 2 * wv + ((lane & 3) >> 1)) + 8 * (lane & 1); ta1 = off_b(8 * g + 4 + (nl >> 2), 2 * wv + ((lane & 3) >> 1)) + 8 * (lane & 1);
                    tb0 = ta0 + 65536; tb1 = ta1 + 65536; qo2 = qo;
                    hw0 = L_HI + ((2 * wv - rot) & 15) * 512 + (lane >> 4) * 128 + (lane & 7) * 16 + ((lane >> 3) & 1) * 8;
                    hw1 = L_HI + ((2 * wv + 1 - rot) & 15) * 512 + (lane >> 4) * 128 + (lane & 7) * 16 + ((lane >> 3) & 1) * 8;
                    hr2 = L_HI + g * 128 + (nl & 7) * 16 + (nl >> 3) * 512;
                    PIN(hr2);
                    qr0 = L_QI + g * 128 + (nl & 7) * 16 + (nl >> 3) * 2048; qr1 = qr0 ^ 2048u;
                    PIN(qr0); PIN(qr1);
                    qw = L_QI + (wv >> 1) * 512 + ((wv & 1) * 2 + (g >> 1)) * 128 + (nl & 7) * 16 + (g & 1) * 8;
                    PIN4(hsrc, hsrc2, ta0, ta1); PIN4(tb0, tb1, qo2, hw0); PIN(hw1); PIN(qw);
                }
                if (ps == 8) { tag_take(); thx = tlo ^ thi ^ TAGC; }
                if (ps == 9) {
#pragma unroll
                    for (int t = 0; t < 4; ++t) PIN4(uk[t][0], uk[t][1], uk[t][2], uk[t][3]);
                    wg_barrier();
                    *LP(unsigned, L_QI + 2048 + wv * 256 + lane * 4) = 0u;
#pragma unroll
                    for (int q = 0; q < 6; ++q) aq[q] = tr_frag(ta0 + q * 8192, ta1 + q * 8192);
                }
                if (ps == 13) {
                    asm volatile("s_barrier" ::: "memory");
#pragma unroll
                    for (int q = 6; q < 10; ++q) aq[q] = q < 8 ? tr_frag(ta0 + q * 8192, ta1 + q * 8192) : tr_frag(tb0 + (q - 8) * 8192, tb1 + (q - 8) * 8192);
                }
            }
            STAMP(15);
            if (HP + 4 > 15) VMW(HP + 4 > 15 ? 15 - HP : 0);
            if (!__all(h_bad() == 0u)) {
                for (unsigned spins = 0;;) {
                    h_load();
                    VMW(0);
                    if (__all(h_bad() == 0u)) break;
                    __builtin_amdgcn_s_sleep(SLPH);
                    if (++spins > kSpinCap) break;
                }
            }
            {
                unsigned hh[4];
#pragma unroll
                for (int q = 0; q < 4; ++q) {
                    const unsigned t = hv[2 + (q >> 1)][(q & 1) * 2], sh = hv[q >> 1][(q & 1) * 2];
                    hh[q] = pk2(lo16(t) + lo16(sh), hi16(t) + hi16(sh));
                }
                *LP(u32x2, hw0) = u32x2{hh[0], hh[1]};
                *LP(u32x2, hw1) = u32x2{hh[2], hh[3]};
            }
            STAMP(3);
            asm volatile("s_waitcnt vmcnt(0) lgkmcnt(0)\n\ts_barrier" : "+v"(fi) :: "memory");
            STAMP(2);
            {
                bf16x8 hb[8];
#pragma unroll
                for (int q = 0; q < 5; ++q) hb[q] = *LP(bf16x8, hr2 + q * 1024);
#pragma unroll
                for (int ps = 10; ps < 16; ++ps) {
                    if (!(ps & 1)) hb[ps >> 1] = *LP(bf16x8, hr2 + (ps >> 1) * 1024);
                    aq[ps] = tr_frag(tb0 + (ps - 8) * 8192, tb1 + (ps - 8) * 8192);
                }
                __builtin_amdgcn_sched_barrier(0);
                f32x4 d0 = {0.f, 0.f, 0.f, 0.f}, d1 = {0.f, 0.f, 0.f, 0.f};
#pragma unroll
                for (int ps = 0; ps < 16; ps += 2) {
                    d0 = MFMA16(aq[ps], hb[ps >> 1], d0); d1 = MFMA16(aq[ps + 1], hb[ps >> 1], d1);
                    if (ps == 6) { unsigned bad = (fi[0] ^ tlo) | (fi[1] ^ thi) | (fi[2] ^ tlo) | (fi[3] ^ thi); PIN(bad); if (!__all(bad == 0u)) flags_wait(fio); }
                }
#pragma unroll
                for (int e = 0; e < 4; ++e) { float v = d0[e] + dppf<0x128>(d1[e]); asm("" : "+v"(v)); d0[e] = v; }
                if (nl < 8) *LP(u32x2, qw) = u32x2{pk2(d0[0], d0[1]), pk2(d0[2], d0[3])};
            }
            wg_barrier();
            if (pub) {
                bf16x8 qb[2][4];
#pragma unroll
                for (int s = 0; s < 4; ++s) { qb[0][s] = *LP(bf16x8, qr0 + s * 512); qb[1][s] = *LP(bf16x8, qr1 + s * 512); }
                __builtin_amdgcn_sched_barrier(0);
                f32x4 a = {0.f, 0.f, 0.f, 0.f}, b = a;
#pragma unroll
                for (int s = 0; s < 4; ++s) {
                    a = MFMA16(uk[0][s], qb[0][s], a); b = MFMA16(uk[1][s], qb[0][s], b);
                    a = MFMA16(uk[2][s], qb[1][s], a); b = MFMA16(uk[3][s], qb[1][s], b);
                }
                const u32x4 qv = {pk2s(a[0], a[1]), pk2s(a[2], a[3]), pk2s(b[0], b[1]), pk2s(b[2], b[3])};
                ST16(qv, qo2, 0);
            }
            STAMP(4);
            wg_barrier();
            if (wv < 3 && j + 12 * wv < 32) {
                const int hh = j + 12 * wv;
#pragma unroll 1
                for (unsigned spins = 0; spins < 64; ++spins) {
                    __builtin_amdgcn_s_sleep(QHELP);
                    const u32x4 v = LD16((unsigned)(WS_Q + (hh & 7) * STR + (x * 4 + (hh >> 3)) * 1024 + lane * 16), 0);
                    if (__all(mx4(v, 0u) != SENT)) break;
                    asm volatile("" ::: "memory");
                }
                STAMP(6);
            }
        } else {
            const int cg = x * 2 + (j & 1);
            if (j < 14) {
                dma_piece(p.w_mod + (size_t)wv * 64 * 3072 + cg * 64, 6144, rgn);
                cnt_load();
                VMW(9);
            } else {
                cnt_load();
                ST16(s4, (unsigned)(WS_Q + wv * STR + cg * 2048 + lane * 16), 0); ST16(s4, (unsigned)(WS_Q + wv * STR + cg * 2048 + lane * 16), 1024);
                VMW(0);
                tag_take();
                wg_barrier();
                if (wv == 7) flag_put(WS_FI, cg);
                STAMP(15);
                if (j == 14) {
                    u32x2 best = {~0u, ~0u};
#pragma unroll
                    for (int r = 0; r < 2 * NCAL; ++r) {
                        const unsigned hf = (r ^ wv) & 1;
                        VMW(0);
                        const unsigned long long t0 = __builtin_amdgcn_s_memtime();
                        if (lane < 8) ST16(s4, WS_CAL + x * STR + hf * 8192 + (wv * 8 + r) * 128 + lane * 16, 0);
                        VMW(0);
                        const unsigned dt = (unsigned)(__builtin_amdgcn_s_memtime() - t0);
                        if (hf) best[1] = min(best[1], dt); else best[0] = min(best[0], dt);
                    }
                    *LP(u32x2, 16384 * 7 + 14336 + wv * 8) = best;
                    wg_barrier();
                    if (wv == 0 && lane < 8) {
                        u32x2 m = *LP(u32x2, 16384 * 7 + 14336 + lane * 8);
#pragma unroll
                        for (int o = 1; o < 8; o <<= 1) { m[0] = min(m[0], (unsigned)__shfl_xor(m[0], o)); m[1] = min(m[1], (unsigned)__shfl_xor(m[1], o)); }
                        ST16((u32x4{(unsigned)max(-1000, min(1000, (int)m[1] - (int)m[0])), 0u, tlo, thi}), (unsigned)(WS_NP + lane * STR + x * 16), 0);
                    }
                    if (wv == 0) {
                        const int d = (int)tagged_wait(WS_NP + x * STR + (lane & 7) * 16)[0];
                        int t = (lane & 4) ? -d : d;
                        t += __builtin_amdgcn_update_dpp(0, t, 0xB1, 0xf, 0xf, true);
                        t += __builtin_amdgcn_update_dpp(0, t, 0x4E, 0xf, 0xf, true);
                        t += __builtin_amdgcn_update_dpp(0, t, 0x141, 0xf, 0xf, true);
                        const bool n1 = abs(d) > 160 ? d < 0 : (t < 0) != ((lane & 4) != 0);
                        const unsigned mk = (unsigned)__ballot(n1) & 0xffu;
                        if (lane == 0) ST16((u32x4{mk, 0u, tlo, thi}), (unsigned)(WS_NM + x * STR), 0);
                    }
                    STAMP(9);
                }
            }
            const u16x8 cf = *LP(u16x8, rgn + 8192 + lane * 16), xa = *LP(u16x8, rgn + 9216 + lane * 16);
            u32x4 af[2];
#pragma unroll
            for (int q = 0; q < 4; ++q) {
                const float a = bf2f(cf[2 * q]), b = bf2f(cf[2 * q + 1]);
                af[0][q] = pk2(a * __builtin_amdgcn_rcpf(1.f + __expf(-a)), b * __builtin_amdgcn_rcpf(1.f + __expf(-b)));
                af[1][q] = (unsigned)__builtin_amdgcn_update_dpp(0, (int)af[0][q], 0x128, 0xf, 0xf, true);
            }
            const unsigned L_MS = L_SCR;
            unsigned wa[4], fr[4];
#pragma unroll
            for (int t = 0; t < 4; ++t) { wa[t] = L_MS + wv * 2048 + (nl & 7) * 256 + (((t * 4 + g) ^ (nl & 7)) << 4) + (nl >> 3) * 16384; fr[t] = rgn + frl + ((t ^ frs) << 5); }
            PIN4(wa[0], wa[1], wa[2], wa[3]); PIN4(fr[0], fr[1], fr[2], fr[3]); PIN(af[0]); PIN(af[1]);
            const unsigned L_XN = 12288;
            auto mod_mm = [&](bool ln) {
                VMW(0);
                bf16x8 f[2][4];
#pragma unroll
                for (int ks = 0; ks < 2; ++ks)
#pragma unroll
                    for (int t = 0; t < 4; ++t) f[ks][t] = tr_frag(fr[t] + ks * 4096, fr[t] + ks * 4096 + 512);
                __builtin_amdgcn_sched_barrier(0);
                f32x4 d[4];
#pragma unroll
                for (int t = 0; t < 4; ++t) d[t] = MFMA16(f[0][t], BF8(af[0]), (f32x4{0.f, 0.f, 0.f, 0.f}));
#pragma unroll
                for (int t = 0; t < 4; ++t) d[t] = MFMA16(f[1][t], BF8(af[1]), d[t]);
#pragma unroll
                for (int t = 0; t < 4; ++t) *LP(f32x4, wa[t]) = d[t];
                if (ln) {
                    float v[8];
#pragma unroll
                    for (int q = 0; q < 8; ++q) v[q] = bf2f(xa[q]);
                    const float p1 = ((v[0] + v[1]) + (v[2] + v[3])) + ((v[4] + v[5]) + (v[6] + v[7]));
                    const float p2 = ((v[0] * v[0] + v[1] * v[1]) + (v[2] * v[2] + v[3] * v[3])) + ((v[4] * v[4] + v[5] * v[5]) + (v[6] * v[6] + v[7] * v[7]));
                    float mb, rden;
                    ln_stats(p1, p2, mb, rden);
                    u32x4 r;
#pragma unroll
                    for (int q = 0; q < 4; ++q) { const unsigned d = pk2(v[2 * q] - mb, v[2 * q + 1] - mb); r[q] = pk2(lo16(d) * rden, hi16(d) * rden); }
                    *LP(u32x4, L_XN + wv * 16384 + lane * 16) = r;
                }
                wg_barrier();
            };
            auto sum8 = [&](unsigned a) {
                f32x4 u[8];
#pragma unroll
                for (int w2 = 0; w2 < 8; ++w2) u[w2] = *LP(f32x4, a + w2 * 2048);
                __builtin_amdgcn_sched_barrier(0);
                return ((u[0] + u[1]) + (u[2] + u[3])) + ((u[4] + u[5]) + (u[6] + u[7]));
            };
            if (j < 14) {
                const int hb = lane & 7, c4 = 2 * wv + (nl >> 3);
                unsigned hoff = (unsigned)(WS_H + g * STR + cg * 2048 + wv * 256 + nl * 16);
                unsigned sa = L_MS + hb * 256 + ((c4 ^ hb) << 4);
                unsigned xa2 = L_XN + hb * 16384 + (cg & 7) * 128 + c4 * 8;
                PIN(hoff); PIN(sa); PIN(xa2);
                STAMP(1);
                mod_mm(cg >= 8);
                STAMP(2);
                const u32x2 xn = *LP(u32x2, xa2);
                const f32x4 v = sum8(sa);
                tag_take();
                const unsigned thx = tlo ^ thi ^ TAGC;
                u32x2 r = {pk2(v[0], v[1]), pk2(v[2], v[3])};
                if (cg >= 8) {
#pragma unroll
                    for (int q = 0; q < 2; ++q) { const unsigned sc = pk2(1.f + lo16(r[q]), 1.f + hi16(r[q])); r[q] = pk2(lo16(xn[q]) * lo16(sc), hi16(xn[q]) * hi16(sc)); }
                }
                ST16((u32x4{r[0], tlo, r[1], thx}), hoff, 0);
                ST16((u32x4{r[0], tlo, r[1], thx}), hoff, 4 * STR);
                STAMP(5);
            }
            wg_barrier();
            while (__builtin_amdgcn_s_memrealtime() < t_start + LMD) __builtin_amdgcn_s_sleep(2);
            const int lg = x * 4 + (j & 3);
            dma_piece(p.w_mod + (size_t)wv * 64 * 3072 + 1024 + lg * 64, 6144, rgn);
            mod_mm(false);
            if (lg >= 8 && lg < 24) {
                if (wv == 0) {
                    const int b = lane >> 3, c8 = lane & 7;
                    const f32x4 v0 = sum8(L_MS + b * 256 + (((2 * c8) ^ b) << 4)), v1 = sum8(L_MS + b * 256 + (((2 * c8 + 1) ^ b) << 4));
                    ST16((u32x4{pk2(v0[0], v0[1]), pk2(v0[2], v0[3]), pk2(v1[0], v1[1]), pk2(v1[2], v1[3])}), (unsigned)(WS_MB + b * STR + (lg - 8) * 128 + c8 * 16), 0);
                }
            } else if (wv < 4) {
                const int b = wv * 2 + (lane >> 5), cp = lane & 31;
                f32x2_t u[8];
#pragma unroll
                for (int w2 = 0; w2 < 8; ++w2) u[w2] = *LP(f32x2_t, L_MS + w2 * 2048 + b * 256 + (((cp >> 1) ^ b) << 4) + (cp & 1) * 8);
                const f32x2_t v = ((u[0] + u[1]) + (u[2] + u[3])) + ((u[4] + u[5]) + (u[6] + u[7]));
                ST16((u32x4{__float_as_uint(rbf(v[0])), __float_as_uint(rbf(v[1])), tlo, thi}), (unsigned)(WS_MOD + b * STR + (lg * 32 + cp) * 16), 0);
            }
            STAMP(6);
        }
        const int bq = (j >> 2) & 1, dh = j >> 3, src = th * 2 + dh, rb = bq * 4 + (src >> 4), rc = src & 15;
        wg_barrier();
        if (j == 0) {
            if (wv == 4 && lane < 5) pf4s((unsigned)((lane + 1) * 2097152 + 8064), ws, L_SCR + 31744);
            if (wv == 5 && lane < 2) { pf4s((unsigned)(lane * 2097152), p.w_uv, L_SCR + 31744); pf4s((unsigned)(lane * 2097152), p.w_o, L_SCR + 31744); }
            if (wv == 6 && lane < 1) { pf4s(0u, p.w1, L_SCR + 31744); pf4s(0u, p.w2, L_SCR + 31744); pf4s(0u, p.out, L_SCR + 31744); }
        }
        while (__builtin_amdgcn_s_memrealtime() < t_start + TWD - 110) __builtin_amdgcn_s_sleep(2);
        const unsigned npm = __builtin_amdgcn_readfirstlane(tagged_wait(WS_NM + x * STR)[0]), np = (npm >> x) & 1u;
        unsigned oo = (unsigned)(WS_OUTP + ((bq * 4 + (nl & 3)) * 16 + wv * 2 + (nl >> 3)) * 16384 + (src ^ wv) * 128 + ((nl >> 2) & 1) * 64 + g * 16) + ((~npm >> ((nl & 3) * 2 + (wv >> 2))) & 1u) * 8192u;
        const int rcol = rc * 32 + (lane & 7) * 4 + (lane >> 5) * 2;
        unsigned xdst = (unsigned)(WS_X1 + ((lane >> 3) & 3) * STR + (rb * 512 + rcol) * 2), xdst2 = xdst + 4 * STR + ((npm >> (4 + ((lane >> 3) & 3))) & 1u) * 8192u;
        xdst += ((npm >> ((lane >> 3) & 3)) & 1u) * 8192u;
        ST16(s4, oo, 0);
        if (wv == 7) { __builtin_amdgcn_raw_buffer_store_b32(SENT, rw, xdst, 0, 16); __builtin_amdgcn_raw_buffer_store_b32(SENT, rw, xdst2, 0, 16); }
        VMW(0);
        wg_barrier();
        if (wv == 0) flag_put(WS_FB, id);
        while (__builtin_amdgcn_s_memrealtime() < t_start + TWD) __builtin_amdgcn_s_sleep(2);
        asm volatile("s_barrier" ::: "memory");
        STAMP(7);
        if (wv == 0 && lane < 32) pf4((unsigned)((j * 32 + lane) * 8192 + x * 1024 + ((3u - ((unsigned)(size_t)p.w_uv >> 7)) & 7u) * 128), p.w_uv, L_SCR + 31744);
        if (bq) dma_piece(p.w_o + (size_t)(th * 128 + dh * 64) * 512 + wv * 64, 1024, rgn + 8192);
        dma_piece(p.w_uv + ((size_t)wv * 64 * NH + th) * 128 + dh * 64, 8192, rgn);
        if (!bq) dma_piece(p.w_o + (size_t)(th * 128 + dh * 64) * 512 + wv * 64, 1024, rgn + 8192);
        u32x4 fb, gm;
        unsigned xg;
        VMW(4);
        if (wv == 7)
            asm volatile("buffer_load_dwordx4 %0, %3, %5, 0 offen sc1\n\tbuffer_load_dwordx4 %1, %4, %5, 0 offen sc1\n\tglobal_load_dword %2, %6, %7" : "=&v"(fb), "=&v"(gm), "=&v"(xg)
                         : "v"((unsigned)(WS_FB + x * STR + lane * 16)), "v"((unsigned)(WS_MOD + rb * STR + rcol * 8)), "s"(rw), "v"((unsigned)(rb * 512 + rcol) * 2u), "s"(p.x) : "memory");
        VMW(0);
        asm volatile("" : "+v"(fb), "+v"(gm), "+v"(xg));
        STAMP(8);
        bf16x8 fuv[2][4], fwo[2][4];
        unsigned pfa[4];
#pragma unroll
        for (int t = 0; t < 4; ++t) { pfa[t] = rgn + frl + ((t ^ frs) << 5); PIN(pfa[t]); }
#pragma unroll
        for (int ks = 0; ks < 2; ++ks)
#pragma unroll
            for (int t = 0; t < 4; ++t) {
                const unsigned a = rgn + (unsigned)((g * 4 + (nl >> 2)) * 128 + (lane & 3) * 8) + ((t ^ (unsigned)((nl >> 3) | (g & 2))) << 5) + ks * 4096;
                fuv[ks][t] = tr_frag(a, a + 2048);
                fwo[ks][t] = tr_frag(pfa[t] + 8192 + ks * 4096, pfa[t] + 8192 + ks * 4096 + 512);
            }
#pragma unroll
        for (int ks = 0; ks < 2; ++ks) { PIN4(fuv[ks][0], fuv[ks][1], fuv[ks][2], fuv[ks][3]); PIN4(fwo[ks][0], fwo[ks][1], fwo[ks][2], fwo[ks][3]); }
        const unsigned L_WGT = L_SCR, L_MSK = L_SCR + 256, L_RED = L_SCR + 1024;
        unsigned psrc = (unsigned)(WS_PART + (bq * 4 * 32 + th) * 32768 + (wv >> 2) * 16384 + (wv & 3) * 2048 + ((lane >> 3) ^ (th & 7)) * 128) + np * 8192u + pcl, psrc1 = (psrc ^ 64u) + 1024;
        unsigned psrc2 = psrc ^ 8192u, psrc3 = psrc1 ^ 8192u;
        PIN(psrc2); PIN(psrc3); PIN(xdst2);
        unsigned rw2 = L_RED + wv * 1024 + (nl & 3) * 256 + g * 16 + (nl >> 2) * 64;
        unsigned wa0 = (nl & 3) == (g >> 1) ? L_WGT + g * 16 : L_WGT + 272, wa1 = (nl & 3) == 2 + (g >> 1) ? L_WGT + 64 + g * 16 : L_WGT + 272;
        unsigned cr = rgn + (nl & 3) * 128 + g * 16;
        PIN(cr);
        unsigned r2r = L_RED + lane * 16, cw = rgn + lane * 8;
        unsigned osrc = (unsigned)(WS_OUTP + (rb * 16 + rc) * 16384 + lane * 16) + (np ^ 1u) * 8192u;
        PIN(psrc); PIN(psrc1); PIN(rw2); PIN(wa0); PIN(wa1); PIN(r2r); PIN4(cw, oo, osrc, xdst);
        float mlm = 0.f, mll = 0.f;
        if (wv == 0) *LP(unsigned, L_WGT + 256 + lane * 4) = 0u;
        wg_barrier();
        const bool early = __all(flags_ok(fb) & (gm[2] == tlo) & (gm[3] == thi));
        u32x2 ga = {gm[0], gm[1]};
        PIN(ga);
        bf16x8 pa[2][4], wh[2];
        {
            u32x4 ml = {0u, 0u, ~tlo, ~thi};
            const unsigned mlsrc = (unsigned)(WS_ML + (bq * 4 + g) * STR + nl * 512 + th * 16) + ((npm >> (x & 6)) & 1u) * 8192u;
            unsigned long long done = 0ull;
            unsigned lm = L_MSK;
            PIN(lm);
#define PLD(bit, off, v, base) "s_bitcmp1_b64 %0, " #bit "\n\ts_cbranch_scc0 1f\n\ts_add_u32 m0, %1, " #off "\n\ts_nop 0\n\tglobal_load_lds_dwordx4 " v ", " base " sc1\n1:\n\t"
            auto send = [&](unsigned long long m) {
                asm volatile(PLD(0, 0, "%2", "%4") PLD(8, 1024, "%3", "%4") PLD(16, 2048, "%2", "%5") PLD(24, 3072, "%3", "%5")
                             PLD(32, 4096, "%8", "%6") PLD(40, 5120, "%9", "%6") PLD(48, 6144, "%8", "%7") PLD(56, 7168, "%9", "%7")
                             :: "s"(m), "s"(rgn), "v"(psrc), "v"(psrc1), "s"(ws), "s"(ws + 1048576), "s"(ws + 2097152), "s"(ws + 3145728), "v"(psrc2), "v"(psrc3) : "memory", "m0", "scc");
            };
            if (wv == 0) {
#pragma unroll 1
                for (unsigned spins = 0; spins < kSpinCap; ++spins) {
                    if ((ml[2] != tlo) | (ml[3] != thi)) ml = LD16(mlsrc, 0);
                    const unsigned long long m = __ballot((ml[2] == tlo) & (ml[3] == thi));
                    if (m == ~0ull) break;
                    unsigned long long f = m & (m >> 4);
                    f &= f >> 2; f &= f >> 1;
                    asm volatile("ds_write_b64 %0, %1" :: "v"(lm), "v"(f) : "memory");
                }
                asm volatile("ds_write_b64 %0, %1" :: "v"(lm), "v"(~0ull) : "memory");
            } else {
#pragma unroll 1
                for (unsigned spins = 0; spins < kSpinCap && __popcll(done) < TK; ++spins) {
                    unsigned long long t;
                    asm volatile("ds_read_b64 %0, %1\n\ts_waitcnt lgkmcnt(0)" : "=v"(t) : "v"(lm) : "memory");
                    const unsigned long long m = (((unsigned long long)(unsigned)__builtin_amdgcn_readfirstlane((unsigned)(t >> 32)) << 32) | (unsigned)__builtin_amdgcn_readfirstlane((unsigned)t)) & 0x0101010101010101ull & ~done;
                    if (m) { done |= m; send(m); }
                }
            }
            asm volatile("s_barrier" ::: "memory");
            send(~done);
            mlm = __uint_as_float(ml[0]); mll = __uint_as_float(ml[1]);
            STAMP(10);
            if (wv == 0) {
                float M = mlm;
                M = fmaxf(M, dppf<0xB1>(M)); M = fmaxf(M, dppf<0x4E>(M)); M = fmaxf(M, dppf<0x141>(M)); M = fmaxf(M, dppf<0x140>(M));
                const float e = __builtin_amdgcn_exp2f(mlm - M);
                *LP(unsigned short, L_WGT + lane * 2) = f2bf(e * __builtin_amdgcn_rcpf(sum16(e * mll)));
            }
            wg_barrier();
            wh[0] = *LP(bf16x8, wa0); wh[1] = *LP(bf16x8, wa1);
            __builtin_amdgcn_sched_barrier(0);
            VMW(0);
#pragma unroll
            for (int ks = 0; ks < 2; ++ks)
#pragma unroll
                for (int t = 0; t < 4; ++t) pa[ks][t] = tr_frag(pfa[t] + ks * 4096, pfa[t] + ks * 4096 + 512);
        }
        STAMP(11);
        {
            f32x4 d[4];
#pragma unroll
            for (int t = 0; t < 4; ++t) d[t] = MFMA16(pa[0][t], wh[0], (f32x4{0.f, 0.f, 0.f, 0.f}));
#pragma unroll
            for (int t = 0; t < 4; ++t) d[t] = MFMA16(pa[1][t], wh[1], d[t]);
            const u32x4 o0 = {pk2(d[0][0], d[0][1]), pk2(d[0][2], d[0][3]), pk2(d[1][0], d[1][1]), pk2(d[1][2], d[1][3])};
            const u32x4 o1 = {pk2(d[2][0], d[2][1]), pk2(d[2][2], d[2][3]), pk2(d[3][0], d[3][1]), pk2(d[3][2], d[3][3])};
            f32x4 y[4];
#pragma unroll
            for (int t = 0; t < 4; ++t) y[t] = MFMA16(fuv[0][t], BF8(o0), (f32x4{0.f, 0.f, 0.f, 0.f}));
#pragma unroll
            for (int t = 0; t < 4; ++t) y[t] = MFMA16(fuv[1][t], BF8(o1), y[t]);
            *LP(f32x4, rw2) = (nl & 8) ? ((nl & 4) ? y[3] : y[2]) : ((nl & 4) ? y[1] : y[0]);
        }
        wg_barrier();
        {
            f32x4 cs[8];
#pragma unroll
            for (int w2 = 0; w2 < 8; ++w2) cs[w2] = *LP(f32x4, r2r + w2 * 1024);
            const f32x4 c4 = ((cs[0] + cs[1]) + (cs[2] + cs[3])) + ((cs[4] + cs[5]) + (cs[6] + cs[7]));
            *LP(u32x2, cw) = u32x2{pk2(c4[0], c4[1]), pk2(c4[2], c4[3])};
            asm volatile("s_waitcnt lgkmcnt(0)" ::: "memory");
            const u32x4 cx0 = *LP(u32x4, cr), cx1 = *LP(u32x4, cr + 64);
            f32x4 o[4];
#pragma unroll
            for (int q = 0; q < 4; ++q) o[q] = MFMA16(fwo[0][q], BF8(cx0), (f32x4{0.f, 0.f, 0.f, 0.f}));
#pragma unroll
            for (int q = 0; q < 4; ++q) o[q] = MFMA16(fwo[1][q], BF8(cx1), o[q]);
            const f32x4 oa = (nl & 8) ? ((nl & 4) ? o[3] : o[2]) : ((nl & 4) ? o[1] : o[0]);
            const u32x4 sv = {nosent(__float_as_uint(oa[0])), nosent(__float_as_uint(oa[1])), nosent(__float_as_uint(oa[2])), nosent(__float_as_uint(oa[3]))};
            ST16(sv, oo, 0);
        }
        STAMP(12);
        wg_barrier();
        if (wv == 7) {
            if (!early) {
                flags_wait(WS_FB + x * STR + lane * 16);
                { const u32x4 v = tagged_wait(WS_MOD + rb * STR + rcol * 8); ga = u32x2{v[0], v[1]}; }
                PIN(ga);
            }
            __builtin_amdgcn_s_sleep(SLPR);
            u32x4 op[8];
            for (unsigned spins = 0;;) {
#pragma unroll
                for (int k = 0; k < 8; ++k) op[k] = LD16(osrc + (k & 3) * 1024, (k >> 2) * 4096);
                unsigned m = 0u;
#pragma unroll
                for (int k = 0; k < 8; ++k) m = mx4(op[k], m);
                if (__all(m != SENT)) break;
                if (++spins > kSpinCap) break;
                asm volatile("" ::: "memory");
            }
            STAMP(13);
            const f32x4 v = ((__builtin_bit_cast(f32x4, op[0]) + __builtin_bit_cast(f32x4, op[1])) + (__builtin_bit_cast(f32x4, op[2]) + __builtin_bit_cast(f32x4, op[3]))) +
                            ((__builtin_bit_cast(f32x4, op[4]) + __builtin_bit_cast(f32x4, op[5])) + (__builtin_bit_cast(f32x4, op[6]) + __builtin_bit_cast(f32x4, op[7])));
            float w[2];
#pragma unroll
            for (int q = 0; q < 2; ++q) {
                auto t = __builtin_amdgcn_permlane32_swap(__float_as_uint(v[q]), __float_as_uint(v[2 + q]), false, false);
                w[q] = __uint_as_float(t[0]) + __uint_as_float(t[1]);
                auto u = __builtin_amdgcn_permlane16_swap(__float_as_uint(w[q]), __float_as_uint(w[q]), false, false);
                w[q] = __uint_as_float(u[0]) + __uint_as_float(u[1]);
                w[q] += dppf<0x128>(w[q]);
            }
            unsigned t = pk2(w[0], w[1]);
            t = pk2(__uint_as_float(ga[0]) * lo16(t), __uint_as_float(ga[1]) * hi16(t));
            t = pk2s(lo16(xg) + lo16(t), hi16(xg) + hi16(t));
            __builtin_amdgcn_raw_buffer_store_b32(t, rw, xdst, 0, 16);
            __builtin_amdgcn_raw_buffer_store_b32(t, rw, xdst2, 0, 16);
            STAMP(14);
        }
        STAMP_FLUSH();
        return;
    }

    const int s = j - 16;
    const unsigned long long t_s = __builtin_amdgcn_s_memrealtime();
    {
        const unsigned ln = (unsigned)(s * 64 + lane), r0 = (unsigned)(s * 4 + (wv & 3));
        if (wv == 0) pf4((ln & 511u) * 6144u + (x * 2 + (ln >> 9)) * 128u, p.w_mod, L_SCR);
        pf4(((r0 >> 5) * 256 + (lane >> 3) * 32 + (r0 & 31)) * 8192u + x * 1024u + (lane & 7u) * 128u, wv < 4 ? p.w_uk : p.w_q, L_SCR);
    }
    const int ug = x * 7 + s;
    const unsigned vb = (unsigned)(g * 1024 + ((nl ^ (g << 2)) << 4)), lb = (unsigned)((wv >> 1) * 32768 + (wv & 1) * 4096);
    const char* ck = (const char*)p.ckv + ((size_t)x * SK + s * 256 + wv * 16) * 1024;
    const unsigned rot = (unsigned)s & 3u;
    const unsigned vbr = vb ^ (rot << 8);
    const char* ck2 = ck + 131072;
    auto ck_dma = [&](unsigned i) { dma16x(vbr, ((i & 3u) << 4) | (((i >> 2) & 3u) << 8) | ((i & 3u) << 12), (i >> 4) ? ck2 : ck, lb + ((i >> 2) & 3u) * 8192u + (i & 3u) * 1024u); };
    bf16x8 ka[16];
    s16x4 va[8][4];
    unsigned kad[4], vad[4], vad2[4];
    auto kq = [](unsigned u) { return (0x78u >> (2 * u)) & 3u; };
    auto ka_reads = [&](int cb) {
#pragma unroll
        for (int u = 0; u < 4; ++u) ka[cb * 4 + u] = *LP(bf16x8, kad[u] + cb * 8192);
    };
    auto va_reads = [&]() {
#pragma unroll
        for (int kk = 0; kk < 8; ++kk)
#pragma unroll
            for (int q = 0; q < 4; ++q) va[kk][q] = tr4((kk < 4 ? vad[q] : vad2[q]) + ((kk >> 1) & 1) * 32768 + (kk & 1) * 4096);
    };
    auto va_read1 = [&](int kk) {
#pragma unroll
        for (int q = 0; q < 4; ++q) va[kk][q] = tr4((kk < 4 ? vad[q] : vad2[q]) + ((kk >> 1) & 1) * 32768 + (kk & 1) * 4096);
    };
    auto va_pin = [&]() {
#pragma unroll
        for (int q = 0; q < 8; ++q) PIN4(va[q][0], va[q][1], va[q][2], va[q][3]);
    };
    u32x4 fa = {0u, 0u, 0u, 0u};
    const unsigned fao = (unsigned)(WS_FI + x * STR + (lane & 7) * 16);
    constexpr int LAST0 = GI + NPOST, LAST = LAST0 + NP2;
    static_assert(GI - WG >= 15 && GI < 20 && LAST >= 19 && LAST < 28, "");
    const unsigned qsrc = (unsigned)(WS_Q + (wv & 1) * 4 * STR + (x * 4 + (wv >> 1)) * 1024) + (g ^ rot) * 256 + ((nl ^ ((wv * 4) & 15)) << 4);
    auto q_dma = [&](int k) {
        unsigned t;
        asm volatile("s_mov_b32 m0, %3\n\tv_xor_b32 %0, %4, %1\n\tglobal_load_lds_dwordx4 %0, %2 sc1" : "=&v"(t) : "v"(qsrc), "s"(ws), "s"(L_SCR + (wv * 4 + k) * 1024), "i"(k * (STR + 16)) : "memory", "m0");
    };
#pragma unroll
    for (int i = 0; i <= GI; ++i) {
        ck_dma(i);
        if (i == 5 && wv == 0) cnt_load();
        if (i == 3) {
#pragma unroll
            for (int u = 0; u < 4; ++u) {
                kad[u] = tile_off(wv * 16 + kq(nl >> 2) * 4 + (nl & 3), 4 * u + g);
                vad[u] = tile_off(8 * (lane >> 5) + 4 * (u & 1) + (nl >> 2), wv * 8 + 4 * (u >> 1) + 2 * (g & 1) + ((lane & 3) >> 1)) + 8 * (lane & 1);
                vad2[u] = vad[u] + 65536;
                PIN(kad[u]); PIN(vad[u]); PIN(vad2[u]);
            }
        }
        if (i >= 4) VMW(4);
        if (i == 15 && wv == 0) {
            PIN(fa);
            tag_take();
            if (!__all(flags_ok(fa))) flags_wait(fao);
        }
        if ((i & 3) == 3 && i >= 7 && i <= 15) {
            const int cb = (i - 7) >> 2;
            if (cb > 0) { PIN4(ka[cb * 4 - 4], ka[cb * 4 - 3], ka[cb * 4 - 2], ka[cb * 4 - 1]); va_pin(); }
            wg_barrier();
            ka_reads(cb);
            if ((wv >> 1) == cb) va_reads();
            STAMP(16 + cb);
        }
        if (i == 10 && wv == 0) asm volatile("buffer_load_dwordx4 %0, %1, %2, 0 offen sc1" : "+v"(fa) : "v"(fao), "s"(rw) : "memory");
    }
    PIN4(ka[8], ka[9], ka[10], ka[11]); va_pin();
    const bool late = __builtin_amdgcn_s_memrealtime() >= t_s + TL;
    while (__builtin_amdgcn_s_memrealtime() < t_s + TG) __builtin_amdgcn_s_sleep(1);
    VMW(WG);
    q_dma(0); q_dma(1); q_dma(2); q_dma(3);
    asm volatile("s_barrier" ::: "memory");
    STAMP(19);
    if (late) {
#pragma unroll
        for (int i = GI + 1; i <= LAST0; ++i) ck_dma(i);
    }
    ka_reads(3);
    if ((wv >> 1) == 3) va_reads();
    if (late) VMW(NPOST); else VMW(0);
    const unsigned L_EXM = L_SCR - 1024, L_BIG = L_SCR + 512 + 128;
    unsigned npm = 0u;
    unsigned qad[4];
    {
        unsigned qck = L_SCR + wv * 4096 + lane * 16;
        u32x4 qb[4];
#pragma unroll
        for (int k = 0; k < 4; ++k) qb[k] = *LP(u32x4, qck + k * 1024);
#pragma unroll
        for (int k = 0; k < 4; ++k) { qad[k] = L_SCR + nl * 1024 + (((4 * k + g) ^ nl) << 4); PIN(qad[k]); }
        STAMP(20);
#pragma unroll 1
        for (unsigned spins = 0; spins < kSpinCap && !__all(mx4(qb[0], mx4(qb[1], mx4(qb[2], mx4(qb[3], 0u)))) != SENT); ++spins) {
            __builtin_amdgcn_s_sleep(SLPQ);
#pragma unroll
            for (int k = 0; k < 4; ++k)
                if (!__all(mx4(qb[k], 0u) != SENT)) q_dma(k);
            VMW(0);
#pragma unroll
            for (int k = 0; k < 4; ++k) qb[k] = *LP(u32x4, qck + k * 1024);
        }
        if (!late) {
#pragma unroll
            for (int i = GI + 1; i <= LAST0; ++i) ck_dma(i);
        }
#pragma unroll
        for (int i = LAST0 + 1; i <= LAST; ++i) ck_dma(i);
    }
    constexpr int N0 = LAST + 1 + SL0, NE = N0 + 6 < 31 ? N0 + 6 : 31;
    const unsigned kg = kq(g);
    unsigned exw = L_EXM + ((nl + (lane >> 5) * 16) * 8 + wv) * 4, exr = L_EXM + nl * 32;
    unsigned pwr = L_SCR + nl * 1024 + 768 + (((2 * wv + (kg >> 1)) ^ nl) << 4) + (kg & 1) * 8, esw = L_SCR + nl * 1024 + 512 + ((wv ^ (nl & 7)) << 4) + g * 4;
    unsigned prd = L_SCR + (lane & 31) * 1024 + 768 + (((lane >> 5) ^ nl) << 4);
    PIN4(exw, exr, pwr, esw); PIN(prd);
    PIN4(ka[12], ka[13], ka[14], ka[15]); va_pin();
    wg_barrier();
    STAMP(21);
    bf16x8 qr[8][2];
    auto q_ld = [&](int ks, int hg) { return *LP(bf16x8, qad[ks & 3] + (ks >> 2) * 256 + hg * 16384); };
    auto p_ld = [&](int kk) { return *LP(bf16x8, prd ^ (kk << 5)); };
    float ls0, ls1, m0, m1;
    f32x4 sa, sb;
    f32x16 o[2];
    unsigned sw[4], srd, p0, mlo, esr, fbo;
    u32x2 tg;
    u32x4 nq;
    auto probs = [&](float (&e)[8]) {
#pragma unroll
        for (int r = 0; r < 4; ++r) { e[r] = __builtin_amdgcn_exp2f(fmaf(sa[r], SCL, -m0)); e[4 + r] = __builtin_amdgcn_exp2f(fmaf(sb[r], SCL, -m1)); }
        *LP(u32x2, pwr) = u32x2{pk2(e[0], e[1]), pk2(e[2], e[3])};
        *LP(u32x2, pwr + 16384) = u32x2{pk2(e[4], e[5]), pk2(e[6], e[7])};
    };
    auto wave_max = [&]() {
        const float a = fmaxf(fmaxf(sa[0], sa[1]), fmaxf(sa[2], sa[3])), b = fmaxf(fmaxf(sb[0], sb[1]), fmaxf(sb[2], sb[3]));
        auto u = __builtin_amdgcn_permlane32_swap(__float_as_uint(a), __float_as_uint(b), false, false);
        const float c = fmax3(__uint_as_float(u[0]), __uint_as_float(u[1]), __uint_as_float(u[1]));
        auto v = __builtin_amdgcn_permlane16_swap(__float_as_uint(c), __float_as_uint(c), false, false);
        return fmax3(__uint_as_float(v[0]), __uint_as_float(v[1]), __uint_as_float(v[1]));
    };
    auto max8 = [&](unsigned a) {
        const f32x4 a0 = *LP(f32x4, a), a1 = *LP(f32x4, a + 16);
        return fmax3(fmax3(a0[0], a0[1], a0[2]), fmax3(a0[3], a1[0], a1[1]), fmax3(a1[2], a1[3], a1[3])) * SCL;
    };
    {
        bf16x8 qs[8][2];
#pragma unroll
        for (int ks = 0; ks < 8; ++ks) { qr[ks][0] = q_ld(ks + 8, 0); qr[ks][1] = q_ld(ks + 8, 1); }
        __builtin_amdgcn_sched_barrier(0);
#pragma unroll
        for (int i = LAST + 1; i < LAST + 1 + SL0; ++i) if (i < NE) ck_dma(i);
        sa = f32x4{0.f, 0.f, 0.f, 0.f}; sb = sa;
        f32x4 s2a = sa, s2b = sa;
        bf16x8 kb[4];
#pragma unroll
        for (int n = 0; n < 16; ++n) {
            const int ks = (n + 8) & 15;
            sa = MFMA16(ka[ks], ks < 8 ? qs[ks][0] : qr[ks - 8][0], sa);
            sb = MFMA16(ka[ks], ks < 8 ? qs[ks][1] : qr[ks - 8][1], sb);
            if (n == 4) { VMW(LAST + SL0 + 1 - 19); for (int u = 0; u < 4; ++u) kb[u] = *LP(bf16x8, kad[u]); }
            if (ks < 4) { s2a = MFMA16(kb[ks], qs[ks][0], s2a); s2b = MFMA16(kb[ks], qs[ks][1], s2b); }
            if (n >= 8 - QD && n < 16 - QD) { qs[n + QD - 8][0] = q_ld(n + QD - 8, 0); qs[n + QD - 8][1] = q_ld(n + QD - 8, 1); }
            if ((n & 3) == 3 && LAST + 1 + SL0 + (n >> 2) < NE) ck_dma(LAST + 1 + SL0 + (n >> 2));
            __builtin_amdgcn_sched_barrier(0);
        }
        *LP(float, exw) = wave_max();
        if (N0 + 4 < NE) ck_dma(N0 + 4);
        wg_barrier();
        m0 = max8(exr); m1 = max8(exr + 512);
#pragma unroll
        for (int ks = 4; ks < 8; ++ks) { qs[ks][0] = q_ld(ks, 0); qs[ks][1] = q_ld(ks, 1); }
        __builtin_amdgcn_sched_barrier(0);
        float e[8];
        probs(e);
        ls0 = (e[0] + e[1]) + (e[2] + e[3]); ls1 = (e[4] + e[5]) + (e[6] + e[7]);
        PIN(ls0); PIN(ls1);
        if (wv == 0) *LP(u32x4, L_BIG) = u32x4{0u, 0u, tlo, thi};
        if (N0 + 5 < NE) ck_dma(N0 + 5);
        VMW(NE - 24);
        ka_reads(1);
        asm volatile("s_waitcnt lgkmcnt(4)\n\ts_barrier" ::: "memory");
        STAMP(22);
        static_assert(NE >= 28, "");
        bf16x8 pf[8];
        tg = *LP(u32x2, L_BIG + 8);
        pf[0] = p_ld(0); pf[1] = p_ld(1);
        ck_dma(NE);
        sa = s2a; sb = s2b;
        __builtin_amdgcn_sched_barrier(0);
#pragma unroll
        for (int ks = 4; ks < 8; ++ks) { sa = MFMA16(ka[ks], qs[ks][0], sa); sb = MFMA16(ka[ks], qs[ks][1], sb); }
        __builtin_amdgcn_sched_barrier(0);
#pragma unroll
        for (int i = 0; i < 16; ++i) { o[0][i] = 0.f; o[1][i] = 0.f; }
#pragma unroll
        for (int kk = 0; kk < 8; ++kk) {
            if (kk == 0) {
#pragma unroll
                for (int q = 2; q < 8; ++q) pf[q] = p_ld(q);
            }
            if (kk == 2) { PIN4(pf[2], pf[3], pf[4], pf[5]); PIN(pf[6]); PIN(pf[7]); PIN(tg); }
            if (kk >= 2 && wv < 4) { va_read1(kk - 2); if (kk == 7) va_read1(6); }
            o[0] = MFMA32(cat8(va[kk][0], va[kk][1]), pf[kk], o[0]);
            o[1] = MFMA32(cat8(va[kk][2], va[kk][3]), pf[kk], o[1]);
            if ((kk & 1) && NE + 1 + (kk >> 1) < 32) ck_dma(NE + 1 + (kk >> 1));
            __builtin_amdgcn_sched_barrier(0);
        }
        if (wv < 4) va_read1(7);
        VMW(4);
        ka_reads(2);
        tlo = __builtin_amdgcn_readfirstlane(tg[0]); thi = __builtin_amdgcn_readfirstlane(tg[1]);
        rederive();
        fbo = (unsigned)(WS_FB + x * STR + lane * 16);
        asm volatile("buffer_load_dwordx4 %0, off, %1, %2 sc1" : "=v"(nq) : "s"(rw), "s"(WS_NM + x * STR) : "memory");
        const unsigned head = lane & 31, r8 = lane >> 3, cw = wv ^ (rot * 2);
        srd = lb + r8 * 128 + (((lane & 7) ^ r8) << 4);
        sw[0] = lb + head * 128 + (((lane >> 5) ^ (head & 7)) << 4);
        sw[1] = sw[0] ^ 32u; sw[2] = sw[0] ^ 64u; sw[3] = sw[0] ^ 96u;
        p0 = (unsigned)(WS_PART + x * 1048576) + (cw >> 2) * 16384 + (cw & 3) * 2048 + r8 * 32768 + ((s ^ r8) * 8 + (lane & 7)) * 16;
        PIN4(sw[0], sw[1], sw[2], sw[3]); PIN(srd); PIN(p0); PIN(fbo);
#pragma unroll
        for (int ks = 8; ks < 12; ++ks) { sa = MFMA16(ka[ks], qr[ks - 8][0], sa); sb = MFMA16(ka[ks], qr[ks - 8][1], sb); }
    }
    STAMP(23);
    unsigned po[4];
    float mrun;
    {
        VMW(1);
        STAMP(24);
        ka_reads(3);
        asm volatile("" : "=v"(fa));
        if (wv == 0 && s < 11) asm volatile("buffer_load_dwordx4 %0, %1, %2, 0 offen sc1" : "+v"(fa) : "v"(fbo), "s"(rw) : "memory");
        __builtin_amdgcn_sched_barrier(0);
#pragma unroll
        for (int ks = 12; ks < 16; ++ks) { sa = MFMA16(ka[ks], qr[ks - 8][0], sa); sb = MFMA16(ka[ks], qr[ks - 8][1], sb); }
        bf16x8 pf[8];
#pragma unroll 1
        for (bool first = true;; first = false) {
            float e[8];
            probs(e);
            const float n0 = (e[0] + e[1]) + (e[2] + e[3]), n1 = (e[4] + e[5]) + (e[6] + e[7]);
            *LP(float, esw) = ls0 + n0; *LP(float, esw + 16384) = ls1 + n1;
            if (__builtin_expect(!(n0 + n1 <= BIGV), 0)) *LP(unsigned, L_BIG) = 1u;
            va_pin();
            wg_barrier();
            STAMP(25);
            PIN(prd);
            const unsigned big = *LP(unsigned, L_BIG);
            if (wv >= 4) va_reads();
#pragma unroll
            for (int kk = 0; kk < 8; ++kk) { pf[kk] = p_ld(kk); __builtin_amdgcn_sched_barrier(0); }
            if (__builtin_expect(__builtin_amdgcn_readfirstlane(big) == 0 || !first, 1)) break;
            const unsigned ex2 = L_SCR + 512 + 160;
            PIN(sa); PIN(sb);
            rederive();
            *LP(float, ex2 + (nl + (lane >> 5) * 16) * 1024 + wv * 4) = wave_max();
            wg_barrier();
            const float x0 = fmaxf(max8(ex2 + nl * 1024), m0), x1 = fmaxf(max8(ex2 + nl * 1024 + 16384), m1);
            const float al0 = __builtin_amdgcn_exp2f(m0 - x0), al1 = __builtin_amdgcn_exp2f(m1 - x1), al = (lane & 16) ? al1 : al0;
            m0 = x0; m1 = x1; ls0 *= al0; ls1 *= al1;
            o[0] *= al; o[1] *= al;
        }
        mrun = (lane & 16) ? m1 : m0;
        auto stage = [&](int t2, int gp) {
            const unsigned lo0 = pk2(o[t2][8 * gp + 0], o[t2][8 * gp + 1]), lo1 = pk2(o[t2][8 * gp + 2], o[t2][8 * gp + 3]);
            const unsigned hi0 = pk2(o[t2][8 * gp + 4], o[t2][8 * gp + 5]), hi1 = pk2(o[t2][8 * gp + 6], o[t2][8 * gp + 7]);
            auto r0 = __builtin_amdgcn_permlane32_swap(lo0, hi0, false, false);
            auto r1 = __builtin_amdgcn_permlane32_swap(lo1, hi1, false, false);
            *LP(u32x4, sw[t2 * 2 + gp]) = u32x4{r0[0], r1[0], r0[1], r1[1]};
        };
#pragma unroll
        for (int kk = 0; kk < 8; ++kk) {
            o[0] = MFMA32(cat8(va[kk][0], va[kk][1]), pf[kk], o[0]);
            if (kk == 1) {
                asm volatile("s_waitcnt vmcnt(0)" : "+v"(nq) :: "memory");
                npm = __builtin_amdgcn_readfirstlane(nq[0]);
                if ((unsigned)__builtin_amdgcn_readfirstlane(nq[2]) != tlo || (unsigned)__builtin_amdgcn_readfirstlane(nq[3]) != thi) npm = __builtin_amdgcn_readfirstlane(tagged_wait(WS_NM + x * STR)[0]);
            }
            if (kk == 3 || kk == 5) {
                const unsigned v = (npm ^ ((x & 2) ? 0xffu : 0u)) >> (lane >> 5);
#pragma unroll
                for (int i = kk - 3; i < kk - 1; ++i) { po[i] = p0 + ((v >> (2 * i)) & 1u) * 8192u; PIN(po[i]); }
            }
            __builtin_amdgcn_sched_barrier(0);
        }
#pragma unroll
        for (int kk = 0; kk < 8; ++kk) {
            o[1] = MFMA32(cat8(va[kk][2], va[kk][3]), pf[kk], o[1]);
            if (kk == 2) { PIN(o[0]); stage(0, 0); }
            if (kk == 4) stage(0, 1);
            __builtin_amdgcn_sched_barrier(0);
        }
        stage(1, 0); stage(1, 1);
        asm volatile("s_waitcnt lgkmcnt(0)" ::: "memory");
        u32x4 w[4];
#pragma unroll
        for (int i = 0; i < 4; ++i) w[i] = *LP(u32x4, srd + i * 1024);
#pragma unroll
        for (int i = 0; i < 4; ++i) ST16(w[i], po[i], i * 262144);
        __builtin_amdgcn_sched_barrier(0);
        STAMP(26);
        u32x4 mlrec;
        if (wv == 0) {
            const unsigned head = lane & 31;
            esr = L_SCR + head * 1024 + 512 + ((head & 7) << 4);
            mlo = (unsigned)(WS_ML + x * STR + s * 512 + head * 16) + ((npm >> ((head >> 3) * 2)) & 1u) * 8192u;
            PIN(mlo);
            f32x4 b8[8];
#pragma unroll
            for (int q = 0; q < 8; ++q) b8[q] = *LP(f32x4, esr ^ (q << 4));
            const f32x4 bs = ((b8[0] + b8[1]) + (b8[2] + b8[3])) + ((b8[4] + b8[5]) + (b8[6] + b8[7]));
            mlrec = u32x4{__float_as_uint(mrun), __float_as_uint((bs[0] + bs[1]) + (bs[2] + bs[3])), tlo, thi};
            PIN(mlrec);
        }
        VMW(0);
        wg_barrier();
        if (wv == 0) {
            if (lane < 32) ST16(mlrec, mlo, 0);
            PIN(fa);
            if (s < 11 && !__all(flags_ok(fa))) flags_wait(fbo);
        }
        STAMP(27);
        wg_barrier();
    }
    const unsigned long long t_att = __builtin_amdgcn_s_memrealtime();
    rederive();
    if (s >= 11) {
        const unsigned n = (unsigned)((s - 11) * 8 + wv);
        if (n < 28) pf4((n * 64 + lane) * 1024u + x * 128u, p.w2, 0);
        { const unsigned l2 = n * 64 + lane; pf4((l2 / 5u) * (DFF * 2) + min(((unsigned)x * 448u & ~127u) + (l2 % 5u) * 128u, DFF * 2 - 4u), p.w1, 0); }
        if (wv == 0) flag_put(WS_FA, id - 128);
        STAMP_FLUSH();
        return;
    }
    const unsigned L_H2S = L_SCR, L_REDM = L_SCR + 16384;
    if (s < 7) {
        {
            const char* w1s = (const char*)(p.w1 + (size_t)wv * 64 * DFF + ug * 32);
#pragma unroll
            for (int i = 0; i < 4; ++i) dma16((unsigned)((lane >> 2) * DFF * 2 + (lane & 3) * 16), w1s + (size_t)i * 16 * DFF * 2, wv * 4096 + i * 1024);
        }
        const int b = wv;
        if (wv == 0) ST16(s4, (unsigned)(WS_U + x * STR + (lane >> 5) * 8192 + s * 512 + (lane & 31) * 16), 0);
        const u32x4 shf = LD16((unsigned)(WS_MB + b * STR + lane * 16), 0), scl = LD16((unsigned)(WS_MB + b * STR + 1024 + lane * 16), 0);
        VMW(0);
        if (wv == 0) flag_put(WS_FA, id - 128);
        wg_barrier();
        bf16x8 wk0[8], wk1[8];
        if (wv >= 6) {
#pragma unroll
            for (int q = 0; q < 8; ++q) {
                const unsigned a = (unsigned)(q * 4096 + (g * 8 + (nl >> 2)) * 64 + (wv & 1) * 32 + (lane & 3) * 8);
                wk0[q] = tr_frag(a, a + 256); wk1[q] = tr_frag(a + 2048, a + 2304);
            }
#pragma unroll
            for (int q = 0; q < 8; q += 2) PIN4(wk0[q], wk0[q + 1], wk1[q], wk1[q + 1]);
        }
        float scv[8], shv[8];
#pragma unroll
        for (int q = 0; q < 4; ++q) {
            scv[2 * q] = rbf(1.f + lo16(scl[q])); scv[2 * q + 1] = rbf(1.f + hi16(scl[q])); shv[2 * q] = lo16(shf[q]); shv[2 * q + 1] = hi16(shf[q]);
            PIN4(scv[2 * q], scv[2 * q + 1], shv[2 * q], shv[2 * q + 1]);
        }
        unsigned har = L_H2S + ((lane & 7) * HS_LD + g * 8 + (nl >> 3) * 32) * 2, haw = L_H2S + (b * HS_LD + lane * 8) * 2;
        unsigned udst = (unsigned)(WS_U + x * STR + (g & 1) * 8192 + (s * 2 + (wv & 1)) * 256 + (nl & 7) * 32 + (g >> 1) * 16), xsrc = (unsigned)(WS_X1 + x * STR + (b * 512 + lane * 8) * 2) + ((npm >> x) & 1u) * 8192u;
        PIN4(har, haw, udst, xsrc);
        STAMP(28);
        while (__builtin_amdgcn_s_memrealtime() < t_att + XGATE) __builtin_amdgcn_s_sleep(2);
        u32x4 xv;
        for (unsigned spins = 0;;) {
            xv = LD16(xsrc, 0);
            if (__all(mx4(xv, 0u) != SENT)) break;
            __builtin_amdgcn_s_sleep(SLPX);
            if (++spins > kSpinCap) break;
            asm volatile("" ::: "memory");
        }
        STAMP(29);
        {
            f32x2_t v[4];
#pragma unroll
            for (int q = 0; q < 4; ++q) v[q] = f32x2_t{lo16(xv[q]), hi16(xv[q])};
            const f32x2_t p1 = (v[0] + v[1]) + (v[2] + v[3]), p2 = (v[0] * v[0] + v[1] * v[1]) + (v[2] * v[2] + v[3] * v[3]);
            float mb, rden;
            ln_stats(p1[0] + p1[1], p2[0] + p2[1], mb, rden);
            u32x4 hr;
#pragma unroll
            for (int q = 0; q < 4; ++q) {
                const f32x2_t h = ((v[q] - mb) * rden) * f32x2_t{scv[2 * q], scv[2 * q + 1]} + f32x2_t{shv[2 * q], shv[2 * q + 1]};
                hr[q] = pk2(h[0], h[1]);
            }
            *LP(u32x4, haw) = hr;
        }
        wg_barrier();
        if (wv >= 6) {
            bf16x8 af[8];
#pragma unroll
            for (int q = 0; q < 8; ++q) af[q] = *LP(bf16x8, har + q * 128);
            __builtin_amdgcn_sched_barrier(0);
            f32x4 d0 = {0.f, 0.f, 0.f, 0.f}, d1 = {0.f, 0.f, 0.f, 0.f};
#pragma unroll
            for (int q = 0; q < 8; ++q) { d0 = MFMA16(wk0[q], af[q], d0); d1 = MFMA16(wk1[q], af[q], d1); }
            const f32x4 own = nl < 8 ? d0 : d1;
#pragma unroll
            for (int e = 0; e < 4; ++e) { float v = own[e] + dppf<0x128>(own[e]); asm("" : "+v"(v)); d0[e] = fmaxf(v, 0.f); }
            const unsigned u0 = pk2s(d0[0], d0[1]), u1 = pk2s(d0[2], d0[3]);
            auto r0 = __builtin_amdgcn_permlane16_swap(u0, u0, false, false);
            auto r1 = __builtin_amdgcn_permlane16_swap(u1, u1, false, false);
            if (nl < 8) ST16((u32x4{r0[0], r1[0], r0[1], r1[1]}), udst, 0);
            STAMP(30);
        }
        STAMP_FLUSH();
        return;
    }
    {
        const int n0 = (x * 4 + (s - 7)) * 16;
        const char* mws = (const char*)(p.w2 + (size_t)wv * 224 * 512 + n0);
#pragma unroll
        for (int i = 0; i < 7; ++i) dma16((unsigned)((lane >> 1) * 1024 + (lane & 1) * 16), mws + i * 32768, rgn + i * 1024);
        const int fb = (lane >> 2) & 7, fc = (lane & 3) * 4 + (lane >> 5) * 2;
        u32x2 gb = {0u, 0u};
        if (wv == 0) {
            flag_put(WS_FA, id - 128);
            { const u32x4 v = tagged_wait(WS_MOD + fb * STR + (1536 + n0 + fc) * 8); gb = u32x2{v[0], v[1]}; }
            flags_wait(WS_FA + x * STR + lane * 16);
            if (id == 23 * 8 && lane == 0) __builtin_amdgcn_raw_buffer_store_b64(u32x2{tlo, thi}, rw, WS_EPOCH, 0, 16);
            PIN(gb);
        }
        VMW(0);
        bf16x8 wfr[7];
        const unsigned trl = (unsigned)(g * 8 + (nl >> 2)) * 32u + (unsigned)(lane & 3) * 8u;
#pragma unroll
        for (int ks = 0; ks < 7; ++ks) { wfr[ks] = tr_frag(rgn + ks * 1024 + trl, rgn + ks * 1024 + trl + 128); PIN(wfr[ks]); }
        unsigned usrc = (unsigned)(WS_U + wv * STR + (g >> 1) * 256 + (nl & 7) * 32 + (g & 1) * 16) + ((npm >> x) & 1u) * 8192u, usrc3 = usrc + 3072;
        usrc += (nl >> 3) * 512;
        PIN(usrc3);
        unsigned rdw = L_REDM + (wv * 2 + (nl >> 3)) * 512 + (nl & 7) * 64 + g * 16, rdr = L_REDM + (lane >> 5) * 4096 + (lane & 31) * 16;
        unsigned short* outp = p.out + fb * 512 + n0 + fc;
        PIN(usrc); PIN(rdw); PIN(rdr); PIN(outp);
        STAMP(28);
        wg_barrier();
        while (__builtin_amdgcn_s_memrealtime() < t_att + XGATE) __builtin_amdgcn_s_sleep(2);
        const unsigned x1b = (unsigned)(WS_X1 + x * STR) + ((npm >> x) & 1u) * 8192u;
        for (unsigned spins = 0;;) {
            const u32x4 xv = LD16(x1b + wv * 1024 + lane * 16, 0);
            if (__all(mx4(xv, 0u) != SENT)) break;
            __builtin_amdgcn_s_sleep(SLPD);
            if (++spins > kSpinCap) break;
            asm volatile("" ::: "memory");
        }
        wg_barrier();
        unsigned xg = 0u;
        if (wv == 0) { xg = __builtin_amdgcn_raw_buffer_load_b32(rw, x1b + (fb * 512 + n0 + fc) * 2, 0, 16); PIN(xg); } else __builtin_amdgcn_s_sleep(5);
        __builtin_amdgcn_s_sleep(SLPG);
        u32x4 uf[4];
        for (unsigned spins = 0;;) {
#pragma unroll
            for (int q = 0; q < 4; ++q) uf[q] = LD16(q < 3 ? usrc + q * 1024 : usrc3, 0);
            if (__all(mx4(uf[0], mx4(uf[1], mx4(uf[2], mx4(uf[3], 0u)))) != SENT)) break;
            if (++spins > kSpinCap) break;
            asm volatile("" ::: "memory");
        }
        STAMP(29);
        f32x4 d = {0.f, 0.f, 0.f, 0.f}, d2 = {0.f, 0.f, 0.f, 0.f};
#pragma unroll
        for (int ks = 0; ks < 7; ++ks) {
            if (ks & 1) d2 = MFMA16(wfr[ks], BF8(uf[ks >> 1]), d2);
            else d = MFMA16(wfr[ks], BF8(uf[ks >> 1]), d);
        }
        *LP(f32x4, rdw) = nl < 8 ? d : d2;
        wg_barrier();
        if (wv == 0) {
            f32x4 c[8];
#pragma unroll
            for (int w2 = 0; w2 < 8; ++w2) c[w2] = *LP(f32x4, rdr + w2 * 512);
            const f32x4 s4v = ((c[0] + c[1]) + (c[2] + c[3])) + ((c[4] + c[5]) + (c[6] + c[7]));
            f32x2_t sm;
#pragma unroll
            for (int q = 0; q < 2; ++q) { auto t2 = __builtin_amdgcn_permlane32_swap(__float_as_uint(s4v[q]), __float_as_uint(s4v[2 + q]), false, false); sm[q] = __uint_as_float(t2[0]) + __uint_as_float(t2[1]); }
            unsigned t = pk2(sm[0], sm[1]);
            t = pk2(__uint_as_float(gb[0]) * lo16(t), __uint_as_float(gb[1]) * hi16(t));
            *(GAS unsigned*)outp = pk2(lo16(xg) + lo16(t), hi16(xg) + hi16(t));
            STAMP(31);
        }
        STAMP_FLUSH();
    }
}

extern "C" void kernel_launch(void* const* d_in, const int* in_sizes, int n_in,
                              void* d_out, int out_size, void* d_ws, size_t ws_size,
                              hipStream_t stream) {
    Params p;
    p.x = (const unsigned short*)d_in[0];
    p.ckv = (const unsigned short*)d_in[1];
    p.w_q = (const unsigned short*)d_in[2];
    p.w_uk = (const unsigned short*)d_in[3];
    p.w_uv = (const unsigned short*)d_in[4];
    p.w_o = (const unsigned short*)d_in[5];
    p.w1 = (const unsigned short*)d_in[7];
    p.w2 = (const unsigned short*)d_in[8];
    p.c = (const unsigned short*)d_in[9];
    p.w_mod = (const unsigned short*)d_in[10];
    p.ws = (char*)(((size_t)d_ws + 2097151) & ~(size_t)2097151);
    p.out = (unsigned short*)d_out;
    (void)hipFuncSetAttribute((const void*)k_main, hipFuncAttributeMaxDynamicSharedMemorySize, 163840);
    k_main<<<256, 512, 163840, stream>>>(p);
}
```

```cpp
#define MEAS 99
#define AVG 0
#include <hip/hip_runtime.h>
#include <hip/hip_bf16.h>
#include <stdint.h>

#define NH 32
#define SK 4096
#define DFF 1792
#define GI 18
#define WG 1
#define NPOST 3
#define NP2 1
#define SL0 1
#define HP 13
#define QD 4
#define TK 5
#define NCAL 3
#define TG 430
#define TL 480
#define LMD 250
#define TWD 570
#define XGATE 270
#define CPFE 36000
#define QHELP 4
#define SLPH 4
#define SLPQ 2
#define SLPR 10
#define SLPX 1
#define SLPD 2
#define SLPG 18

using u16x8 = __attribute__((ext_vector_type(8))) unsigned short;
using s16x4 = __attribute__((ext_vector_type(4))) short;
using bf16x8 = __attribute__((ext_vector_type(8))) short;
using f32x16 = __attribute__((ext_vector_type(16))) float;
using f32x4 = __attribute__((ext_vector_type(4))) float;
using u32x4 = __attribute__((ext_vector_type(4))) unsigned;
using u32x2 = __attribute__((ext_vector_type(2))) unsigned;

#define GAS __attribute__((address_space(1)))
#define LAS __attribute__((address_space(3)))
typedef GAS unsigned long long gu64;
#define RLX_AGENT __ATOMIC_RELAXED, __HIP_MEMORY_SCOPE_AGENT
#define SENT 0xffffffffu
#define TAGC 0x9e3779b9u

__device__ __forceinline__ float bf2f(unsigned short u) { return __uint_as_float(((unsigned)u) << 16); }
typedef __attribute__((ext_vector_type(2))) __bf16 bf16x2_t;
typedef __attribute__((ext_vector_type(2))) float f32x2_t;
__device__ __forceinline__ unsigned pk2(float a, float b) {
    f32x2_t v = {a, b};
    bf16x2_t r = __builtin_convertvector(v, bf16x2_t);
    return __builtin_bit_cast(unsigned, r);
}
__device__ __forceinline__ unsigned nosent(unsigned r) { return min(r, SENT - 1u); }
__device__ __forceinline__ unsigned pk2s(float a, float b) { return nosent(pk2(a, b)); }
__device__ __forceinline__ unsigned short f2bf(float f) { return (unsigned short)(pk2(f, 0.f) & 0xffffu); }
__device__ __forceinline__ float rbf(float f) { return __uint_as_float(pk2(f, 0.f) << 16); }
__device__ __forceinline__ float lo16(unsigned u) { return __uint_as_float(u << 16); }
__device__ __forceinline__ float hi16(unsigned u) { return __uint_as_float(u & 0xffff0000u); }
__device__ __forceinline__ unsigned umax3(unsigned a, unsigned b, unsigned c) { return max(max(a, b), c); }
__device__ __forceinline__ unsigned mx4(const u32x4& v, unsigned m) { return umax3(v[2], v[3], umax3(v[0], v[1], m)); }
__device__ __forceinline__ float fmax3(float a, float b, float c) { float r; asm("v_max3_f32 %0, %1, %2, %3" : "=v"(r) : "v"(a), "v"(b), "v"(c)); return r; }
template <int C> __device__ __forceinline__ float dppf(float v) { return __int_as_float(__builtin_amdgcn_update_dpp(0, __float_as_int(v), C, 0xf, 0xf, true)); }
__device__ __forceinline__ float sum16(float v) { v += dppf<0xB1>(v); v += dppf<0x4E>(v); v += dppf<0x141>(v); v += dppf<0x140>(v); return v; }

__device__ __forceinline__ void ln_stats(float p1, float p2, float& mb, float& rden) {
    auto t = __builtin_amdgcn_permlane32_swap(__float_as_uint(p1), __float_as_uint(p2), false, false);
    float a = sum16(__uint_as_float(t[0]) + __uint_as_float(t[1]));
    auto t2 = __builtin_amdgcn_permlane16_swap(__float_as_uint(a), __float_as_uint(a), false, false);
    a = (__uint_as_float(t2[0]) + __uint_as_float(t2[1])) * (1.f / 512.f);
    auto t3 = __builtin_amdgcn_permlane32_swap(__float_as_uint(a), __float_as_uint(a), false, false);
    const float m = __uint_as_float(t3[0]);
    const float var = rbf(fmaxf(fmaf(-m, m, __uint_as_float(t3[1])), 0.f));
    mb = rbf(m);
    rden = __builtin_amdgcn_rcpf(rbf(__builtin_amdgcn_sqrtf(rbf(var + 1e-5f))));
}
#define STR 262144
#define WS_Q 0
#define WS_H 32768
#define WS_X1 65536
#define WS_ML 81920
#define WS_MOD 98304
#define WS_MB 139264
#define WS_U 114688
#define WS_FI 131072
#define WS_FA 132096
#define WS_FB 133120
#define WS_NP 134144
#define WS_NM 134400
#define WS_EPOCH 135168
#define WS_CAL 147456
#define WS_OUTP 2097152
#define WS_PART 4194304
#define WS_END 12582912

#define LP(T, off) ((LAS T*)(size_t)(unsigned)(off))
#define ABSM 1
#define STAMP(k) do { if ((k) == MEAS) asm volatile("s_memrealtime %0\n\ts_waitcnt lgkmcnt(0)" : "=s"(tA) :: "memory"); if ((k) == 31) fin = true; } while (0)
#define STAMP_FLUSH() do { if ((threadIdx.x & 63) == 0) { __hip_atomic_store((gu64*)(p.ws + (128u << 20)) + blockIdx.x * 8 + wv, t00, RLX_AGENT); __hip_atomic_store((gu64*)(p.ws + (128u << 20)) + 2048 + blockIdx.x * 8 + wv, tA, RLX_AGENT); } \
    if (fin) { \
        const unsigned long long t1 = __builtin_amdgcn_s_memrealtime(); \
        while (__builtin_amdgcn_s_memrealtime() < t1 + 200) __builtin_amdgcn_s_sleep(1); \
        unsigned long long m = ~0ull, mx = 0ull; unsigned sm = 0, cn = 0; \
        for (int q = 0; q < 32; ++q) { const unsigned long long v = __hip_atomic_load((gu64*)(p.ws + (128u << 20)) + q * 64 + (threadIdx.x & 63), RLX_AGENT); if (t1 - v < 10000ull) m = v < m ? v : m; } \
        for (int o = 1; o < 64; o <<= 1) { const unsigned long long v = __shfl_xor(m, o); m = v < m ? v : m; } \
        for (int q = 0; q < 32; ++q) { const unsigned long long v = __hip_atomic_load((gu64*)(p.ws + (128u << 20)) + 2048 + q * 64 + (threadIdx.x & 63), RLX_AGENT); if (v - m < 10000ull) { mx = v > mx ? v : mx; sm += (unsigned)(v - m); ++cn; } } \
        for (int o = 1; o < 64; o <<= 1) { const unsigned long long v = __shfl_xor(mx, o); mx = v > mx ? v : mx; sm += __shfl_xor(sm, o); cn += __shfl_xor(cn, o); } \
        if (AVG) mx = m + sm / max(cn, 1u); \
        const unsigned long long te = m + 2500ull + (MEAS == 99 ? 0u : min((unsigned)(mx - m), 5000u) * 4u); \
        const unsigned tl = __builtin_amdgcn_readfirstlane((unsigned)te); \
        while ((int)((unsigned)__builtin_amdgcn_s_memrealtime() - tl) < 0) __builtin_amdgcn_s_sleep(1); \
    } } while (0)
#define STAMP0() asm volatile("s_waitcnt lgkmcnt(0)" : "+s"(t00) : "s"(ws))
__device__ __forceinline__ s16x4 tr4(unsigned a) { return __builtin_amdgcn_ds_read_tr16_b64_v4i16(LP(s16x4, a)); }
__device__ __forceinline__ bf16x8 cat8(s16x4 a, s16x4 b) { return __builtin_shufflevector(a, b, 0, 1, 2, 3, 4, 5, 6, 7); }
__device__ __forceinline__ bf16x8 tr_frag(unsigned a0, unsigned a1) { return cat8(tr4(a0), tr4(a1)); }
__device__ __forceinline__ unsigned swz(unsigned row) { return ((row & 3) << 2) | ((row >> 2) & 3); }
__device__ __forceinline__ unsigned off_b(unsigned row, unsigned ch) { return 256u * row + 16u * (ch ^ swz(row)); }
__device__ __forceinline__ unsigned tile_off(unsigned key, unsigned chunk) { return ((key >> 5) * 4 + (chunk >> 4)) * 8192u + off_b(key & 31, chunk & 15); }
__device__ __forceinline__ void wg_barrier() { asm volatile("s_waitcnt lgkmcnt(0)\n\ts_barrier" ::: "memory"); }
__device__ __forceinline__ void dma16(unsigned voff, const void* sbase, unsigned lds_dst) {
    asm volatile("s_mov_b32 m0, %2\n\ts_nop 0\n\tglobal_load_lds_dwordx4 %0, %1" :: "v"(voff), "s"(sbase), "s"(lds_dst) : "memory", "m0");
}
__device__ __forceinline__ void dma16x(unsigned voff, unsigned vx, const void* sbase, unsigned lds_dst) {
    unsigned t;
    asm volatile("s_mov_b32 m0, %3\n\tv_xor_b32 %0, %4, %1\n\tglobal_load_lds_dwordx4 %0, %2 nt" : "=&v"(t) : "v"(voff), "s"(sbase), "s"(lds_dst), "i"(vx) : "memory", "m0");
}
__device__ __forceinline__ void pf4(unsigned voff, const void* sbase, unsigned lds_dst) {
    asm volatile("s_mov_b32 m0, %2\n\ts_nop 0\n\tglobal_load_lds_dword %0, %1" :: "v"(voff), "s"(sbase), "s"(lds_dst) : "memory", "m0");
}
__device__ __forceinline__ void pf4s(unsigned voff, const void* sbase, unsigned lds_dst) {
    asm volatile("s_mov_b32 m0, %2\n\ts_nop 0\n\tglobal_load_lds_dword %0, %1 sc1" :: "v"(voff), "s"(sbase), "s"(lds_dst) : "memory", "m0");
}
#define SPINCAP (1u << 20)
constexpr unsigned kSpinCap = SPINCAP;
struct Params {
    const unsigned short* x;
    const unsigned short* ckv;
    const unsigned short* w_q;
    const unsigned short* w_uk;
    const unsigned short* w_uv;
    const unsigned short* w_o;
    const unsigned short* w1;
    const unsigned short* w2;
    const unsigned short* c;
    const unsigned short* w_mod;
    char* ws;
    unsigned short* out;
};
#define VMW(n) asm volatile("s_waitcnt vmcnt(%0)" :: "n"(n) : "memory")
#define PIN(x) asm volatile("" : "+v"(x))
#define PIN4(a, b, c, d) asm volatile("" : "+v"(a), "+v"(b), "+v"(c), "+v"(d) :: "memory")
#define MFMA16(a, b, c) __builtin_amdgcn_mfma_f32_16x16x32_bf16(a, b, c, 0, 0, 0)
#define MFMA32(a, b, c) __builtin_amdgcn_mfma_f32_32x32x16_bf16(a, b, c, 0, 0, 0)
#define BF8(x) __builtin_bit_cast(bf16x8, x)
#define ST16(v, off, soff) __builtin_amdgcn_raw_buffer_store_b128(v, rw, off, soff, 16)
#define LD16(off, soff) __builtin_amdgcn_raw_buffer_load_b128(rw, off, soff, 16)
#define SCL (0.08838834764831845f * 1.4426950408889634f)
#define BIGV 1.0e18f
#define L_SCR 131072
#define HS_LD 520

__global__ __launch_bounds__(512, 2) void k_main(Params p) {
    unsigned long long tA = 0ull; bool fin = false;
    unsigned long long t00; asm volatile("s_memrealtime %0" : "=s"(t00));
    unsigned long long pc;
    asm volatile("s_getpc_b64 %0" : "=s"(pc));
    const int id = blockIdx.x, tid = threadIdx.x;
    int lane = tid & 63;
    const int wv = __builtin_amdgcn_readfirstlane(tid >> 6);
    int g = lane >> 4, nl = lane & 15;
    auto rederive = [&]() {
        asm volatile("v_mbcnt_lo_u32_b32 %0, -1, 0\n\tv_mbcnt_hi_u32_b32 %0, -1, %0" : "=v"(lane));
        g = lane >> 4; nl = lane & 15;
    };
    const int x = id & 7, j0 = id >> 3, j = j0 < 16 ? 15 - j0 : j0;
    if (wv < 5 && j == 15) {
        asm volatile("s_mov_b32 m0, %2\n\ts_nop 0\n\tglobal_load_lds_dword %0, %1" :: "v"(min((unsigned)((wv * 64 + lane) * 128), (unsigned)CPFE)), "s"(pc), "s"(L_SCR + 31744) : "memory", "m0");
    }
    char* ws = p.ws;
    if ((j >> 2) == 3) {
        dma16((unsigned)(((nl & 7) * 512 + (nl >> 3) * 32 + g * 8) * 2 + wv * 128), p.c, wv * 16384 + 8192);
        dma16((unsigned)tid * 16u, p.x, wv * 16384 + 9216);
    }
    if (j == 15 && wv == 5 && lane < 2) pf4s((unsigned)((x * 2 + lane) * 2097152), p.ckv, L_SCR + 31744);
    if (j == 14 && wv >= 4 && lane < 2) pf4s((unsigned)(lane * 2097152 + x * 128), wv == 4 ? p.w_mod : wv == 5 ? p.w_uk : wv == 6 ? p.w_q : (const unsigned short*)(ws + WS_CAL), L_SCR + 31744);
    STAMP0();
    const __amdgpu_buffer_rsrc_t rw = __builtin_amdgcn_make_buffer_rsrc(ws, 0, WS_END, 0x00020000);
    const u32x4 s4 = {SENT, SENT, SENT, SENT};
    unsigned long long cnt = 0ull;
    auto cnt_load = [&]() { asm volatile("global_load_dwordx2 %0, %1, %2 sc1" : "+v"(cnt) : "v"((unsigned)WS_EPOCH), "s"(ws) : "memory"); };
    unsigned tlo = 0u, thi = 0u;
    auto tag_take = [&]() {
        asm volatile("" : "+v"(cnt) :: "memory");
        const unsigned long long t = cnt + 1ull;
        tlo = __builtin_amdgcn_readfirstlane((unsigned)t); thi = __builtin_amdgcn_readfirstlane((unsigned)(t >> 32));
    };
    auto flag_put = [&](unsigned base, unsigned idx) {
        if (lane < 8) __builtin_amdgcn_raw_buffer_store_b64(u32x2{tlo, thi}, rw, base + lane * STR + idx * 8, 0, 16);
    };
    auto tagged_wait = [&](unsigned off) {
        u32x4 v;
#pragma unroll 1
        for (unsigned spins = 0; spins < kSpinCap; ++spins) {
            asm volatile("buffer_load_dwordx4 %0, %1, %2, 0 offen sc1\n\ts_waitcnt vmcnt(0)" : "=v"(v) : "v"(off), "s"(rw) : "memory");
            if (__all((v[2] == tlo) & (v[3] == thi))) break;
            __builtin_amdgcn_s_sleep(8);
        }
        return v;
    };
    auto flags_ok = [&](const u32x4& f) { return (f[0] == tlo) & (f[1] == thi) & (f[2] == tlo) & (f[3] == thi); };
    auto flags_wait = [&](unsigned off) {
        for (unsigned spins = 0;;) {
            u32x4 f;
            asm volatile("buffer_load_dwordx4 %0, %1, %2, 0 offen sc1\n\ts_waitcnt vmcnt(0)" : "=v"(f) : "v"(off), "s"(rw) : "memory");
            if (__all(flags_ok(f))) break;
            __builtin_amdgcn_s_sleep(16);
            if (++spins > kSpinCap) break;
            asm volatile("" ::: "memory");
        }
    };
    unsigned pcl, frl, frs;
    auto dma_piece = [&](const unsigned short* src, unsigned ldb, unsigned dst) {
        const unsigned v0 = (lane >> 3) * ldb + pcl, v1 = v0 ^ 64u;
#pragma unroll
        for (int i = 0; i < 8; ++i) dma16((i & 1) ? v1 : v0, (const char*)src + (size_t)i * 8 * ldb, dst + i * 1024);
    };
    const unsigned rgn = (unsigned)(wv * 16384);

    if (j < 16) {
        const unsigned long long t_start = __builtin_amdgcn_s_memrealtime();
        rederive();
        pcl = (unsigned)(((((lane & 7) >> 1) ^ ((lane >> 4) & 1)) * 32) + (lane & 1) * 16);
        frl = (unsigned)((g * 8 + (nl >> 2)) * 128 + (lane & 3) * 8); frs = (unsigned)((nl >> 3) | ((g & 1) << 1));
        const int th = x * 4 + (j & 3);
        if (j < 12) {
            const int qt = j >> 2, rot = qt * 5;
            const bool pub = wv < (qt < 2 ? 3 : 2);
            const unsigned qo = (unsigned)(WS_Q + (th & 7) * STR + ((nl & 7) * 4 + (th >> 3)) * 1024 + (qt * 192 + wv * 64 + g * 16 + (nl >> 3) * 8) * 2);
            const unsigned vb = (unsigned)(g * 8192 + ((nl ^ (g << 2)) << 4));
            const char* wuk = (const char*)(p.w_uk + th * 128) + (size_t)qt * 192 * 8192;
            const char* wq = (const char*)(p.w_q + th * 128) + (size_t)wv * 32768;
            const unsigned L_U = 114688;
            {
                const int k0 = qt < 2 ? wv * 6 : wv * 4;
#pragma unroll
                for (int i = 0; i < 6; ++i) {
                    if (i < 4 || qt < 2) dma16(vb ^ (((k0 + i) & 3) << 4), wuk + (size_t)(k0 + i) * 32768, L_U + (k0 + i) * 1024);
                    if (i == 4) VMW(4);
                }
            }
            const unsigned vq = vb ^ ((wv & 3) << 4);
            auto wq_dma = [&](int ps) { dma16(vq, wq + (size_t)((ps + rot) & 15) * 262144, ps * 8192 + wv * 1024); };
            bf16x8 uk[4][4], aq[16];
            u32x4 fi, hv[4];
            unsigned fio = (unsigned)(WS_FI + x * STR + (lane & 7) * 16), hsrc, hsrc2, ta0, ta1, tb0, tb1, qo2, hw0, hw1, hr2, qr0, qr1, qw, thx = 0u;
            const unsigned L_HI = L_SCR, L_QI = L_SCR + 8192;
            auto h_load = [&]() {
                asm volatile("buffer_load_dwordx4 %0, %4, %6, 0 offen sc1\n\tbuffer_load_dwordx4 %1, %4, %6, 0 offen offset:1024 sc1\n\t"
                             "buffer_load_dwordx4 %2, %5, %6, 0 offen sc1\n\tbuffer_load_dwordx4 %3, %5, %6, 0 offen offset:1024 sc1"
                             : "+v"(hv[0]), "+v"(hv[1]), "+v"(hv[2]), "+v"(hv[3]) : "v"(hsrc), "v"(hsrc2), "s"(rw) : "memory");
            };
            auto h_bad = [&]() {
                PIN4(hv[0], hv[1], hv[2], hv[3]);
                unsigned bad = (hv[0][1] ^ tlo) | (hv[0][3] ^ thx) | (hv[1][1] ^ tlo) | (hv[1][3] ^ thx) | (hv[2][1] ^ tlo) | (hv[2][3] ^ thx) | (hv[3][1] ^ tlo) | (hv[3][3] ^ thx);
                PIN(bad);
                return bad;
            };
#pragma unroll
            for (int ps = 0; ps < 16; ++ps) {
                wq_dma(ps);
                if (ps == 4) cnt_load();
                if (ps == HP) { h_load(); asm volatile("buffer_load_dwordx4 %0, %1, %2, 0 offen sc1" : "=v"(fi) : "v"(fio), "s"(rw) : "memory"); }
                if (ps >= 4 && ps < 8) VMW(5); else if (ps >= HP && ps < HP + 4) VMW(9); else VMW(4);
                if (ps == 3) {
                    asm volatile("s_barrier" ::: "memory");
                    if (pub) {
#pragma unroll
                        for (int t = 0; t < 4; ++t)
#pragma unroll
                            for (int s = 0; s < 4; ++s) uk[t][s] = *LP(bf16x8, L_U + off_b(wv * 64 + (nl >> 2) * 16 + t * 4 + (nl & 3), s * 4 + g));
                    }
                    STAMP(1);
                }
                if (ps == 5) {
                    hsrc = (unsigned)(WS_H + x * STR + wv * 2048 + lane * 16); hsrc2 = hsrc + 16384;
                    ta0 = off_b(8 * g + (nl >> 2), 2 * wv + ((lane & 3) >> 1)) + 8 * (lane & 1); ta1 = off_b(8 * g + 4 + (nl >> 2), 2 * wv + ((lane & 3) >> 1)) + 8 * (lane & 1);
                    tb0 = ta0 + 65536; tb1 = ta1 + 65536; qo2 = qo;
                    hw0 = L_HI + ((2 * wv - rot) & 15) * 512 + (lane >> 4) * 128 + (lane & 7) * 16 + ((lane >> 3) & 1) * 8;
                    hw1 = L_HI + ((2 * wv + 1 - rot) & 15) * 512 + (lane >> 4) * 128 + (lane & 7) * 16 + ((lane >> 3) & 1) * 8;
                    hr2 = L_HI + g * 128 + (nl & 7) * 16 + (nl >> 3) * 512;
                    PIN(hr2);
                    qr0 = L_QI + g * 128 + (nl & 7) * 16 + (nl >> 3) * 2048; qr1 = qr0 ^ 2048u;
                    PIN(qr0); PIN(qr1);
                    qw = L_QI + (wv >> 1) * 512 + ((wv & 1) * 2 + (g >> 1)) * 128 + (nl & 7) * 16 + (g & 1) * 8;
                    PIN4(hsrc, hsrc2, ta0, ta1); PIN4(tb0, tb1, qo2, hw0); PIN(hw1); PIN(qw);
                }
                if (ps == 8) { tag_take(); thx = tlo ^ thi ^ TAGC; }
                if (ps == 9) {
#pragma unroll
                    for (int t = 0; t < 4; ++t) PIN4(uk[t][0], uk[t][1], uk[t][2], uk[t][3]);
                    wg_barrier();
                    *LP(unsigned, L_QI + 2048 + wv * 256 + lane * 4) = 0u;
#pragma unroll
                    for (int q = 0; q < 6; ++q) aq[q] = tr_frag(ta0 + q * 8192, ta1 + q * 8192);
                }
                if (ps == 13) {
                    asm volatile("s_barrier" ::: "memory");
#pragma unroll
                    for (int q = 6; q < 10; ++q) aq[q] = q < 8 ? tr_frag(ta0 + q * 8192, ta1 + q * 8192) : tr_frag(tb0 + (q - 8) * 8192, tb1 + (q - 8) * 8192);
                }
            }
            STAMP(15);
            if (HP + 4 > 15) VMW(HP + 4 > 15 ? 15 - HP : 0);
            if (!__all(h_bad() == 0u)) {
                for (unsigned spins = 0;;) {
                    h_load();
                    VMW(0);
                    if (__all(h_bad() == 0u)) break;
                    __builtin_amdgcn_s_sleep(SLPH);
                    if (++spins > kSpinCap) break;
                }
            }
            {
                unsigned hh[4];
#pragma unroll
                for (int q = 0; q < 4; ++q) {
                    const unsigned t = hv[2 + (q >> 1)][(q & 1) * 2], sh = hv[q >> 1][(q & 1) * 2];
                    hh[q] = pk2(lo16(t) + lo16(sh), hi16(t) + hi16(sh));
                }
                *LP(u32x2, hw0) = u32x2{hh[0], hh[1]};
                *LP(u32x2, hw1) = u32x2{hh[2], hh[3]};
            }
            STAMP(3);
            asm volatile("s_waitcnt vmcnt(0) lgkmcnt(0)\n\ts_barrier" : "+v"(fi) :: "memory");
            STAMP(2);
            {
                bf16x8 hb[8];
#pragma unroll
                for (int q = 0; q < 5; ++q) hb[q] = *LP(bf16x8, hr2 + q * 1024);
#pragma unroll
                for (int ps = 10; ps < 16; ++ps) {
                    if (!(ps & 1)) hb[ps >> 1] = *LP(bf16x8, hr2 + (ps >> 1) * 1024);
                    aq[ps] = tr_frag(tb0 + (ps - 8) * 8192, tb1 + (ps - 8) * 8192);
                }
                __builtin_amdgcn_sched_barrier(0);
                f32x4 d0 = {0.f, 0.f, 0.f, 0.f}, d1 = {0.f, 0.f, 0.f, 0.f};
#pragma unroll
                for (int ps = 0; ps < 16; ps += 2) {
                    d0 = MFMA16(aq[ps], hb[ps >> 1], d0); d1 = MFMA16(aq[ps + 1], hb[ps >> 1], d1);
                    if (ps == 6) { unsigned bad = (fi[0] ^ tlo) | (fi[1] ^ thi) | (fi[2] ^ tlo) | (fi[3] ^ thi); PIN(bad); if (!__all(bad == 0u)) flags_wait(fio); }
                }
#pragma unroll
                for (int e = 0; e < 4; ++e) { float v = d0[e] + dppf<0x128>(d1[e]); asm("" : "+v"(v)); d0[e] = v; }
                if (nl < 8) *LP(u32x2, qw) = u32x2{pk2(d0[0], d0[1]), pk2(d0[2], d0[3])};
            }
            wg_barrier();
            if (pub) {
                bf16x8 qb[2][4];
#pragma unroll
                for (int s = 0; s < 4; ++s) { qb[0][s] = *LP(bf16x8, qr0 + s * 512); qb[1][s] = *LP(bf16x8, qr1 + s * 512); }
                __builtin_amdgcn_sched_barrier(0);
                f32x4 a = {0.f, 0.f, 0.f, 0.f}, b = a;
#pragma unroll
                for (int s = 0; s < 4; ++s) {
                    a = MFMA16(uk[0][s], qb[0][s], a); b = MFMA16(uk[1][s], qb[0][s], b);
                    a = MFMA16(uk[2][s], qb[1][s], a); b = MFMA16(uk[3][s], qb[1][s], b);
                }
                const u32x4 qv = {pk2s(a[0], a[1]), pk2s(a[2], a[3]), pk2s(b[0], b[1]), pk2s(b[2], b[3])};
                ST16(qv, qo2, 0);
            }
            STAMP(4);
            wg_barrier();
            if (j == 0 && wv == 3) {
                const int d = (int)tagged_wait(WS_NP + x * STR + (lane & 7) * 16)[0];
                int t = (lane & 4) ? -d : d;
                t += __builtin_amdgcn_update_dpp(0, t, 0xB1, 0xf, 0xf, true);
                t += __builtin_amdgcn_update_dpp(0, t, 0x4E, 0xf, 0xf, true);
                t += __builtin_amdgcn_update_dpp(0, t, 0x141, 0xf, 0xf, true);
                const bool n1 = abs(d) > 160 ? d < 0 : (t < 0) != ((lane & 4) != 0);
                const unsigned mk = (unsigned)__ballot(n1) & 0xffu;
                if (lane == 0) ST16((u32x4{mk, 0u, tlo, thi}), (unsigned)(WS_NM + x * STR), 0);
            }
            if (wv < 3 && j + 12 * wv < 32) {
                const int hh = j + 12 * wv;
#pragma unroll 1
                for (unsigned spins = 0; spins < 64; ++spins) {
                    __builtin_amdgcn_s_sleep(QHELP);
                    const u32x4 v = LD16((unsigned)(WS_Q + (hh & 7) * STR + (x * 4 + (hh >> 3)) * 1024 + lane * 16), 0);
                    if (__all(mx4(v, 0u) != SENT)) break;
                    asm volatile("" ::: "memory");
                }
                STAMP(6);
            }
        } else {
            const int cg = x * 2 + (j & 1);
            if (j < 14) {
                dma_piece(p.w_mod + (size_t)wv * 64 * 3072 + cg * 64, 6144, rgn);
                cnt_load();
                VMW(9);
            } else {
                cnt_load();
                if (j == 15) {
#pragma unroll
                    for (int q = 0; q < 4; ++q) ST16(s4, (unsigned)(WS_Q + wv * STR + x * 4096 + lane * 16), q * 1024);
                    VMW(0);
                    tag_take();
                    wg_barrier();
                    if (wv == 7 && lane < 8) ST16((u32x4{tlo, thi, tlo, thi}), (unsigned)(WS_FI + lane * STR + x * 16), 0);
                    STAMP(15);
                } else {
                    u32x2 best = {~0u, ~0u};
#pragma unroll
                    for (int r = 0; r < NCAL; ++r) {
                        const unsigned hf = (r ^ wv) & 1;
                        VMW(0);
                        const unsigned long long t0 = __builtin_amdgcn_s_memtime();
                        if (lane < 8) ST16(s4, WS_CAL + x * STR + hf * 8192 + (wv * 8 + r) * 128 + lane * 16, 0);
                        VMW(0);
                        const unsigned dt = (unsigned)(__builtin_amdgcn_s_memtime() - t0);
                        if (hf) best[1] = min(best[1], dt); else best[0] = min(best[0], dt);
                    }
                    tag_take();
                    *LP(u32x2, 16384 * 7 + 14336 + wv * 8) = best;
                    wg_barrier();
                    if (wv == 0 && lane < 8) {
                        u32x2 m = *LP(u32x2, 16384 * 7 + 14336 + lane * 8);
#pragma unroll
                        for (int o = 1; o < 8; o <<= 1) { m[0] = min(m[0], (unsigned)__shfl_xor(m[0], o)); m[1] = min(m[1], (unsigned)__shfl_xor(m[1], o)); }
                        ST16((u32x4{(unsigned)max(-1000, min(1000, (int)m[1] - (int)m[0])), 0u, tlo, thi}), (unsigned)(WS_NP + lane * STR + x * 16), 0);
                    }
                    STAMP(9);
                }
            }
            const u16x8 cf = *LP(u16x8, rgn + 8192 + lane * 16), xa = *LP(u16x8, rgn + 9216 + lane * 16);
            u32x4 af[2];
#pragma unroll
            for (int q = 0; q < 4; ++q) {
                const float a = bf2f(cf[2 * q]), b = bf2f(cf[2 * q + 1]);
                af[0][q] = pk2(a * __builtin_amdgcn_rcpf(1.f + __expf(-a)), b * __builtin_amdgcn_rcpf(1.f + __expf(-b)));
                af[1][q] = (unsigned)__builtin_amdgcn_update_dpp(0, (int)af[0][q], 0x128, 0xf, 0xf, true);
            }
            const unsigned L_MS = L_SCR;
            unsigned wa[4], fr[4];
#pragma unroll
            for (int t = 0; t < 4; ++t) { wa[t] = L_MS + wv * 2048 + (nl & 7) * 256 + (((t * 4 + g) ^ (nl & 7)) << 4) + (nl >> 3) * 16384; fr[t] = rgn + frl + ((t ^ frs) << 5); }
            PIN4(wa[0], wa[1], wa[2], wa[3]); PIN4(fr[0], fr[1], fr[2], fr[3]); PIN(af[0]); PIN(af[1]);
            const unsigned L_XN = 12288;
            auto mod_mm = [&](bool ln) {
                VMW(0);
                bf16x8 f[2][4];
#pragma unroll
                for (int ks = 0; ks < 2; ++ks)
#pragma unroll
                    for (int t = 0; t < 4; ++t) f[ks][t] = tr_frag(fr[t] + ks * 4096, fr[t] + ks * 4096 + 512);
                __builtin_amdgcn_sched_barrier(0);
                f32x4 d[4];
#pragma unroll
                for (int t = 0; t < 4; ++t) d[t] = MFMA16(f[0][t], BF8(af[0]), (f32x4{0.f, 0.f, 0.f, 0.f}));
#pragma unroll
                for (int t = 0; t < 4; ++t) d[t] = MFMA16(f[1][t], BF8(af[1]), d[t]);
#pragma unroll
                for (int t = 0; t < 4; ++t) *LP(f32x4, wa[t]) = d[t];
                if (ln) {
                    float v[8];
#pragma unroll
                    for (int q = 0; q < 8; ++q) v[q] = bf2f(xa[q]);
                    const float p1 = ((v[0] + v[1]) + (v[2] + v[3])) + ((v[4] + v[5]) + (v[6] + v[7]));
                    const float p2 = ((v[0] * v[0] + v[1] * v[1]) + (v[2] * v[2] + v[3] * v[3])) + ((v[4] * v[4] + v[5] * v[5]) + (v[6] * v[6] + v[7] * v[7]));
                    float mb, rden;
                    ln_stats(p1, p2, mb, rden);
                    u32x4 r;
#pragma unroll
                    for (int q = 0; q < 4; ++q) { const unsigned d = pk2(v[2 * q] - mb, v[2 * q + 1] - mb); r[q] = pk2(lo16(d) * rden, hi16(d) * rden); }
                    *LP(u32x4, L_XN + wv * 16384 + lane * 16) = r;
                }
                wg_barrier();
            };
            auto sum8 = [&](unsigned a) {
                f32x4 u[8];
#pragma unroll
                for (int w2 = 0; w2 < 8; ++w2) u[w2] = *LP(f32x4, a + w2 * 2048);
                __builtin_amdgcn_sched_barrier(0);
                return ((u[0] + u[1]) + (u[2] + u[3])) + ((u[4] + u[5]) + (u[6] + u[7]));
            };
            if (j < 14) {
                const int hb = lane & 7, c4 = 2 * wv + (nl >> 3);
                unsigned hoff = (unsigned)(WS_H + g * STR + cg * 2048 + wv * 256 + nl * 16);
                unsigned sa = L_MS + hb * 256 + ((c4 ^ hb) << 4);
                unsigned xa2 = L_XN + hb * 16384 + (cg & 7) * 128 + c4 * 8;
                PIN(hoff); PIN(sa); PIN(xa2);
                STAMP(1);
                mod_mm(cg >= 8);
                STAMP(2);
                const u32x2 xn = *LP(u32x2, xa2);
                const f32x4 v = sum8(sa);
                tag_take();
                const unsigned thx = tlo ^ thi ^ TAGC;
                u32x2 r = {pk2(v[0], v[1]), pk2(v[2], v[3])};
                if (cg >= 8) {
#pragma unroll
                    for (int q = 0; q < 2; ++q) { const unsigned sc = pk2(1.f + lo16(r[q]), 1.f + hi16(r[q])); r[q] = pk2(lo16(xn[q]) * lo16(sc), hi16(xn[q]) * hi16(sc)); }
                }
                ST16((u32x4{r[0], tlo, r[1], thx}), hoff, 0);
                ST16((u32x4{r[0], tlo, r[1], thx}), hoff, 4 * STR);
                STAMP(5);
            }
            wg_barrier();
            while (__builtin_amdgcn_s_memrealtime() < t_start + LMD) __builtin_amdgcn_s_sleep(2);
            const int lg = x * 4 + (j & 3);
            dma_piece(p.w_mod + (size_t)wv * 64 * 3072 + 1024 + lg * 64, 6144, rgn);
            mod_mm(false);
            if (lg >= 8 && lg < 24) {
                if (wv == 0) {
                    const int b = lane >> 3, c8 = lane & 7;
                    const f32x4 v0 = sum8(L_MS + b * 256 + (((2 * c8) ^ b) << 4)), v1 = sum8(L_MS + b * 256 + (((2 * c8 + 1) ^ b) << 4));
                    ST16((u32x4{pk2(v0[0], v0[1]), pk2(v0[2], v0[3]), pk2(v1[0], v1[1]), pk2(v1[2], v1[3])}), (unsigned)(WS_MB + b * STR + (lg - 8) * 128 + c8 * 16), 0);
                }
            } else if (wv < 4) {
                const int b = wv * 2 + (lane >> 5), cp = lane & 31;
                f32x2_t u[8];
#pragma unroll
                for (int w2 = 0; w2 < 8; ++w2) u[w2] = *LP(f32x2_t, L_MS + w2 * 2048 + b * 256 + (((cp >> 1) ^ b) << 4) + (cp & 1) * 8);
                const f32x2_t v = ((u[0] + u[1]) + (u[2] + u[3])) + ((u[4] + u[5]) + (u[6] + u[7]));
                ST16((u32x4{__float_as_uint(rbf(v[0])), __float_as_uint(rbf(v[1])), tlo, thi}), (unsigned)(WS_MOD + b * STR + (lg * 32 + cp) * 16), 0);
            }
            STAMP(6);
        }
        const int bq = (j >> 2) & 1, dh = j >> 3, src = th * 2 + dh, rb = bq * 4 + (src >> 4), rc = src & 15;
        wg_barrier();
        if (j == 0) {
            if (wv == 4 && lane < 5) pf4s((unsigned)((lane + 1) * 2097152 + 8064), ws, L_SCR + 31744);
            if (wv == 5 && lane < 2) { pf4s((unsigned)(lane * 2097152), p.w_uv, L_SCR + 31744); pf4s((unsigned)(lane * 2097152), p.w_o, L_SCR + 31744); }
            if (wv == 6 && lane < 1) { pf4s(0u, p.w1, L_SCR + 31744); pf4s(0u, p.w2, L_SCR + 31744); pf4s(0u, p.out, L_SCR + 31744); }
        }
        while (__builtin_amdgcn_s_memrealtime() < t_start + TWD - 110) __builtin_amdgcn_s_sleep(2);
        const unsigned npm = __builtin_amdgcn_readfirstlane(tagged_wait(WS_NM + x * STR)[0]), np = (npm >> x) & 1u;
        unsigned oo = (unsigned)(WS_OUTP + ((bq * 4 + (nl & 3)) * 16 + wv * 2 + (nl >> 3)) * 16384 + (src ^ wv) * 128 + ((nl >> 2) & 1) * 64 + g * 16) + ((~npm >> ((nl & 3) * 2 + (wv >> 2))) & 1u) * 8192u;
        const int rcol = rc * 32 + (lane & 7) * 4 + (lane >> 5) * 2;
        unsigned xdst = (unsigned)(WS_X1 + ((lane >> 3) & 3) * STR + (rb * 512 + rcol) * 2), xdst2 = xdst + 4 * STR + ((npm >> (4 + ((lane >> 3) & 3))) & 1u) * 8192u;
        xdst += ((npm >> ((lane >> 3) & 3)) & 1u) * 8192u;
        ST16(s4, oo, 0);
        if (wv == 7) { __builtin_amdgcn_raw_buffer_store_b32(SENT, rw, xdst, 0, 16); __builtin_amdgcn_raw_buffer_store_b32(SENT, rw, xdst2, 0, 16); }
        VMW(0);
        wg_barrier();
        if (wv == 0) flag_put(WS_FB, id);
        while (__builtin_amdgcn_s_memrealtime() < t_start + TWD) __builtin_amdgcn_s_sleep(2);
        asm volatile("s_barrier" ::: "memory");
        STAMP(7);
        if (wv == 0 && lane < 32) pf4((unsigned)((j * 32 + lane) * 8192 + x * 1024 + ((3u - ((unsigned)(size_t)p.w_uv >> 7)) & 7u) * 128), p.w_uv, L_SCR + 31744);
        if (bq) dma_piece(p.w_o + (size_t)(th * 128 + dh * 64) * 512 + wv * 64, 1024, rgn + 8192);
        dma_piece(p.w_uv + ((size_t)wv * 64 * NH + th) * 128 + dh * 64, 8192, rgn);
        if (!bq) dma_piece(p.w_o + (size_t)(th * 128 + dh * 64) * 512 + wv * 64, 1024, rgn + 8192);
        u32x4 fb, gm;
        unsigned xg;
        VMW(4);
        if (wv == 7)
            asm volatile("buffer_load_dwordx4 %0, %3, %5, 0 offen sc1\n\tbuffer_load_dwordx4 %1, %4, %5, 0 offen sc1\n\tglobal_load_dword %2, %6, %7" : "=&v"(fb), "=&v"(gm), "=&v"(xg)
                         : "v"((unsigned)(WS_FB + x * STR + lane * 16)), "v"((unsigned)(WS_MOD + rb * STR + rcol * 8)), "s"(rw), "v"((unsigned)(rb * 512 + rcol) * 2u), "s"(p.x) : "memory");
        VMW(0);
        asm volatile("" : "+v"(fb), "+v"(gm), "+v"(xg));
        STAMP(8);
        bf16x8 fuv[2][4], fwo[2][4];
        unsigned pfa[4];
#pragma unroll
        for (int t = 0; t < 4; ++t) { pfa[t] = rgn + frl + ((t ^ frs) << 5); PIN(pfa[t]); }
#pragma unroll
        for (int ks = 0; ks < 2; ++ks)
#pragma unroll
            for (int t = 0; t < 4; ++t) {
                const unsigned a = rgn + (unsigned)((g * 4 + (nl >> 2)) * 128 + (lane & 3) * 8) + ((t ^ (unsigned)((nl >> 3) | (g & 2))) << 5) + ks * 4096;
                fuv[ks][t] = tr_frag(a, a + 2048);
                fwo[ks][t] = tr_frag(pfa[t] + 8192 + ks * 4096, pfa[t] + 8192 + ks * 4096 + 512);
            }
#pragma unroll
        for (int ks = 0; ks < 2; ++ks) { PIN4(fuv[ks][0], fuv[ks][1], fuv[ks][2], fuv[ks][3]); PIN4(fwo[ks][0], fwo[ks][1], fwo[ks][2], fwo[ks][3]); }
        const unsigned L_WGT = L_SCR, L_MSK = L_SCR + 256, L_RED = L_SCR + 1024;
        unsigned psrc = (unsigned)(WS_PART + (bq * 4 * 32 + th) * 32768 + (wv >> 2) * 16384 + (wv & 3) * 2048 + ((lane >> 3) ^ (th & 7)) * 128) + np * 8192u + pcl, psrc1 = (psrc ^ 64u) + 1024;
        unsigned psrc2 = psrc ^ 8192u, psrc3 = psrc1 ^ 8192u;
        PIN(psrc2); PIN(psrc3); PIN(xdst2);
        unsigned rw2 = L_RED + wv * 1024 + (nl & 3) * 256 + g * 16 + (nl >> 2) * 64;
        unsigned wa0 = (nl & 3) == (g >> 1) ? L_WGT + g * 16 : L_WGT + 272, wa1 = (nl & 3) == 2 + (g >> 1) ? L_WGT + 64 + g * 16 : L_WGT + 272;
        unsigned cr = rgn + (nl & 3) * 128 + g * 16;
        PIN(cr);
        unsigned r2r = L_RED + lane * 16, cw = rgn + lane * 8;
        unsigned osrc = (unsigned)(WS_OUTP + (rb * 16 + rc) * 16384 + lane * 16) + (np ^ 1u) * 8192u;
        PIN(psrc); PIN(psrc1); PIN(rw2); PIN(wa0); PIN(wa1); PIN(r2r); PIN4(cw, oo, osrc, xdst);
        float mlm = 0.f, mll = 0.f;
        if (wv == 0) *LP(unsigned, L_WGT + 256 + lane * 4) = 0u;
        wg_barrier();
        const bool early = __all(flags_ok(fb) & (gm[2] == tlo) & (gm[3] == thi));
        u32x2 ga = {gm[0], gm[1]};
        PIN(ga);
        bf16x8 pa[2][4], wh[2];
        {
            u32x4 ml = {0u, 0u, ~tlo, ~thi};
            const unsigned mlsrc = (unsigned)(WS_ML + (bq * 4 + g) * STR + nl * 512 + th * 16) + ((npm >> (x & 6)) & 1u) * 8192u;
            unsigned long long done = 0ull;
            unsigned lm = L_MSK;
            PIN(lm);
#define PLD(bit, off, v, base) "s_bitcmp1_b64 %0, " #bit "\n\ts_cbranch_scc0 1f\n\ts_add_u32 m0, %1, " #off "\n\ts_nop 0\n\tglobal_load_lds_dwordx4 " v ", " base " sc1\n1:\n\t"
            auto send = [&](unsigned long long m) {
                asm volatile(PLD(0, 0, "%2", "%4") PLD(8, 1024, "%3", "%4") PLD(16, 2048, "%2", "%5") PLD(24, 3072, "%3", "%5")
                             PLD(32, 4096, "%8", "%6") PLD(40, 5120, "%9", "%6") PLD(48, 6144, "%8", "%7") PLD(56, 7168, "%9", "%7")
                             :: "s"(m), "s"(rgn), "v"(psrc), "v"(psrc1), "s"(ws), "s"(ws + 1048576), "s"(ws + 2097152), "s"(ws + 3145728), "v"(psrc2), "v"(psrc3) : "memory", "m0", "scc");
            };
            if (wv == 0) {
#pragma unroll 1
                for (unsigned spins = 0; spins < kSpinCap; ++spins) {
                    if ((ml[2] != tlo) | (ml[3] != thi)) ml = LD16(mlsrc, 0);
                    const unsigned long long m = __ballot((ml[2] == tlo) & (ml[3] == thi));
                    if (m == ~0ull) break;
                    unsigned long long f = m & (m >> 4);
                    f &= f >> 2; f &= f >> 1;
                    asm volatile("ds_write_b64 %0, %1" :: "v"(lm), "v"(f) : "memory");
                }
                asm volatile("ds_write_b64 %0, %1" :: "v"(lm), "v"(~0ull) : "memory");
            } else {
#pragma unroll 1
                for (unsigned spins = 0; spins < kSpinCap && __popcll(done) < TK; ++spins) {
                    unsigned long long t;
                    asm volatile("ds_read_b64 %0, %1\n\ts_waitcnt lgkmcnt(0)" : "=v"(t) : "v"(lm) : "memory");
                    const unsigned long long m = (((unsigned long long)(unsigned)__builtin_amdgcn_readfirstlane((unsigned)(t >> 32)) << 32) | (unsigned)__builtin_amdgcn_readfirstlane((unsigned)t)) & 0x0101010101010101ull & ~done;
                    if (m) { done |= m; send(m); }
                }
            }
            asm volatile("s_barrier" ::: "memory");
            send(~done);
            mlm = __uint_as_float(ml[0]); mll = __uint_as_float(ml[1]);
            STAMP(10);
            if (wv == 0) {
                float M = mlm;
                M = fmaxf(M, dppf<0xB1>(M)); M = fmaxf(M, dppf<0x4E>(M)); M = fmaxf(M, dppf<0x141>(M)); M = fmaxf(M, dppf<0x140>(M));
                const float e = __builtin_amdgcn_exp2f(mlm - M);
                *LP(unsigned short, L_WGT + lane * 2) = f2bf(e * __builtin_amdgcn_rcpf(sum16(e * mll)));
            }
            wg_barrier();
            wh[0] = *LP(bf16x8, wa0); wh[1] = *LP(bf16x8, wa1);
            __builtin_amdgcn_sched_barrier(0);
            VMW(0);
#pragma unroll
            for (int ks = 0; ks < 2; ++ks)
#pragma unroll
                for (int t = 0; t < 4; ++t) pa[ks][t] = tr_frag(pfa[t] + ks * 4096, pfa[t] + ks * 4096 + 512);
        }
        STAMP(11);
        {
            f32x4 d[4];
#pragma unroll
            for (int t = 0; t < 4; ++t) d[t] = MFMA16(pa[0][t], wh[0], (f32x4{0.f, 0.f, 0.f, 0.f}));
#pragma unroll
            for (int t = 0; t < 4; ++t) d[t] = MFMA16(pa[1][t], wh[1], d[t]);
            const u32x4 o0 = {pk2(d[0][0], d[0][1]), pk2(d[0][2], d[0][3]), pk2(d[1][0], d[1][1]), pk2(d[1][2], d[1][3])};
            const u32x4 o1 = {pk2(d[2][0], d[2][1]), pk2(d[2][2], d[2][3]), pk2(d[3][0], d[3][1]), pk2(d[3][2], d[3][3])};
            f32x4 y[4];
#pragma unroll
            for (int t = 0; t < 4; ++t) y[t] = MFMA16(fuv[0][t], BF8(o0), (f32x4{0.f, 0.f, 0.f, 0.f}));
#pragma unroll
            for (int t = 0; t < 4; ++t) y[t] = MFMA16(fuv[1][t], BF8(o1), y[t]);
            *LP(f32x4, rw2) = (nl & 8) ? ((nl & 4) ? y[3] : y[2]) : ((nl & 4) ? y[1] : y[0]);
        }
        wg_barrier();
        {
            f32x4 cs[8];
#pragma unroll
            for (int w2 = 0; w2 < 8; ++w2) cs[w2] = *LP(f32x4, r2r + w2 * 1024);
            const f32x4 c4 = ((cs[0] + cs[1]) + (cs[2] + cs[3])) + ((cs[4] + cs[5]) + (cs[6] + cs[7]));
            *LP(u32x2, cw) = u32x2{pk2(c4[0], c4[1]), pk2(c4[2], c4[3])};
            asm volatile("s_waitcnt lgkmcnt(0)" ::: "memory");
            const u32x4 cx0 = *LP(u32x4, cr), cx1 = *LP(u32x4, cr + 64);
            f32x4 o[4];
#pragma unroll
            for (int q = 0; q < 4; ++q) o[q] = MFMA16(fwo[0][q], BF8(cx0), (f32x4{0.f, 0.f, 0.f, 0.f}));
#pragma unroll
            for (int q = 0; q < 4; ++q) o[q] = MFMA16(fwo[1][q], BF8(cx1), o[q]);
            const f32x4 oa = (nl & 8) ? ((nl & 4) ? o[3] : o[2]) : ((nl & 4) ? o[1] : o[0]);
            const u32x4 sv = {nosent(__float_as_uint(oa[0])), nosent(__float_as_uint(oa[1])), nosent(__float_as_uint(oa[2])), nosent(__float_as_uint(oa[3]))};
            ST16(sv, oo, 0);
        }
        STAMP(12);
        wg_barrier();
        if (wv == 7) {
            if (!early) {
                flags_wait(WS_FB + x * STR + lane * 16);
                { const u32x4 v = tagged_wait(WS_MOD + rb * STR + rcol * 8); ga = u32x2{v[0], v[1]}; }
                PIN(ga);
            }
            __builtin_amdgcn_s_sleep(SLPR);
            u32x4 op[8];
            for (unsigned spins = 0;;) {
#pragma unroll
                for (int k = 0; k < 8; ++k) op[k] = LD16(osrc + (k & 3) * 1024, (k >> 2) * 4096);
                unsigned m = 0u;
#pragma unroll
                for (int k = 0; k < 8; ++k) m = mx4(op[k], m);
                if (__all(m != SENT)) break;
                if (++spins > kSpinCap) break;
                asm volatile("" ::: "memory");
            }
            STAMP(13);
            const f32x4 v = ((__builtin_bit_cast(f32x4, op[0]) + __builtin_bit_cast(f32x4, op[1])) + (__builtin_bit_cast(f32x4, op[2]) + __builtin_bit_cast(f32x4, op[3]))) +
                            ((__builtin_bit_cast(f32x4, op[4]) + __builtin_bit_cast(f32x4, op[5])) + (__builtin_bit_cast(f32x4, op[6]) + __builtin_bit_cast(f32x4, op[7])));
            float w[2];
#pragma unroll
            for (int q = 0; q < 2; ++q) {
                auto t = __builtin_amdgcn_permlane32_swap(__float_as_uint(v[q]), __float_as_uint(v[2 + q]), false, false);
                w[q] = __uint_as_float(t[0]) + __uint_as_float(t[1]);
                auto u = __builtin_amdgcn_permlane16_swap(__float_as_uint(w[q]), __float_as_uint(w[q]), false, false);
                w[q] = __uint_as_float(u[0]) + __uint_as_float(u[1]);
                w[q] += dppf<0x128>(w[q]);
            }
            unsigned t = pk2(w[0], w[1]);
            t = pk2(__uint_as_float(ga[0]) * lo16(t), __uint_as_float(ga[1]) * hi16(t));
            t = pk2s(lo16(xg) + lo16(t), hi16(xg) + hi16(t));
            __builtin_amdgcn_raw_buffer_store_b32(t, rw, xdst, 0, 16);
            __builtin_amdgcn_raw_buffer_store_b32(t, rw, xdst2, 0, 16);
            STAMP(14);
        }
        STAMP_FLUSH();
        return;
    }

    const int s = j - 16;
    const unsigned long long t_s = __builtin_amdgcn_s_memrealtime();
    {
        const unsigned ln = (unsigned)(s * 64 + lane), r0 = (unsigned)(s * 4 + (wv & 3));
        if (wv == 0) pf4((ln & 511u) * 6144u + (x * 2 + (ln >> 9)) * 128u, p.w_mod, L_SCR);
        pf4(((r0 >> 5) * 256 + (lane >> 3) * 32 + (r0 & 31)) * 8192u + x * 1024u + (lane & 7u) * 128u, wv < 4 ? p.w_uk : p.w_q, L_SCR);
    }
    const int ug = x * 7 + s;
    const unsigned vb = (unsigned)(g * 1024 + ((nl ^ (g << 2)) << 4)), lb = (unsigned)((wv >> 1) * 32768 + (wv & 1) * 4096);
    const char* ck = (const char*)p.ckv + ((size_t)x * SK + s * 256 + wv * 16) * 1024;
    const unsigned rot = (unsigned)s & 3u;
    const unsigned vbr = vb ^ (rot << 8);
    const char* ck2 = ck + 131072;
    auto ck_dma = [&](unsigned i) { dma16x(vbr, ((i & 3u) << 4) | (((i >> 2) & 3u) << 8) | ((i & 3u) << 12), (i >> 4) ? ck2 : ck, lb + ((i >> 2) & 3u) * 8192u + (i & 3u) * 1024u); };
    bf16x8 ka[16];
    s16x4 va[8][4];
    unsigned kad[4], vad[4], vad2[4];
    auto kq = [](unsigned u) { return (0x78u >> (2 * u)) & 3u; };
    auto ka_reads = [&](int cb) {
#pragma unroll
        for (int u = 0; u < 4; ++u) ka[cb * 4 + u] = *LP(bf16x8, kad[u] + cb * 8192);
    };
    auto va_reads = [&]() {
#pragma unroll
        for (int kk = 0; kk < 8; ++kk)
#pragma unroll
            for (int q = 0; q < 4; ++q) va[kk][q] = tr4((kk < 4 ? vad[q] : vad2[q]) + ((kk >> 1) & 1) * 32768 + (kk & 1) * 4096);
    };
    auto va_read1 = [&](int kk) {
#pragma unroll
        for (int q = 0; q < 4; ++q) va[kk][q] = tr4((kk < 4 ? vad[q] : vad2[q]) + ((kk >> 1) & 1) * 32768 + (kk & 1) * 4096);
    };
    auto va_pin = [&]() {
#pragma unroll
        for (int q = 0; q < 8; ++q) PIN4(va[q][0], va[q][1], va[q][2], va[q][3]);
    };
    u32x4 fa = {0u, 0u, 0u, 0u};
    const unsigned fao = (unsigned)(WS_FI + x * STR + (lane & 7) * 16);
    constexpr int LAST0 = GI + NPOST, LAST = LAST0 + NP2;
    static_assert(GI - WG >= 15 && GI < 20 && LAST >= 19 && LAST < 28, "");
    const unsigned qsrc = (unsigned)(WS_Q + (wv & 1) * 4 * STR + (x * 4 + (wv >> 1)) * 1024) + (g ^ rot) * 256 + ((nl ^ ((wv * 4) & 15)) << 4);
    auto q_dma = [&](int k) {
        unsigned t;
        asm volatile("s_mov_b32 m0, %3\n\tv_xor_b32 %0, %4, %1\n\tglobal_load_lds_dwordx4 %0, %2 sc1" : "=&v"(t) : "v"(qsrc), "s"(ws), "s"(L_SCR + (wv * 4 + k) * 1024), "i"(k * (STR + 16)) : "memory", "m0");
    };
#pragma unroll
    for (int i = 0; i <= GI; ++i) {
        ck_dma(i);
        if (i == 5 && wv == 0) cnt_load();
        if (i == 3) {
#pragma unroll
            for (int u = 0; u < 4; ++u) {
                kad[u] = tile_off(wv * 16 + kq(nl >> 2) * 4 + (nl & 3), 4 * u + g);
                vad[u] = tile_off(8 * (lane >> 5) + 4 * (u & 1) + (nl >> 2), wv * 8 + 4 * (u >> 1) + 2 * (g & 1) + ((lane & 3) >> 1)) + 8 * (lane & 1);
                vad2[u] = vad[u] + 65536;
                PIN(kad[u]); PIN(vad[u]); PIN(vad2[u]);
            }
        }
        if (i >= 4) VMW(4);
        if (i == 15 && wv == 0) {
            PIN(fa);
            tag_take();
            if (!__all(flags_ok(fa))) flags_wait(fao);
        }
        if ((i & 3) == 3 && i >= 7 && i <= 15) {
            const int cb = (i - 7) >> 2;
            if (cb > 0) { PIN4(ka[cb * 4 - 4], ka[cb * 4 - 3], ka[cb * 4 - 2], ka[cb * 4 - 1]); va_pin(); }
            wg_barrier();
            ka_reads(cb);
            if ((wv >> 1) == cb) va_reads();
            STAMP(16 + cb);
        }
        if (i == 10 && wv == 0) asm volatile("buffer_load_dwordx4 %0, %1, %2, 0 offen sc1" : "+v"(fa) : "v"(fao), "s"(rw) : "memory");
    }
    PIN4(ka[8], ka[9], ka[10], ka[11]); va_pin();
    const bool late = __builtin_amdgcn_s_memrealtime() >= t_s + TL;
    while (__builtin_amdgcn_s_memrealtime() < t_s + TG) __builtin_amdgcn_s_sleep(1);
    VMW(WG);
    q_dma(0); q_dma(1); q_dma(2); q_dma(3);
    asm volatile("s_barrier" ::: "memory");
    STAMP(19);
    if (late) {
#pragma unroll
        for (int i = GI + 1; i <= LAST0; ++i) ck_dma(i);
    }
    ka_reads(3);
    if ((wv >> 1) == 3) va_reads();
    if (late) VMW(NPOST); else VMW(0);
    const unsigned L_EXM = L_SCR - 1024, L_BIG = L_SCR + 512 + 128;
    unsigned npm = 0u;
    unsigned qad[4];
    {
        unsigned qck = L_SCR + wv * 4096 + lane * 16;
        u32x4 qb[4];
#pragma unroll
        for (int k = 0; k < 4; ++k) qb[k] = *LP(u32x4, qck + k * 1024);
#pragma unroll
        for (int k = 0; k < 4; ++k) { qad[k] = L_SCR + nl * 1024 + (((4 * k + g) ^ nl) << 4); PIN(qad[k]); }
        STAMP(20);
#pragma unroll 1
        for (unsigned spins = 0; spins < kSpinCap && !__all(mx4(qb[0], mx4(qb[1], mx4(qb[2], mx4(qb[3], 0u)))) != SENT); ++spins) {
            __builtin_amdgcn_s_sleep(SLPQ);
#pragma unroll
            for (int k = 0; k < 4; ++k)
                if (!__all(mx4(qb[k], 0u) != SENT)) q_dma(k);
            VMW(0);
#pragma unroll
            for (int k = 0; k < 4; ++k) qb[k] = *LP(u32x4, qck + k * 1024);
        }
        if (!late) {
#pragma unroll
            for (int i = GI + 1; i <= LAST0; ++i) ck_dma(i);
        }
#pragma unroll
        for (int i = LAST0 + 1; i <= LAST; ++i) ck_dma(i);
    }
    constexpr int N0 = LAST + 1 + SL0, NE = N0 + 6 < 31 ? N0 + 6 : 31;
    const unsigned kg = kq(g);
    unsigned exw = L_EXM + ((nl + (lane >> 5) * 16) * 8 + wv) * 4, exr = L_EXM + nl * 32;
    unsigned pwr = L_SCR + nl * 1024 + 768 + (((2 * wv + (kg >> 1)) ^ nl) << 4) + (kg & 1) * 8, esw = L_SCR + nl * 1024 + 512 + ((wv ^ (nl & 7)) << 4) + g * 4;
    unsigned prd = L_SCR + (lane & 31) * 1024 + 768 + (((lane >> 5) ^ nl) << 4);
    PIN4(exw, exr, pwr, esw); PIN(prd);
    PIN4(ka[12], ka[13], ka[14], ka[15]); va_pin();
    wg_barrier();
    STAMP(21);
    bf16x8 qr[8][2];
    auto q_ld = [&](int ks, int hg) { return *LP(bf16x8, qad[ks & 3] + (ks >> 2) * 256 + hg * 16384); };
    auto p_ld = [&](int kk) { return *LP(bf16x8, prd ^ (kk << 5)); };
    float ls0, ls1, m0, m1;
    f32x4 sa, sb;
    f32x16 o[2];
    unsigned sw[4], srd, p0, mlo, esr, fbo;
    u32x2 tg;
    u32x4 nq;
    auto probs = [&](float (&e)[8]) {
#pragma unroll
        for (int r = 0; r < 4; ++r) { e[r] = __builtin_amdgcn_exp2f(fmaf(sa[r], SCL, -m0)); e[4 + r] = __builtin_amdgcn_exp2f(fmaf(sb[r], SCL, -m1)); }
        *LP(u32x2, pwr) = u32x2{pk2(e[0], e[1]), pk2(e[2], e[3])};
        *LP(u32x2, pwr + 16384) = u32x2{pk2(e[4], e[5]), pk2(e[6], e[7])};
    };
    auto wave_max = [&]() {
        const float a = fmaxf(fmaxf(sa[0], sa[1]), fmaxf(sa[2], sa[3])), b = fmaxf(fmaxf(sb[0], sb[1]), fmaxf(sb[2], sb[3]));
        auto u = __builtin_amdgcn_permlane32_swap(__float_as_uint(a), __float_as_uint(b), false, false);
        const float c = fmax3(__uint_as_float(u[0]), __uint_as_float(u[1]), __uint_as_float(u[1]));
        auto v = __builtin_amdgcn_permlane16_swap(__float_as_uint(c), __float_as_uint(c), false, false);
        return fmax3(__uint_as_float(v[0]), __uint_as_float(v[1]), __uint_as_float(v[1]));
    };
    auto max8 = [&](unsigned a) {
        const f32x4 a0 = *LP(f32x4, a), a1 = *LP(f32x4, a + 16);
        return fmax3(fmax3(a0[0], a0[1], a0[2]), fmax3(a0[3], a1[0], a1[1]), fmax3(a1[2], a1[3], a1[3])) * SCL;
    };
    {
        bf16x8 qs[8][2];
#pragma unroll
        for (int ks = 0; ks < 8; ++ks) { qr[ks][0] = q_ld(ks + 8, 0); qr[ks][1] = q_ld(ks + 8, 1); }
        __builtin_amdgcn_sched_barrier(0);
#pragma unroll
        for (int i = LAST + 1; i < LAST + 1 + SL0; ++i) if (i < NE) ck_dma(i);
        sa = f32x4{0.f, 0.f, 0.f, 0.f}; sb = sa;
        f32x4 s2a = sa, s2b = sa;
        bf16x8 kb[4];
#pragma unroll
        for (int n = 0; n < 16; ++n) {
            const int ks = (n + 8) & 15;
            sa = MFMA16(ka[ks], ks < 8 ? qs[ks][0] : qr[ks - 8][0], sa);
            sb = MFMA16(ka[ks], ks < 8 ? qs[ks][1] : qr[ks - 8][1], sb);
            if (n == 4) { VMW(LAST + SL0 + 1 - 19); for (int u = 0; u < 4; ++u) kb[u] = *LP(bf16x8, kad[u]); }
            if (ks < 4) { s2a = MFMA16(kb[ks], qs[ks][0], s2a); s2b = MFMA16(kb[ks], qs[ks][1], s2b); }
            if (n >= 8 - QD && n < 16 - QD) { qs[n + QD - 8][0] = q_ld(n + QD - 8, 0); qs[n + QD - 8][1] = q_ld(n + QD - 8, 1); }
            if ((n & 3) == 3 && LAST + 1 + SL0 + (n >> 2) < NE) ck_dma(LAST + 1 + SL0 + (n >> 2));
            __builtin_amdgcn_sched_barrier(0);
        }
        *LP(float, exw) = wave_max();
        if (N0 + 4 < NE) ck_dma(N0 + 4);
        wg_barrier();
        m0 = max8(exr); m1 = max8(exr + 512);
#pragma unroll
        for (int ks = 4; ks < 8; ++ks) { qs[ks][0] = q_ld(ks, 0); qs[ks][1] = q_ld(ks, 1); }
        __builtin_amdgcn_sched_barrier(0);
        float e[8];
        probs(e);
        ls0 = (e[0] + e[1]) + (e[2] + e[3]); ls1 = (e[4] + e[5]) + (e[6] + e[7]);
        PIN(ls0); PIN(ls1);
        if (wv == 0) *LP(u32x4, L_BIG) = u32x4{0u, 0u, tlo, thi};
        if (N0 + 5 < NE) ck_dma(N0 + 5);
        VMW(NE - 24);
        ka_reads(1);
        asm volatile("s_waitcnt lgkmcnt(4)\n\ts_barrier" ::: "memory");
        STAMP(22);
        static_assert(NE >= 28, "");
        bf16x8 pf[8];
        tg = *LP(u32x2, L_BIG + 8);
        pf[0] = p_ld(0); pf[1] = p_ld(1);
        ck_dma(NE);
        sa = s2a; sb = s2b;
        __builtin_amdgcn_sched_barrier(0);
#pragma unroll
        for (int ks = 4; ks < 8; ++ks) { sa = MFMA16(ka[ks], qs[ks][0], sa); sb = MFMA16(ka[ks], qs[ks][1], sb); }
        __builtin_amdgcn_sched_barrier(0);
#pragma unroll
        for (int i = 0; i < 16; ++i) { o[0][i] = 0.f; o[1][i] = 0.f; }
#pragma unroll
        for (int kk = 0; kk < 8; ++kk) {
            if (kk == 0) {
#pragma unroll
                for (int q = 2; q < 8; ++q) pf[q] = p_ld(q);
            }
            if (kk == 2) { PIN4(pf[2], pf[3], pf[4], pf[5]); PIN(pf[6]); PIN(pf[7]); PIN(tg); }
            if (kk >= 2 && wv < 4) { va_read1(kk - 2); if (kk == 7) va_read1(6); }
            o[0] = MFMA32(cat8(va[kk][0], va[kk][1]), pf[kk], o[0]);
            o[1] = MFMA32(cat8(va[kk][2], va[kk][3]), pf[kk], o[1]);
            if ((kk & 1) && NE + 1 + (kk >> 1) < 32) ck_dma(NE + 1 + (kk >> 1));
            __builtin_amdgcn_sched_barrier(0);
        }
        if (wv < 4) va_read1(7);
        VMW(4);
        ka_reads(2);
        tlo = __builtin_amdgcn_readfirstlane(tg[0]); thi = __builtin_amdgcn_readfirstlane(tg[1]);
        rederive();
        fbo = (unsigned)(WS_FB + x * STR + lane * 16);
        asm volatile("buffer_load_dwordx4 %0, off, %1, %2 sc1" : "=v"(nq) : "s"(rw), "s"(WS_NM + x * STR) : "memory");
        const unsigned head = lane & 31, r8 = lane >> 3, cw = wv ^ (rot * 2);
        srd = lb + r8 * 128 + (((lane & 7) ^ r8) << 4);
        sw[0] = lb + head * 128 + (((lane >> 5) ^ (head & 7)) << 4);
        sw[1] = sw[0] ^ 32u; sw[2] = sw[0] ^ 64u; sw[3] = sw[0] ^ 96u;
        p0 = (unsigned)(WS_PART + x * 1048576) + (cw >> 2) * 16384 + (cw & 3) * 2048 + r8 * 32768 + ((s ^ r8) * 8 + (lane & 7)) * 16;
        PIN4(sw[0], sw[1], sw[2], sw[3]); PIN(srd); PIN(p0); PIN(fbo);
#pragma unroll
        for (int ks = 8; ks < 12; ++ks) { sa = MFMA16(ka[ks], qr[ks - 8][0], sa); sb = MFMA16(ka[ks], qr[ks - 8][1], sb); }
    }
    STAMP(23);
    unsigned po[4];
    float mrun;
    {
        VMW(1);
        STAMP(24);
        ka_reads(3);
        asm volatile("" : "=v"(fa));
        if (wv == 1 && s < 7) ST16(s4, (unsigned)(WS_U + x * STR + (lane >> 5) * 8192 + s * 512 + (lane & 31) * 16), 0);
        if (wv == 0 && s < 11) asm volatile("buffer_load_dwordx4 %0, %1, %2, 0 offen sc1" : "+v"(fa) : "v"(fbo), "s"(rw) : "memory");
        __builtin_amdgcn_sched_barrier(0);
#pragma unroll
        for (int ks = 12; ks < 16; ++ks) { sa = MFMA16(ka[ks], qr[ks - 8][0], sa); sb = MFMA16(ka[ks], qr[ks - 8][1], sb); }
        bf16x8 pf[8];
#pragma unroll 1
        for (bool first = true;; first = false) {
            float e[8];
            probs(e);
            const float n0 = (e[0] + e[1]) + (e[2] + e[3]), n1 = (e[4] + e[5]) + (e[6] + e[7]);
            *LP(float, esw) = ls0 + n0; *LP(float, esw + 16384) = ls1 + n1;
            if (__builtin_expect(!(n0 + n1 <= BIGV), 0)) *LP(unsigned, L_BIG) = 1u;
            va_pin();
            wg_barrier();
            STAMP(25);
            PIN(prd);
            const unsigned big = *LP(unsigned, L_BIG);
            if (wv >= 4) va_reads();
#pragma unroll
            for (int kk = 0; kk < 8; ++kk) { pf[kk] = p_ld(kk); __builtin_amdgcn_sched_barrier(0); }
            if (__builtin_expect(__builtin_amdgcn_readfirstlane(big) == 0 || !first, 1)) break;
            const unsigned ex2 = L_SCR + 512 + 160;
            PIN(sa); PIN(sb);
            rederive();
            *LP(float, ex2 + (nl + (lane >> 5) * 16) * 1024 + wv * 4) = wave_max();
            wg_barrier();
            const float x0 = fmaxf(max8(ex2 + nl * 1024), m0), x1 = fmaxf(max8(ex2 + nl * 1024 + 16384), m1);
            const float al0 = __builtin_amdgcn_exp2f(m0 - x0), al1 = __builtin_amdgcn_exp2f(m1 - x1), al = (lane & 16) ? al1 : al0;
            m0 = x0; m1 = x1; ls0 *= al0; ls1 *= al1;
            o[0] *= al; o[1] *= al;
        }
        mrun = (lane & 16) ? m1 : m0;
        auto stage = [&](int t2, int gp) {
            const unsigned lo0 = pk2(o[t2][8 * gp + 0], o[t2][8 * gp + 1]), lo1 = pk2(o[t2][8 * gp + 2], o[t2][8 * gp + 3]);
            const unsigned hi0 = pk2(o[t2][8 * gp + 4], o[t2][8 * gp + 5]), hi1 = pk2(o[t2][8 * gp + 6], o[t2][8 * gp + 7]);
            auto r0 = __builtin_amdgcn_permlane32_swap(lo0, hi0, false, false);
            auto r1 = __builtin_amdgcn_permlane32_swap(lo1, hi1, false, false);
            *LP(u32x4, sw[t2 * 2 + gp]) = u32x4{r0[0], r1[0], r0[1], r1[1]};
        };
#pragma unroll
        for (int kk = 0; kk < 8; ++kk) {
            o[0] = MFMA32(cat8(va[kk][0], va[kk][1]), pf[kk], o[0]);
            if (kk == 1) {
                asm volatile("s_waitcnt vmcnt(0)" : "+v"(nq) :: "memory");
                npm = __builtin_amdgcn_readfirstlane(nq[0]);
                if ((unsigned)__builtin_amdgcn_readfirstlane(nq[2]) != tlo || (unsigned)__builtin_amdgcn_readfirstlane(nq[3]) != thi) npm = __builtin_amdgcn_readfirstlane(tagged_wait(WS_NM + x * STR)[0]);
            }
            if (kk == 3 || kk == 5) {
                const unsigned v = (npm ^ ((x & 2) ? 0xffu : 0u)) >> (lane >> 5);
#pragma unroll
                for (int i = kk - 3; i < kk - 1; ++i) { po[i] = p0 + ((v >> (2 * i)) & 1u) * 8192u; PIN(po[i]); }
            }
            __builtin_amdgcn_sched_barrier(0);
        }
#pragma unroll
        for (int kk = 0; kk < 8; ++kk) {
            o[1] = MFMA32(cat8(va[kk][2], va[kk][3]), pf[kk], o[1]);
            if (kk == 2) { PIN(o[0]); stage(0, 0); }
            if (kk == 4) stage(0, 1);
            __builtin_amdgcn_sched_barrier(0);
        }
        stage(1, 0); stage(1, 1);
        asm volatile("s_waitcnt lgkmcnt(0)" ::: "memory");
        u32x4 w[4];
#pragma unroll
        for (int i = 0; i < 4; ++i) w[i] = *LP(u32x4, srd + i * 1024);
#pragma unroll
        for (int i = 0; i < 4; ++i) ST16(w[i], po[i], i * 262144);
        __builtin_amdgcn_sched_barrier(0);
        STAMP(26);
        u32x4 mlrec;
        if (wv == 0) {
            const unsigned head = lane & 31;
            esr = L_SCR + head * 1024 + 512 + ((head & 7) << 4);
            mlo = (unsigned)(WS_ML + x * STR + s * 512 + head * 16) + ((npm >> ((head >> 3) * 2)) & 1u) * 8192u;
            PIN(mlo);
            f32x4 b8[8];
#pragma unroll
            for (int q = 0; q < 8; ++q) b8[q] = *LP(f32x4, esr ^ (q << 4));
            const f32x4 bs = ((b8[0] + b8[1]) + (b8[2] + b8[3])) + ((b8[4] + b8[5]) + (b8[6] + b8[7]));
            mlrec = u32x4{__float_as_uint(mrun), __float_as_uint((bs[0] + bs[1]) + (bs[2] + bs[3])), tlo, thi};
            PIN(mlrec);
        }
        VMW(0);
        wg_barrier();
        if (wv == 0) {
            if (lane < 32) ST16(mlrec, mlo, 0);
            flag_put(WS_FA, id - 128);
            PIN(fa);
            if (s < 11 && !__all(flags_ok(fa))) flags_wait(fbo);
        }
        STAMP(27);
        wg_barrier();
    }
    const unsigned long long t_att = __builtin_amdgcn_s_memrealtime();
    rederive();
    if (s >= 11) {
        const unsigned n = (unsigned)((s - 11) * 8 + wv);
        if (n < 28) pf4((n * 64 + lane) * 1024u + x * 128u, p.w2, 0);
        { const unsigned l2 = n * 64 + lane; pf4((l2 / 5u) * (DFF * 2) + min(((unsigned)x * 448u & ~127u) + (l2 % 5u) * 128u, DFF * 2 - 4u), p.w1, 0); }
        STAMP_FLUSH();
        return;
    }
    const unsigned L_H2S = L_SCR, L_REDM = L_SCR + 16384;
    if (s < 7) {
        {
            const char* w1s = (const char*)(p.w1 + (size_t)wv * 64 * DFF + ug * 32);
#pragma unroll
            for (int i = 0; i < 4; ++i) dma16((unsigned)((lane >> 2) * DFF * 2 + (lane & 3) * 16), w1s + (size_t)i * 16 * DFF * 2, wv * 4096 + i * 1024);
        }
        const int b = wv;
        const u32x4 shf = LD16((unsigned)(WS_MB + b * STR + lane * 16), 0), scl = LD16((unsigned)(WS_MB + b * STR + 1024 + lane * 16), 0);
        VMW(0);
        wg_barrier();
        bf16x8 wk0[8], wk1[8];
        if (wv >= 6) {
#pragma unroll
            for (int q = 0; q < 8; ++q) {
                const unsigned a = (unsigned)(q * 4096 + (g * 8 + (nl >> 2)) * 64 + (wv & 1) * 32 + (lane & 3) * 8);
                wk0[q] = tr_frag(a, a + 256); wk1[q] = tr_frag(a + 2048, a + 2304);
            }
#pragma unroll
            for (int q = 0; q < 8; q += 2) PIN4(wk0[q], wk0[q + 1], wk1[q], wk1[q + 1]);
        }
        float scv[8], shv[8];
#pragma unroll
        for (int q = 0; q < 4; ++q) {
            scv[2 * q] = rbf(1.f + lo16(scl[q])); scv[2 * q + 1] = rbf(1.f + hi16(scl[q])); shv[2 * q] = lo16(shf[q]); shv[2 * q + 1] = hi16(shf[q]);
            PIN4(scv[2 * q], scv[2 * q + 1], shv[2 * q], shv[2 * q + 1]);
        }
        unsigned har = L_H2S + ((lane & 7) * HS_LD + g * 8 + (nl >> 3) * 32) * 2, haw = L_H2S + (b * HS_LD + lane * 8) * 2;
        unsigned udst = (unsigned)(WS_U + x * STR + (g & 1) * 8192 + (s * 2 + (wv & 1)) * 256 + (nl & 7) * 32 + (g >> 1) * 16), xsrc = (unsigned)(WS_X1 + x * STR + (b * 512 + lane * 8) * 2) + ((npm >> x) & 1u) * 8192u;
        PIN4(har, haw, udst, xsrc);
        STAMP(28);
        while (__builtin_amdgcn_s_memrealtime() < t_att + XGATE) __builtin_amdgcn_s_sleep(2);
        u32x4 xv;
        for (unsigned spins = 0;;) {
            xv = LD16(xsrc, 0);
            if (__all(mx4(xv, 0u) != SENT)) break;
            __builtin_amdgcn_s_sleep(SLPX);
            if (++spins > kSpinCap) break;
            asm volatile("" ::: "memory");
        }
        STAMP(29);
        {
            f32x2_t v[4];
#pragma unroll
            for (int q = 0; q < 4; ++q) v[q] = f32x2_t{lo16(xv[q]), hi16(xv[q])};
            const f32x2_t p1 = (v[0] + v[1]) + (v[2] + v[3]), p2 = (v[0] * v[0] + v[1] * v[1]) + (v[2] * v[2] + v[3] * v[3]);
            float mb, rden;
            ln_stats(p1[0] + p1[1], p2[0] + p2[1], mb, rden);
            u32x4 hr;
#pragma unroll
            for (int q = 0; q < 4; ++q) {
                const f32x2_t h = ((v[q] - mb) * rden) * f32x2_t{scv[2 * q], scv[2 * q + 1]} + f32x2_t{shv[2 * q], shv[2 * q + 1]};
                hr[q] = pk2(h[0], h[1]);
            }
            *LP(u32x4, haw) = hr;
        }
        wg_barrier();
        if (wv >= 6) {
            bf16x8 af[8];
#pragma unroll
            for (int q = 0; q < 8; ++q) af[q] = *LP(bf16x8, har + q * 128);
            __builtin_amdgcn_sched_barrier(0);
            f32x4 d0 = {0.f, 0.f, 0.f, 0.f}, d1 = {0.f, 0.f, 0.f, 0.f};
#pragma unroll
            for (int q = 0; q < 8; ++q) { d0 = MFMA16(wk0[q], af[q], d0); d1 = MFMA16(wk1[q], af[q], d1); }
            const f32x4 own = nl < 8 ? d0 : d1;
#pragma unroll
            for (int e = 0; e < 4; ++e) { float v = own[e] + dppf<0x128>(own[e]); asm("" : "+v"(v)); d0[e] = fmaxf(v, 0.f); }
            const unsigned u0 = pk2s(d0[0], d0[1]), u1 = pk2s(d0[2], d0[3]);
            auto r0 = __builtin_amdgcn_permlane16_swap(u0, u0, false, false);
            auto r1 = __builtin_amdgcn_permlane16_swap(u1, u1, false, false);
            if (nl < 8) ST16((u32x4{r0[0], r1[0], r0[1], r1[1]}), udst, 0);
            STAMP(30);
        }
        STAMP_FLUSH();
        return;
    }
    {
        const int n0 = (x * 4 + (s - 7)) * 16;
        const char* mws = (const char*)(p.w2 + (size_t)wv * 224 * 512 + n0);
#pragma unroll
        for (int i = 0; i < 7; ++i) dma16((unsigned)((lane >> 1) * 1024 + (lane & 1) * 16), mws + i * 32768, rgn + i * 1024);
        const int fb = (lane >> 2) & 7, fc = (lane & 3) * 4 + (lane >> 5) * 2;
        u32x2 gb = {0u, 0u};
        if (wv == 0) {
            { const u32x4 v = tagged_wait(WS_MOD + fb * STR + (1536 + n0 + fc) * 8); gb = u32x2{v[0], v[1]}; }
            flags_wait(WS_FA + x * STR + lane * 16);
            if (id == 23 * 8 && lane == 0) __builtin_amdgcn_raw_buffer_store_b64(u32x2{tlo, thi}, rw, WS_EPOCH, 0, 16);
            PIN(gb);
        }
        VMW(0);
        bf16x8 wfr[7];
        const unsigned trl = (unsigned)(g * 8 + (nl >> 2)) * 32u + (unsigned)(lane & 3) * 8u;
#pragma unroll
        for (int ks = 0; ks < 7; ++ks) { wfr[ks] = tr_frag(rgn + ks * 1024 + trl, rgn + ks * 1024 + trl + 128); PIN(wfr[ks]); }
        unsigned usrc = (unsigned)(WS_U + wv * STR + (g >> 1) * 256 + (nl & 7) * 32 + (g & 1) * 16) + ((npm >> x) & 1u) * 8192u, usrc3 = usrc + 3072;
        usrc += (nl >> 3) * 512;
        PIN(usrc3);
        unsigned rdw = L_REDM + (wv * 2 + (nl >> 3)) * 512 + (nl & 7) * 64 + g * 16, rdr = L_REDM + (lane >> 5) * 4096 + (lane & 31) * 16;
        unsigned short* outp = p.out + fb * 512 + n0 + fc;
        PIN(usrc); PIN(rdw); PIN(rdr); PIN(outp);
        STAMP(28);
        wg_barrier();
        while (__builtin_amdgcn_s_memrealtime() < t_att + XGATE) __builtin_amdgcn_s_sleep(2);
        const unsigned x1b = (unsigned)(WS_X1 + x * STR) + ((npm >> x) & 1u) * 8192u;
        for (unsigned spins = 0;;) {
            const u32x4 xv = LD16(x1b + wv * 1024 + lane * 16, 0);
            if (__all(mx4(xv, 0u) != SENT)) break;
            __builtin_amdgcn_s_sleep(SLPD);
            if (++spins > kSpinCap) break;
            asm volatile("" ::: "memory");
        }
        wg_barrier();
        unsigned xg = 0u;
        if (wv == 0) { xg = __builtin_amdgcn_raw_buffer_load_b32(rw, x1b + (fb * 512 + n0 + fc) * 2, 0, 16); PIN(xg); } else __builtin_amdgcn_s_sleep(5);
        __builtin_amdgcn_s_sleep(SLPG);
        u32x4 uf[4];
        for (unsigned spins = 0;;) {
#pragma unroll
            for (int q = 0; q < 4; ++q) uf[q] = LD16(q < 3 ? usrc + q * 1024 : usrc3, 0);
            if (__all(mx4(uf[0], mx4(uf[1], mx4(uf[2], mx4(uf[3], 0u)))) != SENT)) break;
            if (++spins > kSpinCap) break;
            asm volatile("" ::: "memory");
        }
        STAMP(29);
        f32x4 d = {0.f, 0.f, 0.f, 0.f}, d2 = {0.f, 0.f, 0.f, 0.f};
#pragma unroll
        for (int ks = 0; ks < 7; ++ks) {
            if (ks & 1) d2 = MFMA16(wfr[ks], BF8(uf[ks >> 1]), d2);
            else d = MFMA16(wfr[ks], BF8(uf[ks >> 1]), d);
        }
        *LP(f32x4, rdw) = nl < 8 ? d : d2;
        wg_barrier();
        if (wv == 0) {
            f32x4 c[8];
#pragma unroll
            for (int w2 = 0; w2 < 8; ++w2) c[w2] = *LP(f32x4, rdr + w2 * 512);
            const f32x4 s4v = ((c[0] + c[1]) + (c[2] + c[3])) + ((c[4] + c[5]) + (c[6] + c[7]));
            f32x2_t sm;
#pragma unroll
            for (int q = 0; q < 2; ++q) { auto t2 = __builtin_amdgcn_permlane32_swap(__float_as_uint(s4v[q]), __float_as_uint(s4v[2 + q]), false, false); sm[q] = __uint_as_float(t2[0]) + __uint_as_float(t2[1]); }
            unsigned t = pk2(sm[0], sm[1]);
            t = pk2(__uint_as_float(gb[0]) * lo16(t), __uint_as_float(gb[1]) * hi16(t));
            *(GAS unsigned*)outp = pk2(lo16(xg) + lo16(t), hi16(xg) + hi16(t));
            STAMP(31);
        }
        STAMP_FLUSH();
    }
}

extern "C" void kernel_launch(void* const* d_in, const int* in_sizes, int n_in,
                              void* d_out, int out_size, void* d_ws, size_t ws_size,
                              hipStream_t stream) {
    Params p;
    p.x = (const unsigned short*)d_in[0];
    p.ckv = (const unsigned short*)d_in[1];
    p.w_q = (const unsigned short*)d_in[2];
    p.w_uk = (const unsigned short*)d_in[3];
    p.w_uv = (const unsigned short*)d_in[4];
    p.w_o = (const unsigned short*)d_in[5];
    p.w1 = (const unsigned short*)d_in[7];
    p.w2 = (const unsigned short*)d_in[8];
    p.c = (const unsigned short*)d_in[9];
    p.w_mod = (const unsigned short*)d_in[10];
    p.ws = (char*)(((size_t)d_ws + 2097151) & ~(size_t)2097151);
    p.out = (unsigned short*)d_out;
    (void)hipFuncSetAttribute((const void*)k_main, hipFuncAttributeMaxDynamicSharedMemorySize, 163840);
    k_main<<<256, 512, 163840, stream>>>(p);
}
```

```cpp
#include <hip/hip_runtime.h>
#include <hip/hip_bf16.h>
#include <stdint.h>

#define NH 32
#define SK 4096
#define DFF 1792
#define GI 18
#define WG 1
#define NPOST 3
#define NP2 1
#define SL0 1
#define HP 13
#define QD 4
#define TK 5
#define NCAL 3
#define TG 430
#define TL 480
#define LMD 250
#define TWD 570
#define XGATE 300
#define CPFE 36300
#define QHELP 4
#define SLPH 4
#define SLPQ 2
#define SLPR 10
#define SLPX 1
#define SLPD 2
#define SLPG 18

using u16x8 = __attribute__((ext_vector_type(8))) unsigned short;
using s16x4 = __attribute__((ext_vector_type(4))) short;
using bf16x8 = __attribute__((ext_vector_type(8))) short;
using f32x16 = __attribute__((ext_vector_type(16))) float;
using f32x4 = __attribute__((ext_vector_type(4))) float;
using u32x4 = __attribute__((ext_vector_type(4))) unsigned;
using u32x2 = __attribute__((ext_vector_type(2))) unsigned;

#define GAS __attribute__((address_space(1)))
#define LAS __attribute__((address_space(3)))
typedef GAS unsigned long long gu64;
#define RLX_AGENT __ATOMIC_RELAXED, __HIP_MEMORY_SCOPE_AGENT
#define SENT 0xffffffffu
#define TAGC 0x9e3779b9u

__device__ __forceinline__ float bf2f(unsigned short u) { return __uint_as_float(((unsigned)u) << 16); }
typedef __attribute__((ext_vector_type(2))) __bf16 bf16x2_t;
typedef __attribute__((ext_vector_type(2))) float f32x2_t;
__device__ __forceinline__ unsigned pk2(float a, float b) {
    f32x2_t v = {a, b};
    bf16x2_t r = __builtin_convertvector(v, bf16x2_t);
    return __builtin_bit_cast(unsigned, r);
}
__device__ __forceinline__ unsigned nosent(unsigned r) { return min(r, SENT - 1u); }
__device__ __forceinline__ unsigned pk2s(float a, float b) { return nosent(pk2(a, b)); }
__device__ __forceinline__ unsigned short f2bf(float f) { return (unsigned short)(pk2(f, 0.f) & 0xffffu); }
__device__ __forceinline__ float rbf(float f) { return __uint_as_float(pk2(f, 0.f) << 16); }
__device__ __forceinline__ float lo16(unsigned u) { return __uint_as_float(u << 16); }
__device__ __forceinline__ float hi16(unsigned u) { return __uint_as_float(u & 0xffff0000u); }
__device__ __forceinline__ unsigned umax3(unsigned a, unsigned b, unsigned c) { return max(max(a, b), c); }
__device__ __forceinline__ unsigned mx4(const u32x4& v, unsigned m) { return umax3(v[2], v[3], umax3(v[0], v[1], m)); }
__device__ __forceinline__ float fmax3(float a, float b, float c) { float r; asm("v_max3_f32 %0, %1, %2, %3" : "=v"(r) : "v"(a), "v"(b), "v"(c)); return r; }
template <int C> __device__ __forceinline__ float dppf(float v) { return __int_as_float(__builtin_amdgcn_update_dpp(0, __float_as_int(v), C, 0xf, 0xf, true)); }
__device__ __forceinline__ float sum16(float v) { v += dppf<0xB1>(v); v += dppf<0x4E>(v); v += dppf<0x141>(v); v += dppf<0x140>(v); return v; }

__device__ __forceinline__ void ln_stats(float p1, float p2, float& mb, float& rden) {
    auto t = __builtin_amdgcn_permlane32_swap(__float_as_uint(p1), __float_as_uint(p2), false, false);
    float a = sum16(__uint_as_float(t[0]) + __uint_as_float(t[1]));
    auto t2 = __builtin_amdgcn_permlane16_swap(__float_as_uint(a), __float_as_uint(a), false, false);
    a = (__uint_as_float(t2[0]) + __uint_as_float(t2[1])) * (1.f / 512.f);
    auto t3 = __builtin_amdgcn_permlane32_swap(__float_as_uint(a), __float_as_uint(a), false, false);
    const float m = __uint_as_float(t3[0]);
    const float var = rbf(fmaxf(fmaf(-m, m, __uint_as_float(t3[1])), 0.f));
    mb = rbf(m);
    rden = __builtin_amdgcn_rcpf(rbf(__builtin_amdgcn_sqrtf(rbf(var + 1e-5f))));
}
#define STR 262144
#define WS_Q 0
#define WS_H 32768
#define WS_X1 65536
#define WS_ML 81920
#define WS_MOD 98304
#define WS_MB 139264
#define WS_U 114688
#define WS_FI 131072
#define WS_FA 132096
#define WS_FB 133120
#define WS_NP 134144
#define WS_NM 134400
#define WS_EPOCH 135168
#define WS_CAL 147456
#define WS_OUTP 2097152
#define WS_PART 4194304
#define WS_END 12582912

#define LP(T, off) ((LAS T*)(size_t)(unsigned)(off))
#define STAMP(k) do {} while (0)
#define STAMP_FLUSH() do {} while (0)
#define STAMP0() do {} while (0)
__device__ __forceinline__ s16x4 tr4(unsigned a) { return __builtin_amdgcn_ds_read_tr16_b64_v4i16(LP(s16x4, a)); }
__device__ __forceinline__ bf16x8 cat8(s16x4 a, s16x4 b) { return __builtin_shufflevector(a, b, 0, 1, 2, 3, 4, 5, 6, 7); }
__device__ __forceinline__ bf16x8 tr_frag(unsigned a0, unsigned a1) { return cat8(tr4(a0), tr4(a1)); }
__device__ __forceinline__ unsigned swz(unsigned row) { return ((row & 3) << 2) | ((row >> 2) & 3); }
__device__ __forceinline__ unsigned off_b(unsigned row, unsigned ch) { return 256u * row + 16u * (ch ^ swz(row)); }
__device__ __forceinline__ unsigned tile_off(unsigned key, unsigned chunk) { return ((key >> 5) * 4 + (chunk >> 4)) * 8192u + off_b(key & 31, chunk & 15); }
__device__ __forceinline__ void wg_barrier() { asm volatile("s_waitcnt lgkmcnt(0)\n\ts_barrier" ::: "memory"); }
__device__ __forceinline__ void dma16(unsigned voff, const void* sbase, unsigned lds_dst) {
    asm volatile("s_mov_b32 m0, %2\n\ts_nop 0\n\tglobal_load_lds_dwordx4 %0, %1" :: "v"(voff), "s"(sbase), "s"(lds_dst) : "memory", "m0");
}
__device__ __forceinline__ void dma16x(unsigned voff, unsigned vx, const void* sbase, unsigned lds_dst) {
    unsigned t;
    asm volatile("s_mov_b32 m0, %3\n\tv_xor_b32 %0, %4, %1\n\tglobal_load_lds_dwordx4 %0, %2 nt" : "=&v"(t) : "v"(voff), "s"(sbase), "s"(lds_dst), "i"(vx) : "memory", "m0");
}
__device__ __forceinline__ void pf4(unsigned voff, const void* sbase, unsigned lds_dst) {
    asm volatile("s_mov_b32 m0, %2\n\ts_nop 0\n\tglobal_load_lds_dword %0, %1" :: "v"(voff), "s"(sbase), "s"(lds_dst) : "memory", "m0");
}
__device__ __forceinline__ void pf4s(unsigned voff, const void* sbase, unsigned lds_dst) {
    asm volatile("s_mov_b32 m0, %2\n\ts_nop 0\n\tglobal_load_lds_dword %0, %1 sc1" :: "v"(voff), "s"(sbase), "s"(lds_dst) : "memory", "m0");
}
#define SPINCAP (1u << 20)
constexpr unsigned kSpinCap = SPINCAP;
struct Params {
    const unsigned short* x;
    const unsigned short* ckv;
    const unsigned short* w_q;
    const unsigned short* w_uk;
    const unsigned short* w_uv;
    const unsigned short* w_o;
    const unsigned short* w1;
    const unsigned short* w2;
    const unsigned short* c;
    const unsigned short* w_mod;
    char* ws;
    unsigned short* out;
};
#define VMW(n) asm volatile("s_waitcnt vmcnt(%0)" :: "n"(n) : "memory")
#define PIN(x) asm volatile("" : "+v"(x))
#define PIN4(a, b, c, d) asm volatile("" : "+v"(a), "+v"(b), "+v"(c), "+v"(d) :: "memory")
#define MFMA16(a, b, c) __builtin_amdgcn_mfma_f32_16x16x32_bf16(a, b, c, 0, 0, 0)
#define MFMA32(a, b, c) __builtin_amdgcn_mfma_f32_32x32x16_bf16(a, b, c, 0, 0, 0)
#define BF8(x) __builtin_bit_cast(bf16x8, x)
#define ST16(v, off, soff) __builtin_amdgcn_raw_buffer_store_b128(v, rw, off, soff, 16)
#define LD16(off, soff) __builtin_amdgcn_raw_buffer_load_b128(rw, off, soff, 16)
#define SCL (0.08838834764831845f * 1.4426950408889634f)
#define BIGV 1.0e18f
#define L_SCR 131072
#define HS_LD 520

__global__ __launch_bounds__(512, 2) void k_main(Params p) {
    unsigned long long pc;
    asm volatile("s_getpc_b64 %0" : "=s"(pc));
    const int id = blockIdx.x, tid = threadIdx.x;
    int lane = tid & 63;
    const int wv = __builtin_amdgcn_readfirstlane(tid >> 6);
    int g = lane >> 4, nl = lane & 15;
    auto rederive = [&]() {
        asm volatile("v_mbcnt_lo_u32_b32 %0, -1, 0\n\tv_mbcnt_hi_u32_b32 %0, -1, %0" : "=v"(lane));
        g = lane >> 4; nl = lane & 15;
    };
    const int x = id & 7, j0 = id >> 3, j = j0 < 16 ? 15 - j0 : j0;
    if (wv < 5 && j == 15) {
        asm volatile("s_mov_b32 m0, %2\n\ts_nop 0\n\tglobal_load_lds_dword %0, %1" :: "v"(min((unsigned)((wv * 64 + lane) * 128), (unsigned)CPFE)), "s"(pc), "s"(L_SCR + 31744) : "memory", "m0");
    }
    char* ws = p.ws;
    if ((j >> 2) == 3) {
        dma16((unsigned)(((nl & 7) * 512 + (nl >> 3) * 32 + g * 8) * 2 + wv * 128), p.c, wv * 16384 + 8192);
        dma16((unsigned)tid * 16u, p.x, wv * 16384 + 9216);
    }
    if (j == 15 && wv == 5 && lane < 2) pf4s((unsigned)((x * 2 + lane) * 2097152), p.ckv, L_SCR + 31744);
    if (j == 14 && wv >= 4 && lane < 2) pf4s((unsigned)(lane * 2097152 + x * 128), wv == 4 ? p.w_mod : wv == 5 ? p.w_uk : wv == 6 ? p.w_q : (const unsigned short*)(ws + WS_CAL), L_SCR + 31744);
    STAMP0();
    const __amdgpu_buffer_rsrc_t rw = __builtin_amdgcn_make_buffer_rsrc(ws, 0, WS_END, 0x00020000);
    const u32x4 s4 = {SENT, SENT, SENT, SENT};
    unsigned long long cnt = 0ull;
    auto cnt_load = [&]() { asm volatile("global_load_dwordx2 %0, %1, %2 sc1" : "+v"(cnt) : "v"((unsigned)WS_EPOCH), "s"(ws) : "memory"); };
    unsigned tlo = 0u, thi = 0u;
    auto tag_take = [&]() {
        asm volatile("" : "+v"(cnt) :: "memory");
        const unsigned long long t = cnt + 1ull;
        tlo = __builtin_amdgcn_readfirstlane((unsigned)t); thi = __builtin_amdgcn_readfirstlane((unsigned)(t >> 32));
    };
    auto flag_put = [&](unsigned base, unsigned idx) {
        if (lane < 8) __builtin_amdgcn_raw_buffer_store_b64(u32x2{tlo, thi}, rw, base + lane * STR + idx * 8, 0, 16);
    };
    auto tagged_wait = [&](unsigned off) {
        u32x4 v;
#pragma unroll 1
        for (unsigned spins = 0; spins < kSpinCap; ++spins) {
            asm volatile("buffer_load_dwordx4 %0, %1, %2, 0 offen sc1\n\ts_waitcnt vmcnt(0)" : "=v"(v) : "v"(off), "s"(rw) : "memory");
            if (__all((v[2] == tlo) & (v[3] == thi))) break;
            __builtin_amdgcn_s_sleep(8);
        }
        return v;
    };
    auto flags_ok = [&](const u32x4& f) { return (f[0] == tlo) & (f[1] == thi) & (f[2] == tlo) & (f[3] == thi); };
    auto flags_wait = [&](unsigned off) {
        for (unsigned spins = 0;;) {
            u32x4 f;
            asm volatile("buffer_load_dwordx4 %0, %1, %2, 0 offen sc1\n\ts_waitcnt vmcnt(0)" : "=v"(f) : "v"(off), "s"(rw) : "memory");
            if (__all(flags_ok(f))) break;
            __builtin_amdgcn_s_sleep(16);
            if (++spins > kSpinCap) break;
            asm volatile("" ::: "memory");
        }
    };
    unsigned pcl, frl, frs;
    auto dma_piece = [&](const unsigned short* src, unsigned ldb, unsigned dst) {
        const unsigned v0 = (lane >> 3) * ldb + pcl, v1 = v0 ^ 64u;
#pragma unroll
        for (int i = 0; i < 8; ++i) dma16((i & 1) ? v1 : v0, (const char*)src + (size_t)i * 8 * ldb, dst + i * 1024);
    };
    const unsigned rgn = (unsigned)(wv * 16384);

    if (j < 16) {
        const unsigned long long t_start = __builtin_amdgcn_s_memrealtime();
        rederive();
        pcl = (unsigned)(((((lane & 7) >> 1) ^ ((lane >> 4) & 1)) * 32) + (lane & 1) * 16);
        frl = (unsigned)((g * 8 + (nl >> 2)) * 128 + (lane & 3) * 8); frs = (unsigned)((nl >> 3) | ((g & 1) << 1));
        const int th = x * 4 + (j & 3);
        if (j < 12) {
            const int qt = j >> 2, rot = qt * 5;
            const bool pub = wv < (qt < 2 ? 3 : 2);
            const unsigned qo = (unsigned)(WS_Q + (th & 7) * STR + ((nl & 7) * 4 + (th >> 3)) * 1024 + (qt * 192 + wv * 64 + g * 16 + (nl >> 3) * 8) * 2);
            const unsigned vb = (unsigned)(g * 8192 + ((nl ^ (g << 2)) << 4));
            const char* wuk = (const char*)(p.w_uk + th * 128) + (size_t)qt * 192 * 8192;
            const char* wq = (const char*)(p.w_q + th * 128) + (size_t)wv * 32768;
            const unsigned L_U = 114688;
            {
                const int k0 = qt < 2 ? wv * 6 : wv * 4;
#pragma unroll
                for (int i = 0; i < 6; ++i) {
                    if (i < 4 || qt < 2) dma16(vb ^ (((k0 + i) & 3) << 4), wuk + (size_t)(k0 + i) * 32768, L_U + (k0 + i) * 1024);
                    if (i == 4) VMW(4);
                }
            }
            const unsigned vq = vb ^ ((wv & 3) << 4);
            auto wq_dma = [&](int ps) { dma16(vq, wq + (size_t)((ps + rot) & 15) * 262144, ps * 8192 + wv * 1024); };
            bf16x8 uk[4][4], aq[16];
            u32x4 fi, hv[4];
            unsigned fio = (unsigned)(WS_FI + x * STR + (lane & 7) * 16), hsrc, hsrc2, ta0, ta1, tb0, tb1, qo2, hw0, hw1, hr2, qr0, qr1, qw, thx = 0u;
            const unsigned L_HI = L_SCR, L_QI = L_SCR + 8192;
            auto h_load = [&]() {
                asm volatile("buffer_load_dwordx4 %0, %4, %6, 0 offen sc1\n\tbuffer_load_dwordx4 %1, %4, %6, 0 offen offset:1024 sc1\n\t"
                             "buffer_load_dwordx4 %2, %5, %6, 0 offen sc1\n\tbuffer_load_dwordx4 %3, %5, %6, 0 offen offset:1024 sc1"
                             : "+v"(hv[0]), "+v"(hv[1]), "+v"(hv[2]), "+v"(hv[3]) : "v"(hsrc), "v"(hsrc2), "s"(rw) : "memory");
            };
            auto h_bad = [&]() {
                PIN4(hv[0], hv[1], hv[2], hv[3]);
                unsigned bad = (hv[0][1] ^ tlo) | (hv[0][3] ^ thx) | (hv[1][1] ^ tlo) | (hv[1][3] ^ thx) | (hv[2][1] ^ tlo) | (hv[2][3] ^ thx) | (hv[3][1] ^ tlo) | (hv[3][3] ^ thx);
                PIN(bad);
                return bad;
            };
#pragma unroll
            for (int ps = 0; ps < 16; ++ps) {
                wq_dma(ps);
                if (ps == 4) cnt_load();
                if (ps == HP) { h_load(); asm volatile("buffer_load_dwordx4 %0, %1, %2, 0 offen sc1" : "=v"(fi) : "v"(fio), "s"(rw) : "memory"); }
                if (ps >= 4 && ps < 8) VMW(5); else if (ps >= HP && ps < HP + 4) VMW(9); else VMW(4);
                if (ps == 3) {
                    asm volatile("s_barrier" ::: "memory");
                    if (pub) {
#pragma unroll
                        for (int t = 0; t < 4; ++t)
#pragma unroll
                            for (int s = 0; s < 4; ++s) uk[t][s] = *LP(bf16x8, L_U + off_b(wv * 64 + (nl >> 2) * 16 + t * 4 + (nl & 3), s * 4 + g));
                    }
                    STAMP(1);
                }
                if (ps == 5) {
                    hsrc = (unsigned)(WS_H + x * STR + wv * 2048 + lane * 16); hsrc2 = hsrc + 16384;
                    ta0 = off_b(8 * g + (nl >> 2), 2 * wv + ((lane & 3) >> 1)) + 8 * (lane & 1); ta1 = off_b(8 * g + 4 + (nl >> 2), 2 * wv + ((lane & 3) >> 1)) + 8 * (lane & 1);
                    tb0 = ta0 + 65536; tb1 = ta1 + 65536; qo2 = qo;
                    hw0 = L_HI + ((2 * wv - rot) & 15) * 512 + (lane >> 4) * 128 + (lane & 7) * 16 + ((lane >> 3) & 1) * 8;
                    hw1 = L_HI + ((2 * wv + 1 - rot) & 15) * 512 + (lane >> 4) * 128 + (lane & 7) * 16 + ((lane >> 3) & 1) * 8;
                    hr2 = L_HI + g * 128 + (nl & 7) * 16 + (nl >> 3) * 512;
                    PIN(hr2);
                    qr0 = L_QI + g * 128 + (nl & 7) * 16 + (nl >> 3) * 2048; qr1 = qr0 ^ 2048u;
                    PIN(qr0); PIN(qr1);
                    qw = L_QI + (wv >> 1) * 512 + ((wv & 1) * 2 + (g >> 1)) * 128 + (nl & 7) * 16 + (g & 1) * 8;
                    PIN4(hsrc, hsrc2, ta0, ta1); PIN4(tb0, tb1, qo2, hw0); PIN(hw1); PIN(qw);
                }
                if (ps == 8) { tag_take(); thx = tlo ^ thi ^ TAGC; }
                if (ps == 9) {
#pragma unroll
                    for (int t = 0; t < 4; ++t) PIN4(uk[t][0], uk[t][1], uk[t][2], uk[t][3]);
                    wg_barrier();
                    *LP(unsigned, L_QI + 2048 + wv * 256 + lane * 4) = 0u;
#pragma unroll
                    for (int q = 0; q < 6; ++q) aq[q] = tr_frag(ta0 + q * 8192, ta1 + q * 8192);
                }
                if (ps == 13) {
                    asm volatile("s_barrier" ::: "memory");
#pragma unroll
                    for (int q = 6; q < 10; ++q) aq[q] = q < 8 ? tr_frag(ta0 + q * 8192, ta1 + q * 8192) : tr_frag(tb0 + (q - 8) * 8192, tb1 + (q - 8) * 8192);
                }
            }
            STAMP(15);
            if (HP + 4 > 15) VMW(HP + 4 > 15 ? 15 - HP : 0);
            if (!__all(h_bad() == 0u)) {
                for (unsigned spins = 0;;) {
                    h_load();
                    VMW(0);
                    if (__all(h_bad() == 0u)) break;
                    __builtin_amdgcn_s_sleep(SLPH);
                    if (++spins > kSpinCap) break;
                }
            }
            {
                unsigned hh[4];
#pragma unroll
                for (int q = 0; q < 4; ++q) {
                    const unsigned t = hv[2 + (q >> 1)][(q & 1) * 2], sh = hv[q >> 1][(q & 1) * 2];
                    hh[q] = pk2(lo16(t) + lo16(sh), hi16(t) + hi16(sh));
                }
                *LP(u32x2, hw0) = u32x2{hh[0], hh[1]};
                *LP(u32x2, hw1) = u32x2{hh[2], hh[3]};
            }
            STAMP(3);
            asm volatile("s_waitcnt vmcnt(0) lgkmcnt(0)\n\ts_barrier" : "+v"(fi) :: "memory");
            STAMP(2);
            {
                bf16x8 hb[8];
#pragma unroll
                for (int q = 0; q < 5; ++q) hb[q] = *LP(bf16x8, hr2 + q * 1024);
#pragma unroll
                for (int ps = 10; ps < 16; ++ps) {
                    if (!(ps & 1)) hb[ps >> 1] = *LP(bf16x8, hr2 + (ps >> 1) * 1024);
                    aq[ps] = tr_frag(tb0 + (ps - 8) * 8192, tb1 + (ps - 8) * 8192);
                }
                __builtin_amdgcn_sched_barrier(0);
                f32x4 d0 = {0.f, 0.f, 0.f, 0.f}, d1 = {0.f, 0.f, 0.f, 0.f};
#pragma unroll
                for (int ps = 0; ps < 16; ps += 2) {
                    d0 = MFMA16(aq[ps], hb[ps >> 1], d0); d1 = MFMA16(aq[ps + 1], hb[ps >> 1], d1);
                    if (ps == 6) { unsigned bad = (fi[0] ^ tlo) | (fi[1] ^ thi) | (fi[2] ^ tlo) | (fi[3] ^ thi); PIN(bad); if (!__all(bad == 0u)) flags_wait(fio); }
                }
#pragma unroll
                for (int e = 0; e < 4; ++e) { float v = d0[e] + dppf<0x128>(d1[e]); asm("" : "+v"(v)); d0[e] = v; }
                if (nl < 8) *LP(u32x2, qw) = u32x2{pk2(d0[0], d0[1]), pk2(d0[2], d0[3])};
            }
            wg_barrier();
            if (pub) {
                bf16x8 qb[2][4];
#pragma unroll
                for (int s = 0; s < 4; ++s) { qb[0][s] = *LP(bf16x8, qr0 + s * 512); qb[1][s] = *LP(bf16x8, qr1 + s * 512); }
                __builtin_amdgcn_sched_barrier(0);
                f32x4 a = {0.f, 0.f, 0.f, 0.f}, b = a;
#pragma unroll
                for (int s = 0; s < 4; ++s) {
                    a = MFMA16(uk[0][s], qb[0][s], a); b = MFMA16(uk[1][s], qb[0][s], b);
                    a = MFMA16(uk[2][s], qb[1][s], a); b = MFMA16(uk[3][s], qb[1][s], b);
                }
                const u32x4 qv = {pk2s(a[0], a[1]), pk2s(a[2], a[3]), pk2s(b[0], b[1]), pk2s(b[2], b[3])};
                ST16(qv, qo2, 0);
            }
            STAMP(4);
            wg_barrier();
            if (j == 0 && wv == 3) {
                const int d = (int)tagged_wait(WS_NP + x * STR + (lane & 7) * 16)[0];
                int t = (lane & 4) ? -d : d;
                t += __builtin_amdgcn_update_dpp(0, t, 0xB1, 0xf, 0xf, true);
                t += __builtin_amdgcn_update_dpp(0, t, 0x4E, 0xf, 0xf, true);
                t += __builtin_amdgcn_update_dpp(0, t, 0x141, 0xf, 0xf, true);
                const bool n1 = abs(d) > 160 ? d < 0 : (t < 0) != ((lane & 4) != 0);
                const unsigned mk = (unsigned)__ballot(n1) & 0xffu;
                if (lane == 0) ST16((u32x4{mk, 0u, tlo, thi}), (unsigned)(WS_NM + x * STR), 0);
            }
            if (wv < 3 && j + 12 * wv < 32) {
                const int hh = j + 12 * wv;
#pragma unroll 1
                for (unsigned spins = 0; spins < 64; ++spins) {
                    __builtin_amdgcn_s_sleep(QHELP);
                    const u32x4 v = LD16((unsigned)(WS_Q + (hh & 7) * STR + (x * 4 + (hh >> 3)) * 1024 + lane * 16), 0);
                    if (__all(mx4(v, 0u) != SENT)) break;
                    asm volatile("" ::: "memory");
                }
                STAMP(6);
            }
        } else {
            const int cg = x * 2 + (j & 1);
            if (j < 14) {
                dma_piece(p.w_mod + (size_t)wv * 64 * 3072 + cg * 64, 6144, rgn);
                cnt_load();
                VMW(9);
            } else {
                cnt_load();
                if (j == 15) {
#pragma unroll
                    for (int q = 0; q < 4; ++q) ST16(s4, (unsigned)(WS_Q + wv * STR + x * 4096 + lane * 16), q * 1024);
                    VMW(0);
                    tag_take();
                    wg_barrier();
                    if (wv == 7 && lane < 8) ST16((u32x4{tlo, thi, tlo, thi}), (unsigned)(WS_FI + lane * STR + x * 16), 0);
                    STAMP(15);
                } else {
                    u32x2 best = {~0u, ~0u};
#pragma unroll
                    for (int r = 0; r < NCAL; ++r) {
                        const unsigned hf = (r ^ wv) & 1;
                        VMW(0);
                        const unsigned long long t0 = __builtin_amdgcn_s_memtime();
                        if (lane < 8) ST16(s4, WS_CAL + x * STR + hf * 8192 + (wv * 8 + r) * 128 + lane * 16, 0);
                        VMW(0);
                        const unsigned dt = (unsigned)(__builtin_amdgcn_s_memtime() - t0);
                        if (hf) best[1] = min(best[1], dt); else best[0] = min(best[0], dt);
                    }
                    tag_take();
                    *LP(u32x2, 16384 * 7 + 14336 + wv * 8) = best;
                    wg_barrier();
                    if (wv == 0 && lane < 8) {
                        u32x2 m = *LP(u32x2, 16384 * 7 + 14336 + lane * 8);
#pragma unroll
                        for (int o = 1; o < 8; o <<= 1) { m[0] = min(m[0], (unsigned)__shfl_xor(m[0], o)); m[1] = min(m[1], (unsigned)__shfl_xor(m[1], o)); }
                        ST16((u32x4{(unsigned)max(-1000, min(1000, (int)m[1] - (int)m[0])), 0u, tlo, thi}), (unsigned)(WS_NP + lane * STR + x * 16), 0);
                    }
                    STAMP(9);
                }
            }
            const u16x8 cf = *LP(u16x8, rgn + 8192 + lane * 16), xa = *LP(u16x8, rgn + 9216 + lane * 16);
            u32x4 af[2];
#pragma unroll
            for (int q = 0; q < 4; ++q) {
                const float a = bf2f(cf[2 * q]), b = bf2f(cf[2 * q + 1]);
                af[0][q] = pk2(a * __builtin_amdgcn_rcpf(1.f + __expf(-a)), b * __builtin_amdgcn_rcpf(1.f + __expf(-b)));
                af[1][q] = (unsigned)__builtin_amdgcn_update_dpp(0, (int)af[0][q], 0x128, 0xf, 0xf, true);
            }
            const unsigned L_MS = L_SCR;
            unsigned wa[4], fr[4];
#pragma unroll
            for (int t = 0; t < 4; ++t) { wa[t] = L_MS + wv * 2048 + (nl & 7) * 256 + (((t * 4 + g) ^ (nl & 7)) << 4) + (nl >> 3) * 16384; fr[t] = rgn + frl + ((t ^ frs) << 5); }
            PIN4(wa[0], wa[1], wa[2], wa[3]); PIN4(fr[0], fr[1], fr[2], fr[3]); PIN(af[0]); PIN(af[1]);
            const unsigned L_XN = 12288;
            auto mod_mm = [&](bool ln) {
                VMW(0);
                bf16x8 f[2][4];
#pragma unroll
                for (int ks = 0; ks < 2; ++ks)
#pragma unroll
                    for (int t = 0; t < 4; ++t) f[ks][t] = tr_frag(fr[t] + ks * 4096, fr[t] + ks * 4096 + 512);
                __builtin_amdgcn_sched_barrier(0);
                f32x4 d[4];
#pragma unroll
                for (int t = 0; t < 4; ++t) d[t] = MFMA16(f[0][t], BF8(af[0]), (f32x4{0.f, 0.f, 0.f, 0.f}));
#pragma unroll
                for (int t = 0; t < 4; ++t) d[t] = MFMA16(f[1][t], BF8(af[1]), d[t]);
#pragma unroll
                for (int t = 0; t < 4; ++t) *LP(f32x4, wa[t]) = d[t];
                if (ln) {
                    float v[8];
#pragma unroll
                    for (int q = 0; q < 8; ++q) v[q] = bf2f(xa[q]);
                    const float p1 = ((v[0] + v[1]) + (v[2] + v[3])) + ((v[4] + v[5]) + (v[6] + v[7]));
                    const float p2 = ((v[0] * v[0] + v[1] * v[1]) + (v[2] * v[2] + v[3] * v[3])) + ((v[4] * v[4] + v[5] * v[5]) + (v[6] * v[6] + v[7] * v[7]));
                    float mb, rden;
                    ln_stats(p1, p2, mb, rden);
                    u32x4 r;
#pragma unroll
                    for (int q = 0; q < 4; ++q) { const unsigned d = pk2(v[2 * q] - mb, v[2 * q + 1] - mb); r[q] = pk2(lo16(d) * rden, hi16(d) * rden); }
                    *LP(u32x4, L_XN + wv * 16384 + lane * 16) = r;
                }
                wg_barrier();
            };
            auto sum8 = [&](unsigned a) {
                f32x4 u[8];
#pragma unroll
                for (int w2 = 0; w2 < 8; ++w2) u[w2] = *LP(f32x4, a + w2 * 2048);
                __builtin_amdgcn_sched_barrier(0);
                return ((u[0] + u[1]) + (u[2] + u[3])) + ((u[4] + u[5]) + (u[6] + u[7]));
            };
            if (j < 14) {
                const int hb = lane & 7, c4 = 2 * wv + (nl >> 3);
                unsigned hoff = (unsigned)(WS_H + g * STR + cg * 2048 + wv * 256 + nl * 16);
                unsigned sa = L_MS + hb * 256 + ((c4 ^ hb) << 4);
                unsigned xa2 = L_XN + hb * 16384 + (cg & 7) * 128 + c4 * 8;
                PIN(hoff); PIN(sa); PIN(xa2);
                STAMP(1);
                mod_mm(cg >= 8);
                STAMP(2);
                const u32x2 xn = *LP(u32x2, xa2);
                const f32x4 v = sum8(sa);
                tag_take();
                const unsigned thx = tlo ^ thi ^ TAGC;
                u32x2 r = {pk2(v[0], v[1]), pk2(v[2], v[3])};
                if (cg >= 8) {
#pragma unroll
                    for (int q = 0; q < 2; ++q) { const unsigned sc = pk2(1.f + lo16(r[q]), 1.f + hi16(r[q])); r[q] = pk2(lo16(xn[q]) * lo16(sc), hi16(xn[q]) * hi16(sc)); }
                }
                ST16((u32x4{r[0], tlo, r[1], thx}), hoff, 0);
                ST16((u32x4{r[0], tlo, r[1], thx}), hoff, 4 * STR);
                STAMP(5);
            }
            wg_barrier();
            while (__builtin_amdgcn_s_memrealtime() < t_start + LMD) __builtin_amdgcn_s_sleep(2);
            const int lg = x * 4 + (j & 3);
            dma_piece(p.w_mod + (size_t)wv * 64 * 3072 + 1024 + lg * 64, 6144, rgn);
            mod_mm(false);
            if (lg >= 8 && lg < 24) {
                if (wv == 0) {
                    const int b = lane >> 3, c8 = lane & 7;
                    const f32x4 v0 = sum8(L_MS + b * 256 + (((2 * c8) ^ b) << 4)), v1 = sum8(L_MS + b * 256 + (((2 * c8 + 1) ^ b) << 4));
                    ST16((u32x4{pk2(v0[0], v0[1]), pk2(v0[2], v0[3]), pk2(v1[0], v1[1]), pk2(v1[2], v1[3])}), (unsigned)(WS_MB + b * STR + (lg - 8) * 128 + c8 * 16), 0);
                }
            } else if (wv < 4) {
                const int b = wv * 2 + (lane >> 5), cp = lane & 31;
                f32x2_t u[8];
#pragma unroll
                for (int w2 = 0; w2 < 8; ++w2) u[w2] = *LP(f32x2_t, L_MS + w2 * 2048 + b * 256 + (((cp >> 1) ^ b) << 4) + (cp & 1) * 8);
                const f32x2_t v = ((u[0] + u[1]) + (u[2] + u[3])) + ((u[4] + u[5]) + (u[6] + u[7]));
                ST16((u32x4{__float_as_uint(rbf(v[0])), __float_as_uint(rbf(v[1])), tlo, thi}), (unsigned)(WS_MOD + b * STR + (lg * 32 + cp) * 16), 0);
            }
            STAMP(6);
        }
        const int bq = (j >> 2) & 1, dh = j >> 3, src = th * 2 + dh, rb = bq * 4 + (src >> 4), rc = src & 15;
        wg_barrier();
        if (j == 0) {
            if (wv == 4 && lane < 5) pf4s((unsigned)((lane + 1) * 2097152 + 8064), ws, L_SCR + 31744);
            if (wv == 5 && lane < 2) { pf4s((unsigned)(lane * 2097152), p.w_uv, L_SCR + 31744); pf4s((unsigned)(lane * 2097152), p.w_o, L_SCR + 31744); }
            if (wv == 6 && lane < 1) { pf4s(0u, p.w1, L_SCR + 31744); pf4s(0u, p.w2, L_SCR + 31744); pf4s(0u, p.out, L_SCR + 31744); }
        }
        while (__builtin_amdgcn_s_memrealtime() < t_start + TWD - 110) __builtin_amdgcn_s_sleep(2);
        const unsigned npm = __builtin_amdgcn_readfirstlane(tagged_wait(WS_NM + x * STR)[0]), np = (npm >> x) & 1u;
        unsigned oo = (unsigned)(WS_OUTP + ((bq * 4 + (nl & 3)) * 16 + wv * 2 + (nl >> 3)) * 16384 + (src ^ wv) * 128 + ((nl >> 2) & 1) * 64 + g * 16) + ((~npm >> ((nl & 3) * 2 + (wv >> 2))) & 1u) * 8192u;
        const int rcol = rc * 32 + (lane & 7) * 4 + (lane >> 5) * 2;
        unsigned xdst = (unsigned)(WS_X1 + ((lane >> 3) & 3) * STR + (rb * 512 + rcol) * 2), xdst2 = xdst + 4 * STR + ((npm >> (4 + ((lane >> 3) & 3))) & 1u) * 8192u;
        xdst += ((npm >> ((lane >> 3) & 3)) & 1u) * 8192u;
        ST16(s4, oo, 0);
        if (wv == 7) { __builtin_amdgcn_raw_buffer_store_b32(SENT, rw, xdst, 0, 16); __builtin_amdgcn_raw_buffer_store_b32(SENT, rw, xdst2, 0, 16); }
        VMW(0);
        wg_barrier();
        if (wv == 0) flag_put(WS_FB, id);
        while (__builtin_amdgcn_s_memrealtime() < t_start + TWD) __builtin_amdgcn_s_sleep(2);
        asm volatile("s_barrier" ::: "memory");
        STAMP(7);
        if (wv == 0 && lane < 32) pf4((unsigned)((j * 32 + lane) * 8192 + x * 1024 + ((3u - ((unsigned)(size_t)p.w_uv >> 7)) & 7u) * 128), p.w_uv, L_SCR + 31744);
        if (bq) dma_piece(p.w_o + (size_t)(th * 128 + dh * 64) * 512 + wv * 64, 1024, rgn + 8192);
        dma_piece(p.w_uv + ((size_t)wv * 64 * NH + th) * 128 + dh * 64, 8192, rgn);
        if (!bq) dma_piece(p.w_o + (size_t)(th * 128 + dh * 64) * 512 + wv * 64, 1024, rgn + 8192);
        u32x4 fb, gm;
        unsigned xg;
        VMW(4);
        if (wv == 7)
            asm volatile("buffer_load_dwordx4 %0, %3, %5, 0 offen sc1\n\tbuffer_load_dwordx4 %1, %4, %5, 0 offen sc1\n\tglobal_load_dword %2, %6, %7" : "=&v"(fb), "=&v"(gm), "=&v"(xg)
                         : "v"((unsigned)(WS_FB + x * STR + lane * 16)), "v"((unsigned)(WS_MOD + rb * STR + rcol * 8)), "s"(rw), "v"((unsigned)(rb * 512 + rcol) * 2u), "s"(p.x) : "memory");
        VMW(0);
        asm volatile("" : "+v"(fb), "+v"(gm), "+v"(xg));
        STAMP(8);
        bf16x8 fuv[2][4], fwo[2][4];
        unsigned pfa[4];
#pragma unroll
        for (int t = 0; t < 4; ++t) { pfa[t] = rgn + frl + ((t ^ frs) << 5); PIN(pfa[t]); }
#pragma unroll
        for (int ks = 0; ks < 2; ++ks)
#pragma unroll
            for (int t = 0; t < 4; ++t) {
                const unsigned a = rgn + (unsigned)((g * 4 + (nl >> 2)) * 128 + (lane & 3) * 8) + ((t ^ (unsigned)((nl >> 3) | (g & 2))) << 5) + ks * 4096;
                fuv[ks][t] = tr_frag(a, a + 2048);
                fwo[ks][t] = tr_frag(pfa[t] + 8192 + ks * 4096, pfa[t] + 8192 + ks * 4096 + 512);
            }
#pragma unroll
        for (int ks = 0; ks < 2; ++ks) { PIN4(fuv[ks][0], fuv[ks][1], fuv[ks][2], fuv[ks][3]); PIN4(fwo[ks][0], fwo[ks][1], fwo[ks][2], fwo[ks][3]); }
        const unsigned L_WGT = L_SCR, L_MSK = L_SCR + 256, L_RED = L_SCR + 1024;
        unsigned psrc = (unsigned)(WS_PART + (bq * 4 * 32 + th) * 32768 + (wv >> 2) * 16384 + (wv & 3) * 2048 + ((lane >> 3) ^ (th & 7)) * 128) + np * 8192u + pcl, psrc1 = (psrc ^ 64u) + 1024;
        unsigned psrc2 = psrc ^ 8192u, psrc3 = psrc1 ^ 8192u;
        PIN(psrc2); PIN(psrc3); PIN(xdst2);
        unsigned rw2 = L_RED + wv * 1024 + (nl & 3) * 256 + g * 16 + (nl >> 2) * 64;
        unsigned wa0 = (nl & 3) == (g >> 1) ? L_WGT + g * 16 : L_WGT + 272, wa1 = (nl & 3) == 2 + (g >> 1) ? L_WGT + 64 + g * 16 : L_WGT + 272;
        unsigned cr = rgn + (nl & 3) * 128 + g * 16;
        PIN(cr);
        unsigned r2r = L_RED + lane * 16, cw = rgn + lane * 8;
        unsigned osrc = (unsigned)(WS_OUTP + (rb * 16 + rc) * 16384 + lane * 16) + (np ^ 1u) * 8192u;
        PIN(psrc); PIN(psrc1); PIN(rw2); PIN(wa0); PIN(wa1); PIN(r2r); PIN4(cw, oo, osrc, xdst);
        float mlm = 0.f, mll = 0.f;
        if (wv == 0) *LP(unsigned, L_WGT + 256 + lane * 4) = 0u;
        wg_barrier();
        const bool early = __all(flags_ok(fb) & (gm[2] == tlo) & (gm[3] == thi));
        u32x2 ga = {gm[0], gm[1]};
        PIN(ga);
        bf16x8 pa[2][4], wh[2];
        {
            u32x4 ml = {0u, 0u, ~tlo, ~thi};
            const unsigned mlsrc = (unsigned)(WS_ML + (bq * 4 + g) * STR + nl * 512 + th * 16) + ((npm >> (x & 6)) & 1u) * 8192u;
            unsigned long long done = 0ull;
            unsigned lm = L_MSK;
            PIN(lm);
#define PLD(bit, off, v, base) "s_bitcmp1_b64 %0, " #bit "\n\ts_cbranch_scc0 1f\n\ts_add_u32 m0, %1, " #off "\n\ts_nop 0\n\tglobal_load_lds_dwordx4 " v ", " base " sc1\n1:\n\t"
            auto send = [&](unsigned long long m) {
                asm volatile(PLD(0, 0, "%2", "%4") PLD(8, 1024, "%3", "%4") PLD(16, 2048, "%2", "%5") PLD(24, 3072, "%3", "%5")
                             PLD(32, 4096, "%8", "%6") PLD(40, 5120, "%9", "%6") PLD(48, 6144, "%8", "%7") PLD(56, 7168, "%9", "%7")
                             :: "s"(m), "s"(rgn), "v"(psrc), "v"(psrc1), "s"(ws), "s"(ws + 1048576), "s"(ws + 2097152), "s"(ws + 3145728), "v"(psrc2), "v"(psrc3) : "memory", "m0", "scc");
            };
            if (wv == 0) {
#pragma unroll 1
                for (unsigned spins = 0; spins < kSpinCap; ++spins) {
                    if ((ml[2] != tlo) | (ml[3] != thi)) ml = LD16(mlsrc, 0);
                    const unsigned long long m = __ballot((ml[2] == tlo) & (ml[3] == thi));
                    if (m == ~0ull) break;
                    unsigned long long f = m & (m >> 4);
                    f &= f >> 2; f &= f >> 1;
                    asm volatile("ds_write_b64 %0, %1" :: "v"(lm), "v"(f) : "memory");
                }
                asm volatile("ds_write_b64 %0, %1" :: "v"(lm), "v"(~0ull) : "memory");
            } else {
#pragma unroll 1
                for (unsigned spins = 0; spins < kSpinCap && __popcll(done) < TK; ++spins) {
                    unsigned long long t;
                    asm volatile("ds_read_b64 %0, %1\n\ts_waitcnt lgkmcnt(0)" : "=v"(t) : "v"(lm) : "memory");
                    const unsigned long long m = (((unsigned long long)(unsigned)__builtin_amdgcn_readfirstlane((unsigned)(t >> 32)) << 32) | (unsigned)__builtin_amdgcn_readfirstlane((unsigned)t)) & 0x0101010101010101ull & ~done;
                    if (m) { done |= m; send(m); }
                }
            }
            asm volatile("s_barrier" ::: "memory");
            send(~done);
            mlm = __uint_as_float(ml[0]); mll = __uint_as_float(ml[1]);
            STAMP(10);
            if (wv == 0) {
                float M = mlm;
                M = fmaxf(M, dppf<0xB1>(M)); M = fmaxf(M, dppf<0x4E>(M)); M = fmaxf(M, dppf<0x141>(M)); M = fmaxf(M, dppf<0x140>(M));
                const float e = __builtin_amdgcn_exp2f(mlm - M);
                *LP(unsigned short, L_WGT + lane * 2) = f2bf(e * __builtin_amdgcn_rcpf(sum16(e * mll)));
            }
            wg_barrier();
            wh[0] = *LP(bf16x8, wa0); wh[1] = *LP(bf16x8, wa1);
            __builtin_amdgcn_sched_barrier(0);
            VMW(0);
#pragma unroll
            for (int ks = 0; ks < 2; ++ks)
#pragma unroll
                for (int t = 0; t < 4; ++t) pa[ks][t] = tr_frag(pfa[t] + ks * 4096, pfa[t] + ks * 4096 + 512);
        }
        STAMP(11);
        {
            f32x4 d[4];
#pragma unroll
            for (int t = 0; t < 4; ++t) d[t] = MFMA16(pa[0][t], wh[0], (f32x4{0.f, 0.f, 0.f, 0.f}));
#pragma unroll
            for (int t = 0; t < 4; ++t) d[t] = MFMA16(pa[1][t], wh[1], d[t]);
            const u32x4 o0 = {pk2(d[0][0], d[0][1]), pk2(d[0][2], d[0][3]), pk2(d[1][0], d[1][1]), pk2(d[1][2], d[1][3])};
            const u32x4 o1 = {pk2(d[2][0], d[2][1]), pk2(d[2][2], d[2][3]), pk2(d[3][0], d[3][1]), pk2(d[3][2], d[3][3])};
            f32x4 y[4];
#pragma unroll
            for (int t = 0; t < 4; ++t) y[t] = MFMA16(fuv[0][t], BF8(o0), (f32x4{0.f, 0.f, 0.f, 0.f}));
#pragma unroll
            for (int t = 0; t < 4; ++t) y[t] = MFMA16(fuv[1][t], BF8(o1), y[t]);
            *LP(f32x4, rw2) = (nl & 8) ? ((nl & 4) ? y[3] : y[2]) : ((nl & 4) ? y[1] : y[0]);
        }
        wg_barrier();
        {
            f32x4 cs[8];
#pragma unroll
            for (int w2 = 0; w2 < 8; ++w2) cs[w2] = *LP(f32x4, r2r + w2 * 1024);
            const f32x4 c4 = ((cs[0] + cs[1]) + (cs[2] + cs[3])) + ((cs[4] + cs[5]) + (cs[6] + cs[7]));
            *LP(u32x2, cw) = u32x2{pk2(c4[0], c4[1]), pk2(c4[2], c4[3])};
            asm volatile("s_waitcnt lgkmcnt(0)" ::: "memory");
            const u32x4 cx0 = *LP(u32x4, cr), cx1 = *LP(u32x4, cr + 64);
            f32x4 o[4];
#pragma unroll
            for (int q = 0; q < 4; ++q) o[q] = MFMA16(fwo[0][q], BF8(cx0), (f32x4{0.f, 0.f, 0.f, 0.f}));
#pragma unroll
            for (int q = 0; q < 4; ++q) o[q] = MFMA16(fwo[1][q], BF8(cx1), o[q]);
            const f32x4 oa = (nl & 8) ? ((nl & 4) ? o[3] : o[2]) : ((nl & 4) ? o[1] : o[0]);
            const u32x4 sv = {nosent(__float_as_uint(oa[0])), nosent(__float_as_uint(oa[1])), nosent(__float_as_uint(oa[2])), nosent(__float_as_uint(oa[3]))};
            ST16(sv, oo, 0);
        }
        STAMP(12);
        wg_barrier();
        if (wv == 7) {
            if (!early) {
                flags_wait(WS_FB + x * STR + lane * 16);
                { const u32x4 v = tagged_wait(WS_MOD + rb * STR + rcol * 8); ga = u32x2{v[0], v[1]}; }
                PIN(ga);
            }
            __builtin_amdgcn_s_sleep(SLPR);
            u32x4 op[8];
            for (unsigned spins = 0;;) {
#pragma unroll
                for (int k = 0; k < 8; ++k) op[k] = LD16(osrc + (k & 3) * 1024, (k >> 2) * 4096);
                unsigned m = 0u;
#pragma unroll
                for (int k = 0; k < 8; ++k) m = mx4(op[k], m);
                if (__all(m != SENT)) break;
                if (++spins > kSpinCap) break;
                asm volatile("" ::: "memory");
            }
            STAMP(13);
            const f32x4 v = ((__builtin_bit_cast(f32x4, op[0]) + __builtin_bit_cast(f32x4, op[1])) + (__builtin_bit_cast(f32x4, op[2]) + __builtin_bit_cast(f32x4, op[3]))) +
                            ((__builtin_bit_cast(f32x4, op[4]) + __builtin_bit_cast(f32x4, op[5])) + (__builtin_bit_cast(f32x4, op[6]) + __builtin_bit_cast(f32x4, op[7])));
            float w[2];
#pragma unroll
            for (int q = 0; q < 2; ++q) {
                auto t = __builtin_amdgcn_permlane32_swap(__float_as_uint(v[q]), __float_as_uint(v[2 + q]), false, false);
                w[q] = __uint_as_float(t[0]) + __uint_as_float(t[1]);
                auto u = __builtin_amdgcn_permlane16_swap(__float_as_uint(w[q]), __float_as_uint(w[q]), false, false);
                w[q] = __uint_as_float(u[0]) + __uint_as_float(u[1]);
                w[q] += dppf<0x128>(w[q]);
            }
            unsigned t = pk2(w[0], w[1]);
            t = pk2(__uint_as_float(ga[0]) * lo16(t), __uint_as_float(ga[1]) * hi16(t));
            t = pk2s(lo16(xg) + lo16(t), hi16(xg) + hi16(t));
            __builtin_amdgcn_raw_buffer_store_b32(t, rw, xdst, 0, 16);
            __builtin_amdgcn_raw_buffer_store_b32(t, rw, xdst2, 0, 16);
            STAMP(14);
        }
        STAMP_FLUSH();
        return;
    }

    const int s = j - 16;
    const unsigned long long t_s = __builtin_amdgcn_s_memrealtime();
    {
        const unsigned ln = (unsigned)(s * 64 + lane), r0 = (unsigned)(s * 4 + (wv & 3));
        if (wv == 0) pf4((ln & 511u) * 6144u + (x * 2 + (ln >> 9)) * 128u, p.w_mod, L_SCR);
        pf4(((r0 >> 5) * 256 + (lane >> 3) * 32 + (r0 & 31)) * 8192u + x * 1024u + (lane & 7u) * 128u, wv < 4 ? p.w_uk : p.w_q, L_SCR);
    }
    const int ug = x * 7 + s;
    const unsigned vb = (unsigned)(g * 1024 + ((nl ^ (g << 2)) << 4)), lb = (unsigned)((wv >> 1) * 32768 + (wv & 1) * 4096);
    const char* ck = (const char*)p.ckv + ((size_t)x * SK + s * 256 + wv * 16) * 1024;
    const unsigned rot = (unsigned)s & 3u;
    const unsigned vbr = vb ^ (rot << 8);
    const char* ck2 = ck + 131072;
    auto ck_dma = [&](unsigned i) { dma16x(vbr, ((i & 3u) << 4) | (((i >> 2) & 3u) << 8) | ((i & 3u) << 12), (i >> 4) ? ck2 : ck, lb + ((i >> 2) & 3u) * 8192u + (i & 3u) * 1024u); };
    bf16x8 ka[16];
    s16x4 va[8][4];
    unsigned kad[4], vad[4], vad2[4];
    auto kq = [](unsigned u) { return (0x78u >> (2 * u)) & 3u; };
    auto ka_reads = [&](int cb) {
#pragma unroll
        for (int u = 0; u < 4; ++u) ka[cb * 4 + u] = *LP(bf16x8, kad[u] + cb * 8192);
    };
    auto va_reads = [&]() {
#pragma unroll
        for (int kk = 0; kk < 8; ++kk)
#pragma unroll
            for (int q = 0; q < 4; ++q) va[kk][q] = tr4((kk < 4 ? vad[q] : vad2[q]) + ((kk >> 1) & 1) * 32768 + (kk & 1) * 4096);
    };
    auto va_read1 = [&](int kk) {
#pragma unroll
        for (int q = 0; q < 4; ++q) va[kk][q] = tr4((kk < 4 ? vad[q] : vad2[q]) + ((kk >> 1) & 1) * 32768 + (kk & 1) * 4096);
    };
    auto va_pin = [&]() {
#pragma unroll
        for (int q = 0; q < 8; ++q) PIN4(va[q][0], va[q][1], va[q][2], va[q][3]);
    };
    u32x4 fa = {0u, 0u, 0u, 0u};
    const unsigned fao = (unsigned)(WS_FI + x * STR + (lane & 7) * 16);
    constexpr int LAST0 = GI + NPOST, LAST = LAST0 + NP2;
    static_assert(GI - WG >= 15 && GI < 20 && LAST >= 19 && LAST < 28, "");
    const unsigned qsrc = (unsigned)(WS_Q + (wv & 1) * 4 * STR + (x * 4 + (wv >> 1)) * 1024) + (g ^ rot) * 256 + ((nl ^ ((wv * 4) & 15)) << 4);
    auto q_dma = [&](int k) {
        unsigned t;
        asm volatile("s_mov_b32 m0, %3\n\tv_xor_b32 %0, %4, %1\n\tglobal_load_lds_dwordx4 %0, %2 sc1" : "=&v"(t) : "v"(qsrc), "s"(ws), "s"(L_SCR + (wv * 4 + k) * 1024), "i"(k * (STR + 16)) : "memory", "m0");
    };
#pragma unroll
    for (int i = 0; i <= GI; ++i) {
        ck_dma(i);
        if (i == 5 && wv == 0) cnt_load();
        if (i == 3) {
#pragma unroll
            for (int u = 0; u < 4; ++u) {
                kad[u] = tile_off(wv * 16 + kq(nl >> 2) * 4 + (nl & 3), 4 * u + g);
                vad[u] = tile_off(8 * (lane >> 5) + 4 * (u & 1) + (nl >> 2), wv * 8 + 4 * (u >> 1) + 2 * (g & 1) + ((lane & 3) >> 1)) + 8 * (lane & 1);
                vad2[u] = vad[u] + 65536;
                PIN(kad[u]); PIN(vad[u]); PIN(vad2[u]);
            }
        }
        if (i >= 4) VMW(4);
        if (i == 15 && wv == 0) {
            PIN(fa);
            tag_take();
            if (!__all(flags_ok(fa))) flags_wait(fao);
        }
        if ((i & 3) == 3 && i >= 7 && i <= 15) {
            const int cb = (i - 7) >> 2;
            if (cb > 0) { PIN4(ka[cb * 4 - 4], ka[cb * 4 - 3], ka[cb * 4 - 2], ka[cb * 4 - 1]); va_pin(); }
            wg_barrier();
            ka_reads(cb);
            if ((wv >> 1) == cb) va_reads();
            STAMP(16 + cb);
        }
        if (i == 10 && wv == 0) asm volatile("buffer_load_dwordx4 %0, %1, %2, 0 offen sc1" : "+v"(fa) : "v"(fao), "s"(rw) : "memory");
    }
    PIN4(ka[8], ka[9], ka[10], ka[11]); va_pin();
    const bool late = __builtin_amdgcn_s_memrealtime() >= t_s + TL;
    while (__builtin_amdgcn_s_memrealtime() < t_s + TG) __builtin_amdgcn_s_sleep(1);
    VMW(WG);
    q_dma(0); q_dma(1); q_dma(2); q_dma(3);
    asm volatile("s_barrier" ::: "memory");
    STAMP(19);
    if (late) {
#pragma unroll
        for (int i = GI + 1; i <= LAST0; ++i) ck_dma(i);
    }
    ka_reads(3);
    if ((wv >> 1) == 3) va_reads();
    if (late) VMW(NPOST); else VMW(0);
    const unsigned L_EXM = L_SCR - 1024, L_BIG = L_SCR + 512 + 128;
    unsigned npm = 0u;
    unsigned qad[4];
    {
        unsigned qck = L_SCR + wv * 4096 + lane * 16;
        u32x4 qb[4];
#pragma unroll
        for (int k = 0; k < 4; ++k) qb[k] = *LP(u32x4, qck + k * 1024);
#pragma unroll
        for (int k = 0; k < 4; ++k) { qad[k] = L_SCR + nl * 1024 + (((4 * k + g) ^ nl) << 4); PIN(qad[k]); }
        STAMP(20);
#pragma unroll 1
        for (unsigned spins = 0; spins < kSpinCap && !__all(mx4(qb[0], mx4(qb[1], mx4(qb[2], mx4(qb[3], 0u)))) != SENT); ++spins) {
            __builtin_amdgcn_s_sleep(SLPQ);
#pragma unroll
            for (int k = 0; k < 4; ++k)
                if (!__all(mx4(qb[k], 0u) != SENT)) q_dma(k);
            VMW(0);
#pragma unroll
            for (int k = 0; k < 4; ++k) qb[k] = *LP(u32x4, qck + k * 1024);
        }
        if (!late) {
#pragma unroll
            for (int i = GI + 1; i <= LAST0; ++i) ck_dma(i);
        }
#pragma unroll
        for (int i = LAST0 + 1; i <= LAST; ++i) ck_dma(i);
    }
    constexpr int N0 = LAST + 1 + SL0, NE = N0 + 6 < 31 ? N0 + 6 : 31;
    const unsigned kg = kq(g);
    unsigned exw = L_EXM + ((nl + (lane >> 5) * 16) * 8 + wv) * 4, exr = L_EXM + nl * 32;
    unsigned pwr = L_SCR + nl * 1024 + 768 + (((2 * wv + (kg >> 1)) ^ nl) << 4) + (kg & 1) * 8, esw = L_SCR + nl * 1024 + 512 + ((wv ^ (nl & 7)) << 4) + g * 4;
    unsigned prd = L_SCR + (lane & 31) * 1024 + 768 + (((lane >> 5) ^ nl) << 4);
    PIN4(exw, exr, pwr, esw); PIN(prd);
    PIN4(ka[12], ka[13], ka[14], ka[15]); va_pin();
    wg_barrier();
    STAMP(21);
    bf16x8 qr[8][2];
    auto q_ld = [&](int ks, int hg) { return *LP(bf16x8, qad[ks & 3] + (ks >> 2) * 256 + hg * 16384); };
    auto p_ld = [&](int kk) { return *LP(bf16x8, prd ^ (kk << 5)); };
    float ls0, ls1, m0, m1;
    f32x4 sa, sb;
    f32x16 o[2];
    unsigned sw[4], srd, p0, mlo, esr, fbo;
    u32x2 tg;
    u32x4 nq;
    auto probs = [&](float (&e)[8]) {
#pragma unroll
        for (int r = 0; r < 4; ++r) { e[r] = __builtin_amdgcn_exp2f(fmaf(sa[r], SCL, -m0)); e[4 + r] = __builtin_amdgcn_exp2f(fmaf(sb[r], SCL, -m1)); }
        *LP(u32x2, pwr) = u32x2{pk2(e[0], e[1]), pk2(e[2], e[3])};
        *LP(u32x2, pwr + 16384) = u32x2{pk2(e[4], e[5]), pk2(e[6], e[7])};
    };
    auto wave_max = [&]() {
        const float a = fmaxf(fmaxf(sa[0], sa[1]), fmaxf(sa[2], sa[3])), b = fmaxf(fmaxf(sb[0], sb[1]), fmaxf(sb[2], sb[3]));
        auto u = __builtin_amdgcn_permlane32_swap(__float_as_uint(a), __float_as_uint(b), false, false);
        const float c = fmax3(__uint_as_float(u[0]), __uint_as_float(u[1]), __uint_as_float(u[1]));
        auto v = __builtin_amdgcn_permlane16_swap(__float_as_uint(c), __float_as_uint(c), false, false);
        return fmax3(__uint_as_float(v[0]), __uint_as_float(v[1]), __uint_as_float(v[1]));
    };
    auto max8 = [&](unsigned a) {
        const f32x4 a0 = *LP(f32x4, a), a1 = *LP(f32x4, a + 16);
        return fmax3(fmax3(a0[0], a0[1], a0[2]), fmax3(a0[3], a1[0], a1[1]), fmax3(a1[2], a1[3], a1[3])) * SCL;
    };
    {
        bf16x8 qs[8][2];
#pragma unroll
        for (int ks = 0; ks < 8; ++ks) { qr[ks][0] = q_ld(ks + 8, 0); qr[ks][1] = q_ld(ks + 8, 1); }
        __builtin_amdgcn_sched_barrier(0);
#pragma unroll
        for (int i = LAST + 1; i < LAST + 1 + SL0; ++i) if (i < NE) ck_dma(i);
        sa = f32x4{0.f, 0.f, 0.f, 0.f}; sb = sa;
        f32x4 s2a = sa, s2b = sa;
        bf16x8 kb[4];
#pragma unroll
        for (int n = 0; n < 16; ++n) {
            const int ks = (n + 8) & 15;
            sa = MFMA16(ka[ks], ks < 8 ? qs[ks][0] : qr[ks - 8][0], sa);
            sb = MFMA16(ka[ks], ks < 8 ? qs[ks][1] : qr[ks - 8][1], sb);
            if (n == 4) { VMW(LAST + SL0 + 1 - 19); for (int u = 0; u < 4; ++u) kb[u] = *LP(bf16x8, kad[u]); }
            if (ks < 4) { s2a = MFMA16(kb[ks], qs[ks][0], s2a); s2b = MFMA16(kb[ks], qs[ks][1], s2b); }
            if (n >= 8 - QD && n < 16 - QD) { qs[n + QD - 8][0] = q_ld(n + QD - 8, 0); qs[n + QD - 8][1] = q_ld(n + QD - 8, 1); }
            if ((n & 3) == 3 && LAST + 1 + SL0 + (n >> 2) < NE) ck_dma(LAST + 1 + SL0 + (n >> 2));
            __builtin_amdgcn_sched_barrier(0);
        }
        *LP(float, exw) = wave_max();
        if (N0 + 4 < NE) ck_dma(N0 + 4);
        wg_barrier();
        m0 = max8(exr); m1 = max8(exr + 512);
#pragma unroll
        for (int ks = 4; ks < 8; ++ks) { qs[ks][0] = q_ld(ks, 0); qs[ks][1] = q_ld(ks, 1); }
        __builtin_amdgcn_sched_barrier(0);
        float e[8];
        probs(e);
        ls0 = (e[0] + e[1]) + (e[2] + e[3]); ls1 = (e[4] + e[5]) + (e[6] + e[7]);
        PIN(ls0); PIN(ls1);
        if (wv == 0) *LP(u32x4, L_BIG) = u32x4{0u, 0u, tlo, thi};
        if (N0 + 5 < NE) ck_dma(N0 + 5);
        VMW(NE - 24);
        ka_reads(1);
        asm volatile("s_waitcnt lgkmcnt(4)\n\ts_barrier" ::: "memory");
        STAMP(22);
        static_assert(NE >= 28, "");
        bf16x8 pf[8];
        tg = *LP(u32x2, L_BIG + 8);
        pf[0] = p_ld(0); pf[1] = p_ld(1);
        ck_dma(NE);
        sa = s2a; sb = s2b;
        __builtin_amdgcn_sched_barrier(0);
#pragma unroll
        for (int ks = 4; ks < 8; ++ks) { sa = MFMA16(ka[ks], qs[ks][0], sa); sb = MFMA16(ka[ks], qs[ks][1], sb); }
        __builtin_amdgcn_sched_barrier(0);
#pragma unroll
        for (int i = 0; i < 16; ++i) { o[0][i] = 0.f; o[1][i] = 0.f; }
#pragma unroll
        for (int kk = 0; kk < 8; ++kk) {
            if (kk == 0) {
#pragma unroll
                for (int q = 2; q < 8; ++q) pf[q] = p_ld(q);
            }
            if (kk == 2) { PIN4(pf[2], pf[3], pf[4], pf[5]); PIN(pf[6]); PIN(pf[7]); PIN(tg); }
            if (kk >= 2 && wv < 4) { va_read1(kk - 2); if (kk == 7) va_read1(6); }
            o[0] = MFMA32(cat8(va[kk][0], va[kk][1]), pf[kk], o[0]);
            o[1] = MFMA32(cat8(va[kk][2], va[kk][3]), pf[kk], o[1]);
            if ((kk & 1) && NE + 1 + (kk >> 1) < 32) ck_dma(NE + 1 + (kk >> 1));
            __builtin_amdgcn_sched_barrier(0);
        }
        if (wv < 4) va_read1(7);
        VMW(4);
        ka_reads(2);
        tlo = __builtin_amdgcn_readfirstlane(tg[0]); thi = __builtin_amdgcn_readfirstlane(tg[1]);
        rederive();
        fbo = (unsigned)(WS_FB + x * STR + lane * 16);
        asm volatile("buffer_load_dwordx4 %0, off, %1, %2 sc1" : "=v"(nq) : "s"(rw), "s"(WS_NM + x * STR) : "memory");
        const unsigned head = lane & 31, r8 = lane >> 3, cw = wv ^ (rot * 2);
        srd = lb + r8 * 128 + (((lane & 7) ^ r8) << 4);
        sw[0] = lb + head * 128 + (((lane >> 5) ^ (head & 7)) << 4);
        sw[1] = sw[0] ^ 32u; sw[2] = sw[0] ^ 64u; sw[3] = sw[0] ^ 96u;
        p0 = (unsigned)(WS_PART + x * 1048576) + (cw >> 2) * 16384 + (cw & 3) * 2048 + r8 * 32768 + ((s ^ r8) * 8 + (lane & 7)) * 16;
        PIN4(sw[0], sw[1], sw[2], sw[3]); PIN(srd); PIN(p0); PIN(fbo);
#pragma unroll
        for (int ks = 8; ks < 12; ++ks) { sa = MFMA16(ka[ks], qr[ks - 8][0], sa); sb = MFMA16(ka[ks], qr[ks - 8][1], sb); }
    }
    STAMP(23);
    unsigned po[4];
    float mrun;
    {
        VMW(1);
        STAMP(24);
        ka_reads(3);
        asm volatile("" : "=v"(fa));
        if (wv == 0 && s < 11) asm volatile("buffer_load_dwordx4 %0, %1, %2, 0 offen sc1" : "+v"(fa) : "v"(fbo), "s"(rw) : "memory");
        __builtin_amdgcn_sched_barrier(0);
#pragma unroll
        for (int ks = 12; ks < 16; ++ks) { sa = MFMA16(ka[ks], qr[ks - 8][0], sa); sb = MFMA16(ka[ks], qr[ks - 8][1], sb); }
        bf16x8 pf[8];
#pragma unroll 1
        for (bool first = true;; first = false) {
            float e[8];
            probs(e);
            const float n0 = (e[0] + e[1]) + (e[2] + e[3]), n1 = (e[4] + e[5]) + (e[6] + e[7]);
            *LP(float, esw) = ls0 + n0; *LP(float, esw + 16384) = ls1 + n1;
            if (__builtin_expect(!(n0 + n1 <= BIGV), 0)) *LP(unsigned, L_BIG) = 1u;
            va_pin();
            wg_barrier();
            STAMP(25);
            PIN(prd);
            const unsigned big = *LP(unsigned, L_BIG);
            if (wv >= 4) va_reads();
#pragma unroll
            for (int kk = 0; kk < 8; ++kk) { pf[kk] = p_ld(kk); __builtin_amdgcn_sched_barrier(0); }
            if (__builtin_expect(__builtin_amdgcn_readfirstlane(big) == 0 || !first, 1)) break;
            const unsigned ex2 = L_SCR + 512 + 160;
            PIN(sa); PIN(sb);
            rederive();
            *LP(float, ex2 + (nl + (lane >> 5) * 16) * 1024 + wv * 4) = wave_max();
            wg_barrier();
            const float x0 = fmaxf(max8(ex2 + nl * 1024), m0), x1 = fmaxf(max8(ex2 + nl * 1024 + 16384), m1);
            const float al0 = __builtin_amdgcn_exp2f(m0 - x0), al1 = __builtin_amdgcn_exp2f(m1 - x1), al = (lane & 16) ? al1 : al0;
            m0 = x0; m1 = x1; ls0 *= al0; ls1 *= al1;
            o[0] *= al; o[1] *= al;
        }
        mrun = (lane & 16) ? m1 : m0;
        auto stage = [&](int t2, int gp) {
            const unsigned lo0 = pk2(o[t2][8 * gp + 0], o[t2][8 * gp + 1]), lo1 = pk2(o[t2][8 * gp + 2], o[t2][8 * gp + 3]);
            const unsigned hi0 = pk2(o[t2][8 * gp + 4], o[t2][8 * gp + 5]), hi1 = pk2(o[t2][8 * gp + 6], o[t2][8 * gp + 7]);
            auto r0 = __builtin_amdgcn_permlane32_swap(lo0, hi0, false, false);
            auto r1 = __builtin_amdgcn_permlane32_swap(lo1, hi1, false, false);
            *LP(u32x4, sw[t2 * 2 + gp]) = u32x4{r0[0], r1[0], r0[1], r1[1]};
        };
#pragma unroll
        for (int kk = 0; kk < 8; ++kk) {
            o[0] = MFMA32(cat8(va[kk][0], va[kk][1]), pf[kk], o[0]);
            if (kk == 1) {
                asm volatile("s_waitcnt vmcnt(0)" : "+v"(nq) :: "memory");
                npm = __builtin_amdgcn_readfirstlane(nq[0]);
                if ((unsigned)__builtin_amdgcn_readfirstlane(nq[2]) != tlo || (unsigned)__builtin_amdgcn_readfirstlane(nq[3]) != thi) npm = __builtin_amdgcn_readfirstlane(tagged_wait(WS_NM + x * STR)[0]);
            }
            if (kk == 3 || kk == 5) {
                const unsigned v = (npm ^ ((x & 2) ? 0xffu : 0u)) >> (lane >> 5);
#pragma unroll
                for (int i = kk - 3; i < kk - 1; ++i) { po[i] = p0 + ((v >> (2 * i)) & 1u) * 8192u; PIN(po[i]); }
            }
            __builtin_amdgcn_sched_barrier(0);
        }
#pragma unroll
        for (int kk = 0; kk < 8; ++kk) {
            o[1] = MFMA32(cat8(va[kk][2], va[kk][3]), pf[kk], o[1]);
            if (kk == 2) { PIN(o[0]); stage(0, 0); }
            if (kk == 4) stage(0, 1);
            __builtin_amdgcn_sched_barrier(0);
        }
        stage(1, 0); stage(1, 1);
        asm volatile("s_waitcnt lgkmcnt(0)" ::: "memory");
        u32x4 w[4];
#pragma unroll
        for (int i = 0; i < 4; ++i) w[i] = *LP(u32x4, srd + i * 1024);
#pragma unroll
        for (int i = 0; i < 4; ++i) ST16(w[i], po[i], i * 262144);
        __builtin_amdgcn_sched_barrier(0);
        STAMP(26);
        u32x4 mlrec;
        if (wv == 0) {
            const unsigned head = lane & 31;
            esr = L_SCR + head * 1024 + 512 + ((head & 7) << 4);
            mlo = (unsigned)(WS_ML + x * STR + s * 512 + head * 16) + ((npm >> ((head >> 3) * 2)) & 1u) * 8192u;
            PIN(mlo);
            f32x4 b8[8];
#pragma unroll
            for (int q = 0; q < 8; ++q) b8[q] = *LP(f32x4, esr ^ (q << 4));
            const f32x4 bs = ((b8[0] + b8[1]) + (b8[2] + b8[3])) + ((b8[4] + b8[5]) + (b8[6] + b8[7]));
            mlrec = u32x4{__float_as_uint(mrun), __float_as_uint((bs[0] + bs[1]) + (bs[2] + bs[3])), tlo, thi};
            PIN(mlrec);
        }
        VMW(0);
        wg_barrier();
        if (wv == 0) {
            if (lane < 32) ST16(mlrec, mlo, 0);
            PIN(fa);
            if (s < 11 && !__all(flags_ok(fa))) flags_wait(fbo);
        }
        STAMP(27);
        wg_barrier();
    }
    const unsigned long long t_att = __builtin_amdgcn_s_memrealtime();
    rederive();
    if (s >= 11) {
        const unsigned n = (unsigned)((s - 11) * 8 + wv);
        if (n < 28) pf4((n * 64 + lane) * 1024u + x * 128u, p.w2, 0);
        { const unsigned l2 = n * 64 + lane; pf4((l2 / 5u) * (DFF * 2) + min(((unsigned)x * 448u & ~127u) + (l2 % 5u) * 128u, DFF * 2 - 4u), p.w1, 0); }
        if (wv == 0) flag_put(WS_FA, id - 128);
        STAMP_FLUSH();
        return;
    }
    const unsigned L_H2S = L_SCR, L_REDM = L_SCR + 16384;
    if (s < 7) {
        {
            const char* w1s = (const char*)(p.w1 + (size_t)wv * 64 * DFF + ug * 32);
#pragma unroll
            for (int i = 0; i < 4; ++i) dma16((unsigned)((lane >> 2) * DFF * 2 + (lane & 3) * 16), w1s + (size_t)i * 16 * DFF * 2, wv * 4096 + i * 1024);
        }
        const int b = wv;
        if (wv == 0) ST16(s4, (unsigned)(WS_U + x * STR + (lane >> 5) * 8192 + s * 512 + (lane & 31) * 16), 0);
        const u32x4 shf = LD16((unsigned)(WS_MB + b * STR + lane * 16), 0), scl = LD16((unsigned)(WS_MB + b * STR + 1024 + lane * 16), 0);
        VMW(0);
        if (wv == 0) flag_put(WS_FA, id - 128);
        wg_barrier();
        bf16x8 wk0[8], wk1[8];
        if (wv >= 6) {
#pragma unroll
            for (int q = 0; q < 8; ++q) {
                const unsigned a = (unsigned)(q * 4096 + (g * 8 + (nl >> 2)) * 64 + (wv & 1) * 32 + (lane & 3) * 8);
                wk0[q] = tr_frag(a, a + 256); wk1[q] = tr_frag(a + 2048, a + 2304);
            }
#pragma unroll
            for (int q = 0; q < 8; q += 2) PIN4(wk0[q], wk0[q + 1], wk1[q], wk1[q + 1]);
        }
        float scv[8], shv[8];
#pragma unroll
        for (int q = 0; q < 4; ++q) {
            scv[2 * q] = rbf(1.f + lo16(scl[q])); scv[2 * q + 1] = rbf(1.f + hi16(scl[q])); shv[2 * q] = lo16(shf[q]); shv[2 * q + 1] = hi16(shf[q]);
            PIN4(scv[2 * q], scv[2 * q + 1], shv[2 * q], shv[2 * q + 1]);
        }
        unsigned har = L_H2S + ((lane & 7) * HS_LD + g * 8 + (nl >> 3) * 32) * 2, haw = L_H2S + (b * HS_LD + lane * 8) * 2;
        unsigned udst = (unsigned)(WS_U + x * STR + (g & 1) * 8192 + (s * 2 + (wv & 1)) * 256 + (nl & 7) * 32 + (g >> 1) * 16), xsrc = (unsigned)(WS_X1 + x * STR + (b * 512 + lane * 8) * 2) + ((npm >> x) & 1u) * 8192u;
        PIN4(har, haw, udst, xsrc);
        STAMP(28);
        while (__builtin_amdgcn_s_memrealtime() < t_att + XGATE) __builtin_amdgcn_s_sleep(2);
        u32x4 xv;
        for (unsigned spins = 0;;) {
            xv = LD16(xsrc, 0);
            if (__all(mx4(xv, 0u) != SENT)) break;
            __builtin_amdgcn_s_sleep(SLPX);
            if (++spins > kSpinCap) break;
            asm volatile("" ::: "memory");
        }
        STAMP(29);
        {
            f32x2_t v[4];
#pragma unroll
            for (int q = 0; q < 4; ++q) v[q] = f32x2_t{lo16(xv[q]), hi16(xv[q])};
            const f32x2_t p1 = (v[0] + v[1]) + (v[2] + v[3]), p2 = (v[0] * v[0] + v[1] * v[1]) + (v[2] * v[2] + v[3] * v[3]);
            float mb, rden;
            ln_stats(p1[0] + p1[1], p2[0] + p2[1], mb, rden);
            u32x4 hr;
#pragma unroll
            for (int q = 0; q < 4; ++q) {
                const f32x2_t h = ((v[q] - mb) * rden) * f32x2_t{scv[2 * q], scv[2 * q + 1]} + f32x2_t{shv[2 * q], shv[2 * q + 1]};
                hr[q] = pk2(h[0], h[1]);
            }
            *LP(u32x4, haw) = hr;
        }
        wg_barrier();
        if (wv >= 6) {
            bf16x8 af[8];
#pragma unroll
            for (int q = 0; q < 8; ++q) af[q] = *LP(bf16x8, har + q * 128);
            __builtin_amdgcn_sched_barrier(0);
            f32x4 d0 = {0.f, 0.f, 0.f, 0.f}, d1 = {0.f, 0.f, 0.f, 0.f};
#pragma unroll
            for (int q = 0; q < 8; ++q) { d0 = MFMA16(wk0[q], af[q], d0); d1 = MFMA16(wk1[q], af[q], d1); }
            const f32x4 own = nl < 8 ? d0 : d1;
#pragma unroll
            for (int e = 0; e < 4; ++e) { float v = own[e] + dppf<0x128>(own[e]); asm("" : "+v"(v)); d0[e] = fmaxf(v, 0.f); }
            const unsigned u0 = pk2s(d0[0], d0[1]), u1 = pk2s(d0[2], d0[3]);
            auto r0 = __builtin_amdgcn_permlane16_swap(u0, u0, false, false);
            auto r1 = __builtin_amdgcn_permlane16_swap(u1, u1, false, false);
            if (nl < 8) ST16((u32x4{r0[0], r1[0], r0[1], r1[1]}), udst, 0);
            STAMP(30);
        }
        STAMP_FLUSH();
        return;
    }
    {
        const int n0 = (x * 4 + (s - 7)) * 16;
        const char* mws = (const char*)(p.w2 + (size_t)wv * 224 * 512 + n0);
#pragma unroll
        for (int i = 0; i < 7; ++i) dma16((unsigned)((lane >> 1) * 1024 + (lane & 1) * 16), mws + i * 32768, rgn + i * 1024);
        const int fb = (lane >> 2) & 7, fc = (lane & 3) * 4 + (lane >> 5) * 2;
        u32x2 gb = {0u, 0u};
        if (wv == 0) {
            flag_put(WS_FA, id - 128);
            { const u32x4 v = tagged_wait(WS_MOD + fb * STR + (1536 + n0 + fc) * 8); gb = u32x2{v[0], v[1]}; }
            flags_wait(WS_FA + x * STR + lane * 16);
            if (id == 23 * 8 && lane == 0) __builtin_amdgcn_raw_buffer_store_b64(u32x2{tlo, thi}, rw, WS_EPOCH, 0, 16);
            PIN(gb);
        }
        VMW(0);
        bf16x8 wfr[7];
        const unsigned trl = (unsigned)(g * 8 + (nl >> 2)) * 32u + (unsigned)(lane & 3) * 8u;
#pragma unroll
        for (int ks = 0; ks < 7; ++ks) { wfr[ks] = tr_frag(rgn + ks * 1024 + trl, rgn + ks * 1024 + trl + 128); PIN(wfr[ks]); }
        unsigned usrc = (unsigned)(WS_U + wv * STR + (g >> 1) * 256 + (nl & 7) * 32 + (g & 1) * 16) + ((npm >> x) & 1u) * 8192u, usrc3 = usrc + 3072;
        usrc += (nl >> 3) * 512;
        PIN(usrc3);
        unsigned rdw = L_REDM + (wv * 2 + (nl >> 3)) * 512 + (nl & 7) * 64 + g * 16, rdr = L_REDM + (lane >> 5) * 4096 + (lane & 31) * 16;
        unsigned short* outp = p.out + fb * 512 + n0 + fc;
        PIN(usrc); PIN(rdw); PIN(rdr); PIN(outp);
        STAMP(28);
        wg_barrier();
        while (__builtin_amdgcn_s_memrealtime() < t_att + XGATE) __builtin_amdgcn_s_sleep(2);
        const unsigned x1b = (unsigned)(WS_X1 + x * STR) + ((npm >> x) & 1u) * 8192u;
        for (unsigned spins = 0;;) {
            const u32x4 xv = LD16(x1b + wv * 1024 + lane * 16, 0);
            if (__all(mx4(xv, 0u) != SENT)) break;
            __builtin_amdgcn_s_sleep(SLPD);
            if (++spins > kSpinCap) break;
            asm volatile("" ::: "memory");
        }
        wg_barrier();
        unsigned xg = 0u;
        if (wv == 0) { xg = __builtin_amdgcn_raw_buffer_load_b32(rw, x1b + (fb * 512 + n0 + fc) * 2, 0, 16); PIN(xg); } else __builtin_amdgcn_s_sleep(5);
        __builtin_amdgcn_s_sleep(SLPG);
        u32x4 uf[4];
        for (unsigned spins = 0;;) {
#pragma unroll
            for (int q = 0; q < 4; ++q) uf[q] = LD16(q < 3 ? usrc + q * 1024 : usrc3, 0);
            if (__all(mx4(uf[0], mx4(uf[1], mx4(uf[2], mx4(uf[3], 0u)))) != SENT)) break;
            if (++spins > kSpinCap) break;
            asm volatile("" ::: "memory");
        }
        STAMP(29);
        f32x4 d = {0.f, 0.f, 0.f, 0.f}, d2 = {0.f, 0.f, 0.f, 0.f};
#pragma unroll
        for (int ks = 0; ks < 7; ++ks) {
            if (ks & 1) d2 = MFMA16(wfr[ks], BF8(uf[ks >> 1]), d2);
            else d = MFMA16(wfr[ks], BF8(uf[ks >> 1]), d);
        }
        *LP(f32x4, rdw) = nl < 8 ? d : d2;
        wg_barrier();
        if (wv == 0) {
            f32x4 c[8];
#pragma unroll
            for (int w2 = 0; w2 < 8; ++w2) c[w2] = *LP(f32x4, rdr + w2 * 512);
            const f32x4 s4v = ((c[0] + c[1]) + (c[2] + c[3])) + ((c[4] + c[5]) + (c[6] + c[7]));
            f32x2_t sm;
#pragma unroll
            for (int q = 0; q < 2; ++q) { auto t2 = __builtin_amdgcn_permlane32_swap(__float_as_uint(s4v[q]), __float_as_uint(s4v[2 + q]), false, false); sm[q] = __uint_as_float(t2[0]) + __uint_as_float(t2[1]); }
            unsigned t = pk2(sm[0], sm[1]);
            t = pk2(__uint_as_float(gb[0]) * lo16(t), __uint_as_float(gb[1]) * hi16(t));
            *(GAS unsigned*)outp = pk2(lo16(xg) + lo16(t), hi16(xg) + hi16(t));
            STAMP(31);
        }
        STAMP_FLUSH();
    }
}

extern "C" void kernel_launch(void* const* d_in, const int* in_sizes, int n_in,
                              void* d_out, int out_size, void* d_ws, size_t ws_size,
                              hipStream_t stream) {
    Params p;
    p.x = (const unsigned short*)d_in[0];
    p.ckv = (const unsigned short*)d_in[1];
    p.w_q = (const unsigned short*)d_in[2];
    p.w_uk = (const unsigned short*)d_in[3];
    p.w_uv = (const unsigned short*)d_in[4];
    p.w_o = (const unsigned short*)d_in[5];
    p.w1 = (const unsigned short*)d_in[7];
    p.w2 = (const unsigned short*)d_in[8];
    p.c = (const unsigned short*)d_in[9];
    p.w_mod = (const unsigned short*)d_in[10];
    p.ws = (char*)(((size_t)d_ws + 2097151) & ~(size_t)2097151);
    p.out = (unsigned short*)d_out;
    (void)hipFuncSetAttribute((const void*)k_main, hipFuncAttributeMaxDynamicSharedMemorySize, 163840);
    k_main<<<256, 512, 163840, stream>>>(p);
}
```
